```python
import torch

def _softmax(x, axis=-1):
    return torch.softmax(x, dim=axis)

def _gelu(x):
    return torch.nn.functional.gelu(x, approximate='tanh')

def _silu(x):
    return torch.nn.functional.silu(x)

def _sigmoid(x):
    return torch.sigmoid(x)

def _layernorm(x, axis=-1, eps=1e-05):
    m = x.mean(dim=axis, keepdim=True)
    v = x.var(dim=axis, keepdim=True, unbiased=False)
    return (x - m) / torch.sqrt(v + eps)

def _scatter_add(tgt, idx, src, axis=0):
    return tgt.clone().index_add_(axis, idx, src)

def _segment_sum(src, seg, n):
    out = torch.zeros((n,) + src.shape[1:], dtype=src.dtype)
    return out.index_add_(0, seg, src)

def _pad(x, pw):
    flat = tuple((v for pair in reversed(pw) for v in pair))
    return torch.nn.functional.pad(x, flat)
kernel_name = 'parallel_sum__compressed_kv_cla__mixer_gmlp__prompt'

def setup_inputs():

    _w = lambda a: (lambda t: t.to(torch.bfloat16) if t.is_floating_point() else t)(torch.as_tensor(a))

    def _A_setup():
        B, H, Sq, Sk, D, M = (1, 32, 2048, 2048, 128, 640)
        Dc = 4 * D
        return {'x': _w((lambda *s: torch.randn(*s, dtype=torch.bfloat16) * 0.05)(B, Sq, M)), 'c_kv': _w((lambda *s: torch.randn(*s, dtype=torch.bfloat16) * 1.6)(B, Sk, Dc)), 'w_q': _w((lambda *s: torch.randn(*s, dtype=torch.bfloat16) * 1.6)(M, H, D) * M ** (-0.5)), 'w_uk': _w((lambda *s: torch.randn(*s, dtype=torch.bfloat16) * 1.6)(Dc, H, D) * Dc ** (-0.5)), 'w_uv': _w((lambda *s: torch.randn(*s, dtype=torch.bfloat16) * 1.6)(Dc, H, D) * Dc ** (-0.5)), 'w_o': _w((lambda *s: torch.randn(*s, dtype=torch.bfloat16) * 1.6)(H, D, M) * (H * D) ** (-0.5))}
    _A_in = _A_setup()

    def _B_setup():
        B, S, D, F = (1, 2048, 640, 1728)
        Fh = F // 2
        return {'x': _w((lambda *s: torch.randn(*s, dtype=torch.bfloat16) * 0.05)(B, S, D)), 'w_in': _w((lambda *s: torch.randn(*s, dtype=torch.bfloat16) * 0.4)(D, 2 * Fh)), 'w_sp': _w(0.01 * (lambda *s: torch.randn(*s, dtype=torch.bfloat16) * 0.4)(S, S)), 'b_sp': _w(1.0 + 0.01 * (lambda *s: torch.randn(*s, dtype=torch.bfloat16) * 0.05)(S)), 'w_out': _w((lambda *s: torch.randn(*s, dtype=torch.bfloat16) * 0.05)(Fh, D))}
    _B_in = _B_setup()
    return {'A_x': _A_in['x'], 'A_c_kv': _A_in['c_kv'], 'A_w_q': _A_in['w_q'], 'A_w_uk': _A_in['w_uk'], 'A_w_uv': _A_in['w_uv'], 'A_w_o': _A_in['w_o'], 'B_x': _B_in['x'], 'B_w_in': _B_in['w_in'], 'B_w_sp': _B_in['w_sp'], 'B_b_sp': _B_in['b_sp'], 'B_w_out': _B_in['w_out']}

def reference(A_x, A_c_kv, A_w_q, A_w_uk, A_w_uv, A_w_o, B_x, B_w_in, B_w_sp, B_b_sp, B_w_out):

    def _A_ref(x, c_kv, w_q, w_uk, w_uv, w_o):
        B, Sq, M = (x.shape[0], x.shape[1], x.shape[2])
        Sk = c_kv.shape[1]
        H, D = (w_q.shape[1], w_q.shape[2])
        q = torch.einsum('bqm,mhd->bhqd', x, w_q)
        k = torch.einsum('bkc,chd->bhkd', c_kv, w_uk)
        v = torch.einsum('bkc,chd->bhkd', c_kv, w_uv)
        scores = torch.einsum('bhqd,bhkd->bhqk', q, k) * D ** (-0.5)
        off = Sk - Sq
        qi = torch.arange(Sq)[:, None]
        kj = torch.arange(Sk)[None, :]
        cmask = kj <= qi + off
        scores = torch.where(cmask[None, None], scores, -1000000000.0)
        p = _softmax(scores, axis=-1)
        ctx = torch.einsum('bhqk,bhkd->bhqd', p, v)
        return torch.einsum('bhqd,hdm->bqm', ctx, w_o)

    def _B_ref(x, w_in, w_sp, b_sp, w_out):
        B, S, D = x.shape
        F = w_in.shape[-1]
        Fh = F // 2
        z = _gelu(torch.einsum('bsd,df->bsf', x, w_in))
        z1 = z[..., :Fh].clone()
        z2 = _layernorm(z[..., Fh:], axis=-1)
        g = torch.einsum('ts,bsf->btf', w_sp, z2) + b_sp[None, :, None]
        return torch.einsum('bsf,fd->bsd', z1 * g, w_out)
    x = A_x
    xn = _layernorm(x, axis=-1)
    return x + _A_ref(xn, A_c_kv, A_w_q, A_w_uk, A_w_uv, A_w_o) + _B_ref(xn, B_w_in, B_w_sp, B_b_sp, B_w_out)
```

```cpp
#include <hip/hip_runtime.h>
#include <hip/hip_bf16.h>
#include <string.h>
typedef unsigned short u16;
typedef short bf16x8 __attribute__((ext_vector_type(8)));
typedef float f32x4 __attribute__((ext_vector_type(4)));
typedef float f32x16 __attribute__((ext_vector_type(16)));
typedef unsigned u32x4 __attribute__((ext_vector_type(4)));
#define DI __device__ __forceinline__
constexpr int S = 2048, M = 640, H = 32, D = 128, DC = 512, HD = 4096, FH = 864, FP = 896, CUW = 4992;
constexpr size_t OFF_XN = 0;
constexpr size_t OFF_Q = OFF_XN + (size_t)S * M * 2;
constexpr size_t OFF_K = OFF_Q + (size_t)H * S * D * 2;
constexpr size_t OFF_VT = OFF_K + (size_t)H * S * D * 2;
constexpr size_t OFF_CU = OFF_VT + (size_t)H * S * D * 2;
constexpr size_t OFF_Z1 = OFF_CU + (size_t)S * CUW * 2;
constexpr size_t OFF_Z2R = OFF_Z1 + (size_t)S * FP * 2;
constexpr size_t OFF_Z2N = OFF_Z2R + (size_t)S * FP * 2;
DI float bf2f(u16 b) { return __uint_as_float((unsigned)b << 16); }
DI u16 f2bf(float f) { unsigned u = __float_as_uint(f); u += 0x7FFFu + ((u >> 16) & 1u); return (u16)(u >> 16); }
DI int sw23(int p) { return (p & ~12) | ((p & 4) << 1) | ((p & 8) >> 1); }
DI int sig_inv(int d) { return (d & ~31) | (((d >> 4) & 1) << 2) | (((d >> 2) & 3) << 3) | (d & 3); }

__global__ void ln_kernel(const u16* x, u16* y, int ncol, int ldx, int ldy) {
    int row = blockIdx.x * 4 + (threadIdx.x >> 6), lane = threadIdx.x & 63;
    float s = 0;
    for (int c = lane; c < ncol; c += 64) s += bf2f(x[(size_t)row * ldx + c]);
    for (int o = 32; o; o >>= 1) s += __shfl_xor(s, o);
    float m = s / ncol, q = 0;
    for (int c = lane; c < ncol; c += 64) { float d = bf2f(x[(size_t)row * ldx + c]) - m; q += d * d; }
    for (int o = 32; o; o >>= 1) q += __shfl_xor(q, o);
    float r = 1.f / sqrtf(q / ncol + 1e-5f);
    for (int c = lane; c < ncol; c += 64) y[(size_t)row * ldy + c] = f2bf((bf2f(x[(size_t)row * ldx + c]) - m) * r);
}
DI float gelu_t(float v) { float t = 0.7978845608028654f * (v + 0.044715f * v * v * v); return v / (1.f + __expf(-2.f * t)); }

struct EpiQ { u16* o; DI void operator()(int m, int n, float v) const { int h = n >> 7, d = n & 127; o[((size_t)h * S + m) * D + sig_inv(d)] = f2bf(v * 0.12751743082f); } };
struct EpiK { u16* o; DI void operator()(int m, int n, float v) const { int h = n >> 7, d = n & 127; o[((size_t)h * S + m) * D + sig_inv(d)] = f2bf(v); } };
struct EpiV { u16* o; DI void operator()(int m, int n, float v) const { o[(size_t)n * S + sw23(m)] = f2bf(v); } };
struct EpiZ { u16* z1; u16* z2; DI void operator()(int m, int n, float v) const { float g = gelu_t(v); if (n < FH) z1[(size_t)m * FP + n] = f2bf(g); else z2[(size_t)m * FP + n - FH] = f2bf(g); } };
struct EpiU { const u16* z1; const u16* b; u16* cu; DI void operator()(int m, int n, float v) const { cu[(size_t)m * CUW + HD + n] = f2bf(bf2f(z1[(size_t)m * FP + n]) * (v + bf2f(b[m]))); } };
struct EpiO { const u16* x; u16* o; DI void operator()(int m, int n, float v) const { o[(size_t)m * M + n] = f2bf(v + bf2f(x[(size_t)m * M + n])); } };

template <int MODE, class Epi>
__global__ __launch_bounds__(256) void gemm_simple(const u16* A, int lda, const u16* B, const u16* B2, int ldb, int N, int K, Epi epi) {
    __shared__ __attribute__((aligned(16))) u16 sa[64][40], sb[64][40];
    int tid = threadIdx.x, wid = tid >> 6, lane = tid & 63, wr = wid >> 1, wc = wid & 1;
    int m0 = blockIdx.y * 64, n0 = blockIdx.x * 64;
    f32x4 acc[2][2] = {};
    for (int k0 = 0; k0 < K; k0 += 32) {
        { int r = tid >> 2, c = (tid & 3) * 8; *(u32x4*)&sa[r][c] = *(const u32x4*)&A[(size_t)(m0 + r) * lda + k0 + c]; }
        { int kr = tid >> 3, c = (tid & 7) * 8; int k = k0 + kr;
          for (int j = 0; j < 8; ++j) { int n = n0 + c + j; u16 v = 0;
            if (n < N) { if (MODE == 0) v = B[(size_t)k * ldb + n]; else { if (k < HD) v = B[(size_t)sw23(k) * ldb + n]; else if (k < HD + FH) v = B2[(size_t)(k - HD) * ldb + n]; } }
            sb[c + j][kr] = v; } }
        __syncthreads();
        for (int i = 0; i < 2; ++i) for (int j = 0; j < 2; ++j) {
            bf16x8 a = *(const bf16x8*)&sa[wr * 32 + i * 16 + (lane & 15)][(lane >> 4) * 8];
            bf16x8 b = *(const bf16x8*)&sb[wc * 32 + j * 16 + (lane & 15)][(lane >> 4) * 8];
            acc[i][j] = __builtin_amdgcn_mfma_f32_16x16x32_bf16(a, b, acc[i][j], 0, 0, 0);
        }
        __syncthreads();
    }
    for (int i = 0; i < 2; ++i) for (int j = 0; j < 2; ++j) for (int r = 0; r < 4; ++r) {
        int m = m0 + wr * 32 + i * 16 + (lane >> 4) * 4 + r, n = n0 + wc * 32 + j * 16 + (lane & 15);
        if (n < N) epi(m, n, acc[i][j][r]);
    }
}
DI unsigned cvtpk(float lo, float hi) { return (unsigned)f2bf(lo) | ((unsigned)f2bf(hi) << 16); }
__global__ __launch_bounds__(64) void attn_simple(const u16* Q, const u16* K, const u16* VT, u16* cu) {
    int h = blockIdx.y, q0 = blockIdx.x * 32, lane = threadIdx.x, r = lane & 31, hi = lane >> 5;
    bf16x8 qf[8];
    for (int s = 0; s < 8; ++s) qf[s] = *(const bf16x8*)&Q[((size_t)h * S + q0 + r) * D + 16 * s + 8 * hi];
    f32x16 o[4] = {}; float m = -1e30f, l = 0;
    for (int k0 = 0; k0 <= q0; k0 += 32) {
        f32x16 sc = {};
        for (int s = 0; s < 8; ++s) { bf16x8 kf = *(const bf16x8*)&K[((size_t)h * S + k0 + r) * D + 16 * s + 8 * hi];
            sc = __builtin_amdgcn_mfma_f32_32x32x16_bf16(kf, qf[s], sc, 0, 0, 0); }
        float pm = -INFINITY;
        for (int i = 0; i < 16; ++i) { int key = k0 + (i & 3) + 8 * (i >> 2) + 4 * hi; if (key > q0 + r) sc[i] = -INFINITY; pm = fmaxf(pm, sc[i]); }
        pm = fmaxf(pm, __shfl_xor(pm, 32));
        float mn = fmaxf(m, pm), al = exp2f(m - mn); m = mn;
        float ps = 0;
        for (int i = 0; i < 16; ++i) { sc[i] = exp2f(sc[i] - mn); ps += sc[i]; }
        ps += __shfl_xor(ps, 32); l = l * al + ps;
        for (int db = 0; db < 4; ++db) for (int i = 0; i < 16; ++i) o[db][i] *= al;
        for (int s = 0; s < 2; ++s) {
            u32x4 w = {cvtpk(sc[8 * s], sc[8 * s + 1]), cvtpk(sc[8 * s + 2], sc[8 * s + 3]), cvtpk(sc[8 * s + 4], sc[8 * s + 5]), cvtpk(sc[8 * s + 6], sc[8 * s + 7])};
            bf16x8 pf = __builtin_bit_cast(bf16x8, w);
            for (int db = 0; db < 4; ++db) { bf16x8 vf = *(const bf16x8*)&VT[((size_t)h * D + db * 32 + r) * S + k0 + 16 * s + 8 * hi];
                o[db] = __builtin_amdgcn_mfma_f32_32x32x16_bf16(vf, pf, o[db], 0, 0, 0); }
        }
    }
    float il = 1.f / l;
    for (int db = 0; db < 4; ++db) for (int s = 0; s < 2; ++s) {
        u32x4 w = {cvtpk(o[db][8 * s] * il, o[db][8 * s + 1] * il), cvtpk(o[db][8 * s + 2] * il, o[db][8 * s + 3] * il), cvtpk(o[db][8 * s + 4] * il, o[db][8 * s + 5] * il), cvtpk(o[db][8 * s + 6] * il, o[db][8 * s + 7] * il)};
        *(u32x4*)&cu[(size_t)(q0 + r) * CUW + h * D + db * 32 + 16 * s + 8 * hi] = w;
    }
}
__global__ void zero_pad(u16* cu) { int row = blockIdx.x * 8 + (threadIdx.x >> 5), c = threadIdx.x & 31; cu[(size_t)row * CUW + 4960 + c] = 0; }

extern "C" long long ws_offset(const char* n) {
    if (!strcmp(n, "xn")) return OFF_XN; if (!strcmp(n, "q")) return OFF_Q; if (!strcmp(n, "k")) return OFF_K; if (!strcmp(n, "vt")) return OFF_VT;
    if (!strcmp(n, "cu")) return OFF_CU; if (!strcmp(n, "z1")) return OFF_Z1; if (!strcmp(n, "z2n")) return OFF_Z2N; return -1; }

extern "C" void kernel_launch(void* const* d_in, const int* in_sizes, int n_in, void* d_out, int out_size, void* d_ws, size_t ws_size, hipStream_t stream) {
    const u16* x = (const u16*)d_in[0]; const u16* ckv = (const u16*)d_in[1]; const u16* wq = (const u16*)d_in[2]; const u16* wuk = (const u16*)d_in[3];
    const u16* wuv = (const u16*)d_in[4]; const u16* wo = (const u16*)d_in[5]; const u16* win = (const u16*)d_in[7]; const u16* wsp = (const u16*)d_in[8];
    const u16* bsp = (const u16*)d_in[9]; const u16* wout = (const u16*)d_in[10];
    char* ws = (char*)d_ws; u16* xn = (u16*)(ws + OFF_XN); u16* q = (u16*)(ws + OFF_Q); u16* k = (u16*)(ws + OFF_K); u16* vt = (u16*)(ws + OFF_VT);
    u16* cu = (u16*)(ws + OFF_CU); u16* z1 = (u16*)(ws + OFF_Z1); u16* z2r = (u16*)(ws + OFF_Z2R); u16* z2n = (u16*)(ws + OFF_Z2N);
    ln_kernel<<<S / 4, 256, 0, stream>>>(x, xn, M, M, M);
    zero_pad<<<S / 8, 256, 0, stream>>>(cu);
    gemm_simple<0, EpiQ><<<dim3(HD / 64, S / 64), 256, 0, stream>>>(xn, M, wq, nullptr, HD, HD, M, EpiQ{q});
    gemm_simple<0, EpiK><<<dim3(HD / 64, S / 64), 256, 0, stream>>>(ckv, DC, wuk, nullptr, HD, HD, DC, EpiK{k});
    gemm_simple<0, EpiV><<<dim3(HD / 64, S / 64), 256, 0, stream>>>(ckv, DC, wuv, nullptr, HD, HD, DC, EpiV{vt});
    gemm_simple<0, EpiZ><<<dim3(2 * FH / 64, S / 64), 256, 0, stream>>>(xn, M, win, nullptr, 2 * FH, 2 * FH, M, EpiZ{z1, z2r});
    ln_kernel<<<S / 4, 256, 0, stream>>>(z2r, z2n, FH, FP, FP);
    gemm_simple<0, EpiU><<<dim3((FH + 63) / 64, S / 64), 256, 0, stream>>>(wsp, S, z2n, nullptr, FP, FH, S, EpiU{z1, bsp, cu});
    attn_simple<<<dim3(S / 32, H), 64, 0, stream>>>(q, k, vt, cu);
    gemm_simple<1, EpiO><<<dim3(M / 64, S / 64), 256, 0, stream>>>(cu, CUW, wo, wout, M, M, CUW, EpiO{x, (u16*)d_out});
}
```

```cpp
#include <hip/hip_runtime.h>
#include <hip/hip_bf16.h>
#include <string.h>
typedef unsigned short u16;
typedef short bf16x8 __attribute__((ext_vector_type(8)));
typedef float f32x4 __attribute__((ext_vector_type(4)));
typedef unsigned u32x4 __attribute__((ext_vector_type(4)));
typedef unsigned u32x2 __attribute__((ext_vector_type(2)));
typedef float f32x2 __attribute__((ext_vector_type(2)));
typedef __bf16 bf16x2 __attribute__((ext_vector_type(2)));
#define DI __device__ __forceinline__
constexpr int S = 2048, M = 640, H = 32, D = 128, DC = 512, HD = 4096, FH = 864, FP = 896;
constexpr size_t OFF_CTL = 0;
constexpr size_t OFF_XNI = 65536;
constexpr size_t OFF_KV = OFF_XNI + (size_t)20 * S * 64;
constexpr size_t OFF_CU = OFF_KV + (size_t)H * 32 * 32768;
constexpr size_t OFF_Z1 = OFF_CU + (size_t)156 * S * 64;
constexpr size_t OFF_Z2S = OFF_Z1 + (size_t)S * FP * 2;
constexpr size_t OFF_Z2I = OFF_Z2S + (size_t)16 * 7 * 128 * 16;
constexpr size_t OFF_PART = OFF_Z2I + (size_t)64 * 7 * 8192;
constexpr size_t OFF_QPARK = OFF_PART + (size_t)80 * 3 * 8 * 2 * 4096;
constexpr size_t OFF_DBG = OFF_QPARK + (size_t)1024 * 16384;
constexpr size_t WS_USED = OFF_DBG + 1048576;
constexpr float QK_SCALE = 0.12751743082f;
DI float bflo(unsigned u) { return __uint_as_float(u << 16); }
DI float bfhi(unsigned u) { return __uint_as_float(u & 0xffff0000u); }
DI unsigned cvtpk(float lo, float hi) { f32x2 v = {lo, hi}; bf16x2 b = __builtin_convertvector(v, bf16x2); return __builtin_bit_cast(unsigned, b); }
template <int X> DI float xor_lane(float v) { return __int_as_float(__builtin_amdgcn_ds_swizzle(__float_as_int(v), (X << 10) | 0x1f)); }
DI float xor32_add(float v) { auto t = __builtin_amdgcn_permlane32_swap(__float_as_uint(v), __float_as_uint(v), false, false); return __uint_as_float(t[0]) + __uint_as_float(t[1]); }
DI float xor32_max(float v) { auto t = __builtin_amdgcn_permlane32_swap(__float_as_uint(v), __float_as_uint(v), false, false); return fmaxf(__uint_as_float(t[0]), __uint_as_float(t[1])); }
DI float sum32(float v) { v += xor_lane<16>(v); v += xor_lane<8>(v); v += xor_lane<4>(v); v += xor_lane<2>(v); v += xor_lane<1>(v); return v; }
DI int swz4(int row) { return (4 - ((row >> 2) & 3)) & 3; }
#define LO32(p) ((unsigned)(unsigned long long)(p))
#define HI16(p) ((unsigned)((unsigned long long)(p) >> 32) & 0xffffu)

typedef unsigned long long u64;
typedef __attribute__((address_space(1))) u64 gu64;
constexpr int F_EPOCH = 0, F_LN = 16, F_KV = F_LN + 256, F_Z2N = F_KV + 256, F_Z1 = F_Z2N + 112, F_CTX = F_Z1 + 112, F_U = F_CTX + 512, F_END = F_U + 112;
static_assert(F_END * 8 <= 65536, "flag block");
DI gu64* flagp(char* ws, int i) { return (gu64*)(ws + OFF_CTL) + i; }
DI void await_wave(char* ws, int i, u64 tag) {
    for (unsigned spins = 0; spins < (1u << 16); ++spins) {
        const u64 v = i >= 0 ? __hip_atomic_load(flagp(ws, i), __ATOMIC_RELAXED, __HIP_MEMORY_SCOPE_AGENT) : tag;
        if (__all(v == tag)) break;
        __builtin_amdgcn_s_sleep(2);
    }
    asm volatile("" ::: "memory");
}
DI void st16_wt(const __amdgpu_buffer_rsrc_t r, unsigned off, u32x4 v) { __builtin_amdgcn_raw_buffer_store_b128(v, r, off, 0, 16); }
#define ACC_OUT [c0] "=&v"(c[0]), [c1] "=&v"(c[1]), [c2] "=&v"(c[2]), [c3] "=&v"(c[3]), [c4] "=&v"(c[4]), [c5] "=&v"(c[5]), [c6] "=&v"(c[6]), [c7] "=&v"(c[7]), \
                [c8] "=&v"(c[8]), [c9] "=&v"(c[9]), [c10] "=&v"(c[10]), [c11] "=&v"(c[11]), [c12] "=&v"(c[12]), [c13] "=&v"(c[13]), [c14] "=&v"(c[14]), [c15] "=&v"(c[15])
DI f32x4 gelu4(f32x4 v) {
    const f32x4 t = v * (v * v * (-0.035677408136300125f * 2.885390081777927f) + (-0.7978845608028654f * 2.885390081777927f));
    f32x4 d;
#pragma unroll
    for (int r = 0; r < 4; ++r) d[r] = __builtin_amdgcn_exp2f(t[r]);
    d = d + 1.f;
#pragma unroll
    for (int r = 0; r < 4; ++r) d[r] = __builtin_amdgcn_rcpf(d[r]);
    return v * d;
}
DI f32x4 mfma16(bf16x8 a, bf16x8 b, f32x4 c) { return __builtin_amdgcn_mfma_f32_16x16x32_bf16(a, b, c, 0, 0, 0); }
DI void attn_slow(const char* ws, const __amdgpu_buffer_rsrc_t wsr, int lane, int h, int row0a, int row0b, int wave_gid) {
    const int r16 = lane & 15, g = lane >> 4;
    const char* qp = ws + OFF_QPARK + (size_t)wave_gid * 16384;
    const unsigned kv = (unsigned)OFF_KV + h * 1048576;
    for (int blk = 0; blk < 4; ++blk) {
        const int row0 = (blk < 2 ? row0a : row0b) + 16 * (blk & 1), row = row0 + r16;
        bf16x8 q[4];
        for (int G = 0; G < 4; ++G) q[G] = *(const bf16x8*)(qp + (blk * 4 + G) * 1024 + lane * 16);
        f32x4 o[8]; for (int i = 0; i < 8; ++i) o[i] = (f32x4){0.f, 0.f, 0.f, 0.f};
        float mx = -1e30f, l = 0.f;
        for (int k0 = 0; k0 <= row0; k0 += 32) {
            const unsigned tile = kv + (k0 >> 6) * 32768; const int kb = (k0 >> 5) & 1;
            f32x4 sc[2];
            for (int kt = 0; kt < 2; ++kt) { sc[kt] = (f32x4){0.f, 0.f, 0.f, 0.f}; const int key = 32 * kb + 16 * kt + r16;
                for (int G = 0; G < 4; ++G) sc[kt] = mfma16(__builtin_bit_cast(bf16x8, __builtin_amdgcn_raw_buffer_load_b128(wsr, tile + 4096 * G + 64 * key + 16 * (g ^ swz4(r16)), 0, 16)), q[G], sc[kt]); }
            float pm = -INFINITY;
            for (int kt = 0; kt < 2; ++kt) for (int r = 0; r < 4; ++r) { if (k0 + 16 * kt + 4 * g + r > row) sc[kt][r] = -INFINITY; pm = fmaxf(pm, sc[kt][r]); }
            pm = fmaxf(pm, xor_lane<16>(pm)); pm = xor32_max(pm);
            const float mn = fmaxf(mx, pm), al = exp2f(mx - mn); mx = mn;
            float ps = 0.f;
            for (int kt = 0; kt < 2; ++kt) for (int r = 0; r < 4; ++r) { sc[kt][r] = exp2f(sc[kt][r] - mn); ps += sc[kt][r]; }
            l = l * al + ps;
            const u32x4 pw = {cvtpk(sc[0][0], sc[0][1]), cvtpk(sc[0][2], sc[0][3]), cvtpk(sc[1][0], sc[1][1]), cvtpk(sc[1][2], sc[1][3])};
            const bf16x8 pf = __builtin_bit_cast(bf16x8, pw);
            for (int db = 0; db < 8; ++db) { const int d = 16 * db + r16;
                o[db] = mfma16(__builtin_bit_cast(bf16x8, __builtin_amdgcn_raw_buffer_load_b128(wsr, tile + 16384 + 8192 * kb + 64 * d + 16 * (g ^ swz4(r16)), 0, 16)), pf, o[db] * al); }
        }
        l += xor_lane<16>(l); l = xor32_add(l);
        const float il = 1.f / l;
        for (int G = 0; G < 4; ++G) { const f32x4 a = o[2 * G] * il, b = o[2 * G + 1] * il;
            __builtin_amdgcn_raw_buffer_store_b128((u32x4){cvtpk(a[0], a[1]), cvtpk(a[2], a[3]), cvtpk(b[0], b[1]), cvtpk(b[2], b[3])}, wsr, (unsigned)OFF_CU + ((4 * h + G) * S + row) * 64 + 16 * (g ^ swz4(row)), 0, 16); }
    }
}

struct Args { const u16 *x, *ckv, *wq, *wuk, *wuv, *wo, *win, *wsp, *bsp, *wout; char* ws; u16* out; };

#define ACC32_OUT ACC_OUT, [c16] "=&v"(c[16]), [c17] "=&v"(c[17]), [c18] "=&v"(c[18]), [c19] "=&v"(c[19]), [c20] "=&v"(c[20]), [c21] "=&v"(c[21]), [c22] "=&v"(c[22]), [c23] "=&v"(c[23]), \
                [c24] "=&v"(c[24]), [c25] "=&v"(c[25]), [c26] "=&v"(c[26]), [c27] "=&v"(c[27]), [c28] "=&v"(c[28]), [c29] "=&v"(c[29]), [c30] "=&v"(c[30]), [c31] "=&v"(c[31])

DI void q_pre(const u16* wq, char* ws, char* lds, int w, int h, int bs, int bl) {
    const char* nw = (const char*)wq - 896;
    asm volatile(
R"ASM(s_add_u32 s60, %[nws0], %[xnioff]
s_addc_u32 s61, %[nws1], 0
s_and_b32 s61, s61, 0xffff
s_mov_b32 s62, %[xnin]
s_mov_b32 s63, 0x20000
s_mov_b32 s64, %[nw0]
s_mov_b32 s65, %[nw1]
s_mov_b32 s66, %[nwn]
s_mov_b32 s67, 0x20000
s_mov_b32 s70, %[na0]
s_mov_b32 s71, %[na1]
s_mov_b32 s72, %[na2]
s_lshl_b32 s73, %[w], 11
s_add_u32 s69, %[lds], s73
s_add_u32 s68, s69, s73
v_mbcnt_lo_u32_b32 v105, -1, 0
v_mbcnt_hi_u32_b32 v105, -1, v105
v_lshlrev_b32_e32 v100, 4, v105
v_lshrrev_b32_e32 v103, 4, v105
v_and_b32_e32 v103, 1, v103
s_lshl_b32 s73, %[w], 1
v_add_u32_e32 v103, s73, v103
v_and_b32_e32 v103, 3, v103
v_and_b32_e32 v104, 3, v105
v_xor_b32_e32 v103, v103, v104
v_lshrrev_b32_e32 v104, 5, v105
v_lshl_add_u32 v103, v104, 2, v103
v_lshlrev_b32_e32 v103, 4, v103
v_lshrrev_b32_e32 v104, 2, v105
v_and_b32_e32 v104, 7, v104
v_mul_u32_u24_e32 v104, 8192, v104
v_add_u32_e32 v102, v103, v104
v_add_u32_e32 v101, 896, v102
s_add_u32 m0, s68, 114688
s_nop 0
buffer_load_dwordx4 v100, s[60:63], s70 offen sc1 lds
buffer_load_dwordx4 v100, s[60:63], s70 offen offset:1024 sc1 lds
s_add_u32 m0, s68, 116736
s_nop 0
buffer_load_dwordx4 v100, s[60:63], s71 offen sc1 lds
buffer_load_dwordx4 v100, s[60:63], s71 offen offset:1024 sc1 lds
s_add_u32 m0, s69, 131072
s_nop 0
buffer_load_dwordx4 v101, s[64:67], s72 offen lds
buffer_load_dwordx4 v102, s[64:67], s72 offen offset:1024 lds
s_add_u32 s70, s70, 0x20000
s_add_u32 s71, s71, 0x20000
s_add_u32 s72, s72, 262144
s_add_u32 m0, s68, 139264
s_nop 0
buffer_load_dwordx4 v100, s[60:63], s70 offen sc1 lds
buffer_load_dwordx4 v100, s[60:63], s70 offen offset:1024 sc1 lds
s_add_u32 m0, s68, 141312
s_nop 0
buffer_load_dwordx4 v100, s[60:63], s71 offen sc1 lds
buffer_load_dwordx4 v100, s[60:63], s71 offen offset:1024 sc1 lds
s_add_u32 m0, s69, 155648
s_nop 0
buffer_load_dwordx4 v101, s[64:67], s72 offen lds
buffer_load_dwordx4 v102, s[64:67], s72 offen offset:1024 lds
s_add_u32 s70, s70, 0x20000
s_add_u32 s71, s71, 0x20000
s_add_u32 s72, s72, 262144
s_add_u32 m0, s68, 0
s_nop 0
buffer_load_dwordx4 v100, s[60:63], s70 offen sc1 lds
buffer_load_dwordx4 v100, s[60:63], s70 offen offset:1024 sc1 lds
s_add_u32 m0, s68, 2048
s_nop 0
buffer_load_dwordx4 v100, s[60:63], s71 offen sc1 lds
buffer_load_dwordx4 v100, s[60:63], s71 offen offset:1024 sc1 lds
s_add_u32 m0, s69, 16384
s_nop 0
buffer_load_dwordx4 v101, s[64:67], s72 offen lds
buffer_load_dwordx4 v102, s[64:67], s72 offen offset:1024 lds
s_add_u32 s70, s70, 0x20000
s_add_u32 s71, s71, 0x20000
s_add_u32 s72, s72, 262144
s_add_u32 m0, s68, 24576
s_nop 0
buffer_load_dwordx4 v100, s[60:63], s70 offen sc1 lds
buffer_load_dwordx4 v100, s[60:63], s70 offen offset:1024 sc1 lds
s_add_u32 m0, s68, 26624
s_nop 0
buffer_load_dwordx4 v100, s[60:63], s71 offen sc1 lds
buffer_load_dwordx4 v100, s[60:63], s71 offen offset:1024 sc1 lds
s_add_u32 m0, s69, 40960
s_nop 0
buffer_load_dwordx4 v101, s[64:67], s72 offen lds
buffer_load_dwordx4 v102, s[64:67], s72 offen offset:1024 lds
s_add_u32 s70, s70, 0x20000
s_add_u32 s71, s71, 0x20000
s_add_u32 s72, s72, 262144
s_add_u32 m0, s68, 65536
s_nop 0
buffer_load_dwordx4 v100, s[60:63], s70 offen sc1 lds
buffer_load_dwordx4 v100, s[60:63], s70 offen offset:1024 sc1 lds
s_add_u32 m0, s68, 67584
s_nop 0
buffer_load_dwordx4 v100, s[60:63], s71 offen sc1 lds
buffer_load_dwordx4 v100, s[60:63], s71 offen offset:1024 sc1 lds
s_add_u32 m0, s69, 81920
s_nop 0
buffer_load_dwordx4 v101, s[64:67], s72 offen lds
buffer_load_dwordx4 v102, s[64:67], s72 offen offset:1024 lds
s_add_u32 s70, s70, 0x20000
s_add_u32 s71, s71, 0x20000
s_add_u32 s72, s72, 262144
s_add_u32 m0, s68, 90112
s_nop 0
buffer_load_dwordx4 v100, s[60:63], s70 offen sc1 lds
buffer_load_dwordx4 v100, s[60:63], s70 offen offset:1024 sc1 lds
s_add_u32 m0, s68, 92160
s_nop 0
buffer_load_dwordx4 v100, s[60:63], s71 offen sc1 lds
buffer_load_dwordx4 v100, s[60:63], s71 offen offset:1024 sc1 lds
s_add_u32 m0, s69, 106496
s_nop 0
buffer_load_dwordx4 v101, s[64:67], s72 offen lds
buffer_load_dwordx4 v102, s[64:67], s72 offen offset:1024 lds
s_add_u32 s70, s70, 0x20000
s_add_u32 s71, s71, 0x20000
s_add_u32 s72, s72, 262144
)ASM"
        :
        : [nws0] "s"((unsigned)(unsigned long long)ws), [nws1] "s"((unsigned)((unsigned long long)ws >> 32)), [xnioff] "i"((unsigned)OFF_XNI), [xnin] "i"(20 * S * 64),
          [nw0] "s"(LO32(nw)), [nw1] "s"(HI16(nw)), [nwn] "i"(M * HD * 2 + 896), [na0] "s"((128 * bl + 32 * w) * 64), [na1] "s"((128 * bs + 32 * w) * 64), [na2] "s"(h * 256 + (w << 16)),
          [lds] "s"((unsigned)(size_t)lds), [w] "s"(w)
        :
"v100", "v101", "v102", "v103", "v104", "v105", "s60", "s61", "s62", "s63", "s64", "s65", "s66", "s67", "s68", "s69", "s70", "s71", "s72", "s73", "scc", "memory"
    );
}
constexpr unsigned long long PLAN_EVEN = 0x22830595031000fcull, PLAN_EVEN2 = 0x42760da400ed09b2ull, PLAN_ODD = 0x827615b2731000fcull, PLAN_ODD2 = 0xc285a29300ed19a4ull;
typedef __attribute__((address_space(4))) const Args* cargs_t;
#define PHASE_VARS() \
    cargs_t ap = ap0; int bid = bid0, w = wv0; asm volatile("" : "+s"(ap), "+s"(bid), "+s"(w)); \
    const int xcd = bid & 7, idx = bid >> 3, hq = idx & 3, h = xcd * 4 + hq, j = idx >> 2; \
    unsigned lane_; asm volatile("v_mbcnt_lo_u32_b32 %0, -1, 0\n\tv_mbcnt_hi_u32_b32 %0, -1, %0" : "=v"(lane_)); const int lane = (int)lane_, tid = w * 64 + lane; \
    char* const ws = ap->ws; const __amdgpu_buffer_rsrc_t wsr = __builtin_amdgcn_make_buffer_rsrc(ws, (short)0, (int)WS_USED, 0x00020000); \
    const unsigned plan = (unsigned)((((hq & 1) ? PLAN_ODD : PLAN_EVEN) >> (16 * (j & 3))) >> 0) & 0xffffu, plan2 = (unsigned)((((hq & 1) ? PLAN_ODD2 : PLAN_EVEN2) >> (16 * (j & 3)))) & 0xffffu; \
    const unsigned pln = j < 4 ? plan : plan2; \
    const int bs = pln & 15, bl = (pln >> 4) & 15; const bool zrole = (pln >> 8) & 1, srole = (pln >> 9) & 1; \
    const int rankz = (hq >> 1) * 7 + (int)((pln >> 10) & 7), ranks = (hq >> 1) * 7 + (int)((pln >> 13) & 7);     \
    const int rb = xcd * 2 + rankz / 7, ct = rankz % 7, rbs = xcd * 2 + ranks / 7, cts = ranks % 7; \
    (void)lane; (void)tid; (void)h; (void)bs; (void)bl; (void)zrole; (void)srole; (void)rb; (void)ct; (void)rbs; (void)cts; (void)wsr
#define PUBLISH(i) do { asm volatile("s_waitcnt vmcnt(0)" ::: "memory"); __builtin_amdgcn_s_barrier(); asm volatile("" ::: "memory"); \
    if (tid == 0) __hip_atomic_store(flagp(ws, (i)), tag, __ATOMIC_RELAXED, __HIP_MEMORY_SCOPE_AGENT); } while (0)
#define AWAIT(i) do { if (w == 0) await_wave(ws, (i), tag); asm volatile("s_waitcnt lgkmcnt(0)" ::: "memory"); __builtin_amdgcn_s_barrier(); asm volatile("" ::: "memory"); } while (0)
#define CST(k) do {} while (0)
#define CSTW(k, t) do {} while (0)
#define SYNC() do { asm volatile("s_waitcnt lgkmcnt(0)" ::: "memory"); __builtin_amdgcn_s_barrier(); asm volatile("" ::: "memory"); } while (0)

__global__ __launch_bounds__(256, 1) void k_mega(const Args a_by_value) {
    extern __shared__ __attribute__((aligned(16))) char lds[];
    const cargs_t ap0 = (cargs_t)__builtin_amdgcn_kernarg_segment_ptr();
    const int bid0 = blockIdx.x, wv0 = __builtin_amdgcn_readfirstlane(threadIdx.x >> 6);
    unsigned sink = 0;
    {   unsigned long long pc; unsigned len;
        asm volatile("s_getpc_b64 %0\n.Lmega_pc_ref:\n\ts_mov_b32 %1, .Lmega_code_end-.Lmega_pc_ref" : "=s"(pc), "=s"(len));
        const unsigned i = ((blockIdx.x >> 3) * 4 + (threadIdx.x >> 6)) * 64 + (threadIdx.x & 63);
        if (i * 128u < len) asm volatile("global_load_dword %0, %1, off" : "+v"(sink) : "v"(pc + (unsigned long long)i * 128u) : "memory");
    }
    const u64 epoch = __hip_atomic_load(flagp(ap0->ws, F_EPOCH), __ATOMIC_RELAXED, __HIP_MEMORY_SCOPE_AGENT);
    u64 tag;
    { PHASE_VARS(); CST(14);
    }
    {
        PHASE_VARS();
        const int gw = bid * 4 + w, pi = gw * 64 + lane;
        const int c0 = lane & 31, row = 8 * bid + 2 * w + (lane >> 5);
        const char* kb = (const char*)ap->wuk - 896; const char* vb = (const char*)ap->wuv - 896; const u16* ckv = ap->ckv;
        u32x4 v[3]; float f[24]; float s = 0.f;
#pragma unroll
        for (int i = 0; i < 3; ++i) { const int c = c0 + 32 * i; v[i] = (c < 80) ? *(const u32x4*)(ap->x + (size_t)row * M + c * 8) : (u32x4){0, 0, 0, 0}; }
        asm volatile(
R"ASM(s_mov_b32 s36, %[x0]
s_mov_b32 s37, %[x1]
s_mov_b32 s38, %[xn]
s_mov_b32 s39, 0x20000
s_mov_b32 s40, %[k0]
s_mov_b32 s41, %[k1]
s_mov_b32 s42, %[kn]
s_mov_b32 s43, 0x20000
s_mov_b32 s44, %[v0]
s_mov_b32 s45, %[v1]
s_mov_b32 s46, %[vn]
s_mov_b32 s47, 0x20000
s_mov_b32 s51, %[w]
s_lshl_b32 s52, s51, 12
s_add_u32 s48, %[lds], s52
s_lshl_b32 s52, s51, 11
s_add_u32 s54, %[lds], s52
s_lshl_b32 s52, s51, 16
s_add_u32 s49, %[xoff], s52
s_lshl_b32 s52, s51, 16
s_add_u32 s50, %[woff], s52
v_mbcnt_lo_u32_b32 v48, -1, 0
v_mbcnt_hi_u32_b32 v48, -1, v48
v_lshrrev_b32_e32 v53, 4, v48
v_sub_u32_e32 v53, 4, v53
v_and_b32_e32 v53, 3, v53
v_and_b32_e32 v54, 3, v48
v_xor_b32_e32 v53, v53, v54
v_lshlrev_b32_e32 v53, 4, v53
v_lshrrev_b32_e32 v54, 2, v48
v_lshl_add_u32 v42, v54, 10, v53
v_add_u32_e32 v43, 15360, v42
v_add_u32_e32 v44, 30720, v42
v_add_u32_e32 v45, 46080, v42
v_lshrrev_b32_e32 v53, 4, v48
v_and_b32_e32 v53, 1, v53
s_lshl_b32 s52, s51, 1
v_add_u32_e32 v53, s52, v53
v_and_b32_e32 v53, 3, v53
v_and_b32_e32 v54, 3, v48
v_xor_b32_e32 v53, v53, v54
v_lshrrev_b32_e32 v54, 5, v48
v_lshl_add_u32 v53, v54, 2, v53
v_lshlrev_b32_e32 v53, 4, v53
v_lshrrev_b32_e32 v54, 2, v48
v_and_b32_e32 v54, 7, v54
v_lshl_add_u32 v47, v54, 13, v53
v_add_u32_e32 v46, 896, v47
s_add_u32 m0, s48, 0
s_nop 0
buffer_load_dwordx4 v42, s[36:39], s49 offen lds
buffer_load_dwordx4 v43, s[36:39], s49 offen offset:1024 lds
buffer_load_dwordx4 v44, s[36:39], s49 offen offset:2048 lds
buffer_load_dwordx4 v45, s[36:39], s49 offen offset:3072 lds
s_add_u32 m0, s54, 16384
s_nop 0
buffer_load_dwordx4 v46, s[40:43], s50 offen lds
buffer_load_dwordx4 v47, s[40:43], s50 offen offset:1024 lds
s_add_u32 m0, s54, 24576
s_nop 0
buffer_load_dwordx4 v46, s[44:47], s50 offen lds
buffer_load_dwordx4 v47, s[44:47], s50 offen offset:1024 lds
s_add_u32 s49, s49, 64
s_add_u32 s50, s50, 262144
s_add_u32 m0, s48, 32768
s_nop 0
buffer_load_dwordx4 v42, s[36:39], s49 offen lds
buffer_load_dwordx4 v43, s[36:39], s49 offen offset:1024 lds
buffer_load_dwordx4 v44, s[36:39], s49 offen offset:2048 lds
buffer_load_dwordx4 v45, s[36:39], s49 offen offset:3072 lds
s_add_u32 m0, s54, 49152
s_nop 0
buffer_load_dwordx4 v46, s[40:43], s50 offen lds
buffer_load_dwordx4 v47, s[40:43], s50 offen offset:1024 lds
s_add_u32 m0, s54, 57344
s_nop 0
buffer_load_dwordx4 v46, s[44:47], s50 offen lds
buffer_load_dwordx4 v47, s[44:47], s50 offen offset:1024 lds
s_add_u32 s49, s49, 64
s_add_u32 s50, s50, 262144
)ASM"
            :
            : [x0] "s"(LO32(ckv)), [x1] "s"(HI16(ckv)), [xn] "i"(S * DC * 2), [k0] "s"(LO32(kb)), [k1] "s"(HI16(kb)), [kn] "i"(DC * HD * 2 + 896),
              [v0] "s"(LO32(vb)), [v1] "s"(HI16(vb)), [vn] "i"(DC * HD * 2 + 896), [w] "s"(w), [lds] "s"((unsigned)(size_t)lds), [xoff] "s"(j * 256 * DC * 2), [woff] "s"(h * 256)
            :
"v32", "v33", "v34", "v35", "v36", "v37", "v38", "v39", "v40", "v41", "v42", "v43", "v44", "v45", "v46", "v47", "v48", "v49", "v50", "v51", "v52", "v53", "v54", "v55", "v56", "v57", "v58", "v59", "v60", "v61", "v62", "v63", "s36", "s37", "s38", "s39", "s40", "s41", "s42", "s43", "s44", "s45", "s46", "s47", "s48", "s49", "s50", "s51", "s52", "s53", "s54", "s55", "scc", "memory"
        );
        {   const char* pa = (const char*)ckv + (size_t)min(pi, S * DC * 2 / 128 - 1) * 128; const char* pb = (const char*)ap->wuk + (size_t)min(pi, DC * HD * 2 / 128 - 1) * 128;
            const char* pc = (const char*)ap->wuv + (size_t)min(pi, DC * HD * 2 / 128 - 1) * 128;
            asm volatile("global_load_dword %0, %1, off\n\tglobal_load_dword %0, %2, off\n\tglobal_load_dword %0, %3, off" : "+v"(sink) : "v"(pa), "v"(pb), "v"(pc) : "memory"); }
#pragma unroll
        for (int i = 0; i < 3; ++i)
#pragma unroll
            for (int e = 0; e < 4; ++e) { f[8 * i + 2 * e] = bflo(v[i][e]); f[8 * i + 2 * e + 1] = bfhi(v[i][e]); s += f[8 * i + 2 * e] + f[8 * i + 2 * e + 1]; }
        s = sum32(s);
        const float mean = s * (1.f / M); float q = 0.f;
#pragma unroll
        for (int i = 0; i < 3; ++i) if (c0 + 32 * i < 80)
#pragma unroll
            for (int e = 0; e < 8; ++e) { const float d = f[8 * i + e] - mean; q += d * d; }
        q = sum32(q);
        const float r = 1.f / sqrtf(q * (1.f / M) + 1e-5f);
#pragma unroll
        for (int i = 0; i < 3; ++i) { const int c = c0 + 32 * i; u32x4 o;
#pragma unroll
            for (int e = 0; e < 4; ++e) o[e] = cvtpk((f[8 * i + 2 * e] - mean) * r, (f[8 * i + 2 * e + 1] - mean) * r);
            st16_wt(wsr, c < 80 ? (unsigned)OFF_XNI + ((c >> 2) * S + row) * 64 + 16 * ((c & 3) ^ swz4(row)) : 0xfffffff0u, o); }
        { const u64 e = epoch + 0x9E3779B97F4A7C15ull; tag = ((u64)(unsigned)__builtin_amdgcn_readfirstlane((unsigned)(e >> 32)) << 32) | (unsigned)__builtin_amdgcn_readfirstlane((unsigned)e); }
        const unsigned pl = 8u * (lane < 16 ? F_LN + bs * 16 + lane : lane < 32 ? F_LN + bl * 16 + lane - 16 : (lane < 48 && zrole) ? F_LN + rb * 16 + lane - 32 : F_LN + bid);
        const char* p0 = (const char*)ap->wq + (size_t)min(pi, M * HD * 2 / 128 - 1) * 128; const char* p1 = (const char*)ap->win + (size_t)min(pi, M * 2 * FH * 2 / 128 - 1) * 128;
        const char* p2 = (const char*)ap->wsp + (size_t)pi * 128; const char* p3 = (const char*)ap->wo + (size_t)min(pi, HD * M * 2 / 128 - 1) * 128;
        const char* p4 = (const char*)ap->wout + (size_t)min(pi, FH * M * 2 / 128 - 1) * 128;
        const char* ob = ws + OFF_KV; const char* fb = ws + OFF_CTL;
        const char* nw = (const char*)(zrole ? ap->win : ap->wq) - 896;
        const unsigned nwn = zrole ? M * 2 * FH * 2 + 896 : M * HD * 2 + 896;
        const unsigned na0 = zrole ? (rb * 128 + 32 * w) * 64 : (128 * bl + 32 * w) * 64, na1 = zrole ? ct * 256 + 8 * w * (2 * FH * 2) : (128 * bs + 32 * w) * 64,
                       na2 = zrole ? (FH - 32 + ct * 128) * 2 + 8 * w * (2 * FH * 2) : h * 256 + (w << 16);
        asm volatile(
R"ASM(s_mov_b32 s36, %[x0]
s_mov_b32 s37, %[x1]
s_mov_b32 s38, %[xn]
s_mov_b32 s39, 0x20000
s_mov_b32 s40, %[k0]
s_mov_b32 s41, %[k1]
s_mov_b32 s42, %[kn]
s_mov_b32 s43, 0x20000
s_mov_b32 s44, %[v0]
s_mov_b32 s45, %[v1]
s_mov_b32 s46, %[vn]
s_mov_b32 s47, 0x20000
s_mov_b32 s51, %[w]
s_lshl_b32 s52, s51, 12
s_add_u32 s48, %[lds], s52
s_lshl_b32 s52, s51, 11
s_add_u32 s54, %[lds], s52
s_lshl_b32 s52, s51, 16
s_add_u32 s49, %[xoff], s52
s_lshl_b32 s52, s51, 16
s_add_u32 s50, %[woff], s52
v_mbcnt_lo_u32_b32 v48, -1, 0
v_mbcnt_hi_u32_b32 v48, -1, v48
v_lshrrev_b32_e32 v53, 4, v48
v_sub_u32_e32 v53, 4, v53
v_and_b32_e32 v53, 3, v53
v_and_b32_e32 v54, 3, v48
v_xor_b32_e32 v53, v53, v54
v_lshlrev_b32_e32 v53, 4, v53
v_lshrrev_b32_e32 v54, 2, v48
v_lshl_add_u32 v42, v54, 10, v53
v_add_u32_e32 v43, 15360, v42
v_add_u32_e32 v44, 30720, v42
v_add_u32_e32 v45, 46080, v42
v_lshrrev_b32_e32 v53, 4, v48
v_and_b32_e32 v53, 1, v53
s_lshl_b32 s52, s51, 1
v_add_u32_e32 v53, s52, v53
v_and_b32_e32 v53, 3, v53
v_and_b32_e32 v54, 3, v48
v_xor_b32_e32 v53, v53, v54
v_lshrrev_b32_e32 v54, 5, v48
v_lshl_add_u32 v53, v54, 2, v53
v_lshlrev_b32_e32 v53, 4, v53
v_lshrrev_b32_e32 v54, 2, v48
v_and_b32_e32 v54, 7, v54
v_lshl_add_u32 v47, v54, 13, v53
v_add_u32_e32 v46, 896, v47
s_add_u32 s49, s49, 128
s_add_u32 s50, s50, 524288
s_mov_b32 s56, %[tag0]
s_mov_b32 s57, %[tag1]
v_and_b32_e32 v49, 15, v48
v_lshrrev_b32_e32 v50, 4, v48
v_lshrrev_b32_e32 v53, 2, v49
v_sub_u32_e32 v53, 4, v53
v_and_b32_e32 v53, 3, v53
v_xor_b32_e32 v53, v53, v50
v_lshlrev_b32_e32 v53, 4, v53
v_lshl_add_u32 v53, v49, 6, v53
s_lshr_b32 s52, s51, 1
s_lshl_b32 s52, s52, 13
s_add_u32 s52, s52, %[lds]
v_add_u32_e32 v32, s52, v53
v_add_u32_e32 v33, 0x10000, v32
v_lshrrev_b32_e32 v51, 2, v49
v_and_b32_e32 v52, 3, v48
s_and_b32 s52, s51, 1
s_lshl_b32 s52, s52, 10
s_add_u32 s52, s52, %[lds]
s_add_u32 s52, s52, 16384
v_lshlrev_b32_e32 v53, 1, v50
v_add_u32_e32 v53, 0, v53
v_and_b32_e32 v53, 3, v53
v_lshrrev_b32_e32 v54, 1, v52
v_or_b32_e32 v54, 0, v54
v_xor_b32_e32 v53, v53, v54
v_lshlrev_b32_e32 v53, 4, v53
v_and_b32_e32 v54, 1, v52
v_lshl_add_u32 v53, v54, 3, v53
v_lshl_add_u32 v53, v50, 11, v53
v_add_u32_e32 v54, 0, v51
v_lshl_add_u32 v53, v54, 6, v53
v_add_u32_e32 v34, s52, v53
v_add_u32_e32 v38, 0x10000, v34
v_lshlrev_b32_e32 v53, 1, v50
v_add_u32_e32 v53, 0, v53
v_and_b32_e32 v53, 3, v53
v_lshrrev_b32_e32 v54, 1, v52
v_or_b32_e32 v54, 2, v54
v_xor_b32_e32 v53, v53, v54
v_lshlrev_b32_e32 v53, 4, v53
v_and_b32_e32 v54, 1, v52
v_lshl_add_u32 v53, v54, 3, v53
v_lshl_add_u32 v53, v50, 11, v53
v_add_u32_e32 v54, 0, v51
v_lshl_add_u32 v53, v54, 6, v53
v_add_u32_e32 v35, s52, v53
v_add_u32_e32 v39, 0x10000, v35
v_lshlrev_b32_e32 v53, 1, v50
v_add_u32_e32 v53, 1, v53
v_and_b32_e32 v53, 3, v53
v_lshrrev_b32_e32 v54, 1, v52
v_or_b32_e32 v54, 0, v54
v_xor_b32_e32 v53, v53, v54
v_lshlrev_b32_e32 v53, 4, v53
v_and_b32_e32 v54, 1, v52
v_lshl_add_u32 v53, v54, 3, v53
v_lshl_add_u32 v53, v50, 11, v53
v_add_u32_e32 v54, 4, v51
v_lshl_add_u32 v53, v54, 6, v53
v_add_u32_e32 v36, s52, v53
v_add_u32_e32 v40, 0x10000, v36
v_lshlrev_b32_e32 v53, 1, v50
v_add_u32_e32 v53, 1, v53
v_and_b32_e32 v53, 3, v53
v_lshrrev_b32_e32 v54, 1, v52
v_or_b32_e32 v54, 2, v54
v_xor_b32_e32 v53, v53, v54
v_lshlrev_b32_e32 v53, 4, v53
v_and_b32_e32 v54, 1, v52
v_lshl_add_u32 v53, v54, 3, v53
v_lshl_add_u32 v53, v50, 11, v53
v_add_u32_e32 v54, 4, v51
v_lshl_add_u32 v53, v54, 6, v53
v_add_u32_e32 v37, s52, v53
v_add_u32_e32 v41, 0x10000, v37
s_waitcnt vmcnt(3)
s_barrier
ds_read_b128 a[192:195], v32
ds_read_b64_tr_b16 a[128:129], v34
ds_read_b64_tr_b16 a[130:131], v36
ds_read_b64_tr_b16 a[132:133], v35
ds_read_b64_tr_b16 a[134:135], v37
ds_read_b64_tr_b16 a[136:137], v34 offset:512
ds_read_b64_tr_b16 a[138:139], v36 offset:512
ds_read_b64_tr_b16 a[140:141], v35 offset:512
ds_read_b64_tr_b16 a[142:143], v37 offset:512
ds_read_b64_tr_b16 a[144:145], v34 offset:8192
ds_read_b64_tr_b16 a[146:147], v36 offset:8192
ds_read_b64_tr_b16 a[148:149], v35 offset:8192
ds_read_b64_tr_b16 a[150:151], v37 offset:8192
ds_read_b64_tr_b16 a[152:153], v34 offset:8704
ds_read_b64_tr_b16 a[154:155], v36 offset:8704
ds_read_b64_tr_b16 a[156:157], v35 offset:8704
ds_read_b64_tr_b16 a[158:159], v37 offset:8704
ds_read_b128 a[196:199], v32 offset:1024
s_waitcnt lgkmcnt(1)
v_mfma_f32_16x16x32_bf16 v[128:131], a[128:131], a[192:195], 0
s_barrier
v_mfma_f32_16x16x32_bf16 v[132:135], a[132:135], a[192:195], 0
s_add_u32 m0, s48, 65536
v_mfma_f32_16x16x32_bf16 v[136:139], a[136:139], a[192:195], 0
v_mfma_f32_16x16x32_bf16 v[140:143], a[140:143], a[192:195], 0
buffer_load_dwordx4 v42, s[36:39], s49 offen lds
v_mfma_f32_16x16x32_bf16 a[0:3], a[192:195], a[144:147], 0
ds_read_b128 a[200:203], v32 offset:2048
v_mfma_f32_16x16x32_bf16 a[4:7], a[192:195], a[148:151], 0
buffer_load_dwordx4 v43, s[36:39], s49 offen offset:1024 lds
v_mfma_f32_16x16x32_bf16 a[8:11], a[192:195], a[152:155], 0
ds_read_b64_tr_b16 a[160:161], v34 offset:32768
ds_read_b64_tr_b16 a[162:163], v36 offset:32768
v_mfma_f32_16x16x32_bf16 a[12:15], a[192:195], a[156:159], 0
buffer_load_dwordx4 v44, s[36:39], s49 offen offset:2048 lds
s_waitcnt lgkmcnt(3)
v_mfma_f32_16x16x32_bf16 v[144:147], a[128:131], a[196:199], 0
v_mfma_f32_16x16x32_bf16 v[148:151], a[132:135], a[196:199], 0
buffer_load_dwordx4 v45, s[36:39], s49 offen offset:3072 lds
v_mfma_f32_16x16x32_bf16 v[152:155], a[136:139], a[196:199], 0
v_mfma_f32_16x16x32_bf16 v[156:159], a[140:143], a[196:199], 0
s_add_u32 m0, s54, 81920
v_mfma_f32_16x16x32_bf16 a[16:19], a[196:199], a[144:147], 0
ds_read_b128 a[204:207], v32 offset:3072
ds_read_b64_tr_b16 a[164:165], v35 offset:32768
v_mfma_f32_16x16x32_bf16 a[20:23], a[196:199], a[148:151], 0
ds_read_b64_tr_b16 a[166:167], v37 offset:32768
buffer_load_dwordx4 v46, s[40:43], s50 offen lds
v_mfma_f32_16x16x32_bf16 a[24:27], a[196:199], a[152:155], 0
v_mfma_f32_16x16x32_bf16 a[28:31], a[196:199], a[156:159], 0
buffer_load_dwordx4 v47, s[40:43], s50 offen offset:1024 lds
s_waitcnt lgkmcnt(5)
v_mfma_f32_16x16x32_bf16 v[160:163], a[128:131], a[200:203], 0
v_mfma_f32_16x16x32_bf16 v[164:167], a[132:135], a[200:203], 0
s_add_u32 m0, s54, 90112
v_mfma_f32_16x16x32_bf16 v[168:171], a[136:139], a[200:203], 0
ds_read_b64_tr_b16 a[168:169], v34 offset:33280
ds_read_b64_tr_b16 a[170:171], v36 offset:33280
v_mfma_f32_16x16x32_bf16 v[172:175], a[140:143], a[200:203], 0
buffer_load_dwordx4 v46, s[44:47], s50 offen lds
v_mfma_f32_16x16x32_bf16 a[32:35], a[200:203], a[144:147], 0
ds_read_b128 a[208:211], v32 offset:4096
v_mfma_f32_16x16x32_bf16 a[36:39], a[200:203], a[148:151], 0
buffer_load_dwordx4 v47, s[44:47], s50 offen offset:1024 lds
v_mfma_f32_16x16x32_bf16 a[40:43], a[200:203], a[152:155], 0
v_mfma_f32_16x16x32_bf16 a[44:47], a[200:203], a[156:159], 0
s_add_u32 s49, s49, 64
s_waitcnt lgkmcnt(5)
v_mfma_f32_16x16x32_bf16 v[176:179], a[128:131], a[204:207], 0
ds_read_b64_tr_b16 a[172:173], v35 offset:33280
ds_read_b64_tr_b16 a[174:175], v37 offset:33280
v_mfma_f32_16x16x32_bf16 v[180:183], a[132:135], a[204:207], 0
s_add_u32 s50, s50, 262144
v_mfma_f32_16x16x32_bf16 v[184:187], a[136:139], a[204:207], 0
v_mfma_f32_16x16x32_bf16 v[188:191], a[140:143], a[204:207], 0
s_add_u32 m0, s48, 98304
v_mfma_f32_16x16x32_bf16 a[48:51], a[204:207], a[144:147], 0
ds_read_b128 a[212:215], v32 offset:5120
v_mfma_f32_16x16x32_bf16 a[52:55], a[204:207], a[148:151], 0
buffer_load_dwordx4 v42, s[36:39], s49 offen lds
v_mfma_f32_16x16x32_bf16 a[56:59], a[204:207], a[152:155], 0
ds_read_b64_tr_b16 a[176:177], v34 offset:40960
ds_read_b64_tr_b16 a[178:179], v36 offset:40960
v_mfma_f32_16x16x32_bf16 a[60:63], a[204:207], a[156:159], 0
buffer_load_dwordx4 v43, s[36:39], s49 offen offset:1024 lds
s_waitcnt lgkmcnt(5)
v_mfma_f32_16x16x32_bf16 v[192:195], a[128:131], a[208:211], 0
v_mfma_f32_16x16x32_bf16 v[196:199], a[132:135], a[208:211], 0
buffer_load_dwordx4 v44, s[36:39], s49 offen offset:2048 lds
v_mfma_f32_16x16x32_bf16 v[200:203], a[136:139], a[208:211], 0
v_mfma_f32_16x16x32_bf16 v[204:207], a[140:143], a[208:211], 0
buffer_load_dwordx4 v45, s[36:39], s49 offen offset:3072 lds
v_mfma_f32_16x16x32_bf16 a[64:67], a[208:211], a[144:147], 0
ds_read_b128 a[216:219], v32 offset:6144
ds_read_b64_tr_b16 a[180:181], v35 offset:40960
v_mfma_f32_16x16x32_bf16 a[68:71], a[208:211], a[148:151], 0
s_add_u32 m0, s54, 114688
ds_read_b64_tr_b16 a[182:183], v37 offset:40960
v_mfma_f32_16x16x32_bf16 a[72:75], a[208:211], a[152:155], 0
v_mfma_f32_16x16x32_bf16 a[76:79], a[208:211], a[156:159], 0
buffer_load_dwordx4 v46, s[40:43], s50 offen lds
s_waitcnt lgkmcnt(5)
v_mfma_f32_16x16x32_bf16 v[208:211], a[128:131], a[212:215], 0
v_mfma_f32_16x16x32_bf16 v[212:215], a[132:135], a[212:215], 0
buffer_load_dwordx4 v47, s[40:43], s50 offen offset:1024 lds
v_mfma_f32_16x16x32_bf16 v[216:219], a[136:139], a[212:215], 0
ds_read_b64_tr_b16 a[184:185], v34 offset:41472
ds_read_b64_tr_b16 a[186:187], v36 offset:41472
v_mfma_f32_16x16x32_bf16 v[220:223], a[140:143], a[212:215], 0
s_add_u32 m0, s54, 122880
v_mfma_f32_16x16x32_bf16 a[80:83], a[212:215], a[144:147], 0
ds_read_b128 a[220:223], v32 offset:7168
v_mfma_f32_16x16x32_bf16 a[84:87], a[212:215], a[148:151], 0
buffer_load_dwordx4 v46, s[44:47], s50 offen lds
v_mfma_f32_16x16x32_bf16 a[88:91], a[212:215], a[152:155], 0
v_mfma_f32_16x16x32_bf16 a[92:95], a[212:215], a[156:159], 0
buffer_load_dwordx4 v47, s[44:47], s50 offen offset:1024 lds
s_waitcnt lgkmcnt(5)
v_mfma_f32_16x16x32_bf16 v[224:227], a[128:131], a[216:219], 0
ds_read_b64_tr_b16 a[188:189], v35 offset:41472
ds_read_b64_tr_b16 a[190:191], v37 offset:41472
v_mfma_f32_16x16x32_bf16 v[228:231], a[132:135], a[216:219], 0
s_add_u32 s49, s49, 64
v_mfma_f32_16x16x32_bf16 v[232:235], a[136:139], a[216:219], 0
v_mfma_f32_16x16x32_bf16 v[236:239], a[140:143], a[216:219], 0
s_add_u32 s50, s50, 262144
v_mfma_f32_16x16x32_bf16 a[96:99], a[216:219], a[144:147], 0
ds_read_b128 a[192:195], v32 offset:32768
v_mfma_f32_16x16x32_bf16 a[100:103], a[216:219], a[148:151], 0
v_mfma_f32_16x16x32_bf16 a[104:107], a[216:219], a[152:155], 0
v_mfma_f32_16x16x32_bf16 a[108:111], a[216:219], a[156:159], 0
s_waitcnt lgkmcnt(3)
v_mfma_f32_16x16x32_bf16 v[240:243], a[128:131], a[220:223], 0
v_mfma_f32_16x16x32_bf16 v[244:247], a[132:135], a[220:223], 0
v_mfma_f32_16x16x32_bf16 v[248:251], a[136:139], a[220:223], 0
v_mfma_f32_16x16x32_bf16 v[252:255], a[140:143], a[220:223], 0
v_mfma_f32_16x16x32_bf16 a[112:115], a[220:223], a[144:147], 0
ds_read_b128 a[196:199], v32 offset:33792
v_mfma_f32_16x16x32_bf16 a[116:119], a[220:223], a[148:151], 0
v_mfma_f32_16x16x32_bf16 a[120:123], a[220:223], a[152:155], 0
v_mfma_f32_16x16x32_bf16 a[124:127], a[220:223], a[156:159], 0
s_waitcnt lgkmcnt(1)
v_mfma_f32_16x16x32_bf16 v[128:131], a[160:163], a[192:195], v[128:131]
s_waitcnt vmcnt(8)
s_barrier
s_cmp_lg_u32 s51, 0
s_cbranch_scc1 L_fs1_%=
v_mov_b32 v64, s56
v_mov_b32 v65, s57
s_mov_b64 exec, 1
v_mov_b32 v66, %[fln]
global_store_dwordx2 v66, v[64:65], %[flags] sc1
s_mov_b64 exec, -1
L_fs1_%=:
v_mfma_f32_16x16x32_bf16 v[132:135], a[164:167], a[192:195], v[132:135]
s_add_u32 m0, s48, 0
v_mfma_f32_16x16x32_bf16 v[136:139], a[168:171], a[192:195], v[136:139]
v_mfma_f32_16x16x32_bf16 v[140:143], a[172:175], a[192:195], v[140:143]
buffer_load_dwordx4 v42, s[36:39], s49 offen lds
v_mfma_f32_16x16x32_bf16 a[0:3], a[192:195], a[176:179], a[0:3]
ds_read_b128 a[200:203], v32 offset:34816
v_mfma_f32_16x16x32_bf16 a[4:7], a[192:195], a[180:183], a[4:7]
v_mfma_f32_16x16x32_bf16 a[8:11], a[192:195], a[184:187], a[8:11]
ds_read_b64_tr_b16 a[128:129], v38
ds_read_b64_tr_b16 a[130:131], v40
v_mfma_f32_16x16x32_bf16 a[12:15], a[192:195], a[188:191], a[12:15]
s_waitcnt lgkmcnt(3)
v_mfma_f32_16x16x32_bf16 v[144:147], a[160:163], a[196:199], v[144:147]
v_mfma_f32_16x16x32_bf16 v[148:151], a[164:167], a[196:199], v[148:151]
v_mfma_f32_16x16x32_bf16 v[152:155], a[168:171], a[196:199], v[152:155]
v_mfma_f32_16x16x32_bf16 v[156:159], a[172:175], a[196:199], v[156:159]
buffer_load_dwordx4 v43, s[36:39], s49 offen offset:1024 lds
)ASM"
R"ASM(v_mfma_f32_16x16x32_bf16 a[16:19], a[196:199], a[176:179], a[16:19]
ds_read_b128 a[204:207], v32 offset:35840
ds_read_b64_tr_b16 a[132:133], v39
v_mfma_f32_16x16x32_bf16 a[20:23], a[196:199], a[180:183], a[20:23]
ds_read_b64_tr_b16 a[134:135], v41
v_mfma_f32_16x16x32_bf16 a[24:27], a[196:199], a[184:187], a[24:27]
v_mfma_f32_16x16x32_bf16 a[28:31], a[196:199], a[188:191], a[28:31]
s_waitcnt lgkmcnt(5)
v_mfma_f32_16x16x32_bf16 v[160:163], a[160:163], a[200:203], v[160:163]
v_mfma_f32_16x16x32_bf16 v[164:167], a[164:167], a[200:203], v[164:167]
v_mfma_f32_16x16x32_bf16 v[168:171], a[168:171], a[200:203], v[168:171]
ds_read_b64_tr_b16 a[136:137], v38 offset:512
ds_read_b64_tr_b16 a[138:139], v40 offset:512
v_mfma_f32_16x16x32_bf16 v[172:175], a[172:175], a[200:203], v[172:175]
buffer_load_dwordx4 v44, s[36:39], s49 offen offset:2048 lds
v_mfma_f32_16x16x32_bf16 a[32:35], a[200:203], a[176:179], a[32:35]
ds_read_b128 a[208:211], v32 offset:36864
v_mfma_f32_16x16x32_bf16 a[36:39], a[200:203], a[180:183], a[36:39]
v_mfma_f32_16x16x32_bf16 a[40:43], a[200:203], a[184:187], a[40:43]
v_mfma_f32_16x16x32_bf16 a[44:47], a[200:203], a[188:191], a[44:47]
s_waitcnt lgkmcnt(5)
v_mfma_f32_16x16x32_bf16 v[176:179], a[160:163], a[204:207], v[176:179]
ds_read_b64_tr_b16 a[140:141], v39 offset:512
ds_read_b64_tr_b16 a[142:143], v41 offset:512
v_mfma_f32_16x16x32_bf16 v[180:183], a[164:167], a[204:207], v[180:183]
v_mfma_f32_16x16x32_bf16 v[184:187], a[168:171], a[204:207], v[184:187]
v_mfma_f32_16x16x32_bf16 v[188:191], a[172:175], a[204:207], v[188:191]
buffer_load_dwordx4 v45, s[36:39], s49 offen offset:3072 lds
v_mfma_f32_16x16x32_bf16 a[48:51], a[204:207], a[176:179], a[48:51]
ds_read_b128 a[212:215], v32 offset:37888
v_mfma_f32_16x16x32_bf16 a[52:55], a[204:207], a[180:183], a[52:55]
v_mfma_f32_16x16x32_bf16 a[56:59], a[204:207], a[184:187], a[56:59]
ds_read_b64_tr_b16 a[144:145], v38 offset:8192
ds_read_b64_tr_b16 a[146:147], v40 offset:8192
v_mfma_f32_16x16x32_bf16 a[60:63], a[204:207], a[188:191], a[60:63]
s_waitcnt lgkmcnt(5)
v_mfma_f32_16x16x32_bf16 v[192:195], a[160:163], a[208:211], v[192:195]
v_mfma_f32_16x16x32_bf16 v[196:199], a[164:167], a[208:211], v[196:199]
s_add_u32 m0, s54, 16384
v_mfma_f32_16x16x32_bf16 v[200:203], a[168:171], a[208:211], v[200:203]
v_mfma_f32_16x16x32_bf16 v[204:207], a[172:175], a[208:211], v[204:207]
buffer_load_dwordx4 v46, s[40:43], s50 offen lds
v_mfma_f32_16x16x32_bf16 a[64:67], a[208:211], a[176:179], a[64:67]
ds_read_b128 a[216:219], v32 offset:38912
ds_read_b64_tr_b16 a[148:149], v39 offset:8192
v_mfma_f32_16x16x32_bf16 a[68:71], a[208:211], a[180:183], a[68:71]
ds_read_b64_tr_b16 a[150:151], v41 offset:8192
v_mfma_f32_16x16x32_bf16 a[72:75], a[208:211], a[184:187], a[72:75]
v_mfma_f32_16x16x32_bf16 a[76:79], a[208:211], a[188:191], a[76:79]
s_waitcnt lgkmcnt(5)
v_mfma_f32_16x16x32_bf16 v[208:211], a[160:163], a[212:215], v[208:211]
v_mfma_f32_16x16x32_bf16 v[212:215], a[164:167], a[212:215], v[212:215]
v_mfma_f32_16x16x32_bf16 v[216:219], a[168:171], a[212:215], v[216:219]
ds_read_b64_tr_b16 a[152:153], v38 offset:8704
ds_read_b64_tr_b16 a[154:155], v40 offset:8704
v_mfma_f32_16x16x32_bf16 v[220:223], a[172:175], a[212:215], v[220:223]
buffer_load_dwordx4 v47, s[40:43], s50 offen offset:1024 lds
v_mfma_f32_16x16x32_bf16 a[80:83], a[212:215], a[176:179], a[80:83]
ds_read_b128 a[220:223], v32 offset:39936
v_mfma_f32_16x16x32_bf16 a[84:87], a[212:215], a[180:183], a[84:87]
v_mfma_f32_16x16x32_bf16 a[88:91], a[212:215], a[184:187], a[88:91]
v_mfma_f32_16x16x32_bf16 a[92:95], a[212:215], a[188:191], a[92:95]
s_waitcnt lgkmcnt(5)
v_mfma_f32_16x16x32_bf16 v[224:227], a[160:163], a[216:219], v[224:227]
s_add_u32 m0, s54, 24576
ds_read_b64_tr_b16 a[156:157], v39 offset:8704
ds_read_b64_tr_b16 a[158:159], v41 offset:8704
v_mfma_f32_16x16x32_bf16 v[228:231], a[164:167], a[216:219], v[228:231]
v_mfma_f32_16x16x32_bf16 v[232:235], a[168:171], a[216:219], v[232:235]
buffer_load_dwordx4 v46, s[44:47], s50 offen lds
v_mfma_f32_16x16x32_bf16 v[236:239], a[172:175], a[216:219], v[236:239]
v_mfma_f32_16x16x32_bf16 a[96:99], a[216:219], a[176:179], a[96:99]
ds_read_b128 a[192:195], v33
v_mfma_f32_16x16x32_bf16 a[100:103], a[216:219], a[180:183], a[100:103]
v_mfma_f32_16x16x32_bf16 a[104:107], a[216:219], a[184:187], a[104:107]
v_mfma_f32_16x16x32_bf16 a[108:111], a[216:219], a[188:191], a[108:111]
s_waitcnt lgkmcnt(3)
v_mfma_f32_16x16x32_bf16 v[240:243], a[160:163], a[220:223], v[240:243]
v_mfma_f32_16x16x32_bf16 v[244:247], a[164:167], a[220:223], v[244:247]
buffer_load_dwordx4 v47, s[44:47], s50 offen offset:1024 lds
v_mfma_f32_16x16x32_bf16 v[248:251], a[168:171], a[220:223], v[248:251]
v_mfma_f32_16x16x32_bf16 v[252:255], a[172:175], a[220:223], v[252:255]
s_add_u32 s49, s49, 64
v_mfma_f32_16x16x32_bf16 a[112:115], a[220:223], a[176:179], a[112:115]
s_add_u32 s50, s50, 262144
ds_read_b128 a[196:199], v33 offset:1024
v_mfma_f32_16x16x32_bf16 a[116:119], a[220:223], a[180:183], a[116:119]
v_mfma_f32_16x16x32_bf16 a[120:123], a[220:223], a[184:187], a[120:123]
v_mfma_f32_16x16x32_bf16 a[124:127], a[220:223], a[188:191], a[124:127]
s_waitcnt lgkmcnt(1)
v_mfma_f32_16x16x32_bf16 v[128:131], a[128:131], a[192:195], v[128:131]
s_waitcnt vmcnt(8)
s_barrier
v_mfma_f32_16x16x32_bf16 v[132:135], a[132:135], a[192:195], v[132:135]
s_add_u32 m0, s48, 32768
v_mfma_f32_16x16x32_bf16 v[136:139], a[136:139], a[192:195], v[136:139]
v_mfma_f32_16x16x32_bf16 v[140:143], a[140:143], a[192:195], v[140:143]
buffer_load_dwordx4 v42, s[36:39], s49 offen lds
v_mfma_f32_16x16x32_bf16 a[0:3], a[192:195], a[144:147], a[0:3]
ds_read_b128 a[200:203], v33 offset:2048
v_mfma_f32_16x16x32_bf16 a[4:7], a[192:195], a[148:151], a[4:7]
v_mfma_f32_16x16x32_bf16 a[8:11], a[192:195], a[152:155], a[8:11]
ds_read_b64_tr_b16 a[160:161], v38 offset:32768
ds_read_b64_tr_b16 a[162:163], v40 offset:32768
v_mfma_f32_16x16x32_bf16 a[12:15], a[192:195], a[156:159], a[12:15]
s_waitcnt lgkmcnt(3)
v_mfma_f32_16x16x32_bf16 v[144:147], a[128:131], a[196:199], v[144:147]
v_mfma_f32_16x16x32_bf16 v[148:151], a[132:135], a[196:199], v[148:151]
v_mfma_f32_16x16x32_bf16 v[152:155], a[136:139], a[196:199], v[152:155]
v_mfma_f32_16x16x32_bf16 v[156:159], a[140:143], a[196:199], v[156:159]
buffer_load_dwordx4 v43, s[36:39], s49 offen offset:1024 lds
v_mfma_f32_16x16x32_bf16 a[16:19], a[196:199], a[144:147], a[16:19]
ds_read_b128 a[204:207], v33 offset:3072
ds_read_b64_tr_b16 a[164:165], v39 offset:32768
v_mfma_f32_16x16x32_bf16 a[20:23], a[196:199], a[148:151], a[20:23]
ds_read_b64_tr_b16 a[166:167], v41 offset:32768
v_mfma_f32_16x16x32_bf16 a[24:27], a[196:199], a[152:155], a[24:27]
v_mfma_f32_16x16x32_bf16 a[28:31], a[196:199], a[156:159], a[28:31]
s_waitcnt lgkmcnt(5)
v_mfma_f32_16x16x32_bf16 v[160:163], a[128:131], a[200:203], v[160:163]
v_mfma_f32_16x16x32_bf16 v[164:167], a[132:135], a[200:203], v[164:167]
v_mfma_f32_16x16x32_bf16 v[168:171], a[136:139], a[200:203], v[168:171]
ds_read_b64_tr_b16 a[168:169], v38 offset:33280
ds_read_b64_tr_b16 a[170:171], v40 offset:33280
v_mfma_f32_16x16x32_bf16 v[172:175], a[140:143], a[200:203], v[172:175]
buffer_load_dwordx4 v44, s[36:39], s49 offen offset:2048 lds
v_mfma_f32_16x16x32_bf16 a[32:35], a[200:203], a[144:147], a[32:35]
ds_read_b128 a[208:211], v33 offset:4096
v_mfma_f32_16x16x32_bf16 a[36:39], a[200:203], a[148:151], a[36:39]
v_mfma_f32_16x16x32_bf16 a[40:43], a[200:203], a[152:155], a[40:43]
v_mfma_f32_16x16x32_bf16 a[44:47], a[200:203], a[156:159], a[44:47]
s_waitcnt lgkmcnt(5)
v_mfma_f32_16x16x32_bf16 v[176:179], a[128:131], a[204:207], v[176:179]
ds_read_b64_tr_b16 a[172:173], v39 offset:33280
ds_read_b64_tr_b16 a[174:175], v41 offset:33280
v_mfma_f32_16x16x32_bf16 v[180:183], a[132:135], a[204:207], v[180:183]
v_mfma_f32_16x16x32_bf16 v[184:187], a[136:139], a[204:207], v[184:187]
v_mfma_f32_16x16x32_bf16 v[188:191], a[140:143], a[204:207], v[188:191]
buffer_load_dwordx4 v45, s[36:39], s49 offen offset:3072 lds
v_mfma_f32_16x16x32_bf16 a[48:51], a[204:207], a[144:147], a[48:51]
ds_read_b128 a[212:215], v33 offset:5120
v_mfma_f32_16x16x32_bf16 a[52:55], a[204:207], a[148:151], a[52:55]
v_mfma_f32_16x16x32_bf16 a[56:59], a[204:207], a[152:155], a[56:59]
ds_read_b64_tr_b16 a[176:177], v38 offset:40960
ds_read_b64_tr_b16 a[178:179], v40 offset:40960
v_mfma_f32_16x16x32_bf16 a[60:63], a[204:207], a[156:159], a[60:63]
s_waitcnt lgkmcnt(5)
v_mfma_f32_16x16x32_bf16 v[192:195], a[128:131], a[208:211], v[192:195]
v_mfma_f32_16x16x32_bf16 v[196:199], a[132:135], a[208:211], v[196:199]
s_add_u32 m0, s54, 49152
v_mfma_f32_16x16x32_bf16 v[200:203], a[136:139], a[208:211], v[200:203]
v_mfma_f32_16x16x32_bf16 v[204:207], a[140:143], a[208:211], v[204:207]
buffer_load_dwordx4 v46, s[40:43], s50 offen lds
v_mfma_f32_16x16x32_bf16 a[64:67], a[208:211], a[144:147], a[64:67]
ds_read_b128 a[216:219], v33 offset:6144
ds_read_b64_tr_b16 a[180:181], v39 offset:40960
v_mfma_f32_16x16x32_bf16 a[68:71], a[208:211], a[148:151], a[68:71]
ds_read_b64_tr_b16 a[182:183], v41 offset:40960
v_mfma_f32_16x16x32_bf16 a[72:75], a[208:211], a[152:155], a[72:75]
v_mfma_f32_16x16x32_bf16 a[76:79], a[208:211], a[156:159], a[76:79]
s_waitcnt lgkmcnt(5)
v_mfma_f32_16x16x32_bf16 v[208:211], a[128:131], a[212:215], v[208:211]
v_mfma_f32_16x16x32_bf16 v[212:215], a[132:135], a[212:215], v[212:215]
v_mfma_f32_16x16x32_bf16 v[216:219], a[136:139], a[212:215], v[216:219]
ds_read_b64_tr_b16 a[184:185], v38 offset:41472
ds_read_b64_tr_b16 a[186:187], v40 offset:41472
v_mfma_f32_16x16x32_bf16 v[220:223], a[140:143], a[212:215], v[220:223]
buffer_load_dwordx4 v47, s[40:43], s50 offen offset:1024 lds
v_mfma_f32_16x16x32_bf16 a[80:83], a[212:215], a[144:147], a[80:83]
ds_read_b128 a[220:223], v33 offset:7168
v_mfma_f32_16x16x32_bf16 a[84:87], a[212:215], a[148:151], a[84:87]
v_mfma_f32_16x16x32_bf16 a[88:91], a[212:215], a[152:155], a[88:91]
v_mfma_f32_16x16x32_bf16 a[92:95], a[212:215], a[156:159], a[92:95]
s_waitcnt lgkmcnt(5)
v_mfma_f32_16x16x32_bf16 v[224:227], a[128:131], a[216:219], v[224:227]
s_add_u32 m0, s54, 57344
ds_read_b64_tr_b16 a[188:189], v39 offset:41472
ds_read_b64_tr_b16 a[190:191], v41 offset:41472
v_mfma_f32_16x16x32_bf16 v[228:231], a[132:135], a[216:219], v[228:231]
v_mfma_f32_16x16x32_bf16 v[232:235], a[136:139], a[216:219], v[232:235]
buffer_load_dwordx4 v46, s[44:47], s50 offen lds
v_mfma_f32_16x16x32_bf16 v[236:239], a[140:143], a[216:219], v[236:239]
v_mfma_f32_16x16x32_bf16 a[96:99], a[216:219], a[144:147], a[96:99]
ds_read_b128 a[192:195], v33 offset:32768
v_mfma_f32_16x16x32_bf16 a[100:103], a[216:219], a[148:151], a[100:103]
v_mfma_f32_16x16x32_bf16 a[104:107], a[216:219], a[152:155], a[104:107]
v_mfma_f32_16x16x32_bf16 a[108:111], a[216:219], a[156:159], a[108:111]
s_waitcnt lgkmcnt(3)
v_mfma_f32_16x16x32_bf16 v[240:243], a[128:131], a[220:223], v[240:243]
v_mfma_f32_16x16x32_bf16 v[244:247], a[132:135], a[220:223], v[244:247]
buffer_load_dwordx4 v47, s[44:47], s50 offen offset:1024 lds
v_mfma_f32_16x16x32_bf16 v[248:251], a[136:139], a[220:223], v[248:251]
v_mfma_f32_16x16x32_bf16 v[252:255], a[140:143], a[220:223], v[252:255]
s_add_u32 s49, s49, 64
v_mfma_f32_16x16x32_bf16 a[112:115], a[220:223], a[144:147], a[112:115]
s_add_u32 s50, s50, 262144
ds_read_b128 a[196:199], v33 offset:33792
v_mfma_f32_16x16x32_bf16 a[116:119], a[220:223], a[148:151], a[116:119]
v_mfma_f32_16x16x32_bf16 a[120:123], a[220:223], a[152:155], a[120:123]
v_mfma_f32_16x16x32_bf16 a[124:127], a[220:223], a[156:159], a[124:127]
s_waitcnt lgkmcnt(1)
v_mfma_f32_16x16x32_bf16 v[128:131], a[160:163], a[192:195], v[128:131]
s_waitcnt vmcnt(8)
s_barrier
v_mfma_f32_16x16x32_bf16 v[132:135], a[164:167], a[192:195], v[132:135]
s_add_u32 m0, s48, 65536
v_mfma_f32_16x16x32_bf16 v[136:139], a[168:171], a[192:195], v[136:139]
v_mfma_f32_16x16x32_bf16 v[140:143], a[172:175], a[192:195], v[140:143]
buffer_load_dwordx4 v42, s[36:39], s49 offen lds
v_mfma_f32_16x16x32_bf16 a[0:3], a[192:195], a[176:179], a[0:3]
ds_read_b128 a[200:203], v33 offset:34816
)ASM"
R"ASM(v_mfma_f32_16x16x32_bf16 a[4:7], a[192:195], a[180:183], a[4:7]
v_mfma_f32_16x16x32_bf16 a[8:11], a[192:195], a[184:187], a[8:11]
ds_read_b64_tr_b16 a[128:129], v34
ds_read_b64_tr_b16 a[130:131], v36
v_mfma_f32_16x16x32_bf16 a[12:15], a[192:195], a[188:191], a[12:15]
s_waitcnt lgkmcnt(3)
v_mfma_f32_16x16x32_bf16 v[144:147], a[160:163], a[196:199], v[144:147]
v_mfma_f32_16x16x32_bf16 v[148:151], a[164:167], a[196:199], v[148:151]
v_mfma_f32_16x16x32_bf16 v[152:155], a[168:171], a[196:199], v[152:155]
v_mfma_f32_16x16x32_bf16 v[156:159], a[172:175], a[196:199], v[156:159]
buffer_load_dwordx4 v43, s[36:39], s49 offen offset:1024 lds
v_mfma_f32_16x16x32_bf16 a[16:19], a[196:199], a[176:179], a[16:19]
ds_read_b128 a[204:207], v33 offset:35840
ds_read_b64_tr_b16 a[132:133], v35
v_mfma_f32_16x16x32_bf16 a[20:23], a[196:199], a[180:183], a[20:23]
ds_read_b64_tr_b16 a[134:135], v37
v_mfma_f32_16x16x32_bf16 a[24:27], a[196:199], a[184:187], a[24:27]
v_mfma_f32_16x16x32_bf16 a[28:31], a[196:199], a[188:191], a[28:31]
s_waitcnt lgkmcnt(5)
v_mfma_f32_16x16x32_bf16 v[160:163], a[160:163], a[200:203], v[160:163]
v_mfma_f32_16x16x32_bf16 v[164:167], a[164:167], a[200:203], v[164:167]
v_mfma_f32_16x16x32_bf16 v[168:171], a[168:171], a[200:203], v[168:171]
ds_read_b64_tr_b16 a[136:137], v34 offset:512
ds_read_b64_tr_b16 a[138:139], v36 offset:512
v_mfma_f32_16x16x32_bf16 v[172:175], a[172:175], a[200:203], v[172:175]
buffer_load_dwordx4 v44, s[36:39], s49 offen offset:2048 lds
v_mfma_f32_16x16x32_bf16 a[32:35], a[200:203], a[176:179], a[32:35]
ds_read_b128 a[208:211], v33 offset:36864
v_mfma_f32_16x16x32_bf16 a[36:39], a[200:203], a[180:183], a[36:39]
v_mfma_f32_16x16x32_bf16 a[40:43], a[200:203], a[184:187], a[40:43]
v_mfma_f32_16x16x32_bf16 a[44:47], a[200:203], a[188:191], a[44:47]
s_waitcnt lgkmcnt(5)
v_mfma_f32_16x16x32_bf16 v[176:179], a[160:163], a[204:207], v[176:179]
ds_read_b64_tr_b16 a[140:141], v35 offset:512
ds_read_b64_tr_b16 a[142:143], v37 offset:512
v_mfma_f32_16x16x32_bf16 v[180:183], a[164:167], a[204:207], v[180:183]
v_mfma_f32_16x16x32_bf16 v[184:187], a[168:171], a[204:207], v[184:187]
v_mfma_f32_16x16x32_bf16 v[188:191], a[172:175], a[204:207], v[188:191]
buffer_load_dwordx4 v45, s[36:39], s49 offen offset:3072 lds
v_mfma_f32_16x16x32_bf16 a[48:51], a[204:207], a[176:179], a[48:51]
ds_read_b128 a[212:215], v33 offset:37888
v_mfma_f32_16x16x32_bf16 a[52:55], a[204:207], a[180:183], a[52:55]
v_mfma_f32_16x16x32_bf16 a[56:59], a[204:207], a[184:187], a[56:59]
ds_read_b64_tr_b16 a[144:145], v34 offset:8192
ds_read_b64_tr_b16 a[146:147], v36 offset:8192
v_mfma_f32_16x16x32_bf16 a[60:63], a[204:207], a[188:191], a[60:63]
s_waitcnt lgkmcnt(5)
v_mfma_f32_16x16x32_bf16 v[192:195], a[160:163], a[208:211], v[192:195]
v_mfma_f32_16x16x32_bf16 v[196:199], a[164:167], a[208:211], v[196:199]
s_add_u32 m0, s54, 81920
v_mfma_f32_16x16x32_bf16 v[200:203], a[168:171], a[208:211], v[200:203]
v_mfma_f32_16x16x32_bf16 v[204:207], a[172:175], a[208:211], v[204:207]
buffer_load_dwordx4 v46, s[40:43], s50 offen lds
v_mfma_f32_16x16x32_bf16 a[64:67], a[208:211], a[176:179], a[64:67]
ds_read_b128 a[216:219], v33 offset:38912
ds_read_b64_tr_b16 a[148:149], v35 offset:8192
v_mfma_f32_16x16x32_bf16 a[68:71], a[208:211], a[180:183], a[68:71]
ds_read_b64_tr_b16 a[150:151], v37 offset:8192
v_mfma_f32_16x16x32_bf16 a[72:75], a[208:211], a[184:187], a[72:75]
v_mfma_f32_16x16x32_bf16 a[76:79], a[208:211], a[188:191], a[76:79]
s_waitcnt lgkmcnt(5)
v_mfma_f32_16x16x32_bf16 v[208:211], a[160:163], a[212:215], v[208:211]
v_mfma_f32_16x16x32_bf16 v[212:215], a[164:167], a[212:215], v[212:215]
v_mfma_f32_16x16x32_bf16 v[216:219], a[168:171], a[212:215], v[216:219]
ds_read_b64_tr_b16 a[152:153], v34 offset:8704
ds_read_b64_tr_b16 a[154:155], v36 offset:8704
v_mfma_f32_16x16x32_bf16 v[220:223], a[172:175], a[212:215], v[220:223]
buffer_load_dwordx4 v47, s[40:43], s50 offen offset:1024 lds
v_mfma_f32_16x16x32_bf16 a[80:83], a[212:215], a[176:179], a[80:83]
ds_read_b128 a[220:223], v33 offset:39936
v_mfma_f32_16x16x32_bf16 a[84:87], a[212:215], a[180:183], a[84:87]
v_mfma_f32_16x16x32_bf16 a[88:91], a[212:215], a[184:187], a[88:91]
v_mfma_f32_16x16x32_bf16 a[92:95], a[212:215], a[188:191], a[92:95]
s_waitcnt lgkmcnt(5)
v_mfma_f32_16x16x32_bf16 v[224:227], a[160:163], a[216:219], v[224:227]
s_add_u32 m0, s54, 90112
ds_read_b64_tr_b16 a[156:157], v35 offset:8704
ds_read_b64_tr_b16 a[158:159], v37 offset:8704
v_mfma_f32_16x16x32_bf16 v[228:231], a[164:167], a[216:219], v[228:231]
v_mfma_f32_16x16x32_bf16 v[232:235], a[168:171], a[216:219], v[232:235]
buffer_load_dwordx4 v46, s[44:47], s50 offen lds
v_mfma_f32_16x16x32_bf16 v[236:239], a[172:175], a[216:219], v[236:239]
v_mfma_f32_16x16x32_bf16 a[96:99], a[216:219], a[176:179], a[96:99]
ds_read_b128 a[192:195], v32
v_mfma_f32_16x16x32_bf16 a[100:103], a[216:219], a[180:183], a[100:103]
v_mfma_f32_16x16x32_bf16 a[104:107], a[216:219], a[184:187], a[104:107]
v_mfma_f32_16x16x32_bf16 a[108:111], a[216:219], a[188:191], a[108:111]
s_waitcnt lgkmcnt(3)
v_mfma_f32_16x16x32_bf16 v[240:243], a[160:163], a[220:223], v[240:243]
v_mfma_f32_16x16x32_bf16 v[244:247], a[164:167], a[220:223], v[244:247]
buffer_load_dwordx4 v47, s[44:47], s50 offen offset:1024 lds
v_mfma_f32_16x16x32_bf16 v[248:251], a[168:171], a[220:223], v[248:251]
v_mfma_f32_16x16x32_bf16 v[252:255], a[172:175], a[220:223], v[252:255]
s_add_u32 s49, s49, 64
v_mfma_f32_16x16x32_bf16 a[112:115], a[220:223], a[176:179], a[112:115]
s_add_u32 s50, s50, 262144
ds_read_b128 a[196:199], v32 offset:1024
v_mfma_f32_16x16x32_bf16 a[116:119], a[220:223], a[180:183], a[116:119]
v_mfma_f32_16x16x32_bf16 a[120:123], a[220:223], a[184:187], a[120:123]
global_load_dword %[sink], %[pf0], off
v_mfma_f32_16x16x32_bf16 a[124:127], a[220:223], a[188:191], a[124:127]
s_waitcnt lgkmcnt(1)
v_mfma_f32_16x16x32_bf16 v[128:131], a[128:131], a[192:195], v[128:131]
s_waitcnt vmcnt(9)
s_barrier
v_mfma_f32_16x16x32_bf16 v[132:135], a[132:135], a[192:195], v[132:135]
s_add_u32 m0, s48, 98304
v_mfma_f32_16x16x32_bf16 v[136:139], a[136:139], a[192:195], v[136:139]
v_mfma_f32_16x16x32_bf16 v[140:143], a[140:143], a[192:195], v[140:143]
buffer_load_dwordx4 v42, s[36:39], s49 offen lds
v_mfma_f32_16x16x32_bf16 a[0:3], a[192:195], a[144:147], a[0:3]
ds_read_b128 a[200:203], v32 offset:2048
v_mfma_f32_16x16x32_bf16 a[4:7], a[192:195], a[148:151], a[4:7]
v_mfma_f32_16x16x32_bf16 a[8:11], a[192:195], a[152:155], a[8:11]
ds_read_b64_tr_b16 a[160:161], v34 offset:32768
ds_read_b64_tr_b16 a[162:163], v36 offset:32768
v_mfma_f32_16x16x32_bf16 a[12:15], a[192:195], a[156:159], a[12:15]
s_waitcnt lgkmcnt(3)
v_mfma_f32_16x16x32_bf16 v[144:147], a[128:131], a[196:199], v[144:147]
v_mfma_f32_16x16x32_bf16 v[148:151], a[132:135], a[196:199], v[148:151]
v_mfma_f32_16x16x32_bf16 v[152:155], a[136:139], a[196:199], v[152:155]
v_mfma_f32_16x16x32_bf16 v[156:159], a[140:143], a[196:199], v[156:159]
buffer_load_dwordx4 v43, s[36:39], s49 offen offset:1024 lds
v_mfma_f32_16x16x32_bf16 a[16:19], a[196:199], a[144:147], a[16:19]
ds_read_b128 a[204:207], v32 offset:3072
ds_read_b64_tr_b16 a[164:165], v35 offset:32768
v_mfma_f32_16x16x32_bf16 a[20:23], a[196:199], a[148:151], a[20:23]
ds_read_b64_tr_b16 a[166:167], v37 offset:32768
v_mfma_f32_16x16x32_bf16 a[24:27], a[196:199], a[152:155], a[24:27]
v_mfma_f32_16x16x32_bf16 a[28:31], a[196:199], a[156:159], a[28:31]
s_waitcnt lgkmcnt(5)
v_mfma_f32_16x16x32_bf16 v[160:163], a[128:131], a[200:203], v[160:163]
v_mfma_f32_16x16x32_bf16 v[164:167], a[132:135], a[200:203], v[164:167]
v_mfma_f32_16x16x32_bf16 v[168:171], a[136:139], a[200:203], v[168:171]
ds_read_b64_tr_b16 a[168:169], v34 offset:33280
ds_read_b64_tr_b16 a[170:171], v36 offset:33280
v_mfma_f32_16x16x32_bf16 v[172:175], a[140:143], a[200:203], v[172:175]
buffer_load_dwordx4 v44, s[36:39], s49 offen offset:2048 lds
v_mfma_f32_16x16x32_bf16 a[32:35], a[200:203], a[144:147], a[32:35]
ds_read_b128 a[208:211], v32 offset:4096
v_mfma_f32_16x16x32_bf16 a[36:39], a[200:203], a[148:151], a[36:39]
v_mfma_f32_16x16x32_bf16 a[40:43], a[200:203], a[152:155], a[40:43]
v_mfma_f32_16x16x32_bf16 a[44:47], a[200:203], a[156:159], a[44:47]
s_waitcnt lgkmcnt(5)
v_mfma_f32_16x16x32_bf16 v[176:179], a[128:131], a[204:207], v[176:179]
ds_read_b64_tr_b16 a[172:173], v35 offset:33280
ds_read_b64_tr_b16 a[174:175], v37 offset:33280
v_mfma_f32_16x16x32_bf16 v[180:183], a[132:135], a[204:207], v[180:183]
v_mfma_f32_16x16x32_bf16 v[184:187], a[136:139], a[204:207], v[184:187]
v_mfma_f32_16x16x32_bf16 v[188:191], a[140:143], a[204:207], v[188:191]
buffer_load_dwordx4 v45, s[36:39], s49 offen offset:3072 lds
v_mfma_f32_16x16x32_bf16 a[48:51], a[204:207], a[144:147], a[48:51]
ds_read_b128 a[212:215], v32 offset:5120
v_mfma_f32_16x16x32_bf16 a[52:55], a[204:207], a[148:151], a[52:55]
v_mfma_f32_16x16x32_bf16 a[56:59], a[204:207], a[152:155], a[56:59]
ds_read_b64_tr_b16 a[176:177], v34 offset:40960
ds_read_b64_tr_b16 a[178:179], v36 offset:40960
v_mfma_f32_16x16x32_bf16 a[60:63], a[204:207], a[156:159], a[60:63]
s_waitcnt lgkmcnt(5)
v_mfma_f32_16x16x32_bf16 v[192:195], a[128:131], a[208:211], v[192:195]
v_mfma_f32_16x16x32_bf16 v[196:199], a[132:135], a[208:211], v[196:199]
s_add_u32 m0, s54, 114688
v_mfma_f32_16x16x32_bf16 v[200:203], a[136:139], a[208:211], v[200:203]
v_mfma_f32_16x16x32_bf16 v[204:207], a[140:143], a[208:211], v[204:207]
buffer_load_dwordx4 v46, s[40:43], s50 offen lds
v_mfma_f32_16x16x32_bf16 a[64:67], a[208:211], a[144:147], a[64:67]
ds_read_b128 a[216:219], v32 offset:6144
ds_read_b64_tr_b16 a[180:181], v35 offset:40960
v_mfma_f32_16x16x32_bf16 a[68:71], a[208:211], a[148:151], a[68:71]
ds_read_b64_tr_b16 a[182:183], v37 offset:40960
v_mfma_f32_16x16x32_bf16 a[72:75], a[208:211], a[152:155], a[72:75]
v_mfma_f32_16x16x32_bf16 a[76:79], a[208:211], a[156:159], a[76:79]
s_waitcnt lgkmcnt(5)
v_mfma_f32_16x16x32_bf16 v[208:211], a[128:131], a[212:215], v[208:211]
v_mfma_f32_16x16x32_bf16 v[212:215], a[132:135], a[212:215], v[212:215]
v_mfma_f32_16x16x32_bf16 v[216:219], a[136:139], a[212:215], v[216:219]
ds_read_b64_tr_b16 a[184:185], v34 offset:41472
ds_read_b64_tr_b16 a[186:187], v36 offset:41472
v_mfma_f32_16x16x32_bf16 v[220:223], a[140:143], a[212:215], v[220:223]
buffer_load_dwordx4 v47, s[40:43], s50 offen offset:1024 lds
v_mfma_f32_16x16x32_bf16 a[80:83], a[212:215], a[144:147], a[80:83]
ds_read_b128 a[220:223], v32 offset:7168
v_mfma_f32_16x16x32_bf16 a[84:87], a[212:215], a[148:151], a[84:87]
v_mfma_f32_16x16x32_bf16 a[88:91], a[212:215], a[152:155], a[88:91]
v_mfma_f32_16x16x32_bf16 a[92:95], a[212:215], a[156:159], a[92:95]
s_waitcnt lgkmcnt(5)
v_mfma_f32_16x16x32_bf16 v[224:227], a[128:131], a[216:219], v[224:227]
s_add_u32 m0, s54, 122880
ds_read_b64_tr_b16 a[188:189], v35 offset:41472
ds_read_b64_tr_b16 a[190:191], v37 offset:41472
v_mfma_f32_16x16x32_bf16 v[228:231], a[132:135], a[216:219], v[228:231]
v_mfma_f32_16x16x32_bf16 v[232:235], a[136:139], a[216:219], v[232:235]
buffer_load_dwordx4 v46, s[44:47], s50 offen lds
v_mfma_f32_16x16x32_bf16 v[236:239], a[140:143], a[216:219], v[236:239]
v_mfma_f32_16x16x32_bf16 a[96:99], a[216:219], a[144:147], a[96:99]
ds_read_b128 a[192:195], v32 offset:32768
v_mfma_f32_16x16x32_bf16 a[100:103], a[216:219], a[148:151], a[100:103]
v_mfma_f32_16x16x32_bf16 a[104:107], a[216:219], a[152:155], a[104:107]
v_mfma_f32_16x16x32_bf16 a[108:111], a[216:219], a[156:159], a[108:111]
s_waitcnt lgkmcnt(3)
v_mfma_f32_16x16x32_bf16 v[240:243], a[128:131], a[220:223], v[240:243]
v_mfma_f32_16x16x32_bf16 v[244:247], a[132:135], a[220:223], v[244:247]
buffer_load_dwordx4 v47, s[44:47], s50 offen offset:1024 lds
v_mfma_f32_16x16x32_bf16 v[248:251], a[136:139], a[220:223], v[248:251]
v_mfma_f32_16x16x32_bf16 v[252:255], a[140:143], a[220:223], v[252:255]
s_add_u32 s49, s49, 64
v_mfma_f32_16x16x32_bf16 a[112:115], a[220:223], a[144:147], a[112:115]
s_add_u32 s50, s50, 262144
ds_read_b128 a[196:199], v32 offset:33792
v_mfma_f32_16x16x32_bf16 a[116:119], a[220:223], a[148:151], a[116:119]
)ASM"
R"ASM(v_mfma_f32_16x16x32_bf16 a[120:123], a[220:223], a[152:155], a[120:123]
v_mfma_f32_16x16x32_bf16 a[124:127], a[220:223], a[156:159], a[124:127]
s_waitcnt lgkmcnt(1)
v_mfma_f32_16x16x32_bf16 v[128:131], a[160:163], a[192:195], v[128:131]
s_waitcnt vmcnt(9)
s_barrier
v_mfma_f32_16x16x32_bf16 v[132:135], a[164:167], a[192:195], v[132:135]
s_add_u32 m0, s48, 0
v_mfma_f32_16x16x32_bf16 v[136:139], a[168:171], a[192:195], v[136:139]
v_mfma_f32_16x16x32_bf16 v[140:143], a[172:175], a[192:195], v[140:143]
buffer_load_dwordx4 v42, s[36:39], s49 offen lds
v_mfma_f32_16x16x32_bf16 a[0:3], a[192:195], a[176:179], a[0:3]
ds_read_b128 a[200:203], v32 offset:34816
v_mfma_f32_16x16x32_bf16 a[4:7], a[192:195], a[180:183], a[4:7]
v_mfma_f32_16x16x32_bf16 a[8:11], a[192:195], a[184:187], a[8:11]
ds_read_b64_tr_b16 a[128:129], v38
ds_read_b64_tr_b16 a[130:131], v40
v_mfma_f32_16x16x32_bf16 a[12:15], a[192:195], a[188:191], a[12:15]
s_waitcnt lgkmcnt(3)
v_mfma_f32_16x16x32_bf16 v[144:147], a[160:163], a[196:199], v[144:147]
v_mfma_f32_16x16x32_bf16 v[148:151], a[164:167], a[196:199], v[148:151]
v_mfma_f32_16x16x32_bf16 v[152:155], a[168:171], a[196:199], v[152:155]
v_mfma_f32_16x16x32_bf16 v[156:159], a[172:175], a[196:199], v[156:159]
buffer_load_dwordx4 v43, s[36:39], s49 offen offset:1024 lds
v_mfma_f32_16x16x32_bf16 a[16:19], a[196:199], a[176:179], a[16:19]
ds_read_b128 a[204:207], v32 offset:35840
ds_read_b64_tr_b16 a[132:133], v39
v_mfma_f32_16x16x32_bf16 a[20:23], a[196:199], a[180:183], a[20:23]
ds_read_b64_tr_b16 a[134:135], v41
v_mfma_f32_16x16x32_bf16 a[24:27], a[196:199], a[184:187], a[24:27]
v_mfma_f32_16x16x32_bf16 a[28:31], a[196:199], a[188:191], a[28:31]
s_waitcnt lgkmcnt(5)
v_mfma_f32_16x16x32_bf16 v[160:163], a[160:163], a[200:203], v[160:163]
v_mfma_f32_16x16x32_bf16 v[164:167], a[164:167], a[200:203], v[164:167]
v_mfma_f32_16x16x32_bf16 v[168:171], a[168:171], a[200:203], v[168:171]
ds_read_b64_tr_b16 a[136:137], v38 offset:512
ds_read_b64_tr_b16 a[138:139], v40 offset:512
v_mfma_f32_16x16x32_bf16 v[172:175], a[172:175], a[200:203], v[172:175]
buffer_load_dwordx4 v44, s[36:39], s49 offen offset:2048 lds
v_mfma_f32_16x16x32_bf16 a[32:35], a[200:203], a[176:179], a[32:35]
ds_read_b128 a[208:211], v32 offset:36864
v_mfma_f32_16x16x32_bf16 a[36:39], a[200:203], a[180:183], a[36:39]
v_mfma_f32_16x16x32_bf16 a[40:43], a[200:203], a[184:187], a[40:43]
v_mfma_f32_16x16x32_bf16 a[44:47], a[200:203], a[188:191], a[44:47]
s_waitcnt lgkmcnt(5)
v_mfma_f32_16x16x32_bf16 v[176:179], a[160:163], a[204:207], v[176:179]
ds_read_b64_tr_b16 a[140:141], v39 offset:512
ds_read_b64_tr_b16 a[142:143], v41 offset:512
v_mfma_f32_16x16x32_bf16 v[180:183], a[164:167], a[204:207], v[180:183]
v_mfma_f32_16x16x32_bf16 v[184:187], a[168:171], a[204:207], v[184:187]
v_mfma_f32_16x16x32_bf16 v[188:191], a[172:175], a[204:207], v[188:191]
buffer_load_dwordx4 v45, s[36:39], s49 offen offset:3072 lds
v_mfma_f32_16x16x32_bf16 a[48:51], a[204:207], a[176:179], a[48:51]
ds_read_b128 a[212:215], v32 offset:37888
v_mfma_f32_16x16x32_bf16 a[52:55], a[204:207], a[180:183], a[52:55]
v_mfma_f32_16x16x32_bf16 a[56:59], a[204:207], a[184:187], a[56:59]
ds_read_b64_tr_b16 a[144:145], v38 offset:8192
ds_read_b64_tr_b16 a[146:147], v40 offset:8192
v_mfma_f32_16x16x32_bf16 a[60:63], a[204:207], a[188:191], a[60:63]
s_waitcnt lgkmcnt(5)
v_mfma_f32_16x16x32_bf16 v[192:195], a[160:163], a[208:211], v[192:195]
v_mfma_f32_16x16x32_bf16 v[196:199], a[164:167], a[208:211], v[196:199]
s_add_u32 m0, s54, 16384
v_mfma_f32_16x16x32_bf16 v[200:203], a[168:171], a[208:211], v[200:203]
v_mfma_f32_16x16x32_bf16 v[204:207], a[172:175], a[208:211], v[204:207]
buffer_load_dwordx4 v46, s[40:43], s50 offen lds
v_mfma_f32_16x16x32_bf16 a[64:67], a[208:211], a[176:179], a[64:67]
ds_read_b128 a[216:219], v32 offset:38912
ds_read_b64_tr_b16 a[148:149], v39 offset:8192
v_mfma_f32_16x16x32_bf16 a[68:71], a[208:211], a[180:183], a[68:71]
ds_read_b64_tr_b16 a[150:151], v41 offset:8192
v_mfma_f32_16x16x32_bf16 a[72:75], a[208:211], a[184:187], a[72:75]
v_mfma_f32_16x16x32_bf16 a[76:79], a[208:211], a[188:191], a[76:79]
s_waitcnt lgkmcnt(5)
v_mfma_f32_16x16x32_bf16 v[208:211], a[160:163], a[212:215], v[208:211]
v_mfma_f32_16x16x32_bf16 v[212:215], a[164:167], a[212:215], v[212:215]
v_mfma_f32_16x16x32_bf16 v[216:219], a[168:171], a[212:215], v[216:219]
ds_read_b64_tr_b16 a[152:153], v38 offset:8704
ds_read_b64_tr_b16 a[154:155], v40 offset:8704
v_mfma_f32_16x16x32_bf16 v[220:223], a[172:175], a[212:215], v[220:223]
buffer_load_dwordx4 v47, s[40:43], s50 offen offset:1024 lds
v_mfma_f32_16x16x32_bf16 a[80:83], a[212:215], a[176:179], a[80:83]
ds_read_b128 a[220:223], v32 offset:39936
v_mfma_f32_16x16x32_bf16 a[84:87], a[212:215], a[180:183], a[84:87]
v_mfma_f32_16x16x32_bf16 a[88:91], a[212:215], a[184:187], a[88:91]
v_mfma_f32_16x16x32_bf16 a[92:95], a[212:215], a[188:191], a[92:95]
s_waitcnt lgkmcnt(5)
v_mfma_f32_16x16x32_bf16 v[224:227], a[160:163], a[216:219], v[224:227]
s_add_u32 m0, s54, 24576
ds_read_b64_tr_b16 a[156:157], v39 offset:8704
ds_read_b64_tr_b16 a[158:159], v41 offset:8704
v_mfma_f32_16x16x32_bf16 v[228:231], a[164:167], a[216:219], v[228:231]
v_mfma_f32_16x16x32_bf16 v[232:235], a[168:171], a[216:219], v[232:235]
buffer_load_dwordx4 v46, s[44:47], s50 offen lds
v_mfma_f32_16x16x32_bf16 v[236:239], a[172:175], a[216:219], v[236:239]
v_mfma_f32_16x16x32_bf16 a[96:99], a[216:219], a[176:179], a[96:99]
ds_read_b128 a[192:195], v33
v_mfma_f32_16x16x32_bf16 a[100:103], a[216:219], a[180:183], a[100:103]
v_mfma_f32_16x16x32_bf16 a[104:107], a[216:219], a[184:187], a[104:107]
v_mfma_f32_16x16x32_bf16 a[108:111], a[216:219], a[188:191], a[108:111]
s_waitcnt lgkmcnt(3)
v_mfma_f32_16x16x32_bf16 v[240:243], a[160:163], a[220:223], v[240:243]
v_mfma_f32_16x16x32_bf16 v[244:247], a[164:167], a[220:223], v[244:247]
buffer_load_dwordx4 v47, s[44:47], s50 offen offset:1024 lds
v_mfma_f32_16x16x32_bf16 v[248:251], a[168:171], a[220:223], v[248:251]
v_mfma_f32_16x16x32_bf16 v[252:255], a[172:175], a[220:223], v[252:255]
s_add_u32 s49, s49, 64
v_mfma_f32_16x16x32_bf16 a[112:115], a[220:223], a[176:179], a[112:115]
s_add_u32 s50, s50, 262144
ds_read_b128 a[196:199], v33 offset:1024
v_mfma_f32_16x16x32_bf16 a[116:119], a[220:223], a[180:183], a[116:119]
v_mfma_f32_16x16x32_bf16 a[120:123], a[220:223], a[184:187], a[120:123]
global_load_dword %[sink], %[pf1], off
v_mfma_f32_16x16x32_bf16 a[124:127], a[220:223], a[188:191], a[124:127]
s_waitcnt lgkmcnt(1)
v_mfma_f32_16x16x32_bf16 v[128:131], a[128:131], a[192:195], v[128:131]
s_waitcnt vmcnt(9)
s_barrier
v_mfma_f32_16x16x32_bf16 v[132:135], a[132:135], a[192:195], v[132:135]
s_add_u32 m0, s48, 32768
v_mfma_f32_16x16x32_bf16 v[136:139], a[136:139], a[192:195], v[136:139]
v_mfma_f32_16x16x32_bf16 v[140:143], a[140:143], a[192:195], v[140:143]
buffer_load_dwordx4 v42, s[36:39], s49 offen lds
v_mfma_f32_16x16x32_bf16 a[0:3], a[192:195], a[144:147], a[0:3]
ds_read_b128 a[200:203], v33 offset:2048
v_mfma_f32_16x16x32_bf16 a[4:7], a[192:195], a[148:151], a[4:7]
v_mfma_f32_16x16x32_bf16 a[8:11], a[192:195], a[152:155], a[8:11]
ds_read_b64_tr_b16 a[160:161], v38 offset:32768
ds_read_b64_tr_b16 a[162:163], v40 offset:32768
v_mfma_f32_16x16x32_bf16 a[12:15], a[192:195], a[156:159], a[12:15]
s_waitcnt lgkmcnt(3)
v_mfma_f32_16x16x32_bf16 v[144:147], a[128:131], a[196:199], v[144:147]
v_mfma_f32_16x16x32_bf16 v[148:151], a[132:135], a[196:199], v[148:151]
v_mfma_f32_16x16x32_bf16 v[152:155], a[136:139], a[196:199], v[152:155]
v_mfma_f32_16x16x32_bf16 v[156:159], a[140:143], a[196:199], v[156:159]
buffer_load_dwordx4 v43, s[36:39], s49 offen offset:1024 lds
v_mfma_f32_16x16x32_bf16 a[16:19], a[196:199], a[144:147], a[16:19]
ds_read_b128 a[204:207], v33 offset:3072
ds_read_b64_tr_b16 a[164:165], v39 offset:32768
v_mfma_f32_16x16x32_bf16 a[20:23], a[196:199], a[148:151], a[20:23]
ds_read_b64_tr_b16 a[166:167], v41 offset:32768
v_mfma_f32_16x16x32_bf16 a[24:27], a[196:199], a[152:155], a[24:27]
v_mfma_f32_16x16x32_bf16 a[28:31], a[196:199], a[156:159], a[28:31]
s_waitcnt lgkmcnt(5)
v_mfma_f32_16x16x32_bf16 v[160:163], a[128:131], a[200:203], v[160:163]
v_mfma_f32_16x16x32_bf16 v[164:167], a[132:135], a[200:203], v[164:167]
v_mfma_f32_16x16x32_bf16 v[168:171], a[136:139], a[200:203], v[168:171]
ds_read_b64_tr_b16 a[168:169], v38 offset:33280
ds_read_b64_tr_b16 a[170:171], v40 offset:33280
v_mfma_f32_16x16x32_bf16 v[172:175], a[140:143], a[200:203], v[172:175]
buffer_load_dwordx4 v44, s[36:39], s49 offen offset:2048 lds
v_mfma_f32_16x16x32_bf16 a[32:35], a[200:203], a[144:147], a[32:35]
ds_read_b128 a[208:211], v33 offset:4096
v_mfma_f32_16x16x32_bf16 a[36:39], a[200:203], a[148:151], a[36:39]
v_mfma_f32_16x16x32_bf16 a[40:43], a[200:203], a[152:155], a[40:43]
v_mfma_f32_16x16x32_bf16 a[44:47], a[200:203], a[156:159], a[44:47]
s_waitcnt lgkmcnt(5)
v_mfma_f32_16x16x32_bf16 v[176:179], a[128:131], a[204:207], v[176:179]
ds_read_b64_tr_b16 a[172:173], v39 offset:33280
ds_read_b64_tr_b16 a[174:175], v41 offset:33280
v_mfma_f32_16x16x32_bf16 v[180:183], a[132:135], a[204:207], v[180:183]
v_mfma_f32_16x16x32_bf16 v[184:187], a[136:139], a[204:207], v[184:187]
v_mfma_f32_16x16x32_bf16 v[188:191], a[140:143], a[204:207], v[188:191]
buffer_load_dwordx4 v45, s[36:39], s49 offen offset:3072 lds
v_mfma_f32_16x16x32_bf16 a[48:51], a[204:207], a[144:147], a[48:51]
ds_read_b128 a[212:215], v33 offset:5120
v_mfma_f32_16x16x32_bf16 a[52:55], a[204:207], a[148:151], a[52:55]
v_mfma_f32_16x16x32_bf16 a[56:59], a[204:207], a[152:155], a[56:59]
ds_read_b64_tr_b16 a[176:177], v38 offset:40960
ds_read_b64_tr_b16 a[178:179], v40 offset:40960
v_mfma_f32_16x16x32_bf16 a[60:63], a[204:207], a[156:159], a[60:63]
s_waitcnt lgkmcnt(5)
v_mfma_f32_16x16x32_bf16 v[192:195], a[128:131], a[208:211], v[192:195]
v_mfma_f32_16x16x32_bf16 v[196:199], a[132:135], a[208:211], v[196:199]
s_add_u32 m0, s54, 49152
v_mfma_f32_16x16x32_bf16 v[200:203], a[136:139], a[208:211], v[200:203]
v_mfma_f32_16x16x32_bf16 v[204:207], a[140:143], a[208:211], v[204:207]
buffer_load_dwordx4 v46, s[40:43], s50 offen lds
v_mfma_f32_16x16x32_bf16 a[64:67], a[208:211], a[144:147], a[64:67]
ds_read_b128 a[216:219], v33 offset:6144
ds_read_b64_tr_b16 a[180:181], v39 offset:40960
v_mfma_f32_16x16x32_bf16 a[68:71], a[208:211], a[148:151], a[68:71]
ds_read_b64_tr_b16 a[182:183], v41 offset:40960
v_mfma_f32_16x16x32_bf16 a[72:75], a[208:211], a[152:155], a[72:75]
v_mfma_f32_16x16x32_bf16 a[76:79], a[208:211], a[156:159], a[76:79]
s_waitcnt lgkmcnt(5)
v_mfma_f32_16x16x32_bf16 v[208:211], a[128:131], a[212:215], v[208:211]
v_mfma_f32_16x16x32_bf16 v[212:215], a[132:135], a[212:215], v[212:215]
v_mfma_f32_16x16x32_bf16 v[216:219], a[136:139], a[212:215], v[216:219]
ds_read_b64_tr_b16 a[184:185], v38 offset:41472
ds_read_b64_tr_b16 a[186:187], v40 offset:41472
v_mfma_f32_16x16x32_bf16 v[220:223], a[140:143], a[212:215], v[220:223]
buffer_load_dwordx4 v47, s[40:43], s50 offen offset:1024 lds
v_mfma_f32_16x16x32_bf16 a[80:83], a[212:215], a[144:147], a[80:83]
ds_read_b128 a[220:223], v33 offset:7168
v_mfma_f32_16x16x32_bf16 a[84:87], a[212:215], a[148:151], a[84:87]
v_mfma_f32_16x16x32_bf16 a[88:91], a[212:215], a[152:155], a[88:91]
v_mfma_f32_16x16x32_bf16 a[92:95], a[212:215], a[156:159], a[92:95]
s_waitcnt lgkmcnt(5)
v_mfma_f32_16x16x32_bf16 v[224:227], a[128:131], a[216:219], v[224:227]
s_add_u32 m0, s54, 57344
ds_read_b64_tr_b16 a[188:189], v39 offset:41472
ds_read_b64_tr_b16 a[190:191], v41 offset:41472
v_mfma_f32_16x16x32_bf16 v[228:231], a[132:135], a[216:219], v[228:231]
v_mfma_f32_16x16x32_bf16 v[232:235], a[136:139], a[216:219], v[232:235]
buffer_load_dwordx4 v46, s[44:47], s50 offen lds
v_mfma_f32_16x16x32_bf16 v[236:239], a[140:143], a[216:219], v[236:239]
v_mfma_f32_16x16x32_bf16 a[96:99], a[216:219], a[144:147], a[96:99]
ds_read_b128 a[192:195], v33 offset:32768
v_mfma_f32_16x16x32_bf16 a[100:103], a[216:219], a[148:151], a[100:103]
v_mfma_f32_16x16x32_bf16 a[104:107], a[216:219], a[152:155], a[104:107]
)ASM"
R"ASM(v_mfma_f32_16x16x32_bf16 a[108:111], a[216:219], a[156:159], a[108:111]
s_waitcnt lgkmcnt(3)
v_mfma_f32_16x16x32_bf16 v[240:243], a[128:131], a[220:223], v[240:243]
v_mfma_f32_16x16x32_bf16 v[244:247], a[132:135], a[220:223], v[244:247]
buffer_load_dwordx4 v47, s[44:47], s50 offen offset:1024 lds
v_mfma_f32_16x16x32_bf16 v[248:251], a[136:139], a[220:223], v[248:251]
v_mfma_f32_16x16x32_bf16 v[252:255], a[140:143], a[220:223], v[252:255]
s_add_u32 s49, s49, 64
v_mfma_f32_16x16x32_bf16 a[112:115], a[220:223], a[144:147], a[112:115]
s_add_u32 s50, s50, 262144
ds_read_b128 a[196:199], v33 offset:33792
v_mfma_f32_16x16x32_bf16 a[116:119], a[220:223], a[148:151], a[116:119]
v_mfma_f32_16x16x32_bf16 a[120:123], a[220:223], a[152:155], a[120:123]
v_mfma_f32_16x16x32_bf16 a[124:127], a[220:223], a[156:159], a[124:127]
s_waitcnt lgkmcnt(1)
v_mfma_f32_16x16x32_bf16 v[128:131], a[160:163], a[192:195], v[128:131]
s_waitcnt vmcnt(9)
s_barrier
v_mfma_f32_16x16x32_bf16 v[132:135], a[164:167], a[192:195], v[132:135]
s_add_u32 m0, s48, 65536
v_mfma_f32_16x16x32_bf16 v[136:139], a[168:171], a[192:195], v[136:139]
v_mfma_f32_16x16x32_bf16 v[140:143], a[172:175], a[192:195], v[140:143]
buffer_load_dwordx4 v42, s[36:39], s49 offen lds
v_mfma_f32_16x16x32_bf16 a[0:3], a[192:195], a[176:179], a[0:3]
ds_read_b128 a[200:203], v33 offset:34816
v_mfma_f32_16x16x32_bf16 a[4:7], a[192:195], a[180:183], a[4:7]
v_mfma_f32_16x16x32_bf16 a[8:11], a[192:195], a[184:187], a[8:11]
ds_read_b64_tr_b16 a[128:129], v34
ds_read_b64_tr_b16 a[130:131], v36
v_mfma_f32_16x16x32_bf16 a[12:15], a[192:195], a[188:191], a[12:15]
s_waitcnt lgkmcnt(3)
v_mfma_f32_16x16x32_bf16 v[144:147], a[160:163], a[196:199], v[144:147]
v_mfma_f32_16x16x32_bf16 v[148:151], a[164:167], a[196:199], v[148:151]
v_mfma_f32_16x16x32_bf16 v[152:155], a[168:171], a[196:199], v[152:155]
v_mfma_f32_16x16x32_bf16 v[156:159], a[172:175], a[196:199], v[156:159]
buffer_load_dwordx4 v43, s[36:39], s49 offen offset:1024 lds
v_mfma_f32_16x16x32_bf16 a[16:19], a[196:199], a[176:179], a[16:19]
ds_read_b128 a[204:207], v33 offset:35840
ds_read_b64_tr_b16 a[132:133], v35
v_mfma_f32_16x16x32_bf16 a[20:23], a[196:199], a[180:183], a[20:23]
ds_read_b64_tr_b16 a[134:135], v37
v_mfma_f32_16x16x32_bf16 a[24:27], a[196:199], a[184:187], a[24:27]
v_mfma_f32_16x16x32_bf16 a[28:31], a[196:199], a[188:191], a[28:31]
s_waitcnt lgkmcnt(5)
v_mfma_f32_16x16x32_bf16 v[160:163], a[160:163], a[200:203], v[160:163]
v_mfma_f32_16x16x32_bf16 v[164:167], a[164:167], a[200:203], v[164:167]
v_mfma_f32_16x16x32_bf16 v[168:171], a[168:171], a[200:203], v[168:171]
ds_read_b64_tr_b16 a[136:137], v34 offset:512
ds_read_b64_tr_b16 a[138:139], v36 offset:512
v_mfma_f32_16x16x32_bf16 v[172:175], a[172:175], a[200:203], v[172:175]
buffer_load_dwordx4 v44, s[36:39], s49 offen offset:2048 lds
v_mfma_f32_16x16x32_bf16 a[32:35], a[200:203], a[176:179], a[32:35]
ds_read_b128 a[208:211], v33 offset:36864
v_mfma_f32_16x16x32_bf16 a[36:39], a[200:203], a[180:183], a[36:39]
v_mfma_f32_16x16x32_bf16 a[40:43], a[200:203], a[184:187], a[40:43]
v_mfma_f32_16x16x32_bf16 a[44:47], a[200:203], a[188:191], a[44:47]
s_waitcnt lgkmcnt(5)
v_mfma_f32_16x16x32_bf16 v[176:179], a[160:163], a[204:207], v[176:179]
ds_read_b64_tr_b16 a[140:141], v35 offset:512
ds_read_b64_tr_b16 a[142:143], v37 offset:512
v_mfma_f32_16x16x32_bf16 v[180:183], a[164:167], a[204:207], v[180:183]
v_mfma_f32_16x16x32_bf16 v[184:187], a[168:171], a[204:207], v[184:187]
v_mfma_f32_16x16x32_bf16 v[188:191], a[172:175], a[204:207], v[188:191]
buffer_load_dwordx4 v45, s[36:39], s49 offen offset:3072 lds
v_mfma_f32_16x16x32_bf16 a[48:51], a[204:207], a[176:179], a[48:51]
ds_read_b128 a[212:215], v33 offset:37888
v_mfma_f32_16x16x32_bf16 a[52:55], a[204:207], a[180:183], a[52:55]
v_mfma_f32_16x16x32_bf16 a[56:59], a[204:207], a[184:187], a[56:59]
ds_read_b64_tr_b16 a[144:145], v34 offset:8192
ds_read_b64_tr_b16 a[146:147], v36 offset:8192
v_mfma_f32_16x16x32_bf16 a[60:63], a[204:207], a[188:191], a[60:63]
s_waitcnt lgkmcnt(5)
v_mfma_f32_16x16x32_bf16 v[192:195], a[160:163], a[208:211], v[192:195]
v_mfma_f32_16x16x32_bf16 v[196:199], a[164:167], a[208:211], v[196:199]
s_add_u32 m0, s54, 81920
v_mfma_f32_16x16x32_bf16 v[200:203], a[168:171], a[208:211], v[200:203]
v_mfma_f32_16x16x32_bf16 v[204:207], a[172:175], a[208:211], v[204:207]
buffer_load_dwordx4 v46, s[40:43], s50 offen lds
v_mfma_f32_16x16x32_bf16 a[64:67], a[208:211], a[176:179], a[64:67]
ds_read_b128 a[216:219], v33 offset:38912
ds_read_b64_tr_b16 a[148:149], v35 offset:8192
v_mfma_f32_16x16x32_bf16 a[68:71], a[208:211], a[180:183], a[68:71]
ds_read_b64_tr_b16 a[150:151], v37 offset:8192
v_mfma_f32_16x16x32_bf16 a[72:75], a[208:211], a[184:187], a[72:75]
v_mfma_f32_16x16x32_bf16 a[76:79], a[208:211], a[188:191], a[76:79]
s_waitcnt lgkmcnt(5)
v_mfma_f32_16x16x32_bf16 v[208:211], a[160:163], a[212:215], v[208:211]
v_mfma_f32_16x16x32_bf16 v[212:215], a[164:167], a[212:215], v[212:215]
v_mfma_f32_16x16x32_bf16 v[216:219], a[168:171], a[212:215], v[216:219]
ds_read_b64_tr_b16 a[152:153], v34 offset:8704
ds_read_b64_tr_b16 a[154:155], v36 offset:8704
v_mfma_f32_16x16x32_bf16 v[220:223], a[172:175], a[212:215], v[220:223]
buffer_load_dwordx4 v47, s[40:43], s50 offen offset:1024 lds
v_mfma_f32_16x16x32_bf16 a[80:83], a[212:215], a[176:179], a[80:83]
ds_read_b128 a[220:223], v33 offset:39936
v_mfma_f32_16x16x32_bf16 a[84:87], a[212:215], a[180:183], a[84:87]
v_mfma_f32_16x16x32_bf16 a[88:91], a[212:215], a[184:187], a[88:91]
v_mfma_f32_16x16x32_bf16 a[92:95], a[212:215], a[188:191], a[92:95]
s_waitcnt lgkmcnt(5)
v_mfma_f32_16x16x32_bf16 v[224:227], a[160:163], a[216:219], v[224:227]
s_add_u32 m0, s54, 90112
ds_read_b64_tr_b16 a[156:157], v35 offset:8704
ds_read_b64_tr_b16 a[158:159], v37 offset:8704
v_mfma_f32_16x16x32_bf16 v[228:231], a[164:167], a[216:219], v[228:231]
v_mfma_f32_16x16x32_bf16 v[232:235], a[168:171], a[216:219], v[232:235]
buffer_load_dwordx4 v46, s[44:47], s50 offen lds
v_mfma_f32_16x16x32_bf16 v[236:239], a[172:175], a[216:219], v[236:239]
v_mfma_f32_16x16x32_bf16 a[96:99], a[216:219], a[176:179], a[96:99]
ds_read_b128 a[192:195], v32
v_mfma_f32_16x16x32_bf16 a[100:103], a[216:219], a[180:183], a[100:103]
v_mfma_f32_16x16x32_bf16 a[104:107], a[216:219], a[184:187], a[104:107]
v_mfma_f32_16x16x32_bf16 a[108:111], a[216:219], a[188:191], a[108:111]
s_waitcnt lgkmcnt(3)
v_mfma_f32_16x16x32_bf16 v[240:243], a[160:163], a[220:223], v[240:243]
v_mfma_f32_16x16x32_bf16 v[244:247], a[164:167], a[220:223], v[244:247]
buffer_load_dwordx4 v47, s[44:47], s50 offen offset:1024 lds
v_mfma_f32_16x16x32_bf16 v[248:251], a[168:171], a[220:223], v[248:251]
v_mfma_f32_16x16x32_bf16 v[252:255], a[172:175], a[220:223], v[252:255]
s_add_u32 s49, s49, 64
v_mfma_f32_16x16x32_bf16 a[112:115], a[220:223], a[176:179], a[112:115]
s_add_u32 s50, s50, 262144
ds_read_b128 a[196:199], v32 offset:1024
v_mfma_f32_16x16x32_bf16 a[116:119], a[220:223], a[180:183], a[116:119]
v_mfma_f32_16x16x32_bf16 a[120:123], a[220:223], a[184:187], a[120:123]
global_load_dword %[sink], %[pf2], off
v_mfma_f32_16x16x32_bf16 a[124:127], a[220:223], a[188:191], a[124:127]
s_waitcnt lgkmcnt(1)
v_mfma_f32_16x16x32_bf16 v[128:131], a[128:131], a[192:195], v[128:131]
s_waitcnt vmcnt(9)
s_barrier
v_mfma_f32_16x16x32_bf16 v[132:135], a[132:135], a[192:195], v[132:135]
s_add_u32 m0, s48, 98304
v_mfma_f32_16x16x32_bf16 v[136:139], a[136:139], a[192:195], v[136:139]
v_mfma_f32_16x16x32_bf16 v[140:143], a[140:143], a[192:195], v[140:143]
buffer_load_dwordx4 v42, s[36:39], s49 offen lds
v_mfma_f32_16x16x32_bf16 a[0:3], a[192:195], a[144:147], a[0:3]
ds_read_b128 a[200:203], v32 offset:2048
v_mfma_f32_16x16x32_bf16 a[4:7], a[192:195], a[148:151], a[4:7]
v_mfma_f32_16x16x32_bf16 a[8:11], a[192:195], a[152:155], a[8:11]
ds_read_b64_tr_b16 a[160:161], v34 offset:32768
ds_read_b64_tr_b16 a[162:163], v36 offset:32768
v_mfma_f32_16x16x32_bf16 a[12:15], a[192:195], a[156:159], a[12:15]
s_waitcnt lgkmcnt(3)
v_mfma_f32_16x16x32_bf16 v[144:147], a[128:131], a[196:199], v[144:147]
v_mfma_f32_16x16x32_bf16 v[148:151], a[132:135], a[196:199], v[148:151]
v_mfma_f32_16x16x32_bf16 v[152:155], a[136:139], a[196:199], v[152:155]
v_mfma_f32_16x16x32_bf16 v[156:159], a[140:143], a[196:199], v[156:159]
buffer_load_dwordx4 v43, s[36:39], s49 offen offset:1024 lds
v_mfma_f32_16x16x32_bf16 a[16:19], a[196:199], a[144:147], a[16:19]
ds_read_b128 a[204:207], v32 offset:3072
ds_read_b64_tr_b16 a[164:165], v35 offset:32768
v_mfma_f32_16x16x32_bf16 a[20:23], a[196:199], a[148:151], a[20:23]
ds_read_b64_tr_b16 a[166:167], v37 offset:32768
v_mfma_f32_16x16x32_bf16 a[24:27], a[196:199], a[152:155], a[24:27]
v_mfma_f32_16x16x32_bf16 a[28:31], a[196:199], a[156:159], a[28:31]
s_waitcnt lgkmcnt(5)
v_mfma_f32_16x16x32_bf16 v[160:163], a[128:131], a[200:203], v[160:163]
v_mfma_f32_16x16x32_bf16 v[164:167], a[132:135], a[200:203], v[164:167]
v_mfma_f32_16x16x32_bf16 v[168:171], a[136:139], a[200:203], v[168:171]
ds_read_b64_tr_b16 a[168:169], v34 offset:33280
ds_read_b64_tr_b16 a[170:171], v36 offset:33280
v_mfma_f32_16x16x32_bf16 v[172:175], a[140:143], a[200:203], v[172:175]
buffer_load_dwordx4 v44, s[36:39], s49 offen offset:2048 lds
v_mfma_f32_16x16x32_bf16 a[32:35], a[200:203], a[144:147], a[32:35]
ds_read_b128 a[208:211], v32 offset:4096
v_mfma_f32_16x16x32_bf16 a[36:39], a[200:203], a[148:151], a[36:39]
v_mfma_f32_16x16x32_bf16 a[40:43], a[200:203], a[152:155], a[40:43]
v_mfma_f32_16x16x32_bf16 a[44:47], a[200:203], a[156:159], a[44:47]
s_waitcnt lgkmcnt(5)
v_mfma_f32_16x16x32_bf16 v[176:179], a[128:131], a[204:207], v[176:179]
ds_read_b64_tr_b16 a[172:173], v35 offset:33280
ds_read_b64_tr_b16 a[174:175], v37 offset:33280
v_mfma_f32_16x16x32_bf16 v[180:183], a[132:135], a[204:207], v[180:183]
v_mfma_f32_16x16x32_bf16 v[184:187], a[136:139], a[204:207], v[184:187]
v_mfma_f32_16x16x32_bf16 v[188:191], a[140:143], a[204:207], v[188:191]
buffer_load_dwordx4 v45, s[36:39], s49 offen offset:3072 lds
v_mfma_f32_16x16x32_bf16 a[48:51], a[204:207], a[144:147], a[48:51]
ds_read_b128 a[212:215], v32 offset:5120
v_mfma_f32_16x16x32_bf16 a[52:55], a[204:207], a[148:151], a[52:55]
v_mfma_f32_16x16x32_bf16 a[56:59], a[204:207], a[152:155], a[56:59]
ds_read_b64_tr_b16 a[176:177], v34 offset:40960
ds_read_b64_tr_b16 a[178:179], v36 offset:40960
v_mfma_f32_16x16x32_bf16 a[60:63], a[204:207], a[156:159], a[60:63]
s_waitcnt lgkmcnt(5)
v_mfma_f32_16x16x32_bf16 v[192:195], a[128:131], a[208:211], v[192:195]
v_mfma_f32_16x16x32_bf16 v[196:199], a[132:135], a[208:211], v[196:199]
s_add_u32 m0, s54, 114688
v_mfma_f32_16x16x32_bf16 v[200:203], a[136:139], a[208:211], v[200:203]
v_mfma_f32_16x16x32_bf16 v[204:207], a[140:143], a[208:211], v[204:207]
buffer_load_dwordx4 v46, s[40:43], s50 offen lds
v_mfma_f32_16x16x32_bf16 a[64:67], a[208:211], a[144:147], a[64:67]
ds_read_b128 a[216:219], v32 offset:6144
ds_read_b64_tr_b16 a[180:181], v35 offset:40960
v_mfma_f32_16x16x32_bf16 a[68:71], a[208:211], a[148:151], a[68:71]
ds_read_b64_tr_b16 a[182:183], v37 offset:40960
v_mfma_f32_16x16x32_bf16 a[72:75], a[208:211], a[152:155], a[72:75]
v_mfma_f32_16x16x32_bf16 a[76:79], a[208:211], a[156:159], a[76:79]
s_waitcnt lgkmcnt(5)
v_mfma_f32_16x16x32_bf16 v[208:211], a[128:131], a[212:215], v[208:211]
v_mfma_f32_16x16x32_bf16 v[212:215], a[132:135], a[212:215], v[212:215]
v_mfma_f32_16x16x32_bf16 v[216:219], a[136:139], a[212:215], v[216:219]
ds_read_b64_tr_b16 a[184:185], v34 offset:41472
ds_read_b64_tr_b16 a[186:187], v36 offset:41472
v_mfma_f32_16x16x32_bf16 v[220:223], a[140:143], a[212:215], v[220:223]
buffer_load_dwordx4 v47, s[40:43], s50 offen offset:1024 lds
v_mfma_f32_16x16x32_bf16 a[80:83], a[212:215], a[144:147], a[80:83]
ds_read_b128 a[220:223], v32 offset:7168
v_mfma_f32_16x16x32_bf16 a[84:87], a[212:215], a[148:151], a[84:87]
v_mfma_f32_16x16x32_bf16 a[88:91], a[212:215], a[152:155], a[88:91]
v_mfma_f32_16x16x32_bf16 a[92:95], a[212:215], a[156:159], a[92:95]
s_waitcnt lgkmcnt(5)
v_mfma_f32_16x16x32_bf16 v[224:227], a[128:131], a[216:219], v[224:227]
)ASM"
R"ASM(s_add_u32 m0, s54, 122880
ds_read_b64_tr_b16 a[188:189], v35 offset:41472
ds_read_b64_tr_b16 a[190:191], v37 offset:41472
v_mfma_f32_16x16x32_bf16 v[228:231], a[132:135], a[216:219], v[228:231]
v_mfma_f32_16x16x32_bf16 v[232:235], a[136:139], a[216:219], v[232:235]
buffer_load_dwordx4 v46, s[44:47], s50 offen lds
v_mfma_f32_16x16x32_bf16 v[236:239], a[140:143], a[216:219], v[236:239]
v_mfma_f32_16x16x32_bf16 a[96:99], a[216:219], a[144:147], a[96:99]
ds_read_b128 a[192:195], v32 offset:32768
v_mfma_f32_16x16x32_bf16 a[100:103], a[216:219], a[148:151], a[100:103]
v_mfma_f32_16x16x32_bf16 a[104:107], a[216:219], a[152:155], a[104:107]
v_mfma_f32_16x16x32_bf16 a[108:111], a[216:219], a[156:159], a[108:111]
s_waitcnt lgkmcnt(3)
v_mfma_f32_16x16x32_bf16 v[240:243], a[128:131], a[220:223], v[240:243]
v_mfma_f32_16x16x32_bf16 v[244:247], a[132:135], a[220:223], v[244:247]
buffer_load_dwordx4 v47, s[44:47], s50 offen offset:1024 lds
v_mfma_f32_16x16x32_bf16 v[248:251], a[136:139], a[220:223], v[248:251]
v_mfma_f32_16x16x32_bf16 v[252:255], a[140:143], a[220:223], v[252:255]
s_add_u32 s49, s49, 64
v_mfma_f32_16x16x32_bf16 a[112:115], a[220:223], a[144:147], a[112:115]
s_add_u32 s50, s50, 262144
ds_read_b128 a[196:199], v32 offset:33792
v_mfma_f32_16x16x32_bf16 a[116:119], a[220:223], a[148:151], a[116:119]
v_mfma_f32_16x16x32_bf16 a[120:123], a[220:223], a[152:155], a[120:123]
v_mfma_f32_16x16x32_bf16 a[124:127], a[220:223], a[156:159], a[124:127]
s_waitcnt lgkmcnt(1)
v_mfma_f32_16x16x32_bf16 v[128:131], a[160:163], a[192:195], v[128:131]
s_waitcnt vmcnt(9)
s_barrier
v_mfma_f32_16x16x32_bf16 v[132:135], a[164:167], a[192:195], v[132:135]
s_add_u32 m0, s48, 0
v_mfma_f32_16x16x32_bf16 v[136:139], a[168:171], a[192:195], v[136:139]
v_mfma_f32_16x16x32_bf16 v[140:143], a[172:175], a[192:195], v[140:143]
buffer_load_dwordx4 v42, s[36:39], s49 offen lds
v_mfma_f32_16x16x32_bf16 a[0:3], a[192:195], a[176:179], a[0:3]
ds_read_b128 a[200:203], v32 offset:34816
v_mfma_f32_16x16x32_bf16 a[4:7], a[192:195], a[180:183], a[4:7]
v_mfma_f32_16x16x32_bf16 a[8:11], a[192:195], a[184:187], a[8:11]
ds_read_b64_tr_b16 a[128:129], v38
ds_read_b64_tr_b16 a[130:131], v40
v_mfma_f32_16x16x32_bf16 a[12:15], a[192:195], a[188:191], a[12:15]
s_waitcnt lgkmcnt(3)
v_mfma_f32_16x16x32_bf16 v[144:147], a[160:163], a[196:199], v[144:147]
v_mfma_f32_16x16x32_bf16 v[148:151], a[164:167], a[196:199], v[148:151]
v_mfma_f32_16x16x32_bf16 v[152:155], a[168:171], a[196:199], v[152:155]
v_mfma_f32_16x16x32_bf16 v[156:159], a[172:175], a[196:199], v[156:159]
buffer_load_dwordx4 v43, s[36:39], s49 offen offset:1024 lds
v_mfma_f32_16x16x32_bf16 a[16:19], a[196:199], a[176:179], a[16:19]
ds_read_b128 a[204:207], v32 offset:35840
ds_read_b64_tr_b16 a[132:133], v39
v_mfma_f32_16x16x32_bf16 a[20:23], a[196:199], a[180:183], a[20:23]
ds_read_b64_tr_b16 a[134:135], v41
v_mfma_f32_16x16x32_bf16 a[24:27], a[196:199], a[184:187], a[24:27]
v_mfma_f32_16x16x32_bf16 a[28:31], a[196:199], a[188:191], a[28:31]
s_waitcnt lgkmcnt(5)
v_mfma_f32_16x16x32_bf16 v[160:163], a[160:163], a[200:203], v[160:163]
v_mfma_f32_16x16x32_bf16 v[164:167], a[164:167], a[200:203], v[164:167]
v_mfma_f32_16x16x32_bf16 v[168:171], a[168:171], a[200:203], v[168:171]
ds_read_b64_tr_b16 a[136:137], v38 offset:512
ds_read_b64_tr_b16 a[138:139], v40 offset:512
v_mfma_f32_16x16x32_bf16 v[172:175], a[172:175], a[200:203], v[172:175]
buffer_load_dwordx4 v44, s[36:39], s49 offen offset:2048 lds
v_mfma_f32_16x16x32_bf16 a[32:35], a[200:203], a[176:179], a[32:35]
ds_read_b128 a[208:211], v32 offset:36864
v_mfma_f32_16x16x32_bf16 a[36:39], a[200:203], a[180:183], a[36:39]
v_mfma_f32_16x16x32_bf16 a[40:43], a[200:203], a[184:187], a[40:43]
v_mfma_f32_16x16x32_bf16 a[44:47], a[200:203], a[188:191], a[44:47]
s_waitcnt lgkmcnt(5)
v_mfma_f32_16x16x32_bf16 v[176:179], a[160:163], a[204:207], v[176:179]
ds_read_b64_tr_b16 a[140:141], v39 offset:512
ds_read_b64_tr_b16 a[142:143], v41 offset:512
v_mfma_f32_16x16x32_bf16 v[180:183], a[164:167], a[204:207], v[180:183]
v_mfma_f32_16x16x32_bf16 v[184:187], a[168:171], a[204:207], v[184:187]
v_mfma_f32_16x16x32_bf16 v[188:191], a[172:175], a[204:207], v[188:191]
buffer_load_dwordx4 v45, s[36:39], s49 offen offset:3072 lds
v_mfma_f32_16x16x32_bf16 a[48:51], a[204:207], a[176:179], a[48:51]
ds_read_b128 a[212:215], v32 offset:37888
v_mfma_f32_16x16x32_bf16 a[52:55], a[204:207], a[180:183], a[52:55]
v_mfma_f32_16x16x32_bf16 a[56:59], a[204:207], a[184:187], a[56:59]
ds_read_b64_tr_b16 a[144:145], v38 offset:8192
ds_read_b64_tr_b16 a[146:147], v40 offset:8192
v_mfma_f32_16x16x32_bf16 a[60:63], a[204:207], a[188:191], a[60:63]
s_waitcnt lgkmcnt(5)
v_mfma_f32_16x16x32_bf16 v[192:195], a[160:163], a[208:211], v[192:195]
v_mfma_f32_16x16x32_bf16 v[196:199], a[164:167], a[208:211], v[196:199]
s_add_u32 m0, s54, 16384
v_mfma_f32_16x16x32_bf16 v[200:203], a[168:171], a[208:211], v[200:203]
v_mfma_f32_16x16x32_bf16 v[204:207], a[172:175], a[208:211], v[204:207]
buffer_load_dwordx4 v46, s[40:43], s50 offen lds
v_mfma_f32_16x16x32_bf16 a[64:67], a[208:211], a[176:179], a[64:67]
ds_read_b128 a[216:219], v32 offset:38912
ds_read_b64_tr_b16 a[148:149], v39 offset:8192
v_mfma_f32_16x16x32_bf16 a[68:71], a[208:211], a[180:183], a[68:71]
ds_read_b64_tr_b16 a[150:151], v41 offset:8192
v_mfma_f32_16x16x32_bf16 a[72:75], a[208:211], a[184:187], a[72:75]
v_mfma_f32_16x16x32_bf16 a[76:79], a[208:211], a[188:191], a[76:79]
s_waitcnt lgkmcnt(5)
v_mfma_f32_16x16x32_bf16 v[208:211], a[160:163], a[212:215], v[208:211]
v_mfma_f32_16x16x32_bf16 v[212:215], a[164:167], a[212:215], v[212:215]
v_mfma_f32_16x16x32_bf16 v[216:219], a[168:171], a[212:215], v[216:219]
ds_read_b64_tr_b16 a[152:153], v38 offset:8704
ds_read_b64_tr_b16 a[154:155], v40 offset:8704
v_mfma_f32_16x16x32_bf16 v[220:223], a[172:175], a[212:215], v[220:223]
buffer_load_dwordx4 v47, s[40:43], s50 offen offset:1024 lds
v_mfma_f32_16x16x32_bf16 a[80:83], a[212:215], a[176:179], a[80:83]
ds_read_b128 a[220:223], v32 offset:39936
v_mfma_f32_16x16x32_bf16 a[84:87], a[212:215], a[180:183], a[84:87]
v_mfma_f32_16x16x32_bf16 a[88:91], a[212:215], a[184:187], a[88:91]
v_mfma_f32_16x16x32_bf16 a[92:95], a[212:215], a[188:191], a[92:95]
s_waitcnt lgkmcnt(5)
v_mfma_f32_16x16x32_bf16 v[224:227], a[160:163], a[216:219], v[224:227]
s_add_u32 m0, s54, 24576
ds_read_b64_tr_b16 a[156:157], v39 offset:8704
ds_read_b64_tr_b16 a[158:159], v41 offset:8704
v_mfma_f32_16x16x32_bf16 v[228:231], a[164:167], a[216:219], v[228:231]
v_mfma_f32_16x16x32_bf16 v[232:235], a[168:171], a[216:219], v[232:235]
buffer_load_dwordx4 v46, s[44:47], s50 offen lds
v_mfma_f32_16x16x32_bf16 v[236:239], a[172:175], a[216:219], v[236:239]
v_mfma_f32_16x16x32_bf16 a[96:99], a[216:219], a[176:179], a[96:99]
ds_read_b128 a[192:195], v33
v_mfma_f32_16x16x32_bf16 a[100:103], a[216:219], a[180:183], a[100:103]
v_mfma_f32_16x16x32_bf16 a[104:107], a[216:219], a[184:187], a[104:107]
v_mfma_f32_16x16x32_bf16 a[108:111], a[216:219], a[188:191], a[108:111]
s_waitcnt lgkmcnt(3)
v_mfma_f32_16x16x32_bf16 v[240:243], a[160:163], a[220:223], v[240:243]
v_mfma_f32_16x16x32_bf16 v[244:247], a[164:167], a[220:223], v[244:247]
buffer_load_dwordx4 v47, s[44:47], s50 offen offset:1024 lds
v_mfma_f32_16x16x32_bf16 v[248:251], a[168:171], a[220:223], v[248:251]
v_mfma_f32_16x16x32_bf16 v[252:255], a[172:175], a[220:223], v[252:255]
s_add_u32 s49, s49, 64
v_mfma_f32_16x16x32_bf16 a[112:115], a[220:223], a[176:179], a[112:115]
s_add_u32 s50, s50, 262144
ds_read_b128 a[196:199], v33 offset:1024
v_mfma_f32_16x16x32_bf16 a[116:119], a[220:223], a[180:183], a[116:119]
v_mfma_f32_16x16x32_bf16 a[120:123], a[220:223], a[184:187], a[120:123]
global_load_dword %[sink], %[pf3], off
v_mfma_f32_16x16x32_bf16 a[124:127], a[220:223], a[188:191], a[124:127]
s_waitcnt lgkmcnt(1)
v_mfma_f32_16x16x32_bf16 v[128:131], a[128:131], a[192:195], v[128:131]
s_waitcnt vmcnt(9)
s_barrier
v_mfma_f32_16x16x32_bf16 v[132:135], a[132:135], a[192:195], v[132:135]
s_add_u32 m0, s48, 32768
v_mfma_f32_16x16x32_bf16 v[136:139], a[136:139], a[192:195], v[136:139]
v_mfma_f32_16x16x32_bf16 v[140:143], a[140:143], a[192:195], v[140:143]
buffer_load_dwordx4 v42, s[36:39], s49 offen lds
v_mfma_f32_16x16x32_bf16 a[0:3], a[192:195], a[144:147], a[0:3]
ds_read_b128 a[200:203], v33 offset:2048
v_mfma_f32_16x16x32_bf16 a[4:7], a[192:195], a[148:151], a[4:7]
v_mfma_f32_16x16x32_bf16 a[8:11], a[192:195], a[152:155], a[8:11]
ds_read_b64_tr_b16 a[160:161], v38 offset:32768
ds_read_b64_tr_b16 a[162:163], v40 offset:32768
v_mfma_f32_16x16x32_bf16 a[12:15], a[192:195], a[156:159], a[12:15]
s_waitcnt lgkmcnt(3)
v_mfma_f32_16x16x32_bf16 v[144:147], a[128:131], a[196:199], v[144:147]
v_mfma_f32_16x16x32_bf16 v[148:151], a[132:135], a[196:199], v[148:151]
v_mfma_f32_16x16x32_bf16 v[152:155], a[136:139], a[196:199], v[152:155]
v_mfma_f32_16x16x32_bf16 v[156:159], a[140:143], a[196:199], v[156:159]
buffer_load_dwordx4 v43, s[36:39], s49 offen offset:1024 lds
v_mfma_f32_16x16x32_bf16 a[16:19], a[196:199], a[144:147], a[16:19]
ds_read_b128 a[204:207], v33 offset:3072
ds_read_b64_tr_b16 a[164:165], v39 offset:32768
v_mfma_f32_16x16x32_bf16 a[20:23], a[196:199], a[148:151], a[20:23]
ds_read_b64_tr_b16 a[166:167], v41 offset:32768
v_mfma_f32_16x16x32_bf16 a[24:27], a[196:199], a[152:155], a[24:27]
v_mfma_f32_16x16x32_bf16 a[28:31], a[196:199], a[156:159], a[28:31]
s_waitcnt lgkmcnt(5)
v_mfma_f32_16x16x32_bf16 v[160:163], a[128:131], a[200:203], v[160:163]
v_mfma_f32_16x16x32_bf16 v[164:167], a[132:135], a[200:203], v[164:167]
v_mfma_f32_16x16x32_bf16 v[168:171], a[136:139], a[200:203], v[168:171]
ds_read_b64_tr_b16 a[168:169], v38 offset:33280
ds_read_b64_tr_b16 a[170:171], v40 offset:33280
v_mfma_f32_16x16x32_bf16 v[172:175], a[140:143], a[200:203], v[172:175]
buffer_load_dwordx4 v44, s[36:39], s49 offen offset:2048 lds
v_mfma_f32_16x16x32_bf16 a[32:35], a[200:203], a[144:147], a[32:35]
ds_read_b128 a[208:211], v33 offset:4096
v_mfma_f32_16x16x32_bf16 a[36:39], a[200:203], a[148:151], a[36:39]
v_mfma_f32_16x16x32_bf16 a[40:43], a[200:203], a[152:155], a[40:43]
v_mfma_f32_16x16x32_bf16 a[44:47], a[200:203], a[156:159], a[44:47]
s_waitcnt lgkmcnt(5)
v_mfma_f32_16x16x32_bf16 v[176:179], a[128:131], a[204:207], v[176:179]
ds_read_b64_tr_b16 a[172:173], v39 offset:33280
ds_read_b64_tr_b16 a[174:175], v41 offset:33280
v_mfma_f32_16x16x32_bf16 v[180:183], a[132:135], a[204:207], v[180:183]
v_mfma_f32_16x16x32_bf16 v[184:187], a[136:139], a[204:207], v[184:187]
v_mfma_f32_16x16x32_bf16 v[188:191], a[140:143], a[204:207], v[188:191]
buffer_load_dwordx4 v45, s[36:39], s49 offen offset:3072 lds
v_mfma_f32_16x16x32_bf16 a[48:51], a[204:207], a[144:147], a[48:51]
ds_read_b128 a[212:215], v33 offset:5120
v_mfma_f32_16x16x32_bf16 a[52:55], a[204:207], a[148:151], a[52:55]
v_mfma_f32_16x16x32_bf16 a[56:59], a[204:207], a[152:155], a[56:59]
ds_read_b64_tr_b16 a[176:177], v38 offset:40960
ds_read_b64_tr_b16 a[178:179], v40 offset:40960
v_mfma_f32_16x16x32_bf16 a[60:63], a[204:207], a[156:159], a[60:63]
s_waitcnt lgkmcnt(5)
v_mfma_f32_16x16x32_bf16 v[192:195], a[128:131], a[208:211], v[192:195]
v_mfma_f32_16x16x32_bf16 v[196:199], a[132:135], a[208:211], v[196:199]
s_add_u32 m0, s54, 49152
v_mfma_f32_16x16x32_bf16 v[200:203], a[136:139], a[208:211], v[200:203]
v_mfma_f32_16x16x32_bf16 v[204:207], a[140:143], a[208:211], v[204:207]
buffer_load_dwordx4 v46, s[40:43], s50 offen lds
v_mfma_f32_16x16x32_bf16 a[64:67], a[208:211], a[144:147], a[64:67]
ds_read_b128 a[216:219], v33 offset:6144
ds_read_b64_tr_b16 a[180:181], v39 offset:40960
v_mfma_f32_16x16x32_bf16 a[68:71], a[208:211], a[148:151], a[68:71]
ds_read_b64_tr_b16 a[182:183], v41 offset:40960
v_mfma_f32_16x16x32_bf16 a[72:75], a[208:211], a[152:155], a[72:75]
v_mfma_f32_16x16x32_bf16 a[76:79], a[208:211], a[156:159], a[76:79]
s_waitcnt lgkmcnt(5)
v_mfma_f32_16x16x32_bf16 v[208:211], a[128:131], a[212:215], v[208:211]
v_mfma_f32_16x16x32_bf16 v[212:215], a[132:135], a[212:215], v[212:215]
v_mfma_f32_16x16x32_bf16 v[216:219], a[136:139], a[212:215], v[216:219]
)ASM"
R"ASM(ds_read_b64_tr_b16 a[184:185], v38 offset:41472
ds_read_b64_tr_b16 a[186:187], v40 offset:41472
v_mfma_f32_16x16x32_bf16 v[220:223], a[140:143], a[212:215], v[220:223]
buffer_load_dwordx4 v47, s[40:43], s50 offen offset:1024 lds
v_mfma_f32_16x16x32_bf16 a[80:83], a[212:215], a[144:147], a[80:83]
ds_read_b128 a[220:223], v33 offset:7168
v_mfma_f32_16x16x32_bf16 a[84:87], a[212:215], a[148:151], a[84:87]
v_mfma_f32_16x16x32_bf16 a[88:91], a[212:215], a[152:155], a[88:91]
v_mfma_f32_16x16x32_bf16 a[92:95], a[212:215], a[156:159], a[92:95]
s_waitcnt lgkmcnt(5)
v_mfma_f32_16x16x32_bf16 v[224:227], a[128:131], a[216:219], v[224:227]
s_add_u32 m0, s54, 57344
ds_read_b64_tr_b16 a[188:189], v39 offset:41472
ds_read_b64_tr_b16 a[190:191], v41 offset:41472
v_mfma_f32_16x16x32_bf16 v[228:231], a[132:135], a[216:219], v[228:231]
v_mfma_f32_16x16x32_bf16 v[232:235], a[136:139], a[216:219], v[232:235]
buffer_load_dwordx4 v46, s[44:47], s50 offen lds
v_mfma_f32_16x16x32_bf16 v[236:239], a[140:143], a[216:219], v[236:239]
v_mfma_f32_16x16x32_bf16 a[96:99], a[216:219], a[144:147], a[96:99]
ds_read_b128 a[192:195], v33 offset:32768
v_mfma_f32_16x16x32_bf16 a[100:103], a[216:219], a[148:151], a[100:103]
v_mfma_f32_16x16x32_bf16 a[104:107], a[216:219], a[152:155], a[104:107]
v_mfma_f32_16x16x32_bf16 a[108:111], a[216:219], a[156:159], a[108:111]
s_waitcnt lgkmcnt(3)
v_mfma_f32_16x16x32_bf16 v[240:243], a[128:131], a[220:223], v[240:243]
v_mfma_f32_16x16x32_bf16 v[244:247], a[132:135], a[220:223], v[244:247]
buffer_load_dwordx4 v47, s[44:47], s50 offen offset:1024 lds
v_mfma_f32_16x16x32_bf16 v[248:251], a[136:139], a[220:223], v[248:251]
v_mfma_f32_16x16x32_bf16 v[252:255], a[140:143], a[220:223], v[252:255]
s_add_u32 s49, s49, 64
v_mfma_f32_16x16x32_bf16 a[112:115], a[220:223], a[144:147], a[112:115]
s_add_u32 s50, s50, 262144
ds_read_b128 a[196:199], v33 offset:33792
v_mfma_f32_16x16x32_bf16 a[116:119], a[220:223], a[148:151], a[116:119]
v_mfma_f32_16x16x32_bf16 a[120:123], a[220:223], a[152:155], a[120:123]
v_mfma_f32_16x16x32_bf16 a[124:127], a[220:223], a[156:159], a[124:127]
s_waitcnt lgkmcnt(1)
v_mfma_f32_16x16x32_bf16 v[128:131], a[160:163], a[192:195], v[128:131]
s_waitcnt vmcnt(9)
s_barrier
v_mfma_f32_16x16x32_bf16 v[132:135], a[164:167], a[192:195], v[132:135]
s_add_u32 m0, s48, 65536
v_mfma_f32_16x16x32_bf16 v[136:139], a[168:171], a[192:195], v[136:139]
v_mfma_f32_16x16x32_bf16 v[140:143], a[172:175], a[192:195], v[140:143]
buffer_load_dwordx4 v42, s[36:39], s49 offen lds
v_mfma_f32_16x16x32_bf16 a[0:3], a[192:195], a[176:179], a[0:3]
ds_read_b128 a[200:203], v33 offset:34816
v_mfma_f32_16x16x32_bf16 a[4:7], a[192:195], a[180:183], a[4:7]
v_mfma_f32_16x16x32_bf16 a[8:11], a[192:195], a[184:187], a[8:11]
ds_read_b64_tr_b16 a[128:129], v34
ds_read_b64_tr_b16 a[130:131], v36
v_mfma_f32_16x16x32_bf16 a[12:15], a[192:195], a[188:191], a[12:15]
s_waitcnt lgkmcnt(3)
v_mfma_f32_16x16x32_bf16 v[144:147], a[160:163], a[196:199], v[144:147]
v_mfma_f32_16x16x32_bf16 v[148:151], a[164:167], a[196:199], v[148:151]
v_mfma_f32_16x16x32_bf16 v[152:155], a[168:171], a[196:199], v[152:155]
v_mfma_f32_16x16x32_bf16 v[156:159], a[172:175], a[196:199], v[156:159]
buffer_load_dwordx4 v43, s[36:39], s49 offen offset:1024 lds
v_mfma_f32_16x16x32_bf16 a[16:19], a[196:199], a[176:179], a[16:19]
ds_read_b128 a[204:207], v33 offset:35840
ds_read_b64_tr_b16 a[132:133], v35
v_mfma_f32_16x16x32_bf16 a[20:23], a[196:199], a[180:183], a[20:23]
ds_read_b64_tr_b16 a[134:135], v37
v_mfma_f32_16x16x32_bf16 a[24:27], a[196:199], a[184:187], a[24:27]
v_mfma_f32_16x16x32_bf16 a[28:31], a[196:199], a[188:191], a[28:31]
s_waitcnt lgkmcnt(5)
v_mfma_f32_16x16x32_bf16 v[160:163], a[160:163], a[200:203], v[160:163]
v_mfma_f32_16x16x32_bf16 v[164:167], a[164:167], a[200:203], v[164:167]
v_mfma_f32_16x16x32_bf16 v[168:171], a[168:171], a[200:203], v[168:171]
ds_read_b64_tr_b16 a[136:137], v34 offset:512
ds_read_b64_tr_b16 a[138:139], v36 offset:512
v_mfma_f32_16x16x32_bf16 v[172:175], a[172:175], a[200:203], v[172:175]
buffer_load_dwordx4 v44, s[36:39], s49 offen offset:2048 lds
v_mfma_f32_16x16x32_bf16 a[32:35], a[200:203], a[176:179], a[32:35]
ds_read_b128 a[208:211], v33 offset:36864
v_mfma_f32_16x16x32_bf16 a[36:39], a[200:203], a[180:183], a[36:39]
v_mfma_f32_16x16x32_bf16 a[40:43], a[200:203], a[184:187], a[40:43]
v_mfma_f32_16x16x32_bf16 a[44:47], a[200:203], a[188:191], a[44:47]
s_waitcnt lgkmcnt(5)
v_mfma_f32_16x16x32_bf16 v[176:179], a[160:163], a[204:207], v[176:179]
ds_read_b64_tr_b16 a[140:141], v35 offset:512
ds_read_b64_tr_b16 a[142:143], v37 offset:512
v_mfma_f32_16x16x32_bf16 v[180:183], a[164:167], a[204:207], v[180:183]
v_mfma_f32_16x16x32_bf16 v[184:187], a[168:171], a[204:207], v[184:187]
v_mfma_f32_16x16x32_bf16 v[188:191], a[172:175], a[204:207], v[188:191]
buffer_load_dwordx4 v45, s[36:39], s49 offen offset:3072 lds
v_mfma_f32_16x16x32_bf16 a[48:51], a[204:207], a[176:179], a[48:51]
ds_read_b128 a[212:215], v33 offset:37888
v_mfma_f32_16x16x32_bf16 a[52:55], a[204:207], a[180:183], a[52:55]
v_mfma_f32_16x16x32_bf16 a[56:59], a[204:207], a[184:187], a[56:59]
ds_read_b64_tr_b16 a[144:145], v34 offset:8192
ds_read_b64_tr_b16 a[146:147], v36 offset:8192
v_mfma_f32_16x16x32_bf16 a[60:63], a[204:207], a[188:191], a[60:63]
s_waitcnt lgkmcnt(5)
v_mfma_f32_16x16x32_bf16 v[192:195], a[160:163], a[208:211], v[192:195]
v_mfma_f32_16x16x32_bf16 v[196:199], a[164:167], a[208:211], v[196:199]
s_add_u32 m0, s54, 81920
v_mfma_f32_16x16x32_bf16 v[200:203], a[168:171], a[208:211], v[200:203]
v_mfma_f32_16x16x32_bf16 v[204:207], a[172:175], a[208:211], v[204:207]
buffer_load_dwordx4 v46, s[40:43], s50 offen lds
v_mfma_f32_16x16x32_bf16 a[64:67], a[208:211], a[176:179], a[64:67]
ds_read_b128 a[216:219], v33 offset:38912
ds_read_b64_tr_b16 a[148:149], v35 offset:8192
v_mfma_f32_16x16x32_bf16 a[68:71], a[208:211], a[180:183], a[68:71]
ds_read_b64_tr_b16 a[150:151], v37 offset:8192
v_mfma_f32_16x16x32_bf16 a[72:75], a[208:211], a[184:187], a[72:75]
v_mfma_f32_16x16x32_bf16 a[76:79], a[208:211], a[188:191], a[76:79]
s_waitcnt lgkmcnt(5)
v_mfma_f32_16x16x32_bf16 v[208:211], a[160:163], a[212:215], v[208:211]
v_mfma_f32_16x16x32_bf16 v[212:215], a[164:167], a[212:215], v[212:215]
v_mfma_f32_16x16x32_bf16 v[216:219], a[168:171], a[212:215], v[216:219]
ds_read_b64_tr_b16 a[152:153], v34 offset:8704
ds_read_b64_tr_b16 a[154:155], v36 offset:8704
v_mfma_f32_16x16x32_bf16 v[220:223], a[172:175], a[212:215], v[220:223]
buffer_load_dwordx4 v47, s[40:43], s50 offen offset:1024 lds
v_mfma_f32_16x16x32_bf16 a[80:83], a[212:215], a[176:179], a[80:83]
ds_read_b128 a[220:223], v33 offset:39936
v_mfma_f32_16x16x32_bf16 a[84:87], a[212:215], a[180:183], a[84:87]
v_mfma_f32_16x16x32_bf16 a[88:91], a[212:215], a[184:187], a[88:91]
v_mfma_f32_16x16x32_bf16 a[92:95], a[212:215], a[188:191], a[92:95]
s_waitcnt lgkmcnt(5)
v_mfma_f32_16x16x32_bf16 v[224:227], a[160:163], a[216:219], v[224:227]
s_add_u32 m0, s54, 90112
ds_read_b64_tr_b16 a[156:157], v35 offset:8704
ds_read_b64_tr_b16 a[158:159], v37 offset:8704
v_mfma_f32_16x16x32_bf16 v[228:231], a[164:167], a[216:219], v[228:231]
v_mfma_f32_16x16x32_bf16 v[232:235], a[168:171], a[216:219], v[232:235]
buffer_load_dwordx4 v46, s[44:47], s50 offen lds
v_mfma_f32_16x16x32_bf16 v[236:239], a[172:175], a[216:219], v[236:239]
v_mfma_f32_16x16x32_bf16 a[96:99], a[216:219], a[176:179], a[96:99]
ds_read_b128 a[192:195], v32
v_mfma_f32_16x16x32_bf16 a[100:103], a[216:219], a[180:183], a[100:103]
v_mfma_f32_16x16x32_bf16 a[104:107], a[216:219], a[184:187], a[104:107]
v_mfma_f32_16x16x32_bf16 a[108:111], a[216:219], a[188:191], a[108:111]
s_waitcnt lgkmcnt(3)
v_mfma_f32_16x16x32_bf16 v[240:243], a[160:163], a[220:223], v[240:243]
v_mfma_f32_16x16x32_bf16 v[244:247], a[164:167], a[220:223], v[244:247]
buffer_load_dwordx4 v47, s[44:47], s50 offen offset:1024 lds
v_mfma_f32_16x16x32_bf16 v[248:251], a[168:171], a[220:223], v[248:251]
v_mfma_f32_16x16x32_bf16 v[252:255], a[172:175], a[220:223], v[252:255]
s_add_u32 s49, s49, 64
v_mfma_f32_16x16x32_bf16 a[112:115], a[220:223], a[176:179], a[112:115]
s_add_u32 s50, s50, 262144
ds_read_b128 a[196:199], v32 offset:1024
v_mfma_f32_16x16x32_bf16 a[116:119], a[220:223], a[180:183], a[116:119]
v_mfma_f32_16x16x32_bf16 a[120:123], a[220:223], a[184:187], a[120:123]
global_load_dword %[sink], %[pf4], off
v_mfma_f32_16x16x32_bf16 a[124:127], a[220:223], a[188:191], a[124:127]
global_load_dwordx2 v[64:65], %[pl], %[flags] sc1
s_waitcnt lgkmcnt(1)
v_mfma_f32_16x16x32_bf16 v[128:131], a[128:131], a[192:195], v[128:131]
s_waitcnt vmcnt(10)
s_barrier
v_mfma_f32_16x16x32_bf16 v[132:135], a[132:135], a[192:195], v[132:135]
s_add_u32 m0, s48, 98304
v_mfma_f32_16x16x32_bf16 v[136:139], a[136:139], a[192:195], v[136:139]
v_mfma_f32_16x16x32_bf16 v[140:143], a[140:143], a[192:195], v[140:143]
buffer_load_dwordx4 v42, s[36:39], s49 offen lds
v_mfma_f32_16x16x32_bf16 a[0:3], a[192:195], a[144:147], a[0:3]
ds_read_b128 a[200:203], v32 offset:2048
v_mfma_f32_16x16x32_bf16 a[4:7], a[192:195], a[148:151], a[4:7]
v_mfma_f32_16x16x32_bf16 a[8:11], a[192:195], a[152:155], a[8:11]
ds_read_b64_tr_b16 a[160:161], v34 offset:32768
ds_read_b64_tr_b16 a[162:163], v36 offset:32768
v_mfma_f32_16x16x32_bf16 a[12:15], a[192:195], a[156:159], a[12:15]
s_waitcnt lgkmcnt(3)
v_mfma_f32_16x16x32_bf16 v[144:147], a[128:131], a[196:199], v[144:147]
v_mfma_f32_16x16x32_bf16 v[148:151], a[132:135], a[196:199], v[148:151]
v_mfma_f32_16x16x32_bf16 v[152:155], a[136:139], a[196:199], v[152:155]
v_mfma_f32_16x16x32_bf16 v[156:159], a[140:143], a[196:199], v[156:159]
buffer_load_dwordx4 v43, s[36:39], s49 offen offset:1024 lds
v_mfma_f32_16x16x32_bf16 a[16:19], a[196:199], a[144:147], a[16:19]
ds_read_b128 a[204:207], v32 offset:3072
ds_read_b64_tr_b16 a[164:165], v35 offset:32768
v_mfma_f32_16x16x32_bf16 a[20:23], a[196:199], a[148:151], a[20:23]
ds_read_b64_tr_b16 a[166:167], v37 offset:32768
v_mfma_f32_16x16x32_bf16 a[24:27], a[196:199], a[152:155], a[24:27]
v_mfma_f32_16x16x32_bf16 a[28:31], a[196:199], a[156:159], a[28:31]
s_waitcnt lgkmcnt(5)
v_mfma_f32_16x16x32_bf16 v[160:163], a[128:131], a[200:203], v[160:163]
v_mfma_f32_16x16x32_bf16 v[164:167], a[132:135], a[200:203], v[164:167]
v_mfma_f32_16x16x32_bf16 v[168:171], a[136:139], a[200:203], v[168:171]
ds_read_b64_tr_b16 a[168:169], v34 offset:33280
ds_read_b64_tr_b16 a[170:171], v36 offset:33280
v_mfma_f32_16x16x32_bf16 v[172:175], a[140:143], a[200:203], v[172:175]
buffer_load_dwordx4 v44, s[36:39], s49 offen offset:2048 lds
v_mfma_f32_16x16x32_bf16 a[32:35], a[200:203], a[144:147], a[32:35]
ds_read_b128 a[208:211], v32 offset:4096
v_mfma_f32_16x16x32_bf16 a[36:39], a[200:203], a[148:151], a[36:39]
v_mfma_f32_16x16x32_bf16 a[40:43], a[200:203], a[152:155], a[40:43]
v_mfma_f32_16x16x32_bf16 a[44:47], a[200:203], a[156:159], a[44:47]
s_waitcnt lgkmcnt(5)
v_mfma_f32_16x16x32_bf16 v[176:179], a[128:131], a[204:207], v[176:179]
ds_read_b64_tr_b16 a[172:173], v35 offset:33280
ds_read_b64_tr_b16 a[174:175], v37 offset:33280
v_mfma_f32_16x16x32_bf16 v[180:183], a[132:135], a[204:207], v[180:183]
v_mfma_f32_16x16x32_bf16 v[184:187], a[136:139], a[204:207], v[184:187]
v_mfma_f32_16x16x32_bf16 v[188:191], a[140:143], a[204:207], v[188:191]
buffer_load_dwordx4 v45, s[36:39], s49 offen offset:3072 lds
v_mfma_f32_16x16x32_bf16 a[48:51], a[204:207], a[144:147], a[48:51]
ds_read_b128 a[212:215], v32 offset:5120
v_mfma_f32_16x16x32_bf16 a[52:55], a[204:207], a[148:151], a[52:55]
v_mfma_f32_16x16x32_bf16 a[56:59], a[204:207], a[152:155], a[56:59]
ds_read_b64_tr_b16 a[176:177], v34 offset:40960
ds_read_b64_tr_b16 a[178:179], v36 offset:40960
v_mfma_f32_16x16x32_bf16 a[60:63], a[204:207], a[156:159], a[60:63]
s_waitcnt lgkmcnt(5)
v_mfma_f32_16x16x32_bf16 v[192:195], a[128:131], a[208:211], v[192:195]
v_mfma_f32_16x16x32_bf16 v[196:199], a[132:135], a[208:211], v[196:199]
s_add_u32 m0, s54, 114688
v_mfma_f32_16x16x32_bf16 v[200:203], a[136:139], a[208:211], v[200:203]
v_mfma_f32_16x16x32_bf16 v[204:207], a[140:143], a[208:211], v[204:207]
)ASM"
R"ASM(buffer_load_dwordx4 v46, s[40:43], s50 offen lds
v_mfma_f32_16x16x32_bf16 a[64:67], a[208:211], a[144:147], a[64:67]
ds_read_b128 a[216:219], v32 offset:6144
ds_read_b64_tr_b16 a[180:181], v35 offset:40960
v_mfma_f32_16x16x32_bf16 a[68:71], a[208:211], a[148:151], a[68:71]
ds_read_b64_tr_b16 a[182:183], v37 offset:40960
v_mfma_f32_16x16x32_bf16 a[72:75], a[208:211], a[152:155], a[72:75]
v_mfma_f32_16x16x32_bf16 a[76:79], a[208:211], a[156:159], a[76:79]
s_waitcnt lgkmcnt(5)
v_mfma_f32_16x16x32_bf16 v[208:211], a[128:131], a[212:215], v[208:211]
v_mfma_f32_16x16x32_bf16 v[212:215], a[132:135], a[212:215], v[212:215]
v_mfma_f32_16x16x32_bf16 v[216:219], a[136:139], a[212:215], v[216:219]
ds_read_b64_tr_b16 a[184:185], v34 offset:41472
ds_read_b64_tr_b16 a[186:187], v36 offset:41472
v_mfma_f32_16x16x32_bf16 v[220:223], a[140:143], a[212:215], v[220:223]
buffer_load_dwordx4 v47, s[40:43], s50 offen offset:1024 lds
v_mfma_f32_16x16x32_bf16 a[80:83], a[212:215], a[144:147], a[80:83]
ds_read_b128 a[220:223], v32 offset:7168
v_mfma_f32_16x16x32_bf16 a[84:87], a[212:215], a[148:151], a[84:87]
v_mfma_f32_16x16x32_bf16 a[88:91], a[212:215], a[152:155], a[88:91]
v_mfma_f32_16x16x32_bf16 a[92:95], a[212:215], a[156:159], a[92:95]
s_waitcnt lgkmcnt(5)
v_mfma_f32_16x16x32_bf16 v[224:227], a[128:131], a[216:219], v[224:227]
s_add_u32 m0, s54, 122880
ds_read_b64_tr_b16 a[188:189], v35 offset:41472
ds_read_b64_tr_b16 a[190:191], v37 offset:41472
v_mfma_f32_16x16x32_bf16 v[228:231], a[132:135], a[216:219], v[228:231]
v_mfma_f32_16x16x32_bf16 v[232:235], a[136:139], a[216:219], v[232:235]
buffer_load_dwordx4 v46, s[44:47], s50 offen lds
v_mfma_f32_16x16x32_bf16 v[236:239], a[140:143], a[216:219], v[236:239]
v_mfma_f32_16x16x32_bf16 a[96:99], a[216:219], a[144:147], a[96:99]
ds_read_b128 a[192:195], v32 offset:32768
v_mfma_f32_16x16x32_bf16 a[100:103], a[216:219], a[148:151], a[100:103]
v_mfma_f32_16x16x32_bf16 a[104:107], a[216:219], a[152:155], a[104:107]
v_mfma_f32_16x16x32_bf16 a[108:111], a[216:219], a[156:159], a[108:111]
s_waitcnt lgkmcnt(3)
v_mfma_f32_16x16x32_bf16 v[240:243], a[128:131], a[220:223], v[240:243]
v_mfma_f32_16x16x32_bf16 v[244:247], a[132:135], a[220:223], v[244:247]
buffer_load_dwordx4 v47, s[44:47], s50 offen offset:1024 lds
v_mfma_f32_16x16x32_bf16 v[248:251], a[136:139], a[220:223], v[248:251]
v_mfma_f32_16x16x32_bf16 v[252:255], a[140:143], a[220:223], v[252:255]
s_add_u32 s49, s49, 64
v_mfma_f32_16x16x32_bf16 a[112:115], a[220:223], a[144:147], a[112:115]
s_add_u32 s50, s50, 262144
ds_read_b128 a[196:199], v32 offset:33792
v_mfma_f32_16x16x32_bf16 a[116:119], a[220:223], a[148:151], a[116:119]
v_mfma_f32_16x16x32_bf16 a[120:123], a[220:223], a[152:155], a[120:123]
v_mfma_f32_16x16x32_bf16 a[124:127], a[220:223], a[156:159], a[124:127]
s_waitcnt lgkmcnt(1)
v_mfma_f32_16x16x32_bf16 v[128:131], a[160:163], a[192:195], v[128:131]
s_waitcnt vmcnt(10)
s_barrier
s_waitcnt vmcnt(8)
v_cmp_ne_u64_e32 vcc, s[56:57], v[64:65]
s_nop 4
s_cmp_lg_u64 vcc, 0
s_cbranch_scc0 L_pok2_%=
s_mov_b32 s58, 0x10000
L_plp2_%=:
s_sleep 2
global_load_dwordx2 v[64:65], %[pl], %[flags] sc1
s_waitcnt vmcnt(0)
v_cmp_ne_u64_e32 vcc, s[56:57], v[64:65]
s_nop 4
s_cmp_lg_u64 vcc, 0
s_cbranch_scc0 L_pok2_%=
s_sub_u32 s58, s58, 1
s_cmp_lg_u32 s58, 0
s_cbranch_scc1 L_plp2_%=
L_pok2_%=:
v_mfma_f32_16x16x32_bf16 v[132:135], a[164:167], a[192:195], v[132:135]
v_mfma_f32_16x16x32_bf16 v[136:139], a[168:171], a[192:195], v[136:139]
v_mfma_f32_16x16x32_bf16 v[140:143], a[172:175], a[192:195], v[140:143]
v_mfma_f32_16x16x32_bf16 a[0:3], a[192:195], a[176:179], a[0:3]
ds_read_b128 a[200:203], v32 offset:34816
v_mfma_f32_16x16x32_bf16 a[4:7], a[192:195], a[180:183], a[4:7]
v_mfma_f32_16x16x32_bf16 a[8:11], a[192:195], a[184:187], a[8:11]
ds_read_b64_tr_b16 a[128:129], v38
ds_read_b64_tr_b16 a[130:131], v40
v_mfma_f32_16x16x32_bf16 a[12:15], a[192:195], a[188:191], a[12:15]
s_waitcnt lgkmcnt(3)
v_mfma_f32_16x16x32_bf16 v[144:147], a[160:163], a[196:199], v[144:147]
v_mfma_f32_16x16x32_bf16 v[148:151], a[164:167], a[196:199], v[148:151]
v_mfma_f32_16x16x32_bf16 v[152:155], a[168:171], a[196:199], v[152:155]
v_mfma_f32_16x16x32_bf16 v[156:159], a[172:175], a[196:199], v[156:159]
v_mfma_f32_16x16x32_bf16 a[16:19], a[196:199], a[176:179], a[16:19]
ds_read_b128 a[204:207], v32 offset:35840
ds_read_b64_tr_b16 a[132:133], v39
v_mfma_f32_16x16x32_bf16 a[20:23], a[196:199], a[180:183], a[20:23]
ds_read_b64_tr_b16 a[134:135], v41
v_mfma_f32_16x16x32_bf16 a[24:27], a[196:199], a[184:187], a[24:27]
v_mfma_f32_16x16x32_bf16 a[28:31], a[196:199], a[188:191], a[28:31]
s_waitcnt lgkmcnt(5)
v_mfma_f32_16x16x32_bf16 v[160:163], a[160:163], a[200:203], v[160:163]
v_mfma_f32_16x16x32_bf16 v[164:167], a[164:167], a[200:203], v[164:167]
v_mfma_f32_16x16x32_bf16 v[168:171], a[168:171], a[200:203], v[168:171]
ds_read_b64_tr_b16 a[136:137], v38 offset:512
ds_read_b64_tr_b16 a[138:139], v40 offset:512
v_mfma_f32_16x16x32_bf16 v[172:175], a[172:175], a[200:203], v[172:175]
v_mfma_f32_16x16x32_bf16 a[32:35], a[200:203], a[176:179], a[32:35]
ds_read_b128 a[208:211], v32 offset:36864
v_mfma_f32_16x16x32_bf16 a[36:39], a[200:203], a[180:183], a[36:39]
v_mfma_f32_16x16x32_bf16 a[40:43], a[200:203], a[184:187], a[40:43]
v_mfma_f32_16x16x32_bf16 a[44:47], a[200:203], a[188:191], a[44:47]
s_waitcnt lgkmcnt(5)
v_mfma_f32_16x16x32_bf16 v[176:179], a[160:163], a[204:207], v[176:179]
ds_read_b64_tr_b16 a[140:141], v39 offset:512
ds_read_b64_tr_b16 a[142:143], v41 offset:512
v_mfma_f32_16x16x32_bf16 v[180:183], a[164:167], a[204:207], v[180:183]
v_mfma_f32_16x16x32_bf16 v[184:187], a[168:171], a[204:207], v[184:187]
v_mfma_f32_16x16x32_bf16 v[188:191], a[172:175], a[204:207], v[188:191]
v_mfma_f32_16x16x32_bf16 a[48:51], a[204:207], a[176:179], a[48:51]
ds_read_b128 a[212:215], v32 offset:37888
v_mfma_f32_16x16x32_bf16 a[52:55], a[204:207], a[180:183], a[52:55]
v_mfma_f32_16x16x32_bf16 a[56:59], a[204:207], a[184:187], a[56:59]
ds_read_b64_tr_b16 a[144:145], v38 offset:8192
ds_read_b64_tr_b16 a[146:147], v40 offset:8192
v_mfma_f32_16x16x32_bf16 a[60:63], a[204:207], a[188:191], a[60:63]
s_waitcnt lgkmcnt(5)
v_mfma_f32_16x16x32_bf16 v[192:195], a[160:163], a[208:211], v[192:195]
v_mfma_f32_16x16x32_bf16 v[196:199], a[164:167], a[208:211], v[196:199]
v_mfma_f32_16x16x32_bf16 v[200:203], a[168:171], a[208:211], v[200:203]
v_mfma_f32_16x16x32_bf16 v[204:207], a[172:175], a[208:211], v[204:207]
v_mfma_f32_16x16x32_bf16 a[64:67], a[208:211], a[176:179], a[64:67]
ds_read_b128 a[216:219], v32 offset:38912
ds_read_b64_tr_b16 a[148:149], v39 offset:8192
v_mfma_f32_16x16x32_bf16 a[68:71], a[208:211], a[180:183], a[68:71]
ds_read_b64_tr_b16 a[150:151], v41 offset:8192
v_mfma_f32_16x16x32_bf16 a[72:75], a[208:211], a[184:187], a[72:75]
v_mfma_f32_16x16x32_bf16 a[76:79], a[208:211], a[188:191], a[76:79]
s_waitcnt lgkmcnt(5)
v_mfma_f32_16x16x32_bf16 v[208:211], a[160:163], a[212:215], v[208:211]
v_mfma_f32_16x16x32_bf16 v[212:215], a[164:167], a[212:215], v[212:215]
v_mfma_f32_16x16x32_bf16 v[216:219], a[168:171], a[212:215], v[216:219]
ds_read_b64_tr_b16 a[152:153], v38 offset:8704
ds_read_b64_tr_b16 a[154:155], v40 offset:8704
v_mfma_f32_16x16x32_bf16 v[220:223], a[172:175], a[212:215], v[220:223]
v_mfma_f32_16x16x32_bf16 a[80:83], a[212:215], a[176:179], a[80:83]
ds_read_b128 a[220:223], v32 offset:39936
v_mfma_f32_16x16x32_bf16 a[84:87], a[212:215], a[180:183], a[84:87]
v_mfma_f32_16x16x32_bf16 a[88:91], a[212:215], a[184:187], a[88:91]
v_mfma_f32_16x16x32_bf16 a[92:95], a[212:215], a[188:191], a[92:95]
s_waitcnt lgkmcnt(5)
v_mfma_f32_16x16x32_bf16 v[224:227], a[160:163], a[216:219], v[224:227]
ds_read_b64_tr_b16 a[156:157], v39 offset:8704
ds_read_b64_tr_b16 a[158:159], v41 offset:8704
v_mfma_f32_16x16x32_bf16 v[228:231], a[164:167], a[216:219], v[228:231]
v_mfma_f32_16x16x32_bf16 v[232:235], a[168:171], a[216:219], v[232:235]
v_mfma_f32_16x16x32_bf16 v[236:239], a[172:175], a[216:219], v[236:239]
v_mfma_f32_16x16x32_bf16 a[96:99], a[216:219], a[176:179], a[96:99]
ds_read_b128 a[192:195], v33
v_mfma_f32_16x16x32_bf16 a[100:103], a[216:219], a[180:183], a[100:103]
v_mfma_f32_16x16x32_bf16 a[104:107], a[216:219], a[184:187], a[104:107]
v_mfma_f32_16x16x32_bf16 a[108:111], a[216:219], a[188:191], a[108:111]
s_waitcnt lgkmcnt(3)
v_mfma_f32_16x16x32_bf16 v[240:243], a[160:163], a[220:223], v[240:243]
v_mfma_f32_16x16x32_bf16 v[244:247], a[164:167], a[220:223], v[244:247]
v_mfma_f32_16x16x32_bf16 v[248:251], a[168:171], a[220:223], v[248:251]
v_mfma_f32_16x16x32_bf16 v[252:255], a[172:175], a[220:223], v[252:255]
v_mfma_f32_16x16x32_bf16 a[112:115], a[220:223], a[176:179], a[112:115]
ds_read_b128 a[196:199], v33 offset:1024
v_mfma_f32_16x16x32_bf16 a[116:119], a[220:223], a[180:183], a[116:119]
v_mfma_f32_16x16x32_bf16 a[120:123], a[220:223], a[184:187], a[120:123]
v_mfma_f32_16x16x32_bf16 a[124:127], a[220:223], a[188:191], a[124:127]
s_waitcnt lgkmcnt(1)
v_mfma_f32_16x16x32_bf16 v[128:131], a[128:131], a[192:195], v[128:131]
s_waitcnt vmcnt(0)
s_barrier
v_mfma_f32_16x16x32_bf16 v[132:135], a[132:135], a[192:195], v[132:135]
v_mfma_f32_16x16x32_bf16 v[136:139], a[136:139], a[192:195], v[136:139]
v_mfma_f32_16x16x32_bf16 v[140:143], a[140:143], a[192:195], v[140:143]
v_mfma_f32_16x16x32_bf16 a[0:3], a[192:195], a[144:147], a[0:3]
ds_read_b128 a[200:203], v33 offset:2048
v_mfma_f32_16x16x32_bf16 a[4:7], a[192:195], a[148:151], a[4:7]
v_mfma_f32_16x16x32_bf16 a[8:11], a[192:195], a[152:155], a[8:11]
ds_read_b64_tr_b16 a[160:161], v38 offset:32768
ds_read_b64_tr_b16 a[162:163], v40 offset:32768
v_mfma_f32_16x16x32_bf16 a[12:15], a[192:195], a[156:159], a[12:15]
s_waitcnt lgkmcnt(3)
v_mfma_f32_16x16x32_bf16 v[144:147], a[128:131], a[196:199], v[144:147]
v_mfma_f32_16x16x32_bf16 v[148:151], a[132:135], a[196:199], v[148:151]
v_mfma_f32_16x16x32_bf16 v[152:155], a[136:139], a[196:199], v[152:155]
v_mfma_f32_16x16x32_bf16 v[156:159], a[140:143], a[196:199], v[156:159]
v_mfma_f32_16x16x32_bf16 a[16:19], a[196:199], a[144:147], a[16:19]
ds_read_b128 a[204:207], v33 offset:3072
ds_read_b64_tr_b16 a[164:165], v39 offset:32768
v_mfma_f32_16x16x32_bf16 a[20:23], a[196:199], a[148:151], a[20:23]
ds_read_b64_tr_b16 a[166:167], v41 offset:32768
v_mfma_f32_16x16x32_bf16 a[24:27], a[196:199], a[152:155], a[24:27]
v_mfma_f32_16x16x32_bf16 a[28:31], a[196:199], a[156:159], a[28:31]
s_waitcnt lgkmcnt(5)
v_mfma_f32_16x16x32_bf16 v[160:163], a[128:131], a[200:203], v[160:163]
v_mfma_f32_16x16x32_bf16 v[164:167], a[132:135], a[200:203], v[164:167]
v_mfma_f32_16x16x32_bf16 v[168:171], a[136:139], a[200:203], v[168:171]
ds_read_b64_tr_b16 a[168:169], v38 offset:33280
ds_read_b64_tr_b16 a[170:171], v40 offset:33280
v_mfma_f32_16x16x32_bf16 v[172:175], a[140:143], a[200:203], v[172:175]
v_mfma_f32_16x16x32_bf16 a[32:35], a[200:203], a[144:147], a[32:35]
ds_read_b128 a[208:211], v33 offset:4096
v_mfma_f32_16x16x32_bf16 a[36:39], a[200:203], a[148:151], a[36:39]
v_mfma_f32_16x16x32_bf16 a[40:43], a[200:203], a[152:155], a[40:43]
v_mfma_f32_16x16x32_bf16 a[44:47], a[200:203], a[156:159], a[44:47]
s_waitcnt lgkmcnt(5)
v_mfma_f32_16x16x32_bf16 v[176:179], a[128:131], a[204:207], v[176:179]
ds_read_b64_tr_b16 a[172:173], v39 offset:33280
ds_read_b64_tr_b16 a[174:175], v41 offset:33280
v_mfma_f32_16x16x32_bf16 v[180:183], a[132:135], a[204:207], v[180:183]
v_mfma_f32_16x16x32_bf16 v[184:187], a[136:139], a[204:207], v[184:187]
v_mfma_f32_16x16x32_bf16 v[188:191], a[140:143], a[204:207], v[188:191]
v_mfma_f32_16x16x32_bf16 a[48:51], a[204:207], a[144:147], a[48:51]
ds_read_b128 a[212:215], v33 offset:5120
v_mfma_f32_16x16x32_bf16 a[52:55], a[204:207], a[148:151], a[52:55]
v_mfma_f32_16x16x32_bf16 a[56:59], a[204:207], a[152:155], a[56:59]
ds_read_b64_tr_b16 a[176:177], v38 offset:40960
ds_read_b64_tr_b16 a[178:179], v40 offset:40960
v_mfma_f32_16x16x32_bf16 a[60:63], a[204:207], a[156:159], a[60:63]
s_waitcnt lgkmcnt(5)
v_mfma_f32_16x16x32_bf16 v[192:195], a[128:131], a[208:211], v[192:195]
v_mfma_f32_16x16x32_bf16 v[196:199], a[132:135], a[208:211], v[196:199]
)ASM"
R"ASM(v_mfma_f32_16x16x32_bf16 v[200:203], a[136:139], a[208:211], v[200:203]
v_mfma_f32_16x16x32_bf16 v[204:207], a[140:143], a[208:211], v[204:207]
v_mfma_f32_16x16x32_bf16 a[64:67], a[208:211], a[144:147], a[64:67]
ds_read_b128 a[216:219], v33 offset:6144
ds_read_b64_tr_b16 a[180:181], v39 offset:40960
v_mfma_f32_16x16x32_bf16 a[68:71], a[208:211], a[148:151], a[68:71]
ds_read_b64_tr_b16 a[182:183], v41 offset:40960
v_mfma_f32_16x16x32_bf16 a[72:75], a[208:211], a[152:155], a[72:75]
v_mfma_f32_16x16x32_bf16 a[76:79], a[208:211], a[156:159], a[76:79]
s_waitcnt lgkmcnt(5)
v_mfma_f32_16x16x32_bf16 v[208:211], a[128:131], a[212:215], v[208:211]
v_mfma_f32_16x16x32_bf16 v[212:215], a[132:135], a[212:215], v[212:215]
v_mfma_f32_16x16x32_bf16 v[216:219], a[136:139], a[212:215], v[216:219]
ds_read_b64_tr_b16 a[184:185], v38 offset:41472
ds_read_b64_tr_b16 a[186:187], v40 offset:41472
v_mfma_f32_16x16x32_bf16 v[220:223], a[140:143], a[212:215], v[220:223]
v_mfma_f32_16x16x32_bf16 a[80:83], a[212:215], a[144:147], a[80:83]
ds_read_b128 a[220:223], v33 offset:7168
v_mfma_f32_16x16x32_bf16 a[84:87], a[212:215], a[148:151], a[84:87]
v_mfma_f32_16x16x32_bf16 a[88:91], a[212:215], a[152:155], a[88:91]
v_mfma_f32_16x16x32_bf16 a[92:95], a[212:215], a[156:159], a[92:95]
s_waitcnt lgkmcnt(5)
v_mfma_f32_16x16x32_bf16 v[224:227], a[128:131], a[216:219], v[224:227]
ds_read_b64_tr_b16 a[188:189], v39 offset:41472
ds_read_b64_tr_b16 a[190:191], v41 offset:41472
v_mfma_f32_16x16x32_bf16 v[228:231], a[132:135], a[216:219], v[228:231]
v_mfma_f32_16x16x32_bf16 v[232:235], a[136:139], a[216:219], v[232:235]
v_mfma_f32_16x16x32_bf16 v[236:239], a[140:143], a[216:219], v[236:239]
v_mfma_f32_16x16x32_bf16 a[96:99], a[216:219], a[144:147], a[96:99]
ds_read_b128 a[192:195], v33 offset:32768
v_mfma_f32_16x16x32_bf16 a[100:103], a[216:219], a[148:151], a[100:103]
v_mfma_f32_16x16x32_bf16 a[104:107], a[216:219], a[152:155], a[104:107]
v_mfma_f32_16x16x32_bf16 a[108:111], a[216:219], a[156:159], a[108:111]
s_waitcnt lgkmcnt(3)
v_mfma_f32_16x16x32_bf16 v[240:243], a[128:131], a[220:223], v[240:243]
v_mfma_f32_16x16x32_bf16 v[244:247], a[132:135], a[220:223], v[244:247]
v_mfma_f32_16x16x32_bf16 v[248:251], a[136:139], a[220:223], v[248:251]
v_mfma_f32_16x16x32_bf16 v[252:255], a[140:143], a[220:223], v[252:255]
v_mfma_f32_16x16x32_bf16 a[112:115], a[220:223], a[144:147], a[112:115]
ds_read_b128 a[196:199], v33 offset:33792
v_mfma_f32_16x16x32_bf16 a[116:119], a[220:223], a[148:151], a[116:119]
v_mfma_f32_16x16x32_bf16 a[120:123], a[220:223], a[152:155], a[120:123]
v_mfma_f32_16x16x32_bf16 a[124:127], a[220:223], a[156:159], a[124:127]
s_waitcnt lgkmcnt(1)
v_mfma_f32_16x16x32_bf16 v[128:131], a[160:163], a[192:195], v[128:131]
v_mfma_f32_16x16x32_bf16 v[132:135], a[164:167], a[192:195], v[132:135]
v_mfma_f32_16x16x32_bf16 v[136:139], a[168:171], a[192:195], v[136:139]
v_mfma_f32_16x16x32_bf16 v[140:143], a[172:175], a[192:195], v[140:143]
v_mfma_f32_16x16x32_bf16 a[0:3], a[192:195], a[176:179], a[0:3]
ds_read_b128 a[200:203], v33 offset:34816
v_mfma_f32_16x16x32_bf16 a[4:7], a[192:195], a[180:183], a[4:7]
v_mfma_f32_16x16x32_bf16 a[8:11], a[192:195], a[184:187], a[8:11]
v_mfma_f32_16x16x32_bf16 a[12:15], a[192:195], a[188:191], a[12:15]
s_waitcnt lgkmcnt(1)
v_mfma_f32_16x16x32_bf16 v[144:147], a[160:163], a[196:199], v[144:147]
v_mfma_f32_16x16x32_bf16 v[148:151], a[164:167], a[196:199], v[148:151]
v_mfma_f32_16x16x32_bf16 v[152:155], a[168:171], a[196:199], v[152:155]
v_mfma_f32_16x16x32_bf16 v[156:159], a[172:175], a[196:199], v[156:159]
v_mfma_f32_16x16x32_bf16 a[16:19], a[196:199], a[176:179], a[16:19]
ds_read_b128 a[204:207], v33 offset:35840
v_mfma_f32_16x16x32_bf16 a[20:23], a[196:199], a[180:183], a[20:23]
v_mfma_f32_16x16x32_bf16 a[24:27], a[196:199], a[184:187], a[24:27]
v_mfma_f32_16x16x32_bf16 a[28:31], a[196:199], a[188:191], a[28:31]
s_waitcnt lgkmcnt(1)
v_mfma_f32_16x16x32_bf16 v[160:163], a[160:163], a[200:203], v[160:163]
v_mfma_f32_16x16x32_bf16 v[164:167], a[164:167], a[200:203], v[164:167]
v_mfma_f32_16x16x32_bf16 v[168:171], a[168:171], a[200:203], v[168:171]
v_mfma_f32_16x16x32_bf16 v[172:175], a[172:175], a[200:203], v[172:175]
v_mfma_f32_16x16x32_bf16 a[32:35], a[200:203], a[176:179], a[32:35]
ds_read_b128 a[208:211], v33 offset:36864
v_mfma_f32_16x16x32_bf16 a[36:39], a[200:203], a[180:183], a[36:39]
v_mfma_f32_16x16x32_bf16 a[40:43], a[200:203], a[184:187], a[40:43]
v_mfma_f32_16x16x32_bf16 a[44:47], a[200:203], a[188:191], a[44:47]
s_waitcnt lgkmcnt(1)
v_mfma_f32_16x16x32_bf16 v[176:179], a[160:163], a[204:207], v[176:179]
v_mfma_f32_16x16x32_bf16 v[180:183], a[164:167], a[204:207], v[180:183]
v_mfma_f32_16x16x32_bf16 v[184:187], a[168:171], a[204:207], v[184:187]
v_mfma_f32_16x16x32_bf16 v[188:191], a[172:175], a[204:207], v[188:191]
v_mfma_f32_16x16x32_bf16 a[48:51], a[204:207], a[176:179], a[48:51]
ds_read_b128 a[212:215], v33 offset:37888
v_mfma_f32_16x16x32_bf16 a[52:55], a[204:207], a[180:183], a[52:55]
v_mfma_f32_16x16x32_bf16 a[56:59], a[204:207], a[184:187], a[56:59]
v_mfma_f32_16x16x32_bf16 a[60:63], a[204:207], a[188:191], a[60:63]
s_waitcnt lgkmcnt(1)
v_mfma_f32_16x16x32_bf16 v[192:195], a[160:163], a[208:211], v[192:195]
v_mfma_f32_16x16x32_bf16 v[196:199], a[164:167], a[208:211], v[196:199]
v_mfma_f32_16x16x32_bf16 v[200:203], a[168:171], a[208:211], v[200:203]
v_mfma_f32_16x16x32_bf16 v[204:207], a[172:175], a[208:211], v[204:207]
v_mfma_f32_16x16x32_bf16 a[64:67], a[208:211], a[176:179], a[64:67]
ds_read_b128 a[216:219], v33 offset:38912
v_mfma_f32_16x16x32_bf16 a[68:71], a[208:211], a[180:183], a[68:71]
v_mfma_f32_16x16x32_bf16 a[72:75], a[208:211], a[184:187], a[72:75]
v_mfma_f32_16x16x32_bf16 a[76:79], a[208:211], a[188:191], a[76:79]
s_waitcnt lgkmcnt(1)
v_mfma_f32_16x16x32_bf16 v[208:211], a[160:163], a[212:215], v[208:211]
v_mfma_f32_16x16x32_bf16 v[212:215], a[164:167], a[212:215], v[212:215]
v_mfma_f32_16x16x32_bf16 v[216:219], a[168:171], a[212:215], v[216:219]
v_mfma_f32_16x16x32_bf16 v[220:223], a[172:175], a[212:215], v[220:223]
v_mfma_f32_16x16x32_bf16 a[80:83], a[212:215], a[176:179], a[80:83]
ds_read_b128 a[220:223], v33 offset:39936
v_mfma_f32_16x16x32_bf16 a[84:87], a[212:215], a[180:183], a[84:87]
v_mfma_f32_16x16x32_bf16 a[88:91], a[212:215], a[184:187], a[88:91]
v_mfma_f32_16x16x32_bf16 a[92:95], a[212:215], a[188:191], a[92:95]
s_waitcnt lgkmcnt(1)
v_mfma_f32_16x16x32_bf16 v[224:227], a[160:163], a[216:219], v[224:227]
v_mfma_f32_16x16x32_bf16 v[228:231], a[164:167], a[216:219], v[228:231]
v_mfma_f32_16x16x32_bf16 v[232:235], a[168:171], a[216:219], v[232:235]
v_mfma_f32_16x16x32_bf16 v[236:239], a[172:175], a[216:219], v[236:239]
v_mfma_f32_16x16x32_bf16 a[96:99], a[216:219], a[176:179], a[96:99]
v_mfma_f32_16x16x32_bf16 a[100:103], a[216:219], a[180:183], a[100:103]
v_mfma_f32_16x16x32_bf16 a[104:107], a[216:219], a[184:187], a[104:107]
v_mfma_f32_16x16x32_bf16 a[108:111], a[216:219], a[188:191], a[108:111]
s_waitcnt lgkmcnt(0)
v_mfma_f32_16x16x32_bf16 v[240:243], a[160:163], a[220:223], v[240:243]
v_mfma_f32_16x16x32_bf16 v[244:247], a[164:167], a[220:223], v[244:247]
v_mfma_f32_16x16x32_bf16 v[248:251], a[168:171], a[220:223], v[248:251]
v_mfma_f32_16x16x32_bf16 v[252:255], a[172:175], a[220:223], v[252:255]
v_mfma_f32_16x16x32_bf16 a[112:115], a[220:223], a[176:179], a[112:115]
v_mfma_f32_16x16x32_bf16 a[116:119], a[220:223], a[180:183], a[116:119]
v_mfma_f32_16x16x32_bf16 a[120:123], a[220:223], a[184:187], a[120:123]
v_mfma_f32_16x16x32_bf16 a[124:127], a[220:223], a[188:191], a[124:127]
s_barrier
s_cmp_lg_u32 %[role], 0
s_cbranch_scc1 L_ZPRE_%=
s_add_u32 s60, %[nws0], %[xnioff]
s_addc_u32 s61, %[nws1], 0
s_and_b32 s61, s61, 0xffff
s_mov_b32 s62, %[xnin]
s_mov_b32 s63, 0x20000
s_mov_b32 s64, %[nw0]
s_mov_b32 s65, %[nw1]
s_mov_b32 s66, %[nwn]
s_mov_b32 s67, 0x20000
s_mov_b32 s70, %[na0]
s_mov_b32 s71, %[na1]
s_mov_b32 s72, %[na2]
s_lshl_b32 s73, %[w], 11
s_add_u32 s69, %[lds], s73
s_add_u32 s68, s69, s73
v_mbcnt_lo_u32_b32 v105, -1, 0
v_mbcnt_hi_u32_b32 v105, -1, v105
v_lshlrev_b32_e32 v100, 4, v105
v_lshrrev_b32_e32 v103, 4, v105
v_and_b32_e32 v103, 1, v103
s_lshl_b32 s73, %[w], 1
v_add_u32_e32 v103, s73, v103
v_and_b32_e32 v103, 3, v103
v_and_b32_e32 v104, 3, v105
v_xor_b32_e32 v103, v103, v104
v_lshrrev_b32_e32 v104, 5, v105
v_lshl_add_u32 v103, v104, 2, v103
v_lshlrev_b32_e32 v103, 4, v103
v_lshrrev_b32_e32 v104, 2, v105
v_and_b32_e32 v104, 7, v104
v_mul_u32_u24_e32 v104, 8192, v104
v_add_u32_e32 v102, v103, v104
v_add_u32_e32 v101, 896, v102
s_add_u32 m0, s68, 114688
s_nop 0
buffer_load_dwordx4 v100, s[60:63], s70 offen sc1 lds
buffer_load_dwordx4 v100, s[60:63], s70 offen offset:1024 sc1 lds
s_add_u32 m0, s68, 116736
s_nop 0
buffer_load_dwordx4 v100, s[60:63], s71 offen sc1 lds
buffer_load_dwordx4 v100, s[60:63], s71 offen offset:1024 sc1 lds
s_add_u32 m0, s69, 131072
s_nop 0
buffer_load_dwordx4 v101, s[64:67], s72 offen lds
buffer_load_dwordx4 v102, s[64:67], s72 offen offset:1024 lds
s_add_u32 s70, s70, 0x20000
s_add_u32 s71, s71, 0x20000
s_add_u32 s72, s72, 262144
s_add_u32 m0, s68, 139264
s_nop 0
buffer_load_dwordx4 v100, s[60:63], s70 offen sc1 lds
buffer_load_dwordx4 v100, s[60:63], s70 offen offset:1024 sc1 lds
s_add_u32 m0, s68, 141312
s_nop 0
buffer_load_dwordx4 v100, s[60:63], s71 offen sc1 lds
buffer_load_dwordx4 v100, s[60:63], s71 offen offset:1024 sc1 lds
s_add_u32 m0, s69, 155648
s_nop 0
buffer_load_dwordx4 v101, s[64:67], s72 offen lds
buffer_load_dwordx4 v102, s[64:67], s72 offen offset:1024 lds
s_add_u32 s70, s70, 0x20000
s_add_u32 s71, s71, 0x20000
s_add_u32 s72, s72, 262144
s_add_u32 m0, s68, 0
s_nop 0
buffer_load_dwordx4 v100, s[60:63], s70 offen sc1 lds
buffer_load_dwordx4 v100, s[60:63], s70 offen offset:1024 sc1 lds
s_add_u32 m0, s68, 2048
s_nop 0
buffer_load_dwordx4 v100, s[60:63], s71 offen sc1 lds
buffer_load_dwordx4 v100, s[60:63], s71 offen offset:1024 sc1 lds
s_add_u32 m0, s69, 16384
s_nop 0
buffer_load_dwordx4 v101, s[64:67], s72 offen lds
buffer_load_dwordx4 v102, s[64:67], s72 offen offset:1024 lds
s_add_u32 s70, s70, 0x20000
s_add_u32 s71, s71, 0x20000
s_add_u32 s72, s72, 262144
s_add_u32 m0, s68, 24576
s_nop 0
buffer_load_dwordx4 v100, s[60:63], s70 offen sc1 lds
buffer_load_dwordx4 v100, s[60:63], s70 offen offset:1024 sc1 lds
s_add_u32 m0, s68, 26624
s_nop 0
buffer_load_dwordx4 v100, s[60:63], s71 offen sc1 lds
buffer_load_dwordx4 v100, s[60:63], s71 offen offset:1024 sc1 lds
s_add_u32 m0, s69, 40960
s_nop 0
buffer_load_dwordx4 v101, s[64:67], s72 offen lds
buffer_load_dwordx4 v102, s[64:67], s72 offen offset:1024 lds
s_add_u32 s70, s70, 0x20000
s_add_u32 s71, s71, 0x20000
s_add_u32 s72, s72, 262144
s_add_u32 m0, s68, 65536
s_nop 0
buffer_load_dwordx4 v100, s[60:63], s70 offen sc1 lds
buffer_load_dwordx4 v100, s[60:63], s70 offen offset:1024 sc1 lds
s_add_u32 m0, s68, 67584
s_nop 0
buffer_load_dwordx4 v100, s[60:63], s71 offen sc1 lds
buffer_load_dwordx4 v100, s[60:63], s71 offen offset:1024 sc1 lds
s_add_u32 m0, s69, 81920
s_nop 0
buffer_load_dwordx4 v101, s[64:67], s72 offen lds
buffer_load_dwordx4 v102, s[64:67], s72 offen offset:1024 lds
s_add_u32 s70, s70, 0x20000
s_add_u32 s71, s71, 0x20000
s_add_u32 s72, s72, 262144
s_add_u32 m0, s68, 90112
s_nop 0
buffer_load_dwordx4 v100, s[60:63], s70 offen sc1 lds
buffer_load_dwordx4 v100, s[60:63], s70 offen offset:1024 sc1 lds
s_add_u32 m0, s68, 92160
s_nop 0
buffer_load_dwordx4 v100, s[60:63], s71 offen sc1 lds
buffer_load_dwordx4 v100, s[60:63], s71 offen offset:1024 sc1 lds
s_add_u32 m0, s69, 106496
s_nop 0
buffer_load_dwordx4 v101, s[64:67], s72 offen lds
buffer_load_dwordx4 v102, s[64:67], s72 offen offset:1024 lds
s_add_u32 s70, s70, 0x20000
s_add_u32 s71, s71, 0x20000
s_add_u32 s72, s72, 262144
s_branch L_PREDONE_%=
L_ZPRE_%=:
s_add_u32 s60, %[nws0], %[xnioff]
s_addc_u32 s61, %[nws1], 0
s_and_b32 s61, s61, 0xffff
s_mov_b32 s62, %[xnin]
s_mov_b32 s63, 0x20000
s_mov_b32 s64, %[nw0]
s_mov_b32 s65, %[nw1]
s_mov_b32 s66, %[nwn]
s_mov_b32 s67, 0x20000
s_mov_b32 s70, %[na0]
)ASM"
R"ASM(s_mov_b32 s71, %[na1]
s_mov_b32 s72, %[na2]
s_lshl_b32 s73, %[w], 11
s_add_u32 s69, %[lds], s73
v_mbcnt_lo_u32_b32 v105, -1, 0
v_mbcnt_hi_u32_b32 v105, -1, v105
v_lshlrev_b32_e32 v100, 4, v105
v_lshrrev_b32_e32 v103, 4, v105
v_and_b32_e32 v103, 1, v103
s_lshl_b32 s73, %[w], 1
v_add_u32_e32 v103, s73, v103
v_and_b32_e32 v103, 3, v103
v_and_b32_e32 v104, 3, v105
v_xor_b32_e32 v103, v103, v104
v_lshrrev_b32_e32 v104, 5, v105
v_lshl_add_u32 v103, v104, 2, v103
v_lshlrev_b32_e32 v103, 4, v103
v_lshrrev_b32_e32 v104, 2, v105
v_and_b32_e32 v104, 7, v104
v_mul_u32_u24_e32 v104, 3456, v104
v_add_u32_e32 v102, v103, v104
v_add_u32_e32 v101, 896, v102
s_add_u32 m0, s69, 0
s_nop 0
buffer_load_dwordx4 v100, s[60:63], s70 offen sc1 lds
buffer_load_dwordx4 v100, s[60:63], s70 offen offset:1024 sc1 lds
s_add_u32 m0, s69, 8192
s_nop 0
buffer_load_dwordx4 v101, s[64:67], s71 offen lds
buffer_load_dwordx4 v102, s[64:67], s71 offen offset:1024 lds
s_add_u32 m0, s69, 16384
s_nop 0
buffer_load_dwordx4 v101, s[64:67], s72 offen lds
buffer_load_dwordx4 v102, s[64:67], s72 offen offset:1024 lds
s_add_u32 s70, s70, 0x20000
s_add_u32 s71, s71, 110592
s_add_u32 s72, s72, 110592
s_add_u32 m0, s69, 24576
s_nop 0
buffer_load_dwordx4 v100, s[60:63], s70 offen sc1 lds
buffer_load_dwordx4 v100, s[60:63], s70 offen offset:1024 sc1 lds
s_add_u32 m0, s69, 32768
s_nop 0
buffer_load_dwordx4 v101, s[64:67], s71 offen lds
buffer_load_dwordx4 v102, s[64:67], s71 offen offset:1024 lds
s_add_u32 m0, s69, 40960
s_nop 0
buffer_load_dwordx4 v101, s[64:67], s72 offen lds
buffer_load_dwordx4 v102, s[64:67], s72 offen offset:1024 lds
s_add_u32 s70, s70, 0x20000
s_add_u32 s71, s71, 110592
s_add_u32 s72, s72, 110592
s_add_u32 m0, s69, 49152
s_nop 0
buffer_load_dwordx4 v100, s[60:63], s70 offen sc1 lds
buffer_load_dwordx4 v100, s[60:63], s70 offen offset:1024 sc1 lds
s_add_u32 m0, s69, 57344
s_nop 0
buffer_load_dwordx4 v101, s[64:67], s71 offen lds
buffer_load_dwordx4 v102, s[64:67], s71 offen offset:1024 lds
s_add_u32 m0, s69, 65536
s_nop 0
buffer_load_dwordx4 v101, s[64:67], s72 offen lds
buffer_load_dwordx4 v102, s[64:67], s72 offen offset:1024 lds
s_add_u32 s70, s70, 0x20000
s_add_u32 s71, s71, 110592
s_add_u32 s72, s72, 110592
s_add_u32 m0, s69, 73728
s_nop 0
buffer_load_dwordx4 v100, s[60:63], s70 offen sc1 lds
buffer_load_dwordx4 v100, s[60:63], s70 offen offset:1024 sc1 lds
s_add_u32 m0, s69, 81920
s_nop 0
buffer_load_dwordx4 v101, s[64:67], s71 offen lds
buffer_load_dwordx4 v102, s[64:67], s71 offen offset:1024 lds
s_add_u32 m0, s69, 90112
s_nop 0
buffer_load_dwordx4 v101, s[64:67], s72 offen lds
buffer_load_dwordx4 v102, s[64:67], s72 offen offset:1024 lds
s_add_u32 s70, s70, 0x20000
s_add_u32 s71, s71, 110592
s_add_u32 s72, s72, 110592
s_add_u32 m0, s69, 98304
s_nop 0
buffer_load_dwordx4 v100, s[60:63], s70 offen sc1 lds
buffer_load_dwordx4 v100, s[60:63], s70 offen offset:1024 sc1 lds
s_add_u32 m0, s69, 106496
s_nop 0
buffer_load_dwordx4 v101, s[64:67], s71 offen lds
buffer_load_dwordx4 v102, s[64:67], s71 offen offset:1024 lds
s_add_u32 m0, s69, 114688
s_nop 0
buffer_load_dwordx4 v101, s[64:67], s72 offen lds
buffer_load_dwordx4 v102, s[64:67], s72 offen offset:1024 lds
s_add_u32 s70, s70, 0x20000
s_add_u32 s71, s71, 110592
s_add_u32 s72, s72, 110592
s_add_u32 m0, s69, 122880
s_nop 0
buffer_load_dwordx4 v100, s[60:63], s70 offen sc1 lds
buffer_load_dwordx4 v100, s[60:63], s70 offen offset:1024 sc1 lds
s_add_u32 m0, s69, 131072
s_nop 0
buffer_load_dwordx4 v101, s[64:67], s71 offen lds
buffer_load_dwordx4 v102, s[64:67], s71 offen offset:1024 lds
s_add_u32 m0, s69, 139264
s_nop 0
buffer_load_dwordx4 v101, s[64:67], s72 offen lds
buffer_load_dwordx4 v102, s[64:67], s72 offen offset:1024 lds
s_add_u32 s70, s70, 0x20000
s_add_u32 s71, s71, 110592
s_add_u32 s72, s72, 110592
L_PREDONE_%=:
s_nop 7
s_mov_b32 s36, %[o0]
s_mov_b32 s37, %[o1]
s_mov_b32 s38, %[on]
v_mbcnt_lo_u32_b32 v48, -1, 0
v_mbcnt_hi_u32_b32 v48, -1, v48
v_and_b32_e32 v49, 15, v48
v_lshrrev_b32_e32 v50, 4, v48
v_lshrrev_b32_e32 v51, 2, v49
v_sub_u32_e32 v51, 4, v51
v_and_b32_e32 v51, 3, v51
v_xor_b32_e32 v51, v51, v50
v_lshlrev_b32_e32 v51, 4, v51
v_lshl_add_u32 v52, v49, 6, v51
s_lshr_b32 s52, s51, 1
s_lshl_b32 s52, s52, 16
s_add_u32 s52, s52, %[obase]
s_and_b32 s50, s51, 1
s_lshl_b32 s53, s50, 13
s_add_u32 s53, s53, s52
s_lshl_b32 s50, s50, 12
s_add_u32 s50, s50, s52
s_add_u32 s50, s50, 16384
s_mov_b32 s55, %[scale]
s_add_u32 s49, s53, 0
v_mul_f32_e32 v128, s55, v128
v_mul_f32_e32 v129, s55, v129
v_mul_f32_e32 v130, s55, v130
v_mul_f32_e32 v131, s55, v131
v_mul_f32_e32 v132, s55, v132
v_mul_f32_e32 v133, s55, v133
v_mul_f32_e32 v134, s55, v134
v_mul_f32_e32 v135, s55, v135
v_cvt_pk_bf16_f32 v60, v128, v129
v_cvt_pk_bf16_f32 v61, v130, v131
v_cvt_pk_bf16_f32 v62, v132, v133
v_cvt_pk_bf16_f32 v63, v134, v135
buffer_store_dwordx4 v[60:63], v52, s[36:39], s49 offen sc1
s_add_u32 s49, s53, 4096
v_mul_f32_e32 v136, s55, v136
v_mul_f32_e32 v137, s55, v137
v_mul_f32_e32 v138, s55, v138
v_mul_f32_e32 v139, s55, v139
v_mul_f32_e32 v140, s55, v140
v_mul_f32_e32 v141, s55, v141
v_mul_f32_e32 v142, s55, v142
v_mul_f32_e32 v143, s55, v143
v_cvt_pk_bf16_f32 v64, v136, v137
v_cvt_pk_bf16_f32 v65, v138, v139
v_cvt_pk_bf16_f32 v66, v140, v141
v_cvt_pk_bf16_f32 v67, v142, v143
buffer_store_dwordx4 v[64:67], v52, s[36:39], s49 offen sc1
s_add_u32 s49, s53, 1024
v_mul_f32_e32 v144, s55, v144
v_mul_f32_e32 v145, s55, v145
v_mul_f32_e32 v146, s55, v146
v_mul_f32_e32 v147, s55, v147
v_mul_f32_e32 v148, s55, v148
v_mul_f32_e32 v149, s55, v149
v_mul_f32_e32 v150, s55, v150
v_mul_f32_e32 v151, s55, v151
v_cvt_pk_bf16_f32 v68, v144, v145
v_cvt_pk_bf16_f32 v69, v146, v147
v_cvt_pk_bf16_f32 v70, v148, v149
v_cvt_pk_bf16_f32 v71, v150, v151
buffer_store_dwordx4 v[68:71], v52, s[36:39], s49 offen sc1
s_add_u32 s49, s53, 5120
v_mul_f32_e32 v152, s55, v152
v_mul_f32_e32 v153, s55, v153
v_mul_f32_e32 v154, s55, v154
v_mul_f32_e32 v155, s55, v155
v_mul_f32_e32 v156, s55, v156
v_mul_f32_e32 v157, s55, v157
v_mul_f32_e32 v158, s55, v158
v_mul_f32_e32 v159, s55, v159
v_cvt_pk_bf16_f32 v72, v152, v153
v_cvt_pk_bf16_f32 v73, v154, v155
v_cvt_pk_bf16_f32 v74, v156, v157
v_cvt_pk_bf16_f32 v75, v158, v159
buffer_store_dwordx4 v[72:75], v52, s[36:39], s49 offen sc1
s_add_u32 s49, s53, 2048
v_mul_f32_e32 v160, s55, v160
v_mul_f32_e32 v161, s55, v161
v_mul_f32_e32 v162, s55, v162
v_mul_f32_e32 v163, s55, v163
v_mul_f32_e32 v164, s55, v164
v_mul_f32_e32 v165, s55, v165
v_mul_f32_e32 v166, s55, v166
v_mul_f32_e32 v167, s55, v167
v_cvt_pk_bf16_f32 v60, v160, v161
v_cvt_pk_bf16_f32 v61, v162, v163
v_cvt_pk_bf16_f32 v62, v164, v165
v_cvt_pk_bf16_f32 v63, v166, v167
buffer_store_dwordx4 v[60:63], v52, s[36:39], s49 offen sc1
s_add_u32 s49, s53, 6144
v_mul_f32_e32 v168, s55, v168
v_mul_f32_e32 v169, s55, v169
v_mul_f32_e32 v170, s55, v170
v_mul_f32_e32 v171, s55, v171
v_mul_f32_e32 v172, s55, v172
v_mul_f32_e32 v173, s55, v173
v_mul_f32_e32 v174, s55, v174
v_mul_f32_e32 v175, s55, v175
v_cvt_pk_bf16_f32 v64, v168, v169
v_cvt_pk_bf16_f32 v65, v170, v171
v_cvt_pk_bf16_f32 v66, v172, v173
v_cvt_pk_bf16_f32 v67, v174, v175
buffer_store_dwordx4 v[64:67], v52, s[36:39], s49 offen sc1
s_add_u32 s49, s53, 3072
v_mul_f32_e32 v176, s55, v176
v_mul_f32_e32 v177, s55, v177
v_mul_f32_e32 v178, s55, v178
v_mul_f32_e32 v179, s55, v179
v_mul_f32_e32 v180, s55, v180
v_mul_f32_e32 v181, s55, v181
v_mul_f32_e32 v182, s55, v182
v_mul_f32_e32 v183, s55, v183
v_cvt_pk_bf16_f32 v68, v176, v177
v_cvt_pk_bf16_f32 v69, v178, v179
v_cvt_pk_bf16_f32 v70, v180, v181
v_cvt_pk_bf16_f32 v71, v182, v183
buffer_store_dwordx4 v[68:71], v52, s[36:39], s49 offen sc1
s_add_u32 s49, s53, 7168
v_mul_f32_e32 v184, s55, v184
v_mul_f32_e32 v185, s55, v185
v_mul_f32_e32 v186, s55, v186
v_mul_f32_e32 v187, s55, v187
v_mul_f32_e32 v188, s55, v188
v_mul_f32_e32 v189, s55, v189
v_mul_f32_e32 v190, s55, v190
v_mul_f32_e32 v191, s55, v191
v_cvt_pk_bf16_f32 v72, v184, v185
v_cvt_pk_bf16_f32 v73, v186, v187
v_cvt_pk_bf16_f32 v74, v188, v189
v_cvt_pk_bf16_f32 v75, v190, v191
buffer_store_dwordx4 v[72:75], v52, s[36:39], s49 offen sc1
s_add_u32 s49, s53, 32768
v_mul_f32_e32 v192, s55, v192
v_mul_f32_e32 v193, s55, v193
v_mul_f32_e32 v194, s55, v194
v_mul_f32_e32 v195, s55, v195
v_mul_f32_e32 v196, s55, v196
v_mul_f32_e32 v197, s55, v197
v_mul_f32_e32 v198, s55, v198
v_mul_f32_e32 v199, s55, v199
v_cvt_pk_bf16_f32 v60, v192, v193
v_cvt_pk_bf16_f32 v61, v194, v195
v_cvt_pk_bf16_f32 v62, v196, v197
v_cvt_pk_bf16_f32 v63, v198, v199
buffer_store_dwordx4 v[60:63], v52, s[36:39], s49 offen sc1
s_add_u32 s49, s53, 36864
v_mul_f32_e32 v200, s55, v200
v_mul_f32_e32 v201, s55, v201
v_mul_f32_e32 v202, s55, v202
v_mul_f32_e32 v203, s55, v203
v_mul_f32_e32 v204, s55, v204
v_mul_f32_e32 v205, s55, v205
v_mul_f32_e32 v206, s55, v206
v_mul_f32_e32 v207, s55, v207
v_cvt_pk_bf16_f32 v64, v200, v201
v_cvt_pk_bf16_f32 v65, v202, v203
v_cvt_pk_bf16_f32 v66, v204, v205
v_cvt_pk_bf16_f32 v67, v206, v207
buffer_store_dwordx4 v[64:67], v52, s[36:39], s49 offen sc1
s_add_u32 s49, s53, 33792
v_mul_f32_e32 v208, s55, v208
v_mul_f32_e32 v209, s55, v209
v_mul_f32_e32 v210, s55, v210
v_mul_f32_e32 v211, s55, v211
v_mul_f32_e32 v212, s55, v212
v_mul_f32_e32 v213, s55, v213
v_mul_f32_e32 v214, s55, v214
v_mul_f32_e32 v215, s55, v215
v_cvt_pk_bf16_f32 v68, v208, v209
v_cvt_pk_bf16_f32 v69, v210, v211
v_cvt_pk_bf16_f32 v70, v212, v213
v_cvt_pk_bf16_f32 v71, v214, v215
buffer_store_dwordx4 v[68:71], v52, s[36:39], s49 offen sc1
s_add_u32 s49, s53, 37888
v_mul_f32_e32 v216, s55, v216
v_mul_f32_e32 v217, s55, v217
v_mul_f32_e32 v218, s55, v218
v_mul_f32_e32 v219, s55, v219
v_mul_f32_e32 v220, s55, v220
v_mul_f32_e32 v221, s55, v221
v_mul_f32_e32 v222, s55, v222
v_mul_f32_e32 v223, s55, v223
v_cvt_pk_bf16_f32 v72, v216, v217
v_cvt_pk_bf16_f32 v73, v218, v219
v_cvt_pk_bf16_f32 v74, v220, v221
v_cvt_pk_bf16_f32 v75, v222, v223
buffer_store_dwordx4 v[72:75], v52, s[36:39], s49 offen sc1
s_add_u32 s49, s53, 34816
v_mul_f32_e32 v224, s55, v224
v_mul_f32_e32 v225, s55, v225
v_mul_f32_e32 v226, s55, v226
v_mul_f32_e32 v227, s55, v227
v_mul_f32_e32 v228, s55, v228
v_mul_f32_e32 v229, s55, v229
v_mul_f32_e32 v230, s55, v230
v_mul_f32_e32 v231, s55, v231
v_cvt_pk_bf16_f32 v60, v224, v225
v_cvt_pk_bf16_f32 v61, v226, v227
v_cvt_pk_bf16_f32 v62, v228, v229
v_cvt_pk_bf16_f32 v63, v230, v231
buffer_store_dwordx4 v[60:63], v52, s[36:39], s49 offen sc1
s_add_u32 s49, s53, 38912
v_mul_f32_e32 v232, s55, v232
v_mul_f32_e32 v233, s55, v233
v_mul_f32_e32 v234, s55, v234
v_mul_f32_e32 v235, s55, v235
v_mul_f32_e32 v236, s55, v236
v_mul_f32_e32 v237, s55, v237
v_mul_f32_e32 v238, s55, v238
v_mul_f32_e32 v239, s55, v239
v_cvt_pk_bf16_f32 v64, v232, v233
v_cvt_pk_bf16_f32 v65, v234, v235
v_cvt_pk_bf16_f32 v66, v236, v237
v_cvt_pk_bf16_f32 v67, v238, v239
buffer_store_dwordx4 v[64:67], v52, s[36:39], s49 offen sc1
s_add_u32 s49, s53, 35840
v_mul_f32_e32 v240, s55, v240
v_mul_f32_e32 v241, s55, v241
v_mul_f32_e32 v242, s55, v242
v_mul_f32_e32 v243, s55, v243
v_mul_f32_e32 v244, s55, v244
v_mul_f32_e32 v245, s55, v245
v_mul_f32_e32 v246, s55, v246
v_mul_f32_e32 v247, s55, v247
v_cvt_pk_bf16_f32 v68, v240, v241
v_cvt_pk_bf16_f32 v69, v242, v243
v_cvt_pk_bf16_f32 v70, v244, v245
v_cvt_pk_bf16_f32 v71, v246, v247
buffer_store_dwordx4 v[68:71], v52, s[36:39], s49 offen sc1
s_add_u32 s49, s53, 39936
v_mul_f32_e32 v248, s55, v248
v_mul_f32_e32 v249, s55, v249
v_mul_f32_e32 v250, s55, v250
v_mul_f32_e32 v251, s55, v251
v_mul_f32_e32 v252, s55, v252
v_mul_f32_e32 v253, s55, v253
v_mul_f32_e32 v254, s55, v254
v_mul_f32_e32 v255, s55, v255
v_cvt_pk_bf16_f32 v72, v248, v249
v_cvt_pk_bf16_f32 v73, v250, v251
v_cvt_pk_bf16_f32 v74, v252, v253
v_cvt_pk_bf16_f32 v75, v254, v255
buffer_store_dwordx4 v[72:75], v52, s[36:39], s49 offen sc1
s_add_u32 s49, s50, 0
v_accvgpr_read_b32 v76, a0
v_accvgpr_read_b32 v77, a1
v_accvgpr_read_b32 v78, a2
v_accvgpr_read_b32 v79, a3
v_accvgpr_read_b32 v80, a16
v_accvgpr_read_b32 v81, a17
v_accvgpr_read_b32 v82, a18
)ASM"
R"ASM(v_accvgpr_read_b32 v83, a19
v_cvt_pk_bf16_f32 v84, v76, v77
v_cvt_pk_bf16_f32 v85, v78, v79
v_cvt_pk_bf16_f32 v86, v80, v81
v_cvt_pk_bf16_f32 v87, v82, v83
buffer_store_dwordx4 v[84:87], v52, s[36:39], s49 offen sc1
s_add_u32 s49, s50, 1024
v_accvgpr_read_b32 v88, a4
v_accvgpr_read_b32 v89, a5
v_accvgpr_read_b32 v90, a6
v_accvgpr_read_b32 v91, a7
v_accvgpr_read_b32 v92, a20
v_accvgpr_read_b32 v93, a21
v_accvgpr_read_b32 v94, a22
v_accvgpr_read_b32 v95, a23
v_cvt_pk_bf16_f32 v96, v88, v89
v_cvt_pk_bf16_f32 v97, v90, v91
v_cvt_pk_bf16_f32 v98, v92, v93
v_cvt_pk_bf16_f32 v99, v94, v95
buffer_store_dwordx4 v[96:99], v52, s[36:39], s49 offen sc1
s_add_u32 s49, s50, 2048
v_accvgpr_read_b32 v76, a8
v_accvgpr_read_b32 v77, a9
v_accvgpr_read_b32 v78, a10
v_accvgpr_read_b32 v79, a11
v_accvgpr_read_b32 v80, a24
v_accvgpr_read_b32 v81, a25
v_accvgpr_read_b32 v82, a26
v_accvgpr_read_b32 v83, a27
v_cvt_pk_bf16_f32 v84, v76, v77
v_cvt_pk_bf16_f32 v85, v78, v79
v_cvt_pk_bf16_f32 v86, v80, v81
v_cvt_pk_bf16_f32 v87, v82, v83
buffer_store_dwordx4 v[84:87], v52, s[36:39], s49 offen sc1
s_add_u32 s49, s50, 3072
v_accvgpr_read_b32 v88, a12
v_accvgpr_read_b32 v89, a13
v_accvgpr_read_b32 v90, a14
v_accvgpr_read_b32 v91, a15
v_accvgpr_read_b32 v92, a28
v_accvgpr_read_b32 v93, a29
v_accvgpr_read_b32 v94, a30
v_accvgpr_read_b32 v95, a31
v_cvt_pk_bf16_f32 v96, v88, v89
v_cvt_pk_bf16_f32 v97, v90, v91
v_cvt_pk_bf16_f32 v98, v92, v93
v_cvt_pk_bf16_f32 v99, v94, v95
buffer_store_dwordx4 v[96:99], v52, s[36:39], s49 offen sc1
s_add_u32 s49, s50, 8192
v_accvgpr_read_b32 v76, a32
v_accvgpr_read_b32 v77, a33
v_accvgpr_read_b32 v78, a34
v_accvgpr_read_b32 v79, a35
v_accvgpr_read_b32 v80, a48
v_accvgpr_read_b32 v81, a49
v_accvgpr_read_b32 v82, a50
v_accvgpr_read_b32 v83, a51
v_cvt_pk_bf16_f32 v84, v76, v77
v_cvt_pk_bf16_f32 v85, v78, v79
v_cvt_pk_bf16_f32 v86, v80, v81
v_cvt_pk_bf16_f32 v87, v82, v83
buffer_store_dwordx4 v[84:87], v52, s[36:39], s49 offen sc1
s_add_u32 s49, s50, 9216
v_accvgpr_read_b32 v88, a36
v_accvgpr_read_b32 v89, a37
v_accvgpr_read_b32 v90, a38
v_accvgpr_read_b32 v91, a39
v_accvgpr_read_b32 v92, a52
v_accvgpr_read_b32 v93, a53
v_accvgpr_read_b32 v94, a54
v_accvgpr_read_b32 v95, a55
v_cvt_pk_bf16_f32 v96, v88, v89
v_cvt_pk_bf16_f32 v97, v90, v91
v_cvt_pk_bf16_f32 v98, v92, v93
v_cvt_pk_bf16_f32 v99, v94, v95
buffer_store_dwordx4 v[96:99], v52, s[36:39], s49 offen sc1
s_add_u32 s49, s50, 10240
v_accvgpr_read_b32 v76, a40
v_accvgpr_read_b32 v77, a41
v_accvgpr_read_b32 v78, a42
v_accvgpr_read_b32 v79, a43
v_accvgpr_read_b32 v80, a56
v_accvgpr_read_b32 v81, a57
v_accvgpr_read_b32 v82, a58
v_accvgpr_read_b32 v83, a59
v_cvt_pk_bf16_f32 v84, v76, v77
v_cvt_pk_bf16_f32 v85, v78, v79
v_cvt_pk_bf16_f32 v86, v80, v81
v_cvt_pk_bf16_f32 v87, v82, v83
buffer_store_dwordx4 v[84:87], v52, s[36:39], s49 offen sc1
s_add_u32 s49, s50, 11264
v_accvgpr_read_b32 v88, a44
v_accvgpr_read_b32 v89, a45
v_accvgpr_read_b32 v90, a46
v_accvgpr_read_b32 v91, a47
v_accvgpr_read_b32 v92, a60
v_accvgpr_read_b32 v93, a61
v_accvgpr_read_b32 v94, a62
v_accvgpr_read_b32 v95, a63
v_cvt_pk_bf16_f32 v96, v88, v89
v_cvt_pk_bf16_f32 v97, v90, v91
v_cvt_pk_bf16_f32 v98, v92, v93
v_cvt_pk_bf16_f32 v99, v94, v95
buffer_store_dwordx4 v[96:99], v52, s[36:39], s49 offen sc1
s_add_u32 s49, s50, 32768
v_accvgpr_read_b32 v76, a64
v_accvgpr_read_b32 v77, a65
v_accvgpr_read_b32 v78, a66
v_accvgpr_read_b32 v79, a67
v_accvgpr_read_b32 v80, a80
v_accvgpr_read_b32 v81, a81
v_accvgpr_read_b32 v82, a82
v_accvgpr_read_b32 v83, a83
v_cvt_pk_bf16_f32 v84, v76, v77
v_cvt_pk_bf16_f32 v85, v78, v79
v_cvt_pk_bf16_f32 v86, v80, v81
v_cvt_pk_bf16_f32 v87, v82, v83
buffer_store_dwordx4 v[84:87], v52, s[36:39], s49 offen sc1
s_add_u32 s49, s50, 33792
v_accvgpr_read_b32 v88, a68
v_accvgpr_read_b32 v89, a69
v_accvgpr_read_b32 v90, a70
v_accvgpr_read_b32 v91, a71
v_accvgpr_read_b32 v92, a84
v_accvgpr_read_b32 v93, a85
v_accvgpr_read_b32 v94, a86
v_accvgpr_read_b32 v95, a87
v_cvt_pk_bf16_f32 v96, v88, v89
v_cvt_pk_bf16_f32 v97, v90, v91
v_cvt_pk_bf16_f32 v98, v92, v93
v_cvt_pk_bf16_f32 v99, v94, v95
buffer_store_dwordx4 v[96:99], v52, s[36:39], s49 offen sc1
s_add_u32 s49, s50, 34816
v_accvgpr_read_b32 v76, a72
v_accvgpr_read_b32 v77, a73
v_accvgpr_read_b32 v78, a74
v_accvgpr_read_b32 v79, a75
v_accvgpr_read_b32 v80, a88
v_accvgpr_read_b32 v81, a89
v_accvgpr_read_b32 v82, a90
v_accvgpr_read_b32 v83, a91
v_cvt_pk_bf16_f32 v84, v76, v77
v_cvt_pk_bf16_f32 v85, v78, v79
v_cvt_pk_bf16_f32 v86, v80, v81
v_cvt_pk_bf16_f32 v87, v82, v83
buffer_store_dwordx4 v[84:87], v52, s[36:39], s49 offen sc1
s_add_u32 s49, s50, 35840
v_accvgpr_read_b32 v88, a76
v_accvgpr_read_b32 v89, a77
v_accvgpr_read_b32 v90, a78
v_accvgpr_read_b32 v91, a79
v_accvgpr_read_b32 v92, a92
v_accvgpr_read_b32 v93, a93
v_accvgpr_read_b32 v94, a94
v_accvgpr_read_b32 v95, a95
v_cvt_pk_bf16_f32 v96, v88, v89
v_cvt_pk_bf16_f32 v97, v90, v91
v_cvt_pk_bf16_f32 v98, v92, v93
v_cvt_pk_bf16_f32 v99, v94, v95
buffer_store_dwordx4 v[96:99], v52, s[36:39], s49 offen sc1
s_add_u32 s49, s50, 40960
v_accvgpr_read_b32 v76, a96
v_accvgpr_read_b32 v77, a97
v_accvgpr_read_b32 v78, a98
v_accvgpr_read_b32 v79, a99
v_accvgpr_read_b32 v80, a112
v_accvgpr_read_b32 v81, a113
v_accvgpr_read_b32 v82, a114
v_accvgpr_read_b32 v83, a115
v_cvt_pk_bf16_f32 v84, v76, v77
v_cvt_pk_bf16_f32 v85, v78, v79
v_cvt_pk_bf16_f32 v86, v80, v81
v_cvt_pk_bf16_f32 v87, v82, v83
buffer_store_dwordx4 v[84:87], v52, s[36:39], s49 offen sc1
s_add_u32 s49, s50, 41984
v_accvgpr_read_b32 v88, a100
v_accvgpr_read_b32 v89, a101
v_accvgpr_read_b32 v90, a102
v_accvgpr_read_b32 v91, a103
v_accvgpr_read_b32 v92, a116
v_accvgpr_read_b32 v93, a117
v_accvgpr_read_b32 v94, a118
v_accvgpr_read_b32 v95, a119
v_cvt_pk_bf16_f32 v96, v88, v89
v_cvt_pk_bf16_f32 v97, v90, v91
v_cvt_pk_bf16_f32 v98, v92, v93
v_cvt_pk_bf16_f32 v99, v94, v95
buffer_store_dwordx4 v[96:99], v52, s[36:39], s49 offen sc1
s_add_u32 s49, s50, 43008
v_accvgpr_read_b32 v76, a104
v_accvgpr_read_b32 v77, a105
v_accvgpr_read_b32 v78, a106
v_accvgpr_read_b32 v79, a107
v_accvgpr_read_b32 v80, a120
v_accvgpr_read_b32 v81, a121
v_accvgpr_read_b32 v82, a122
v_accvgpr_read_b32 v83, a123
v_cvt_pk_bf16_f32 v84, v76, v77
v_cvt_pk_bf16_f32 v85, v78, v79
v_cvt_pk_bf16_f32 v86, v80, v81
v_cvt_pk_bf16_f32 v87, v82, v83
buffer_store_dwordx4 v[84:87], v52, s[36:39], s49 offen sc1
s_add_u32 s49, s50, 44032
v_accvgpr_read_b32 v88, a108
v_accvgpr_read_b32 v89, a109
v_accvgpr_read_b32 v90, a110
v_accvgpr_read_b32 v91, a111
v_accvgpr_read_b32 v92, a124
v_accvgpr_read_b32 v93, a125
v_accvgpr_read_b32 v94, a126
v_accvgpr_read_b32 v95, a127
v_cvt_pk_bf16_f32 v96, v88, v89
v_cvt_pk_bf16_f32 v97, v90, v91
v_cvt_pk_bf16_f32 v98, v92, v93
v_cvt_pk_bf16_f32 v99, v94, v95
buffer_store_dwordx4 v[96:99], v52, s[36:39], s49 offen sc1
)ASM"
            : [sink] "+v"(sink)
            : [x0] "s"(LO32(ckv)), [x1] "s"(HI16(ckv)), [xn] "i"(S * DC * 2), [k0] "s"(LO32(kb)), [k1] "s"(HI16(kb)), [kn] "i"(DC * HD * 2 + 896),
              [v0] "s"(LO32(vb)), [v1] "s"(HI16(vb)), [vn] "i"(DC * HD * 2 + 896), [w] "s"(w), [lds] "s"((unsigned)(size_t)lds), [xoff] "s"(j * 256 * DC * 2), [woff] "s"(h * 256),
              [o0] "s"(LO32(ob)), [o1] "s"(HI16(ob)), [on] "i"(H * 32 * 32768), [obase] "s"(h * 1048576 + j * 4 * 32768), [scale] "i"(0x3e0293eeu),
              [tag0] "s"((unsigned)tag), [tag1] "s"((unsigned)(tag >> 32)), [fln] "s"(8 * (F_LN + bid)), [flags] "s"(fb), [pl] "v"(pl),
              [pf0] "v"(p0), [pf1] "v"(p1), [pf2] "v"(p2), [pf3] "v"(p3), [pf4] "v"(p4),
              [role] "s"(zrole ? 1 : 0), [nws0] "s"((unsigned)(unsigned long long)ws), [nws1] "s"((unsigned)((unsigned long long)ws >> 32)), [xnioff] "i"((unsigned)OFF_XNI), [xnin] "i"(20 * S * 64),
              [nw0] "s"(LO32(nw)), [nw1] "s"(HI16(nw)), [nwn] "s"(nwn), [na0] "s"(na0), [na1] "s"(na1), [na2] "s"(na2), [dbg] "s"(ws + OFF_DBG + 131072 + (bid * 4 + w) * 64)
            :
"v32", "v33", "v34", "v35", "v36", "v37", "v38", "v39", "v40", "v41", "v42", "v43", "v44", "v45", "v46", "v47", "v48", "v49", "v50", "v51", "v52", "v53", "v54", "v55", "v56", "v57", "v58", "v59", "v60", "v61", "v62", "v63", "v64", "v65", "v66", "v67", "v68", "v69", "v70", "v71", "v72", "v73", "v74", "v75", "v76", "v77", "v78", "v79", "v80", "v81", "v82", "v83", "v84", "v85", "v86", "v87", "v88", "v89", "v90", "v91", "v92", "v93", "v94", "v95", "v96", "v97", "v98", "v99", "v100", "v101", "v102", "v103", "v104", "v105", "v106", "v107", "v108", "v109", "v110", "v111", "v112", "v113", "v114", "v115", "v116", "v117", "v118", "v119", "v120", "v121", "v122", "v123", "v124", "v125", "v126", "v127", "v128", "v129", "v130", "v131", "v132", "v133", "v134", "v135", "v136", "v137", "v138", "v139", "v140", "v141", "v142", "v143", "v144", "v145", "v146", "v147", "v148", "v149", "v150", "v151", "v152", "v153", "v154", "v155", "v156", "v157", "v158", "v159", "v160", "v161", "v162", "v163", "v164", "v165", "v166", "v167", "v168", "v169", "v170", "v171", "v172", "v173", "v174", "v175", "v176", "v177", "v178", "v179", "v180", "v181", "v182", "v183", "v184", "v185", "v186", "v187", "v188", "v189", "v190", "v191", "v192", "v193", "v194", "v195", "v196", "v197", "v198", "v199", "v200", "v201", "v202", "v203", "v204", "v205", "v206", "v207", "v208", "v209", "v210", "v211", "v212", "v213", "v214", "v215", "v216", "v217", "v218", "v219", "v220", "v221", "v222", "v223", "v224", "v225", "v226", "v227", "v228", "v229", "v230", "v231", "v232", "v233", "v234", "v235", "v236", "v237", "v238", "v239", "v240", "v241", "v242", "v243", "v244", "v245", "v246", "v247", "v248", "v249", "v250", "v251", "v252", "v253", "v254", "v255", "a0", "a1", "a2", "a3", "a4", "a5", "a6", "a7", "a8", "a9", "a10", "a11", "a12", "a13", "a14", "a15", "a16", "a17", "a18", "a19", "a20", "a21", "a22", "a23", "a24", "a25", "a26", "a27", "a28", "a29", "a30", "a31", "a32", "a33", "a34", "a35", "a36", "a37", "a38", "a39", "a40", "a41", "a42", "a43", "a44", "a45", "a46", "a47", "a48", "a49", "a50", "a51", "a52", "a53", "a54", "a55", "a56", "a57", "a58", "a59", "a60", "a61", "a62", "a63", "a64", "a65", "a66", "a67", "a68", "a69", "a70", "a71", "a72", "a73", "a74", "a75", "a76", "a77", "a78", "a79", "a80", "a81", "a82", "a83", "a84", "a85", "a86", "a87", "a88", "a89", "a90", "a91", "a92", "a93", "a94", "a95", "a96", "a97", "a98", "a99", "a100", "a101", "a102", "a103", "a104", "a105", "a106", "a107", "a108", "a109", "a110", "a111", "a112", "a113", "a114", "a115", "a116", "a117", "a118", "a119", "a120", "a121", "a122", "a123", "a124", "a125", "a126", "a127", "a128", "a129", "a130", "a131", "a132", "a133", "a134", "a135", "a136", "a137", "a138", "a139", "a140", "a141", "a142", "a143", "a144", "a145", "a146", "a147", "a148", "a149", "a150", "a151", "a152", "a153", "a154", "a155", "a156", "a157", "a158", "a159", "a160", "a161", "a162", "a163", "a164", "a165", "a166", "a167", "a168", "a169", "a170", "a171", "a172", "a173", "a174", "a175", "a176", "a177", "a178", "a179", "a180", "a181", "a182", "a183", "a184", "a185", "a186", "a187", "a188", "a189", "a190", "a191", "a192", "a193", "a194", "a195", "a196", "a197", "a198", "a199", "a200", "a201", "a202", "a203", "a204", "a205", "a206", "a207", "a208", "a209", "a210", "a211", "a212", "a213", "a214", "a215", "a216", "a217", "a218", "a219", "a220", "a221", "a222", "a223", "a224", "a225", "a226", "a227", "a228", "a229", "a230", "a231", "a232", "a233", "a234", "a235", "a236", "a237", "a238", "a239", "a240", "a241", "a242", "a243", "a244", "a245", "a246", "a247", "a248", "a249", "a250", "a251", "a252", "a253", "a254", "a255", "s36", "s37", "s38", "s39", "s40", "s41", "s42", "s43", "s44", "s45", "s46", "s47", "s48", "s49", "s50", "s51", "s52", "s53", "s54", "s55", "s56", "s57", "s58", "s59", "s60", "s61", "s62", "s63", "s64", "s65", "s66", "s67", "s68", "s69", "s70", "s71", "s72", "s73", "s74", "s75", "s76", "s77", "s78", "s79", "s80", "s81", "s82", "s83", "s84", "s85", "s86", "s87", "s88", "s89", "s90", "s91", "s92", "s93", "s94", "s95", "s96", "s97", "s98", "s99", "vcc", "scc", "memory"
        );
    }
    {
        PHASE_VARS();
        if (zrole) {
            const int r16 = lane & 15, fq = lane >> 4, wr = w >> 1, wc = w & 1;
            const unsigned tg = (unsigned)tag;
            const char* xb = ws + OFF_XNI; const char* wb = (const char*)ap->win - 896; const char* fb = ws + OFF_CTL;
            f32x4 c[32];
            CST(0);
            asm volatile(
R"ASM(s_mov_b32 s36, %[x0]
s_mov_b32 s37, %[x1]
s_mov_b32 s38, %[xn]
s_mov_b32 s39, 0x20000
s_mov_b32 s40, %[w0]
s_mov_b32 s41, %[w1]
s_mov_b32 s42, %[wn]
s_mov_b32 s43, 0x20000
s_mov_b32 s48, %[w]
s_mov_b32 s52, %[tag0]
s_mov_b32 s53, %[tag1]
s_lshl_b32 s49, s48, 11
s_add_u32 s44, %[lds], s49
s_mov_b32 s45, %[xo]
s_mov_b32 s46, %[wo]
s_mov_b32 s47, %[wo1]
v_mbcnt_lo_u32_b32 v52, -1, 0
v_mbcnt_hi_u32_b32 v52, -1, v52
v_lshlrev_b32_e32 v32, 4, v52
v_lshrrev_b32_e32 v57, 4, v52
v_and_b32_e32 v57, 1, v57
s_lshl_b32 s49, s48, 1
v_add_u32_e32 v57, s49, v57
v_and_b32_e32 v57, 3, v57
v_and_b32_e32 v58, 3, v52
v_xor_b32_e32 v57, v57, v58
v_lshrrev_b32_e32 v58, 5, v52
v_lshl_add_u32 v57, v58, 2, v57
v_lshlrev_b32_e32 v57, 4, v57
v_lshrrev_b32_e32 v58, 2, v52
v_and_b32_e32 v58, 7, v58
v_mul_u32_u24_e32 v58, %[ws], v58
v_add_u32_e32 v34, v57, v58
v_add_u32_e32 v33, 896, v34
s_add_u32 s45, s45, 786432
s_add_u32 s46, s46, 663552
s_add_u32 s47, s47, 663552
v_and_b32_e32 v53, 15, v52
v_lshrrev_b32_e32 v54, 4, v52
v_lshrrev_b32_e32 v57, 2, v53
v_sub_u32_e32 v57, 4, v57
v_and_b32_e32 v57, 3, v57
v_xor_b32_e32 v57, v57, v54
v_lshlrev_b32_e32 v57, 4, v57
v_lshl_add_u32 v57, v53, 6, v57
s_lshr_b32 s49, s48, 1
s_lshl_b32 s49, s49, 12
s_add_u32 s49, s49, %[lds]
v_add_u32_e32 v57, s49, v57
v_add_u32_e32 v35, 0, v57
v_add_u32_e32 v36, 49152, v57
v_add_u32_e32 v37, 98304, v57
v_lshrrev_b32_e32 v55, 2, v53
v_and_b32_e32 v56, 3, v52
s_and_b32 s49, s48, 1
s_lshl_b32 s49, s49, 13
s_add_u32 s49, s49, %[lds]
s_add_u32 s49, s49, 8192
v_lshlrev_b32_e32 v57, 1, v54
v_add_u32_e32 v57, 0, v57
v_and_b32_e32 v57, 3, v57
v_lshrrev_b32_e32 v58, 1, v56
v_or_b32_e32 v58, 0, v58
v_xor_b32_e32 v57, v57, v58
v_lshlrev_b32_e32 v57, 4, v57
v_and_b32_e32 v58, 1, v56
v_lshl_add_u32 v57, v58, 3, v57
v_lshl_add_u32 v57, v54, 11, v57
v_add_u32_e32 v58, 0, v55
v_lshl_add_u32 v57, v58, 6, v57
v_add_u32_e32 v57, s49, v57
v_add_u32_e32 v38, 0, v57
v_add_u32_e32 v42, 49152, v57
v_add_u32_e32 v46, 98304, v57
v_lshlrev_b32_e32 v57, 1, v54
v_add_u32_e32 v57, 0, v57
v_and_b32_e32 v57, 3, v57
v_lshrrev_b32_e32 v58, 1, v56
v_or_b32_e32 v58, 2, v58
v_xor_b32_e32 v57, v57, v58
v_lshlrev_b32_e32 v57, 4, v57
v_and_b32_e32 v58, 1, v56
v_lshl_add_u32 v57, v58, 3, v57
v_lshl_add_u32 v57, v54, 11, v57
v_add_u32_e32 v58, 0, v55
v_lshl_add_u32 v57, v58, 6, v57
v_add_u32_e32 v57, s49, v57
v_add_u32_e32 v39, 0, v57
v_add_u32_e32 v43, 49152, v57
v_add_u32_e32 v47, 98304, v57
v_lshlrev_b32_e32 v57, 1, v54
v_add_u32_e32 v57, 1, v57
v_and_b32_e32 v57, 3, v57
v_lshrrev_b32_e32 v58, 1, v56
v_or_b32_e32 v58, 0, v58
v_xor_b32_e32 v57, v57, v58
v_lshlrev_b32_e32 v57, 4, v57
v_and_b32_e32 v58, 1, v56
v_lshl_add_u32 v57, v58, 3, v57
v_lshl_add_u32 v57, v54, 11, v57
v_add_u32_e32 v58, 4, v55
v_lshl_add_u32 v57, v58, 6, v57
v_add_u32_e32 v57, s49, v57
v_add_u32_e32 v40, 0, v57
v_add_u32_e32 v44, 49152, v57
v_add_u32_e32 v48, 98304, v57
v_lshlrev_b32_e32 v57, 1, v54
v_add_u32_e32 v57, 1, v57
v_and_b32_e32 v57, 3, v57
v_lshrrev_b32_e32 v58, 1, v56
v_or_b32_e32 v58, 2, v58
v_xor_b32_e32 v57, v57, v58
v_lshlrev_b32_e32 v57, 4, v57
v_and_b32_e32 v58, 1, v56
v_lshl_add_u32 v57, v58, 3, v57
v_lshl_add_u32 v57, v54, 11, v57
v_add_u32_e32 v58, 4, v55
v_lshl_add_u32 v57, v58, 6, v57
v_add_u32_e32 v57, s49, v57
v_add_u32_e32 v41, 0, v57
v_add_u32_e32 v45, 49152, v57
v_add_u32_e32 v49, 98304, v57
s_waitcnt vmcnt(62)
s_barrier
ds_read_b128 a[64:67], v35
ds_read_b128 a[68:71], v35 offset:1024
ds_read_b128 a[72:75], v35 offset:2048
ds_read_b64_tr_b16 a[0:1], v38
ds_read_b64_tr_b16 a[2:3], v40
ds_read_b128 a[76:79], v35 offset:3072
ds_read_b64_tr_b16 a[4:5], v39
ds_read_b64_tr_b16 a[6:7], v41
ds_read_b64_tr_b16 a[8:9], v38 offset:512
ds_read_b64_tr_b16 a[10:11], v40 offset:512
s_waitcnt lgkmcnt(2)
v_mfma_f32_16x16x32_bf16 %[c0], a[0:3], a[64:67], 0
ds_read_b64_tr_b16 a[12:13], v39 offset:512
ds_read_b64_tr_b16 a[14:15], v41 offset:512
v_mfma_f32_16x16x32_bf16 %[c8], a[0:3], a[68:71], 0
v_mfma_f32_16x16x32_bf16 %[c16], a[0:3], a[72:75], 0
v_mfma_f32_16x16x32_bf16 %[c24], a[0:3], a[76:79], 0
v_mfma_f32_16x16x32_bf16 %[c1], a[4:7], a[64:67], 0
ds_read_b64_tr_b16 a[16:17], v38 offset:1024
ds_read_b64_tr_b16 a[18:19], v40 offset:1024
v_mfma_f32_16x16x32_bf16 %[c9], a[4:7], a[68:71], 0
v_mfma_f32_16x16x32_bf16 %[c17], a[4:7], a[72:75], 0
v_mfma_f32_16x16x32_bf16 %[c25], a[4:7], a[76:79], 0
s_waitcnt lgkmcnt(2)
v_mfma_f32_16x16x32_bf16 %[c2], a[8:11], a[64:67], 0
ds_read_b64_tr_b16 a[20:21], v39 offset:1024
ds_read_b64_tr_b16 a[22:23], v41 offset:1024
v_mfma_f32_16x16x32_bf16 %[c10], a[8:11], a[68:71], 0
v_mfma_f32_16x16x32_bf16 %[c18], a[8:11], a[72:75], 0
s_waitcnt vmcnt(56)
s_barrier
v_mfma_f32_16x16x32_bf16 %[c26], a[8:11], a[76:79], 0
v_mfma_f32_16x16x32_bf16 %[c3], a[12:15], a[64:67], 0
ds_read_b64_tr_b16 a[24:25], v38 offset:1536
ds_read_b64_tr_b16 a[26:27], v40 offset:1536
v_mfma_f32_16x16x32_bf16 %[c11], a[12:15], a[68:71], 0
ds_read_b128 a[80:83], v35 offset:24576
v_mfma_f32_16x16x32_bf16 %[c19], a[12:15], a[72:75], 0
v_mfma_f32_16x16x32_bf16 %[c27], a[12:15], a[76:79], 0
s_waitcnt lgkmcnt(3)
v_mfma_f32_16x16x32_bf16 %[c4], a[16:19], a[64:67], 0
ds_read_b64_tr_b16 a[28:29], v39 offset:1536
ds_read_b64_tr_b16 a[30:31], v41 offset:1536
v_mfma_f32_16x16x32_bf16 %[c12], a[16:19], a[68:71], 0
ds_read_b128 a[84:87], v35 offset:25600
v_mfma_f32_16x16x32_bf16 %[c20], a[16:19], a[72:75], 0
v_mfma_f32_16x16x32_bf16 %[c28], a[16:19], a[76:79], 0
v_mfma_f32_16x16x32_bf16 %[c5], a[20:23], a[64:67], 0
ds_read_b128 a[88:91], v35 offset:26624
ds_read_b64_tr_b16 a[32:33], v38 offset:24576
v_mfma_f32_16x16x32_bf16 %[c13], a[20:23], a[68:71], 0
ds_read_b64_tr_b16 a[34:35], v40 offset:24576
v_mfma_f32_16x16x32_bf16 %[c21], a[20:23], a[72:75], 0
v_mfma_f32_16x16x32_bf16 %[c29], a[20:23], a[76:79], 0
s_waitcnt lgkmcnt(4)
v_mfma_f32_16x16x32_bf16 %[c6], a[24:27], a[64:67], 0
ds_read_b128 a[92:95], v35 offset:27648
ds_read_b64_tr_b16 a[36:37], v39 offset:24576
v_mfma_f32_16x16x32_bf16 %[c14], a[24:27], a[68:71], 0
ds_read_b64_tr_b16 a[38:39], v41 offset:24576
v_mfma_f32_16x16x32_bf16 %[c22], a[24:27], a[72:75], 0
v_mfma_f32_16x16x32_bf16 %[c30], a[24:27], a[76:79], 0
v_mfma_f32_16x16x32_bf16 %[c7], a[28:31], a[64:67], 0
ds_read_b64_tr_b16 a[40:41], v38 offset:25088
ds_read_b64_tr_b16 a[42:43], v40 offset:25088
v_mfma_f32_16x16x32_bf16 %[c15], a[28:31], a[68:71], 0
v_mfma_f32_16x16x32_bf16 %[c23], a[28:31], a[72:75], 0
v_mfma_f32_16x16x32_bf16 %[c31], a[28:31], a[76:79], 0
s_waitcnt lgkmcnt(2)
v_mfma_f32_16x16x32_bf16 %[c0], a[32:35], a[80:83], %[c0]
ds_read_b64_tr_b16 a[44:45], v39 offset:25088
ds_read_b64_tr_b16 a[46:47], v41 offset:25088
v_mfma_f32_16x16x32_bf16 %[c8], a[32:35], a[84:87], %[c8]
v_mfma_f32_16x16x32_bf16 %[c16], a[32:35], a[88:91], %[c16]
v_mfma_f32_16x16x32_bf16 %[c24], a[32:35], a[92:95], %[c24]
v_mfma_f32_16x16x32_bf16 %[c1], a[36:39], a[80:83], %[c1]
ds_read_b64_tr_b16 a[48:49], v38 offset:25600
ds_read_b64_tr_b16 a[50:51], v40 offset:25600
v_mfma_f32_16x16x32_bf16 %[c9], a[36:39], a[84:87], %[c9]
v_mfma_f32_16x16x32_bf16 %[c17], a[36:39], a[88:91], %[c17]
v_mfma_f32_16x16x32_bf16 %[c25], a[36:39], a[92:95], %[c25]
s_waitcnt lgkmcnt(2)
v_mfma_f32_16x16x32_bf16 %[c2], a[40:43], a[80:83], %[c2]
ds_read_b64_tr_b16 a[52:53], v39 offset:25600
ds_read_b64_tr_b16 a[54:55], v41 offset:25600
v_mfma_f32_16x16x32_bf16 %[c10], a[40:43], a[84:87], %[c10]
v_mfma_f32_16x16x32_bf16 %[c18], a[40:43], a[88:91], %[c18]
s_waitcnt vmcnt(50)
s_barrier
v_mfma_f32_16x16x32_bf16 %[c26], a[40:43], a[92:95], %[c26]
s_add_u32 m0, s44, 0
v_mfma_f32_16x16x32_bf16 %[c3], a[44:47], a[80:83], %[c3]
ds_read_b64_tr_b16 a[56:57], v38 offset:26112
ds_read_b64_tr_b16 a[58:59], v40 offset:26112
v_mfma_f32_16x16x32_bf16 %[c11], a[44:47], a[84:87], %[c11]
ds_read_b128 a[64:67], v36
buffer_load_dwordx4 v32, s[36:39], s45 offen sc1 lds
v_mfma_f32_16x16x32_bf16 %[c19], a[44:47], a[88:91], %[c19]
v_mfma_f32_16x16x32_bf16 %[c27], a[44:47], a[92:95], %[c27]
s_waitcnt lgkmcnt(3)
v_mfma_f32_16x16x32_bf16 %[c4], a[48:51], a[80:83], %[c4]
ds_read_b64_tr_b16 a[60:61], v39 offset:26112
ds_read_b64_tr_b16 a[62:63], v41 offset:26112
buffer_load_dwordx4 v32, s[36:39], s45 offen offset:1024 sc1 lds
v_mfma_f32_16x16x32_bf16 %[c12], a[48:51], a[84:87], %[c12]
ds_read_b128 a[68:71], v36 offset:1024
v_mfma_f32_16x16x32_bf16 %[c20], a[48:51], a[88:91], %[c20]
s_add_u32 m0, s44, 8192
v_mfma_f32_16x16x32_bf16 %[c28], a[48:51], a[92:95], %[c28]
v_mfma_f32_16x16x32_bf16 %[c5], a[52:55], a[80:83], %[c5]
ds_read_b128 a[72:75], v36 offset:2048
ds_read_b64_tr_b16 a[0:1], v42
v_mfma_f32_16x16x32_bf16 %[c13], a[52:55], a[84:87], %[c13]
ds_read_b64_tr_b16 a[2:3], v44
buffer_load_dwordx4 v33, s[40:43], s46 offen lds
v_mfma_f32_16x16x32_bf16 %[c21], a[52:55], a[88:91], %[c21]
v_mfma_f32_16x16x32_bf16 %[c29], a[52:55], a[92:95], %[c29]
buffer_load_dwordx4 v34, s[40:43], s46 offen offset:1024 lds
s_waitcnt lgkmcnt(4)
v_mfma_f32_16x16x32_bf16 %[c6], a[56:59], a[80:83], %[c6]
ds_read_b128 a[76:79], v36 offset:3072
ds_read_b64_tr_b16 a[4:5], v43
v_mfma_f32_16x16x32_bf16 %[c14], a[56:59], a[84:87], %[c14]
ds_read_b64_tr_b16 a[6:7], v45
v_mfma_f32_16x16x32_bf16 %[c22], a[56:59], a[88:91], %[c22]
s_add_u32 m0, s44, 16384
v_mfma_f32_16x16x32_bf16 %[c30], a[56:59], a[92:95], %[c30]
v_mfma_f32_16x16x32_bf16 %[c7], a[60:63], a[80:83], %[c7]
ds_read_b64_tr_b16 a[8:9], v42 offset:512
ds_read_b64_tr_b16 a[10:11], v44 offset:512
v_mfma_f32_16x16x32_bf16 %[c15], a[60:63], a[84:87], %[c15]
buffer_load_dwordx4 v33, s[40:43], s47 offen lds
v_mfma_f32_16x16x32_bf16 %[c23], a[60:63], a[88:91], %[c23]
v_mfma_f32_16x16x32_bf16 %[c31], a[60:63], a[92:95], %[c31]
buffer_load_dwordx4 v34, s[40:43], s47 offen offset:1024 lds
s_waitcnt lgkmcnt(2)
v_mfma_f32_16x16x32_bf16 %[c0], a[0:3], a[64:67], %[c0]
ds_read_b64_tr_b16 a[12:13], v43 offset:512
ds_read_b64_tr_b16 a[14:15], v45 offset:512
v_mfma_f32_16x16x32_bf16 %[c8], a[0:3], a[68:71], %[c8]
v_mfma_f32_16x16x32_bf16 %[c16], a[0:3], a[72:75], %[c16]
s_add_u32 s45, s45, %[xadv]
v_mfma_f32_16x16x32_bf16 %[c24], a[0:3], a[76:79], %[c24]
v_mfma_f32_16x16x32_bf16 %[c1], a[4:7], a[64:67], %[c1]
s_add_u32 s46, s46, %[wadv]
ds_read_b64_tr_b16 a[16:17], v42 offset:1024
ds_read_b64_tr_b16 a[18:19], v44 offset:1024
v_mfma_f32_16x16x32_bf16 %[c9], a[4:7], a[68:71], %[c9]
v_mfma_f32_16x16x32_bf16 %[c17], a[4:7], a[72:75], %[c17]
v_mfma_f32_16x16x32_bf16 %[c25], a[4:7], a[76:79], %[c25]
s_add_u32 s47, s47, %[wadv]
s_waitcnt lgkmcnt(2)
v_mfma_f32_16x16x32_bf16 %[c2], a[8:11], a[64:67], %[c2]
ds_read_b64_tr_b16 a[20:21], v43 offset:1024
ds_read_b64_tr_b16 a[22:23], v45 offset:1024
v_mfma_f32_16x16x32_bf16 %[c10], a[8:11], a[68:71], %[c10]
v_mfma_f32_16x16x32_bf16 %[c18], a[8:11], a[72:75], %[c18]
s_waitcnt vmcnt(50)
s_barrier
v_mfma_f32_16x16x32_bf16 %[c26], a[8:11], a[76:79], %[c26]
s_add_u32 m0, s44, 24576
v_mfma_f32_16x16x32_bf16 %[c3], a[12:15], a[64:67], %[c3]
ds_read_b64_tr_b16 a[24:25], v42 offset:1536
ds_read_b64_tr_b16 a[26:27], v44 offset:1536
v_mfma_f32_16x16x32_bf16 %[c11], a[12:15], a[68:71], %[c11]
ds_read_b128 a[80:83], v36 offset:24576
buffer_load_dwordx4 v32, s[36:39], s45 offen sc1 lds
v_mfma_f32_16x16x32_bf16 %[c19], a[12:15], a[72:75], %[c19]
v_mfma_f32_16x16x32_bf16 %[c27], a[12:15], a[76:79], %[c27]
s_waitcnt lgkmcnt(3)
v_mfma_f32_16x16x32_bf16 %[c4], a[16:19], a[64:67], %[c4]
ds_read_b64_tr_b16 a[28:29], v43 offset:1536
ds_read_b64_tr_b16 a[30:31], v45 offset:1536
buffer_load_dwordx4 v32, s[36:39], s45 offen offset:1024 sc1 lds
v_mfma_f32_16x16x32_bf16 %[c12], a[16:19], a[68:71], %[c12]
ds_read_b128 a[84:87], v36 offset:25600
v_mfma_f32_16x16x32_bf16 %[c20], a[16:19], a[72:75], %[c20]
s_add_u32 m0, s44, 32768
v_mfma_f32_16x16x32_bf16 %[c28], a[16:19], a[76:79], %[c28]
v_mfma_f32_16x16x32_bf16 %[c5], a[20:23], a[64:67], %[c5]
ds_read_b128 a[88:91], v36 offset:26624
ds_read_b64_tr_b16 a[32:33], v42 offset:24576
v_mfma_f32_16x16x32_bf16 %[c13], a[20:23], a[68:71], %[c13]
ds_read_b64_tr_b16 a[34:35], v44 offset:24576
buffer_load_dwordx4 v33, s[40:43], s46 offen lds
)ASM"
R"ASM(v_mfma_f32_16x16x32_bf16 %[c21], a[20:23], a[72:75], %[c21]
v_mfma_f32_16x16x32_bf16 %[c29], a[20:23], a[76:79], %[c29]
buffer_load_dwordx4 v34, s[40:43], s46 offen offset:1024 lds
s_waitcnt lgkmcnt(4)
v_mfma_f32_16x16x32_bf16 %[c6], a[24:27], a[64:67], %[c6]
ds_read_b128 a[92:95], v36 offset:27648
ds_read_b64_tr_b16 a[36:37], v43 offset:24576
v_mfma_f32_16x16x32_bf16 %[c14], a[24:27], a[68:71], %[c14]
ds_read_b64_tr_b16 a[38:39], v45 offset:24576
v_mfma_f32_16x16x32_bf16 %[c22], a[24:27], a[72:75], %[c22]
s_add_u32 m0, s44, 40960
v_mfma_f32_16x16x32_bf16 %[c30], a[24:27], a[76:79], %[c30]
v_mfma_f32_16x16x32_bf16 %[c7], a[28:31], a[64:67], %[c7]
ds_read_b64_tr_b16 a[40:41], v42 offset:25088
ds_read_b64_tr_b16 a[42:43], v44 offset:25088
v_mfma_f32_16x16x32_bf16 %[c15], a[28:31], a[68:71], %[c15]
buffer_load_dwordx4 v33, s[40:43], s47 offen lds
v_mfma_f32_16x16x32_bf16 %[c23], a[28:31], a[72:75], %[c23]
v_mfma_f32_16x16x32_bf16 %[c31], a[28:31], a[76:79], %[c31]
buffer_load_dwordx4 v34, s[40:43], s47 offen offset:1024 lds
s_waitcnt lgkmcnt(2)
v_mfma_f32_16x16x32_bf16 %[c0], a[32:35], a[80:83], %[c0]
ds_read_b64_tr_b16 a[44:45], v43 offset:25088
ds_read_b64_tr_b16 a[46:47], v45 offset:25088
v_mfma_f32_16x16x32_bf16 %[c8], a[32:35], a[84:87], %[c8]
v_mfma_f32_16x16x32_bf16 %[c16], a[32:35], a[88:91], %[c16]
s_add_u32 s45, s45, %[xadv]
v_mfma_f32_16x16x32_bf16 %[c24], a[32:35], a[92:95], %[c24]
v_mfma_f32_16x16x32_bf16 %[c1], a[36:39], a[80:83], %[c1]
s_add_u32 s46, s46, %[wadv]
ds_read_b64_tr_b16 a[48:49], v42 offset:25600
ds_read_b64_tr_b16 a[50:51], v44 offset:25600
v_mfma_f32_16x16x32_bf16 %[c9], a[36:39], a[84:87], %[c9]
v_mfma_f32_16x16x32_bf16 %[c17], a[36:39], a[88:91], %[c17]
v_mfma_f32_16x16x32_bf16 %[c25], a[36:39], a[92:95], %[c25]
s_add_u32 s47, s47, %[wadv]
s_waitcnt lgkmcnt(2)
v_mfma_f32_16x16x32_bf16 %[c2], a[40:43], a[80:83], %[c2]
ds_read_b64_tr_b16 a[52:53], v43 offset:25600
ds_read_b64_tr_b16 a[54:55], v45 offset:25600
v_mfma_f32_16x16x32_bf16 %[c10], a[40:43], a[84:87], %[c10]
v_mfma_f32_16x16x32_bf16 %[c18], a[40:43], a[88:91], %[c18]
s_waitcnt vmcnt(50)
s_barrier
v_mfma_f32_16x16x32_bf16 %[c26], a[40:43], a[92:95], %[c26]
s_add_u32 m0, s44, 49152
v_mfma_f32_16x16x32_bf16 %[c3], a[44:47], a[80:83], %[c3]
ds_read_b64_tr_b16 a[56:57], v42 offset:26112
ds_read_b64_tr_b16 a[58:59], v44 offset:26112
v_mfma_f32_16x16x32_bf16 %[c11], a[44:47], a[84:87], %[c11]
ds_read_b128 a[64:67], v37
buffer_load_dwordx4 v32, s[36:39], s45 offen sc1 lds
v_mfma_f32_16x16x32_bf16 %[c19], a[44:47], a[88:91], %[c19]
v_mfma_f32_16x16x32_bf16 %[c27], a[44:47], a[92:95], %[c27]
s_waitcnt lgkmcnt(3)
v_mfma_f32_16x16x32_bf16 %[c4], a[48:51], a[80:83], %[c4]
ds_read_b64_tr_b16 a[60:61], v43 offset:26112
ds_read_b64_tr_b16 a[62:63], v45 offset:26112
buffer_load_dwordx4 v32, s[36:39], s45 offen offset:1024 sc1 lds
v_mfma_f32_16x16x32_bf16 %[c12], a[48:51], a[84:87], %[c12]
ds_read_b128 a[68:71], v37 offset:1024
v_mfma_f32_16x16x32_bf16 %[c20], a[48:51], a[88:91], %[c20]
s_add_u32 m0, s44, 57344
v_mfma_f32_16x16x32_bf16 %[c28], a[48:51], a[92:95], %[c28]
v_mfma_f32_16x16x32_bf16 %[c5], a[52:55], a[80:83], %[c5]
ds_read_b128 a[72:75], v37 offset:2048
ds_read_b64_tr_b16 a[0:1], v46
v_mfma_f32_16x16x32_bf16 %[c13], a[52:55], a[84:87], %[c13]
ds_read_b64_tr_b16 a[2:3], v48
buffer_load_dwordx4 v33, s[40:43], s46 offen lds
v_mfma_f32_16x16x32_bf16 %[c21], a[52:55], a[88:91], %[c21]
v_mfma_f32_16x16x32_bf16 %[c29], a[52:55], a[92:95], %[c29]
buffer_load_dwordx4 v34, s[40:43], s46 offen offset:1024 lds
s_waitcnt lgkmcnt(4)
v_mfma_f32_16x16x32_bf16 %[c6], a[56:59], a[80:83], %[c6]
ds_read_b128 a[76:79], v37 offset:3072
ds_read_b64_tr_b16 a[4:5], v47
v_mfma_f32_16x16x32_bf16 %[c14], a[56:59], a[84:87], %[c14]
ds_read_b64_tr_b16 a[6:7], v49
v_mfma_f32_16x16x32_bf16 %[c22], a[56:59], a[88:91], %[c22]
s_add_u32 m0, s44, 65536
v_mfma_f32_16x16x32_bf16 %[c30], a[56:59], a[92:95], %[c30]
v_mfma_f32_16x16x32_bf16 %[c7], a[60:63], a[80:83], %[c7]
ds_read_b64_tr_b16 a[8:9], v46 offset:512
ds_read_b64_tr_b16 a[10:11], v48 offset:512
v_mfma_f32_16x16x32_bf16 %[c15], a[60:63], a[84:87], %[c15]
buffer_load_dwordx4 v33, s[40:43], s47 offen lds
v_mfma_f32_16x16x32_bf16 %[c23], a[60:63], a[88:91], %[c23]
v_mfma_f32_16x16x32_bf16 %[c31], a[60:63], a[92:95], %[c31]
buffer_load_dwordx4 v34, s[40:43], s47 offen offset:1024 lds
s_waitcnt lgkmcnt(2)
v_mfma_f32_16x16x32_bf16 %[c0], a[0:3], a[64:67], %[c0]
ds_read_b64_tr_b16 a[12:13], v47 offset:512
ds_read_b64_tr_b16 a[14:15], v49 offset:512
v_mfma_f32_16x16x32_bf16 %[c8], a[0:3], a[68:71], %[c8]
v_mfma_f32_16x16x32_bf16 %[c16], a[0:3], a[72:75], %[c16]
s_add_u32 s45, s45, %[xadv]
v_mfma_f32_16x16x32_bf16 %[c24], a[0:3], a[76:79], %[c24]
v_mfma_f32_16x16x32_bf16 %[c1], a[4:7], a[64:67], %[c1]
s_add_u32 s46, s46, %[wadv]
ds_read_b64_tr_b16 a[16:17], v46 offset:1024
ds_read_b64_tr_b16 a[18:19], v48 offset:1024
v_mfma_f32_16x16x32_bf16 %[c9], a[4:7], a[68:71], %[c9]
v_mfma_f32_16x16x32_bf16 %[c17], a[4:7], a[72:75], %[c17]
v_mfma_f32_16x16x32_bf16 %[c25], a[4:7], a[76:79], %[c25]
s_add_u32 s47, s47, %[wadv]
s_waitcnt lgkmcnt(2)
v_mfma_f32_16x16x32_bf16 %[c2], a[8:11], a[64:67], %[c2]
ds_read_b64_tr_b16 a[20:21], v47 offset:1024
ds_read_b64_tr_b16 a[22:23], v49 offset:1024
v_mfma_f32_16x16x32_bf16 %[c10], a[8:11], a[68:71], %[c10]
v_mfma_f32_16x16x32_bf16 %[c18], a[8:11], a[72:75], %[c18]
s_waitcnt vmcnt(50)
s_barrier
v_mfma_f32_16x16x32_bf16 %[c26], a[8:11], a[76:79], %[c26]
s_add_u32 m0, s44, 73728
v_mfma_f32_16x16x32_bf16 %[c3], a[12:15], a[64:67], %[c3]
ds_read_b64_tr_b16 a[24:25], v46 offset:1536
ds_read_b64_tr_b16 a[26:27], v48 offset:1536
v_mfma_f32_16x16x32_bf16 %[c11], a[12:15], a[68:71], %[c11]
ds_read_b128 a[80:83], v37 offset:24576
buffer_load_dwordx4 v32, s[36:39], s45 offen sc1 lds
v_mfma_f32_16x16x32_bf16 %[c19], a[12:15], a[72:75], %[c19]
v_mfma_f32_16x16x32_bf16 %[c27], a[12:15], a[76:79], %[c27]
s_waitcnt lgkmcnt(3)
v_mfma_f32_16x16x32_bf16 %[c4], a[16:19], a[64:67], %[c4]
ds_read_b64_tr_b16 a[28:29], v47 offset:1536
ds_read_b64_tr_b16 a[30:31], v49 offset:1536
buffer_load_dwordx4 v32, s[36:39], s45 offen offset:1024 sc1 lds
v_mfma_f32_16x16x32_bf16 %[c12], a[16:19], a[68:71], %[c12]
ds_read_b128 a[84:87], v37 offset:25600
v_mfma_f32_16x16x32_bf16 %[c20], a[16:19], a[72:75], %[c20]
s_add_u32 m0, s44, 81920
v_mfma_f32_16x16x32_bf16 %[c28], a[16:19], a[76:79], %[c28]
v_mfma_f32_16x16x32_bf16 %[c5], a[20:23], a[64:67], %[c5]
ds_read_b128 a[88:91], v37 offset:26624
ds_read_b64_tr_b16 a[32:33], v46 offset:24576
v_mfma_f32_16x16x32_bf16 %[c13], a[20:23], a[68:71], %[c13]
ds_read_b64_tr_b16 a[34:35], v48 offset:24576
buffer_load_dwordx4 v33, s[40:43], s46 offen lds
v_mfma_f32_16x16x32_bf16 %[c21], a[20:23], a[72:75], %[c21]
v_mfma_f32_16x16x32_bf16 %[c29], a[20:23], a[76:79], %[c29]
buffer_load_dwordx4 v34, s[40:43], s46 offen offset:1024 lds
s_waitcnt lgkmcnt(4)
v_mfma_f32_16x16x32_bf16 %[c6], a[24:27], a[64:67], %[c6]
ds_read_b128 a[92:95], v37 offset:27648
ds_read_b64_tr_b16 a[36:37], v47 offset:24576
v_mfma_f32_16x16x32_bf16 %[c14], a[24:27], a[68:71], %[c14]
ds_read_b64_tr_b16 a[38:39], v49 offset:24576
v_mfma_f32_16x16x32_bf16 %[c22], a[24:27], a[72:75], %[c22]
s_add_u32 m0, s44, 90112
v_mfma_f32_16x16x32_bf16 %[c30], a[24:27], a[76:79], %[c30]
v_mfma_f32_16x16x32_bf16 %[c7], a[28:31], a[64:67], %[c7]
ds_read_b64_tr_b16 a[40:41], v46 offset:25088
ds_read_b64_tr_b16 a[42:43], v48 offset:25088
v_mfma_f32_16x16x32_bf16 %[c15], a[28:31], a[68:71], %[c15]
buffer_load_dwordx4 v33, s[40:43], s47 offen lds
v_mfma_f32_16x16x32_bf16 %[c23], a[28:31], a[72:75], %[c23]
v_mfma_f32_16x16x32_bf16 %[c31], a[28:31], a[76:79], %[c31]
buffer_load_dwordx4 v34, s[40:43], s47 offen offset:1024 lds
s_waitcnt lgkmcnt(2)
v_mfma_f32_16x16x32_bf16 %[c0], a[32:35], a[80:83], %[c0]
ds_read_b64_tr_b16 a[44:45], v47 offset:25088
ds_read_b64_tr_b16 a[46:47], v49 offset:25088
v_mfma_f32_16x16x32_bf16 %[c8], a[32:35], a[84:87], %[c8]
v_mfma_f32_16x16x32_bf16 %[c16], a[32:35], a[88:91], %[c16]
s_add_u32 s45, s45, %[xadv]
v_mfma_f32_16x16x32_bf16 %[c24], a[32:35], a[92:95], %[c24]
v_mfma_f32_16x16x32_bf16 %[c1], a[36:39], a[80:83], %[c1]
s_add_u32 s46, s46, %[wadv]
ds_read_b64_tr_b16 a[48:49], v46 offset:25600
ds_read_b64_tr_b16 a[50:51], v48 offset:25600
v_mfma_f32_16x16x32_bf16 %[c9], a[36:39], a[84:87], %[c9]
v_mfma_f32_16x16x32_bf16 %[c17], a[36:39], a[88:91], %[c17]
v_mfma_f32_16x16x32_bf16 %[c25], a[36:39], a[92:95], %[c25]
s_add_u32 s47, s47, %[wadv]
s_waitcnt lgkmcnt(2)
v_mfma_f32_16x16x32_bf16 %[c2], a[40:43], a[80:83], %[c2]
ds_read_b64_tr_b16 a[52:53], v47 offset:25600
ds_read_b64_tr_b16 a[54:55], v49 offset:25600
v_mfma_f32_16x16x32_bf16 %[c10], a[40:43], a[84:87], %[c10]
v_mfma_f32_16x16x32_bf16 %[c18], a[40:43], a[88:91], %[c18]
s_waitcnt vmcnt(18)
s_barrier
v_mfma_f32_16x16x32_bf16 %[c26], a[40:43], a[92:95], %[c26]
s_add_u32 m0, s44, 98304
v_mfma_f32_16x16x32_bf16 %[c3], a[44:47], a[80:83], %[c3]
ds_read_b64_tr_b16 a[56:57], v46 offset:26112
ds_read_b64_tr_b16 a[58:59], v48 offset:26112
v_mfma_f32_16x16x32_bf16 %[c11], a[44:47], a[84:87], %[c11]
ds_read_b128 a[64:67], v35
buffer_load_dwordx4 v32, s[36:39], s45 offen sc1 lds
v_mfma_f32_16x16x32_bf16 %[c19], a[44:47], a[88:91], %[c19]
v_mfma_f32_16x16x32_bf16 %[c27], a[44:47], a[92:95], %[c27]
s_waitcnt lgkmcnt(3)
v_mfma_f32_16x16x32_bf16 %[c4], a[48:51], a[80:83], %[c4]
ds_read_b64_tr_b16 a[60:61], v47 offset:26112
ds_read_b64_tr_b16 a[62:63], v49 offset:26112
buffer_load_dwordx4 v32, s[36:39], s45 offen offset:1024 sc1 lds
v_mfma_f32_16x16x32_bf16 %[c12], a[48:51], a[84:87], %[c12]
ds_read_b128 a[68:71], v35 offset:1024
v_mfma_f32_16x16x32_bf16 %[c20], a[48:51], a[88:91], %[c20]
s_add_u32 m0, s44, 106496
v_mfma_f32_16x16x32_bf16 %[c28], a[48:51], a[92:95], %[c28]
v_mfma_f32_16x16x32_bf16 %[c5], a[52:55], a[80:83], %[c5]
ds_read_b128 a[72:75], v35 offset:2048
ds_read_b64_tr_b16 a[0:1], v38
v_mfma_f32_16x16x32_bf16 %[c13], a[52:55], a[84:87], %[c13]
ds_read_b64_tr_b16 a[2:3], v40
buffer_load_dwordx4 v33, s[40:43], s46 offen lds
v_mfma_f32_16x16x32_bf16 %[c21], a[52:55], a[88:91], %[c21]
v_mfma_f32_16x16x32_bf16 %[c29], a[52:55], a[92:95], %[c29]
buffer_load_dwordx4 v34, s[40:43], s46 offen offset:1024 lds
s_waitcnt lgkmcnt(4)
v_mfma_f32_16x16x32_bf16 %[c6], a[56:59], a[80:83], %[c6]
ds_read_b128 a[76:79], v35 offset:3072
ds_read_b64_tr_b16 a[4:5], v39
v_mfma_f32_16x16x32_bf16 %[c14], a[56:59], a[84:87], %[c14]
ds_read_b64_tr_b16 a[6:7], v41
v_mfma_f32_16x16x32_bf16 %[c22], a[56:59], a[88:91], %[c22]
s_add_u32 m0, s44, 114688
v_mfma_f32_16x16x32_bf16 %[c30], a[56:59], a[92:95], %[c30]
v_mfma_f32_16x16x32_bf16 %[c7], a[60:63], a[80:83], %[c7]
ds_read_b64_tr_b16 a[8:9], v38 offset:512
ds_read_b64_tr_b16 a[10:11], v40 offset:512
v_mfma_f32_16x16x32_bf16 %[c15], a[60:63], a[84:87], %[c15]
buffer_load_dwordx4 v33, s[40:43], s47 offen lds
v_mfma_f32_16x16x32_bf16 %[c23], a[60:63], a[88:91], %[c23]
v_mfma_f32_16x16x32_bf16 %[c31], a[60:63], a[92:95], %[c31]
buffer_load_dwordx4 v34, s[40:43], s47 offen offset:1024 lds
s_waitcnt lgkmcnt(2)
v_mfma_f32_16x16x32_bf16 %[c0], a[0:3], a[64:67], %[c0]
ds_read_b64_tr_b16 a[12:13], v39 offset:512
ds_read_b64_tr_b16 a[14:15], v41 offset:512
v_mfma_f32_16x16x32_bf16 %[c8], a[0:3], a[68:71], %[c8]
v_mfma_f32_16x16x32_bf16 %[c16], a[0:3], a[72:75], %[c16]
s_add_u32 s45, s45, %[xadv]
v_mfma_f32_16x16x32_bf16 %[c24], a[0:3], a[76:79], %[c24]
v_mfma_f32_16x16x32_bf16 %[c1], a[4:7], a[64:67], %[c1]
s_add_u32 s46, s46, %[wadv]
ds_read_b64_tr_b16 a[16:17], v38 offset:1024
ds_read_b64_tr_b16 a[18:19], v40 offset:1024
v_mfma_f32_16x16x32_bf16 %[c9], a[4:7], a[68:71], %[c9]
v_mfma_f32_16x16x32_bf16 %[c17], a[4:7], a[72:75], %[c17]
v_mfma_f32_16x16x32_bf16 %[c25], a[4:7], a[76:79], %[c25]
s_add_u32 s47, s47, %[wadv]
s_waitcnt lgkmcnt(2)
v_mfma_f32_16x16x32_bf16 %[c2], a[8:11], a[64:67], %[c2]
ds_read_b64_tr_b16 a[20:21], v39 offset:1024
ds_read_b64_tr_b16 a[22:23], v41 offset:1024
v_mfma_f32_16x16x32_bf16 %[c10], a[8:11], a[68:71], %[c10]
)ASM"
R"ASM(v_mfma_f32_16x16x32_bf16 %[c18], a[8:11], a[72:75], %[c18]
s_waitcnt vmcnt(18)
s_barrier
v_mfma_f32_16x16x32_bf16 %[c26], a[8:11], a[76:79], %[c26]
s_add_u32 m0, s44, 122880
v_mfma_f32_16x16x32_bf16 %[c3], a[12:15], a[64:67], %[c3]
ds_read_b64_tr_b16 a[24:25], v38 offset:1536
ds_read_b64_tr_b16 a[26:27], v40 offset:1536
v_mfma_f32_16x16x32_bf16 %[c11], a[12:15], a[68:71], %[c11]
ds_read_b128 a[80:83], v35 offset:24576
buffer_load_dwordx4 v32, s[36:39], s45 offen sc1 lds
v_mfma_f32_16x16x32_bf16 %[c19], a[12:15], a[72:75], %[c19]
v_mfma_f32_16x16x32_bf16 %[c27], a[12:15], a[76:79], %[c27]
s_waitcnt lgkmcnt(3)
v_mfma_f32_16x16x32_bf16 %[c4], a[16:19], a[64:67], %[c4]
ds_read_b64_tr_b16 a[28:29], v39 offset:1536
ds_read_b64_tr_b16 a[30:31], v41 offset:1536
buffer_load_dwordx4 v32, s[36:39], s45 offen offset:1024 sc1 lds
v_mfma_f32_16x16x32_bf16 %[c12], a[16:19], a[68:71], %[c12]
ds_read_b128 a[84:87], v35 offset:25600
v_mfma_f32_16x16x32_bf16 %[c20], a[16:19], a[72:75], %[c20]
s_add_u32 m0, s44, 131072
v_mfma_f32_16x16x32_bf16 %[c28], a[16:19], a[76:79], %[c28]
v_mfma_f32_16x16x32_bf16 %[c5], a[20:23], a[64:67], %[c5]
ds_read_b128 a[88:91], v35 offset:26624
ds_read_b64_tr_b16 a[32:33], v38 offset:24576
v_mfma_f32_16x16x32_bf16 %[c13], a[20:23], a[68:71], %[c13]
ds_read_b64_tr_b16 a[34:35], v40 offset:24576
buffer_load_dwordx4 v33, s[40:43], s46 offen lds
v_mfma_f32_16x16x32_bf16 %[c21], a[20:23], a[72:75], %[c21]
v_mfma_f32_16x16x32_bf16 %[c29], a[20:23], a[76:79], %[c29]
buffer_load_dwordx4 v34, s[40:43], s46 offen offset:1024 lds
s_waitcnt lgkmcnt(4)
v_mfma_f32_16x16x32_bf16 %[c6], a[24:27], a[64:67], %[c6]
ds_read_b128 a[92:95], v35 offset:27648
ds_read_b64_tr_b16 a[36:37], v39 offset:24576
v_mfma_f32_16x16x32_bf16 %[c14], a[24:27], a[68:71], %[c14]
ds_read_b64_tr_b16 a[38:39], v41 offset:24576
v_mfma_f32_16x16x32_bf16 %[c22], a[24:27], a[72:75], %[c22]
s_add_u32 m0, s44, 139264
v_mfma_f32_16x16x32_bf16 %[c30], a[24:27], a[76:79], %[c30]
v_mfma_f32_16x16x32_bf16 %[c7], a[28:31], a[64:67], %[c7]
ds_read_b64_tr_b16 a[40:41], v38 offset:25088
ds_read_b64_tr_b16 a[42:43], v40 offset:25088
v_mfma_f32_16x16x32_bf16 %[c15], a[28:31], a[68:71], %[c15]
buffer_load_dwordx4 v33, s[40:43], s47 offen lds
v_mfma_f32_16x16x32_bf16 %[c23], a[28:31], a[72:75], %[c23]
v_mfma_f32_16x16x32_bf16 %[c31], a[28:31], a[76:79], %[c31]
buffer_load_dwordx4 v34, s[40:43], s47 offen offset:1024 lds
s_waitcnt lgkmcnt(2)
v_mfma_f32_16x16x32_bf16 %[c0], a[32:35], a[80:83], %[c0]
ds_read_b64_tr_b16 a[44:45], v39 offset:25088
ds_read_b64_tr_b16 a[46:47], v41 offset:25088
v_mfma_f32_16x16x32_bf16 %[c8], a[32:35], a[84:87], %[c8]
v_mfma_f32_16x16x32_bf16 %[c16], a[32:35], a[88:91], %[c16]
s_add_u32 s45, s45, %[xadv]
v_mfma_f32_16x16x32_bf16 %[c24], a[32:35], a[92:95], %[c24]
v_mfma_f32_16x16x32_bf16 %[c1], a[36:39], a[80:83], %[c1]
s_add_u32 s46, s46, %[wadv]
ds_read_b64_tr_b16 a[48:49], v38 offset:25600
ds_read_b64_tr_b16 a[50:51], v40 offset:25600
v_mfma_f32_16x16x32_bf16 %[c9], a[36:39], a[84:87], %[c9]
v_mfma_f32_16x16x32_bf16 %[c17], a[36:39], a[88:91], %[c17]
v_mfma_f32_16x16x32_bf16 %[c25], a[36:39], a[92:95], %[c25]
s_add_u32 s47, s47, %[wadv]
s_waitcnt lgkmcnt(2)
v_mfma_f32_16x16x32_bf16 %[c2], a[40:43], a[80:83], %[c2]
ds_read_b64_tr_b16 a[52:53], v39 offset:25600
ds_read_b64_tr_b16 a[54:55], v41 offset:25600
v_mfma_f32_16x16x32_bf16 %[c10], a[40:43], a[84:87], %[c10]
v_mfma_f32_16x16x32_bf16 %[c18], a[40:43], a[88:91], %[c18]
s_waitcnt vmcnt(18)
s_barrier
v_mfma_f32_16x16x32_bf16 %[c26], a[40:43], a[92:95], %[c26]
s_add_u32 m0, s44, 0
v_mfma_f32_16x16x32_bf16 %[c3], a[44:47], a[80:83], %[c3]
ds_read_b64_tr_b16 a[56:57], v38 offset:26112
ds_read_b64_tr_b16 a[58:59], v40 offset:26112
v_mfma_f32_16x16x32_bf16 %[c11], a[44:47], a[84:87], %[c11]
ds_read_b128 a[64:67], v36
buffer_load_dwordx4 v32, s[36:39], s45 offen sc1 lds
v_mfma_f32_16x16x32_bf16 %[c19], a[44:47], a[88:91], %[c19]
v_mfma_f32_16x16x32_bf16 %[c27], a[44:47], a[92:95], %[c27]
s_waitcnt lgkmcnt(3)
v_mfma_f32_16x16x32_bf16 %[c4], a[48:51], a[80:83], %[c4]
ds_read_b64_tr_b16 a[60:61], v39 offset:26112
ds_read_b64_tr_b16 a[62:63], v41 offset:26112
buffer_load_dwordx4 v32, s[36:39], s45 offen offset:1024 sc1 lds
v_mfma_f32_16x16x32_bf16 %[c12], a[48:51], a[84:87], %[c12]
ds_read_b128 a[68:71], v36 offset:1024
v_mfma_f32_16x16x32_bf16 %[c20], a[48:51], a[88:91], %[c20]
s_add_u32 m0, s44, 8192
v_mfma_f32_16x16x32_bf16 %[c28], a[48:51], a[92:95], %[c28]
v_mfma_f32_16x16x32_bf16 %[c5], a[52:55], a[80:83], %[c5]
ds_read_b128 a[72:75], v36 offset:2048
ds_read_b64_tr_b16 a[0:1], v42
v_mfma_f32_16x16x32_bf16 %[c13], a[52:55], a[84:87], %[c13]
ds_read_b64_tr_b16 a[2:3], v44
buffer_load_dwordx4 v33, s[40:43], s46 offen lds
v_mfma_f32_16x16x32_bf16 %[c21], a[52:55], a[88:91], %[c21]
v_mfma_f32_16x16x32_bf16 %[c29], a[52:55], a[92:95], %[c29]
buffer_load_dwordx4 v34, s[40:43], s46 offen offset:1024 lds
s_waitcnt lgkmcnt(4)
v_mfma_f32_16x16x32_bf16 %[c6], a[56:59], a[80:83], %[c6]
ds_read_b128 a[76:79], v36 offset:3072
ds_read_b64_tr_b16 a[4:5], v43
v_mfma_f32_16x16x32_bf16 %[c14], a[56:59], a[84:87], %[c14]
ds_read_b64_tr_b16 a[6:7], v45
v_mfma_f32_16x16x32_bf16 %[c22], a[56:59], a[88:91], %[c22]
s_add_u32 m0, s44, 16384
v_mfma_f32_16x16x32_bf16 %[c30], a[56:59], a[92:95], %[c30]
v_mfma_f32_16x16x32_bf16 %[c7], a[60:63], a[80:83], %[c7]
ds_read_b64_tr_b16 a[8:9], v42 offset:512
ds_read_b64_tr_b16 a[10:11], v44 offset:512
v_mfma_f32_16x16x32_bf16 %[c15], a[60:63], a[84:87], %[c15]
buffer_load_dwordx4 v33, s[40:43], s47 offen lds
v_mfma_f32_16x16x32_bf16 %[c23], a[60:63], a[88:91], %[c23]
v_mfma_f32_16x16x32_bf16 %[c31], a[60:63], a[92:95], %[c31]
buffer_load_dwordx4 v34, s[40:43], s47 offen offset:1024 lds
s_waitcnt lgkmcnt(2)
v_mfma_f32_16x16x32_bf16 %[c0], a[0:3], a[64:67], %[c0]
ds_read_b64_tr_b16 a[12:13], v43 offset:512
ds_read_b64_tr_b16 a[14:15], v45 offset:512
v_mfma_f32_16x16x32_bf16 %[c8], a[0:3], a[68:71], %[c8]
v_mfma_f32_16x16x32_bf16 %[c16], a[0:3], a[72:75], %[c16]
s_add_u32 s45, s45, %[xadv]
v_mfma_f32_16x16x32_bf16 %[c24], a[0:3], a[76:79], %[c24]
v_mfma_f32_16x16x32_bf16 %[c1], a[4:7], a[64:67], %[c1]
s_add_u32 s46, s46, %[wadv]
ds_read_b64_tr_b16 a[16:17], v42 offset:1024
ds_read_b64_tr_b16 a[18:19], v44 offset:1024
v_mfma_f32_16x16x32_bf16 %[c9], a[4:7], a[68:71], %[c9]
v_mfma_f32_16x16x32_bf16 %[c17], a[4:7], a[72:75], %[c17]
v_mfma_f32_16x16x32_bf16 %[c25], a[4:7], a[76:79], %[c25]
s_add_u32 s47, s47, %[wadv]
s_waitcnt lgkmcnt(2)
v_mfma_f32_16x16x32_bf16 %[c2], a[8:11], a[64:67], %[c2]
ds_read_b64_tr_b16 a[20:21], v43 offset:1024
ds_read_b64_tr_b16 a[22:23], v45 offset:1024
v_mfma_f32_16x16x32_bf16 %[c10], a[8:11], a[68:71], %[c10]
v_mfma_f32_16x16x32_bf16 %[c18], a[8:11], a[72:75], %[c18]
s_waitcnt vmcnt(18)
s_barrier
s_cmp_lg_u32 s48, 0
s_cbranch_scc1 L_fs1_%=
v_mov_b32 v60, s52
v_mov_b32 v61, s53
s_mov_b64 exec, 1
v_mov_b32 v62, %[fdef]
global_store_dwordx2 v62, v[60:61], %[flags] sc1
s_mov_b64 exec, -1
L_fs1_%=:
v_mfma_f32_16x16x32_bf16 %[c26], a[8:11], a[76:79], %[c26]
s_add_u32 m0, s44, 24576
v_mfma_f32_16x16x32_bf16 %[c3], a[12:15], a[64:67], %[c3]
ds_read_b64_tr_b16 a[24:25], v42 offset:1536
ds_read_b64_tr_b16 a[26:27], v44 offset:1536
v_mfma_f32_16x16x32_bf16 %[c11], a[12:15], a[68:71], %[c11]
ds_read_b128 a[80:83], v36 offset:24576
buffer_load_dwordx4 v32, s[36:39], s45 offen sc1 lds
v_mfma_f32_16x16x32_bf16 %[c19], a[12:15], a[72:75], %[c19]
v_mfma_f32_16x16x32_bf16 %[c27], a[12:15], a[76:79], %[c27]
s_waitcnt lgkmcnt(3)
v_mfma_f32_16x16x32_bf16 %[c4], a[16:19], a[64:67], %[c4]
ds_read_b64_tr_b16 a[28:29], v43 offset:1536
ds_read_b64_tr_b16 a[30:31], v45 offset:1536
buffer_load_dwordx4 v32, s[36:39], s45 offen offset:1024 sc1 lds
v_mfma_f32_16x16x32_bf16 %[c12], a[16:19], a[68:71], %[c12]
ds_read_b128 a[84:87], v36 offset:25600
v_mfma_f32_16x16x32_bf16 %[c20], a[16:19], a[72:75], %[c20]
s_add_u32 m0, s44, 32768
v_mfma_f32_16x16x32_bf16 %[c28], a[16:19], a[76:79], %[c28]
v_mfma_f32_16x16x32_bf16 %[c5], a[20:23], a[64:67], %[c5]
ds_read_b128 a[88:91], v36 offset:26624
ds_read_b64_tr_b16 a[32:33], v42 offset:24576
v_mfma_f32_16x16x32_bf16 %[c13], a[20:23], a[68:71], %[c13]
ds_read_b64_tr_b16 a[34:35], v44 offset:24576
buffer_load_dwordx4 v33, s[40:43], s46 offen lds
v_mfma_f32_16x16x32_bf16 %[c21], a[20:23], a[72:75], %[c21]
v_mfma_f32_16x16x32_bf16 %[c29], a[20:23], a[76:79], %[c29]
buffer_load_dwordx4 v34, s[40:43], s46 offen offset:1024 lds
s_waitcnt lgkmcnt(4)
v_mfma_f32_16x16x32_bf16 %[c6], a[24:27], a[64:67], %[c6]
ds_read_b128 a[92:95], v36 offset:27648
ds_read_b64_tr_b16 a[36:37], v43 offset:24576
v_mfma_f32_16x16x32_bf16 %[c14], a[24:27], a[68:71], %[c14]
ds_read_b64_tr_b16 a[38:39], v45 offset:24576
v_mfma_f32_16x16x32_bf16 %[c22], a[24:27], a[72:75], %[c22]
s_add_u32 m0, s44, 40960
v_mfma_f32_16x16x32_bf16 %[c30], a[24:27], a[76:79], %[c30]
v_mfma_f32_16x16x32_bf16 %[c7], a[28:31], a[64:67], %[c7]
ds_read_b64_tr_b16 a[40:41], v42 offset:25088
ds_read_b64_tr_b16 a[42:43], v44 offset:25088
v_mfma_f32_16x16x32_bf16 %[c15], a[28:31], a[68:71], %[c15]
buffer_load_dwordx4 v33, s[40:43], s47 offen lds
v_mfma_f32_16x16x32_bf16 %[c23], a[28:31], a[72:75], %[c23]
v_mfma_f32_16x16x32_bf16 %[c31], a[28:31], a[76:79], %[c31]
buffer_load_dwordx4 v34, s[40:43], s47 offen offset:1024 lds
s_waitcnt lgkmcnt(2)
v_mfma_f32_16x16x32_bf16 %[c0], a[32:35], a[80:83], %[c0]
ds_read_b64_tr_b16 a[44:45], v43 offset:25088
ds_read_b64_tr_b16 a[46:47], v45 offset:25088
v_mfma_f32_16x16x32_bf16 %[c8], a[32:35], a[84:87], %[c8]
v_mfma_f32_16x16x32_bf16 %[c16], a[32:35], a[88:91], %[c16]
s_add_u32 s45, s45, %[xadv]
v_mfma_f32_16x16x32_bf16 %[c24], a[32:35], a[92:95], %[c24]
v_mfma_f32_16x16x32_bf16 %[c1], a[36:39], a[80:83], %[c1]
s_add_u32 s46, s46, %[wadv]
ds_read_b64_tr_b16 a[48:49], v42 offset:25600
ds_read_b64_tr_b16 a[50:51], v44 offset:25600
v_mfma_f32_16x16x32_bf16 %[c9], a[36:39], a[84:87], %[c9]
v_mfma_f32_16x16x32_bf16 %[c17], a[36:39], a[88:91], %[c17]
v_mfma_f32_16x16x32_bf16 %[c25], a[36:39], a[92:95], %[c25]
s_add_u32 s47, s47, %[wadv]
s_waitcnt lgkmcnt(2)
v_mfma_f32_16x16x32_bf16 %[c2], a[40:43], a[80:83], %[c2]
ds_read_b64_tr_b16 a[52:53], v43 offset:25600
ds_read_b64_tr_b16 a[54:55], v45 offset:25600
v_mfma_f32_16x16x32_bf16 %[c10], a[40:43], a[84:87], %[c10]
v_mfma_f32_16x16x32_bf16 %[c18], a[40:43], a[88:91], %[c18]
s_waitcnt vmcnt(18)
s_barrier
v_mfma_f32_16x16x32_bf16 %[c26], a[40:43], a[92:95], %[c26]
s_add_u32 m0, s44, 49152
v_mfma_f32_16x16x32_bf16 %[c3], a[44:47], a[80:83], %[c3]
ds_read_b64_tr_b16 a[56:57], v42 offset:26112
ds_read_b64_tr_b16 a[58:59], v44 offset:26112
v_mfma_f32_16x16x32_bf16 %[c11], a[44:47], a[84:87], %[c11]
ds_read_b128 a[64:67], v37
buffer_load_dwordx4 v32, s[36:39], s45 offen sc1 lds
v_mfma_f32_16x16x32_bf16 %[c19], a[44:47], a[88:91], %[c19]
v_mfma_f32_16x16x32_bf16 %[c27], a[44:47], a[92:95], %[c27]
s_waitcnt lgkmcnt(3)
v_mfma_f32_16x16x32_bf16 %[c4], a[48:51], a[80:83], %[c4]
ds_read_b64_tr_b16 a[60:61], v43 offset:26112
ds_read_b64_tr_b16 a[62:63], v45 offset:26112
buffer_load_dwordx4 v32, s[36:39], s45 offen offset:1024 sc1 lds
v_mfma_f32_16x16x32_bf16 %[c12], a[48:51], a[84:87], %[c12]
ds_read_b128 a[68:71], v37 offset:1024
v_mfma_f32_16x16x32_bf16 %[c20], a[48:51], a[88:91], %[c20]
s_add_u32 m0, s44, 57344
v_mfma_f32_16x16x32_bf16 %[c28], a[48:51], a[92:95], %[c28]
v_mfma_f32_16x16x32_bf16 %[c5], a[52:55], a[80:83], %[c5]
ds_read_b128 a[72:75], v37 offset:2048
ds_read_b64_tr_b16 a[0:1], v46
v_mfma_f32_16x16x32_bf16 %[c13], a[52:55], a[84:87], %[c13]
ds_read_b64_tr_b16 a[2:3], v48
buffer_load_dwordx4 v33, s[40:43], s46 offen lds
v_mfma_f32_16x16x32_bf16 %[c21], a[52:55], a[88:91], %[c21]
v_mfma_f32_16x16x32_bf16 %[c29], a[52:55], a[92:95], %[c29]
buffer_load_dwordx4 v34, s[40:43], s46 offen offset:1024 lds
s_waitcnt lgkmcnt(4)
v_mfma_f32_16x16x32_bf16 %[c6], a[56:59], a[80:83], %[c6]
ds_read_b128 a[76:79], v37 offset:3072
ds_read_b64_tr_b16 a[4:5], v47
v_mfma_f32_16x16x32_bf16 %[c14], a[56:59], a[84:87], %[c14]
)ASM"
R"ASM(ds_read_b64_tr_b16 a[6:7], v49
v_mfma_f32_16x16x32_bf16 %[c22], a[56:59], a[88:91], %[c22]
s_add_u32 m0, s44, 65536
v_mfma_f32_16x16x32_bf16 %[c30], a[56:59], a[92:95], %[c30]
v_mfma_f32_16x16x32_bf16 %[c7], a[60:63], a[80:83], %[c7]
ds_read_b64_tr_b16 a[8:9], v46 offset:512
ds_read_b64_tr_b16 a[10:11], v48 offset:512
v_mfma_f32_16x16x32_bf16 %[c15], a[60:63], a[84:87], %[c15]
buffer_load_dwordx4 v33, s[40:43], s47 offen lds
v_mfma_f32_16x16x32_bf16 %[c23], a[60:63], a[88:91], %[c23]
v_mfma_f32_16x16x32_bf16 %[c31], a[60:63], a[92:95], %[c31]
buffer_load_dwordx4 v34, s[40:43], s47 offen offset:1024 lds
s_waitcnt lgkmcnt(2)
v_mfma_f32_16x16x32_bf16 %[c0], a[0:3], a[64:67], %[c0]
ds_read_b64_tr_b16 a[12:13], v47 offset:512
ds_read_b64_tr_b16 a[14:15], v49 offset:512
v_mfma_f32_16x16x32_bf16 %[c8], a[0:3], a[68:71], %[c8]
v_mfma_f32_16x16x32_bf16 %[c16], a[0:3], a[72:75], %[c16]
s_add_u32 s45, s45, %[xadv]
v_mfma_f32_16x16x32_bf16 %[c24], a[0:3], a[76:79], %[c24]
v_mfma_f32_16x16x32_bf16 %[c1], a[4:7], a[64:67], %[c1]
s_add_u32 s46, s46, %[wadv]
ds_read_b64_tr_b16 a[16:17], v46 offset:1024
ds_read_b64_tr_b16 a[18:19], v48 offset:1024
v_mfma_f32_16x16x32_bf16 %[c9], a[4:7], a[68:71], %[c9]
v_mfma_f32_16x16x32_bf16 %[c17], a[4:7], a[72:75], %[c17]
v_mfma_f32_16x16x32_bf16 %[c25], a[4:7], a[76:79], %[c25]
s_add_u32 s47, s47, %[wadv]
s_waitcnt lgkmcnt(2)
v_mfma_f32_16x16x32_bf16 %[c2], a[8:11], a[64:67], %[c2]
ds_read_b64_tr_b16 a[20:21], v47 offset:1024
ds_read_b64_tr_b16 a[22:23], v49 offset:1024
v_mfma_f32_16x16x32_bf16 %[c10], a[8:11], a[68:71], %[c10]
v_mfma_f32_16x16x32_bf16 %[c18], a[8:11], a[72:75], %[c18]
s_waitcnt vmcnt(18)
s_barrier
v_mfma_f32_16x16x32_bf16 %[c26], a[8:11], a[76:79], %[c26]
s_add_u32 m0, s44, 73728
v_mfma_f32_16x16x32_bf16 %[c3], a[12:15], a[64:67], %[c3]
ds_read_b64_tr_b16 a[24:25], v46 offset:1536
ds_read_b64_tr_b16 a[26:27], v48 offset:1536
v_mfma_f32_16x16x32_bf16 %[c11], a[12:15], a[68:71], %[c11]
ds_read_b128 a[80:83], v37 offset:24576
buffer_load_dwordx4 v32, s[36:39], s45 offen sc1 lds
v_mfma_f32_16x16x32_bf16 %[c19], a[12:15], a[72:75], %[c19]
v_mfma_f32_16x16x32_bf16 %[c27], a[12:15], a[76:79], %[c27]
s_waitcnt lgkmcnt(3)
v_mfma_f32_16x16x32_bf16 %[c4], a[16:19], a[64:67], %[c4]
ds_read_b64_tr_b16 a[28:29], v47 offset:1536
ds_read_b64_tr_b16 a[30:31], v49 offset:1536
buffer_load_dwordx4 v32, s[36:39], s45 offen offset:1024 sc1 lds
v_mfma_f32_16x16x32_bf16 %[c12], a[16:19], a[68:71], %[c12]
ds_read_b128 a[84:87], v37 offset:25600
v_mfma_f32_16x16x32_bf16 %[c20], a[16:19], a[72:75], %[c20]
s_add_u32 m0, s44, 81920
v_mfma_f32_16x16x32_bf16 %[c28], a[16:19], a[76:79], %[c28]
v_mfma_f32_16x16x32_bf16 %[c5], a[20:23], a[64:67], %[c5]
ds_read_b128 a[88:91], v37 offset:26624
ds_read_b64_tr_b16 a[32:33], v46 offset:24576
v_mfma_f32_16x16x32_bf16 %[c13], a[20:23], a[68:71], %[c13]
ds_read_b64_tr_b16 a[34:35], v48 offset:24576
buffer_load_dwordx4 v33, s[40:43], s46 offen lds
v_mfma_f32_16x16x32_bf16 %[c21], a[20:23], a[72:75], %[c21]
v_mfma_f32_16x16x32_bf16 %[c29], a[20:23], a[76:79], %[c29]
buffer_load_dwordx4 v34, s[40:43], s46 offen offset:1024 lds
s_waitcnt lgkmcnt(4)
v_mfma_f32_16x16x32_bf16 %[c6], a[24:27], a[64:67], %[c6]
ds_read_b128 a[92:95], v37 offset:27648
ds_read_b64_tr_b16 a[36:37], v47 offset:24576
v_mfma_f32_16x16x32_bf16 %[c14], a[24:27], a[68:71], %[c14]
ds_read_b64_tr_b16 a[38:39], v49 offset:24576
v_mfma_f32_16x16x32_bf16 %[c22], a[24:27], a[72:75], %[c22]
s_add_u32 m0, s44, 90112
v_mfma_f32_16x16x32_bf16 %[c30], a[24:27], a[76:79], %[c30]
v_mfma_f32_16x16x32_bf16 %[c7], a[28:31], a[64:67], %[c7]
ds_read_b64_tr_b16 a[40:41], v46 offset:25088
ds_read_b64_tr_b16 a[42:43], v48 offset:25088
v_mfma_f32_16x16x32_bf16 %[c15], a[28:31], a[68:71], %[c15]
buffer_load_dwordx4 v33, s[40:43], s47 offen lds
v_mfma_f32_16x16x32_bf16 %[c23], a[28:31], a[72:75], %[c23]
v_mfma_f32_16x16x32_bf16 %[c31], a[28:31], a[76:79], %[c31]
buffer_load_dwordx4 v34, s[40:43], s47 offen offset:1024 lds
s_waitcnt lgkmcnt(2)
v_mfma_f32_16x16x32_bf16 %[c0], a[32:35], a[80:83], %[c0]
ds_read_b64_tr_b16 a[44:45], v47 offset:25088
ds_read_b64_tr_b16 a[46:47], v49 offset:25088
v_mfma_f32_16x16x32_bf16 %[c8], a[32:35], a[84:87], %[c8]
v_mfma_f32_16x16x32_bf16 %[c16], a[32:35], a[88:91], %[c16]
s_add_u32 s45, s45, %[xadv]
v_mfma_f32_16x16x32_bf16 %[c24], a[32:35], a[92:95], %[c24]
v_mfma_f32_16x16x32_bf16 %[c1], a[36:39], a[80:83], %[c1]
s_add_u32 s46, s46, %[wadv]
ds_read_b64_tr_b16 a[48:49], v46 offset:25600
ds_read_b64_tr_b16 a[50:51], v48 offset:25600
v_mfma_f32_16x16x32_bf16 %[c9], a[36:39], a[84:87], %[c9]
v_mfma_f32_16x16x32_bf16 %[c17], a[36:39], a[88:91], %[c17]
v_mfma_f32_16x16x32_bf16 %[c25], a[36:39], a[92:95], %[c25]
s_add_u32 s47, s47, %[wadv]
s_waitcnt lgkmcnt(2)
v_mfma_f32_16x16x32_bf16 %[c2], a[40:43], a[80:83], %[c2]
ds_read_b64_tr_b16 a[52:53], v47 offset:25600
ds_read_b64_tr_b16 a[54:55], v49 offset:25600
v_mfma_f32_16x16x32_bf16 %[c10], a[40:43], a[84:87], %[c10]
v_mfma_f32_16x16x32_bf16 %[c18], a[40:43], a[88:91], %[c18]
s_waitcnt vmcnt(18)
s_barrier
v_mfma_f32_16x16x32_bf16 %[c26], a[40:43], a[92:95], %[c26]
s_add_u32 m0, s44, 98304
v_mfma_f32_16x16x32_bf16 %[c3], a[44:47], a[80:83], %[c3]
ds_read_b64_tr_b16 a[56:57], v46 offset:26112
ds_read_b64_tr_b16 a[58:59], v48 offset:26112
v_mfma_f32_16x16x32_bf16 %[c11], a[44:47], a[84:87], %[c11]
ds_read_b128 a[64:67], v35
buffer_load_dwordx4 v32, s[36:39], s45 offen sc1 lds
v_mfma_f32_16x16x32_bf16 %[c19], a[44:47], a[88:91], %[c19]
v_mfma_f32_16x16x32_bf16 %[c27], a[44:47], a[92:95], %[c27]
s_waitcnt lgkmcnt(3)
v_mfma_f32_16x16x32_bf16 %[c4], a[48:51], a[80:83], %[c4]
ds_read_b64_tr_b16 a[60:61], v47 offset:26112
ds_read_b64_tr_b16 a[62:63], v49 offset:26112
buffer_load_dwordx4 v32, s[36:39], s45 offen offset:1024 sc1 lds
v_mfma_f32_16x16x32_bf16 %[c12], a[48:51], a[84:87], %[c12]
ds_read_b128 a[68:71], v35 offset:1024
v_mfma_f32_16x16x32_bf16 %[c20], a[48:51], a[88:91], %[c20]
s_add_u32 m0, s44, 106496
v_mfma_f32_16x16x32_bf16 %[c28], a[48:51], a[92:95], %[c28]
v_mfma_f32_16x16x32_bf16 %[c5], a[52:55], a[80:83], %[c5]
ds_read_b128 a[72:75], v35 offset:2048
ds_read_b64_tr_b16 a[0:1], v38
v_mfma_f32_16x16x32_bf16 %[c13], a[52:55], a[84:87], %[c13]
ds_read_b64_tr_b16 a[2:3], v40
buffer_load_dwordx4 v33, s[40:43], s46 offen lds
v_mfma_f32_16x16x32_bf16 %[c21], a[52:55], a[88:91], %[c21]
v_mfma_f32_16x16x32_bf16 %[c29], a[52:55], a[92:95], %[c29]
buffer_load_dwordx4 v34, s[40:43], s46 offen offset:1024 lds
s_waitcnt lgkmcnt(4)
v_mfma_f32_16x16x32_bf16 %[c6], a[56:59], a[80:83], %[c6]
ds_read_b128 a[76:79], v35 offset:3072
ds_read_b64_tr_b16 a[4:5], v39
v_mfma_f32_16x16x32_bf16 %[c14], a[56:59], a[84:87], %[c14]
ds_read_b64_tr_b16 a[6:7], v41
v_mfma_f32_16x16x32_bf16 %[c22], a[56:59], a[88:91], %[c22]
s_add_u32 m0, s44, 114688
v_mfma_f32_16x16x32_bf16 %[c30], a[56:59], a[92:95], %[c30]
v_mfma_f32_16x16x32_bf16 %[c7], a[60:63], a[80:83], %[c7]
ds_read_b64_tr_b16 a[8:9], v38 offset:512
ds_read_b64_tr_b16 a[10:11], v40 offset:512
v_mfma_f32_16x16x32_bf16 %[c15], a[60:63], a[84:87], %[c15]
buffer_load_dwordx4 v33, s[40:43], s47 offen lds
v_mfma_f32_16x16x32_bf16 %[c23], a[60:63], a[88:91], %[c23]
v_mfma_f32_16x16x32_bf16 %[c31], a[60:63], a[92:95], %[c31]
buffer_load_dwordx4 v34, s[40:43], s47 offen offset:1024 lds
s_waitcnt lgkmcnt(2)
v_mfma_f32_16x16x32_bf16 %[c0], a[0:3], a[64:67], %[c0]
ds_read_b64_tr_b16 a[12:13], v39 offset:512
ds_read_b64_tr_b16 a[14:15], v41 offset:512
v_mfma_f32_16x16x32_bf16 %[c8], a[0:3], a[68:71], %[c8]
v_mfma_f32_16x16x32_bf16 %[c16], a[0:3], a[72:75], %[c16]
s_add_u32 s45, s45, %[xadv]
v_mfma_f32_16x16x32_bf16 %[c24], a[0:3], a[76:79], %[c24]
v_mfma_f32_16x16x32_bf16 %[c1], a[4:7], a[64:67], %[c1]
s_add_u32 s46, s46, %[wadv]
ds_read_b64_tr_b16 a[16:17], v38 offset:1024
ds_read_b64_tr_b16 a[18:19], v40 offset:1024
v_mfma_f32_16x16x32_bf16 %[c9], a[4:7], a[68:71], %[c9]
v_mfma_f32_16x16x32_bf16 %[c17], a[4:7], a[72:75], %[c17]
v_mfma_f32_16x16x32_bf16 %[c25], a[4:7], a[76:79], %[c25]
s_add_u32 s47, s47, %[wadv]
s_waitcnt lgkmcnt(2)
v_mfma_f32_16x16x32_bf16 %[c2], a[8:11], a[64:67], %[c2]
ds_read_b64_tr_b16 a[20:21], v39 offset:1024
ds_read_b64_tr_b16 a[22:23], v41 offset:1024
v_mfma_f32_16x16x32_bf16 %[c10], a[8:11], a[68:71], %[c10]
v_mfma_f32_16x16x32_bf16 %[c18], a[8:11], a[72:75], %[c18]
s_waitcnt vmcnt(18)
s_barrier
v_mfma_f32_16x16x32_bf16 %[c26], a[8:11], a[76:79], %[c26]
s_add_u32 m0, s44, 122880
v_mfma_f32_16x16x32_bf16 %[c3], a[12:15], a[64:67], %[c3]
ds_read_b64_tr_b16 a[24:25], v38 offset:1536
ds_read_b64_tr_b16 a[26:27], v40 offset:1536
v_mfma_f32_16x16x32_bf16 %[c11], a[12:15], a[68:71], %[c11]
ds_read_b128 a[80:83], v35 offset:24576
buffer_load_dwordx4 v32, s[36:39], s45 offen sc1 lds
v_mfma_f32_16x16x32_bf16 %[c19], a[12:15], a[72:75], %[c19]
v_mfma_f32_16x16x32_bf16 %[c27], a[12:15], a[76:79], %[c27]
s_waitcnt lgkmcnt(3)
v_mfma_f32_16x16x32_bf16 %[c4], a[16:19], a[64:67], %[c4]
ds_read_b64_tr_b16 a[28:29], v39 offset:1536
ds_read_b64_tr_b16 a[30:31], v41 offset:1536
buffer_load_dwordx4 v32, s[36:39], s45 offen offset:1024 sc1 lds
v_mfma_f32_16x16x32_bf16 %[c12], a[16:19], a[68:71], %[c12]
ds_read_b128 a[84:87], v35 offset:25600
v_mfma_f32_16x16x32_bf16 %[c20], a[16:19], a[72:75], %[c20]
s_add_u32 m0, s44, 131072
v_mfma_f32_16x16x32_bf16 %[c28], a[16:19], a[76:79], %[c28]
v_mfma_f32_16x16x32_bf16 %[c5], a[20:23], a[64:67], %[c5]
ds_read_b128 a[88:91], v35 offset:26624
ds_read_b64_tr_b16 a[32:33], v38 offset:24576
v_mfma_f32_16x16x32_bf16 %[c13], a[20:23], a[68:71], %[c13]
ds_read_b64_tr_b16 a[34:35], v40 offset:24576
buffer_load_dwordx4 v33, s[40:43], s46 offen lds
v_mfma_f32_16x16x32_bf16 %[c21], a[20:23], a[72:75], %[c21]
v_mfma_f32_16x16x32_bf16 %[c29], a[20:23], a[76:79], %[c29]
buffer_load_dwordx4 v34, s[40:43], s46 offen offset:1024 lds
s_waitcnt lgkmcnt(4)
v_mfma_f32_16x16x32_bf16 %[c6], a[24:27], a[64:67], %[c6]
ds_read_b128 a[92:95], v35 offset:27648
ds_read_b64_tr_b16 a[36:37], v39 offset:24576
v_mfma_f32_16x16x32_bf16 %[c14], a[24:27], a[68:71], %[c14]
ds_read_b64_tr_b16 a[38:39], v41 offset:24576
v_mfma_f32_16x16x32_bf16 %[c22], a[24:27], a[72:75], %[c22]
s_add_u32 m0, s44, 139264
v_mfma_f32_16x16x32_bf16 %[c30], a[24:27], a[76:79], %[c30]
v_mfma_f32_16x16x32_bf16 %[c7], a[28:31], a[64:67], %[c7]
ds_read_b64_tr_b16 a[40:41], v38 offset:25088
ds_read_b64_tr_b16 a[42:43], v40 offset:25088
v_mfma_f32_16x16x32_bf16 %[c15], a[28:31], a[68:71], %[c15]
buffer_load_dwordx4 v33, s[40:43], s47 offen lds
v_mfma_f32_16x16x32_bf16 %[c23], a[28:31], a[72:75], %[c23]
v_mfma_f32_16x16x32_bf16 %[c31], a[28:31], a[76:79], %[c31]
buffer_load_dwordx4 v34, s[40:43], s47 offen offset:1024 lds
s_waitcnt lgkmcnt(2)
v_mfma_f32_16x16x32_bf16 %[c0], a[32:35], a[80:83], %[c0]
ds_read_b64_tr_b16 a[44:45], v39 offset:25088
ds_read_b64_tr_b16 a[46:47], v41 offset:25088
v_mfma_f32_16x16x32_bf16 %[c8], a[32:35], a[84:87], %[c8]
v_mfma_f32_16x16x32_bf16 %[c16], a[32:35], a[88:91], %[c16]
s_add_u32 s45, s45, %[xadv]
v_mfma_f32_16x16x32_bf16 %[c24], a[32:35], a[92:95], %[c24]
v_mfma_f32_16x16x32_bf16 %[c1], a[36:39], a[80:83], %[c1]
s_add_u32 s46, s46, %[wadv]
ds_read_b64_tr_b16 a[48:49], v38 offset:25600
ds_read_b64_tr_b16 a[50:51], v40 offset:25600
v_mfma_f32_16x16x32_bf16 %[c9], a[36:39], a[84:87], %[c9]
v_mfma_f32_16x16x32_bf16 %[c17], a[36:39], a[88:91], %[c17]
v_mfma_f32_16x16x32_bf16 %[c25], a[36:39], a[92:95], %[c25]
s_add_u32 s47, s47, %[wadv]
s_waitcnt lgkmcnt(2)
v_mfma_f32_16x16x32_bf16 %[c2], a[40:43], a[80:83], %[c2]
ds_read_b64_tr_b16 a[52:53], v39 offset:25600
ds_read_b64_tr_b16 a[54:55], v41 offset:25600
v_mfma_f32_16x16x32_bf16 %[c10], a[40:43], a[84:87], %[c10]
v_mfma_f32_16x16x32_bf16 %[c18], a[40:43], a[88:91], %[c18]
s_waitcnt vmcnt(18)
s_barrier
v_mfma_f32_16x16x32_bf16 %[c26], a[40:43], a[92:95], %[c26]
s_add_u32 m0, s44, 0
v_mfma_f32_16x16x32_bf16 %[c3], a[44:47], a[80:83], %[c3]
ds_read_b64_tr_b16 a[56:57], v38 offset:26112
ds_read_b64_tr_b16 a[58:59], v40 offset:26112
)ASM"
R"ASM(v_mfma_f32_16x16x32_bf16 %[c11], a[44:47], a[84:87], %[c11]
ds_read_b128 a[64:67], v36
buffer_load_dwordx4 v32, s[36:39], s45 offen sc1 lds
v_mfma_f32_16x16x32_bf16 %[c19], a[44:47], a[88:91], %[c19]
v_mfma_f32_16x16x32_bf16 %[c27], a[44:47], a[92:95], %[c27]
s_waitcnt lgkmcnt(3)
v_mfma_f32_16x16x32_bf16 %[c4], a[48:51], a[80:83], %[c4]
ds_read_b64_tr_b16 a[60:61], v39 offset:26112
ds_read_b64_tr_b16 a[62:63], v41 offset:26112
buffer_load_dwordx4 v32, s[36:39], s45 offen offset:1024 sc1 lds
v_mfma_f32_16x16x32_bf16 %[c12], a[48:51], a[84:87], %[c12]
ds_read_b128 a[68:71], v36 offset:1024
v_mfma_f32_16x16x32_bf16 %[c20], a[48:51], a[88:91], %[c20]
s_add_u32 m0, s44, 8192
v_mfma_f32_16x16x32_bf16 %[c28], a[48:51], a[92:95], %[c28]
v_mfma_f32_16x16x32_bf16 %[c5], a[52:55], a[80:83], %[c5]
ds_read_b128 a[72:75], v36 offset:2048
ds_read_b64_tr_b16 a[0:1], v42
v_mfma_f32_16x16x32_bf16 %[c13], a[52:55], a[84:87], %[c13]
ds_read_b64_tr_b16 a[2:3], v44
buffer_load_dwordx4 v33, s[40:43], s46 offen lds
v_mfma_f32_16x16x32_bf16 %[c21], a[52:55], a[88:91], %[c21]
v_mfma_f32_16x16x32_bf16 %[c29], a[52:55], a[92:95], %[c29]
buffer_load_dwordx4 v34, s[40:43], s46 offen offset:1024 lds
s_waitcnt lgkmcnt(4)
v_mfma_f32_16x16x32_bf16 %[c6], a[56:59], a[80:83], %[c6]
ds_read_b128 a[76:79], v36 offset:3072
ds_read_b64_tr_b16 a[4:5], v43
v_mfma_f32_16x16x32_bf16 %[c14], a[56:59], a[84:87], %[c14]
ds_read_b64_tr_b16 a[6:7], v45
v_mfma_f32_16x16x32_bf16 %[c22], a[56:59], a[88:91], %[c22]
s_add_u32 m0, s44, 16384
v_mfma_f32_16x16x32_bf16 %[c30], a[56:59], a[92:95], %[c30]
v_mfma_f32_16x16x32_bf16 %[c7], a[60:63], a[80:83], %[c7]
ds_read_b64_tr_b16 a[8:9], v42 offset:512
ds_read_b64_tr_b16 a[10:11], v44 offset:512
v_mfma_f32_16x16x32_bf16 %[c15], a[60:63], a[84:87], %[c15]
buffer_load_dwordx4 v33, s[40:43], s47 offen lds
v_mfma_f32_16x16x32_bf16 %[c23], a[60:63], a[88:91], %[c23]
v_mfma_f32_16x16x32_bf16 %[c31], a[60:63], a[92:95], %[c31]
buffer_load_dwordx4 v34, s[40:43], s47 offen offset:1024 lds
s_waitcnt lgkmcnt(2)
v_mfma_f32_16x16x32_bf16 %[c0], a[0:3], a[64:67], %[c0]
ds_read_b64_tr_b16 a[12:13], v43 offset:512
ds_read_b64_tr_b16 a[14:15], v45 offset:512
v_mfma_f32_16x16x32_bf16 %[c8], a[0:3], a[68:71], %[c8]
v_mfma_f32_16x16x32_bf16 %[c16], a[0:3], a[72:75], %[c16]
s_add_u32 s45, s45, %[xadv]
v_mfma_f32_16x16x32_bf16 %[c24], a[0:3], a[76:79], %[c24]
v_mfma_f32_16x16x32_bf16 %[c1], a[4:7], a[64:67], %[c1]
s_add_u32 s46, s46, %[wadv]
ds_read_b64_tr_b16 a[16:17], v42 offset:1024
ds_read_b64_tr_b16 a[18:19], v44 offset:1024
v_mfma_f32_16x16x32_bf16 %[c9], a[4:7], a[68:71], %[c9]
v_mfma_f32_16x16x32_bf16 %[c17], a[4:7], a[72:75], %[c17]
v_mfma_f32_16x16x32_bf16 %[c25], a[4:7], a[76:79], %[c25]
s_add_u32 s47, s47, %[wadv]
s_waitcnt lgkmcnt(2)
v_mfma_f32_16x16x32_bf16 %[c2], a[8:11], a[64:67], %[c2]
ds_read_b64_tr_b16 a[20:21], v43 offset:1024
ds_read_b64_tr_b16 a[22:23], v45 offset:1024
v_mfma_f32_16x16x32_bf16 %[c10], a[8:11], a[68:71], %[c10]
v_mfma_f32_16x16x32_bf16 %[c18], a[8:11], a[72:75], %[c18]
s_waitcnt vmcnt(18)
s_barrier
v_mfma_f32_16x16x32_bf16 %[c26], a[8:11], a[76:79], %[c26]
s_add_u32 m0, s44, 24576
v_mfma_f32_16x16x32_bf16 %[c3], a[12:15], a[64:67], %[c3]
ds_read_b64_tr_b16 a[24:25], v42 offset:1536
ds_read_b64_tr_b16 a[26:27], v44 offset:1536
v_mfma_f32_16x16x32_bf16 %[c11], a[12:15], a[68:71], %[c11]
ds_read_b128 a[80:83], v36 offset:24576
buffer_load_dwordx4 v32, s[36:39], s45 offen sc1 lds
v_mfma_f32_16x16x32_bf16 %[c19], a[12:15], a[72:75], %[c19]
v_mfma_f32_16x16x32_bf16 %[c27], a[12:15], a[76:79], %[c27]
s_waitcnt lgkmcnt(3)
v_mfma_f32_16x16x32_bf16 %[c4], a[16:19], a[64:67], %[c4]
ds_read_b64_tr_b16 a[28:29], v43 offset:1536
ds_read_b64_tr_b16 a[30:31], v45 offset:1536
buffer_load_dwordx4 v32, s[36:39], s45 offen offset:1024 sc1 lds
v_mfma_f32_16x16x32_bf16 %[c12], a[16:19], a[68:71], %[c12]
ds_read_b128 a[84:87], v36 offset:25600
v_mfma_f32_16x16x32_bf16 %[c20], a[16:19], a[72:75], %[c20]
s_add_u32 m0, s44, 32768
v_mfma_f32_16x16x32_bf16 %[c28], a[16:19], a[76:79], %[c28]
v_mfma_f32_16x16x32_bf16 %[c5], a[20:23], a[64:67], %[c5]
ds_read_b128 a[88:91], v36 offset:26624
ds_read_b64_tr_b16 a[32:33], v42 offset:24576
v_mfma_f32_16x16x32_bf16 %[c13], a[20:23], a[68:71], %[c13]
ds_read_b64_tr_b16 a[34:35], v44 offset:24576
buffer_load_dwordx4 v33, s[40:43], s46 offen lds
v_mfma_f32_16x16x32_bf16 %[c21], a[20:23], a[72:75], %[c21]
v_mfma_f32_16x16x32_bf16 %[c29], a[20:23], a[76:79], %[c29]
buffer_load_dwordx4 v34, s[40:43], s46 offen offset:1024 lds
s_waitcnt lgkmcnt(4)
v_mfma_f32_16x16x32_bf16 %[c6], a[24:27], a[64:67], %[c6]
ds_read_b128 a[92:95], v36 offset:27648
ds_read_b64_tr_b16 a[36:37], v43 offset:24576
v_mfma_f32_16x16x32_bf16 %[c14], a[24:27], a[68:71], %[c14]
ds_read_b64_tr_b16 a[38:39], v45 offset:24576
v_mfma_f32_16x16x32_bf16 %[c22], a[24:27], a[72:75], %[c22]
s_add_u32 m0, s44, 40960
v_mfma_f32_16x16x32_bf16 %[c30], a[24:27], a[76:79], %[c30]
v_mfma_f32_16x16x32_bf16 %[c7], a[28:31], a[64:67], %[c7]
ds_read_b64_tr_b16 a[40:41], v42 offset:25088
ds_read_b64_tr_b16 a[42:43], v44 offset:25088
v_mfma_f32_16x16x32_bf16 %[c15], a[28:31], a[68:71], %[c15]
buffer_load_dwordx4 v33, s[40:43], s47 offen lds
v_mfma_f32_16x16x32_bf16 %[c23], a[28:31], a[72:75], %[c23]
v_mfma_f32_16x16x32_bf16 %[c31], a[28:31], a[76:79], %[c31]
buffer_load_dwordx4 v34, s[40:43], s47 offen offset:1024 lds
s_waitcnt lgkmcnt(2)
v_mfma_f32_16x16x32_bf16 %[c0], a[32:35], a[80:83], %[c0]
ds_read_b64_tr_b16 a[44:45], v43 offset:25088
ds_read_b64_tr_b16 a[46:47], v45 offset:25088
v_mfma_f32_16x16x32_bf16 %[c8], a[32:35], a[84:87], %[c8]
v_mfma_f32_16x16x32_bf16 %[c16], a[32:35], a[88:91], %[c16]
s_add_u32 s45, s45, %[xadv]
v_mfma_f32_16x16x32_bf16 %[c24], a[32:35], a[92:95], %[c24]
v_mfma_f32_16x16x32_bf16 %[c1], a[36:39], a[80:83], %[c1]
s_add_u32 s46, s46, %[wadv]
ds_read_b64_tr_b16 a[48:49], v42 offset:25600
ds_read_b64_tr_b16 a[50:51], v44 offset:25600
v_mfma_f32_16x16x32_bf16 %[c9], a[36:39], a[84:87], %[c9]
v_mfma_f32_16x16x32_bf16 %[c17], a[36:39], a[88:91], %[c17]
v_mfma_f32_16x16x32_bf16 %[c25], a[36:39], a[92:95], %[c25]
s_add_u32 s47, s47, %[wadv]
s_waitcnt lgkmcnt(2)
v_mfma_f32_16x16x32_bf16 %[c2], a[40:43], a[80:83], %[c2]
ds_read_b64_tr_b16 a[52:53], v43 offset:25600
ds_read_b64_tr_b16 a[54:55], v45 offset:25600
v_mfma_f32_16x16x32_bf16 %[c10], a[40:43], a[84:87], %[c10]
v_mfma_f32_16x16x32_bf16 %[c18], a[40:43], a[88:91], %[c18]
s_waitcnt vmcnt(18)
s_barrier
v_mfma_f32_16x16x32_bf16 %[c26], a[40:43], a[92:95], %[c26]
v_mfma_f32_16x16x32_bf16 %[c3], a[44:47], a[80:83], %[c3]
ds_read_b64_tr_b16 a[56:57], v42 offset:26112
ds_read_b64_tr_b16 a[58:59], v44 offset:26112
v_mfma_f32_16x16x32_bf16 %[c11], a[44:47], a[84:87], %[c11]
ds_read_b128 a[64:67], v37
v_mfma_f32_16x16x32_bf16 %[c19], a[44:47], a[88:91], %[c19]
v_mfma_f32_16x16x32_bf16 %[c27], a[44:47], a[92:95], %[c27]
s_waitcnt lgkmcnt(3)
v_mfma_f32_16x16x32_bf16 %[c4], a[48:51], a[80:83], %[c4]
ds_read_b64_tr_b16 a[60:61], v43 offset:26112
ds_read_b64_tr_b16 a[62:63], v45 offset:26112
v_mfma_f32_16x16x32_bf16 %[c12], a[48:51], a[84:87], %[c12]
ds_read_b128 a[68:71], v37 offset:1024
v_mfma_f32_16x16x32_bf16 %[c20], a[48:51], a[88:91], %[c20]
v_mfma_f32_16x16x32_bf16 %[c28], a[48:51], a[92:95], %[c28]
v_mfma_f32_16x16x32_bf16 %[c5], a[52:55], a[80:83], %[c5]
ds_read_b128 a[72:75], v37 offset:2048
ds_read_b64_tr_b16 a[0:1], v46
v_mfma_f32_16x16x32_bf16 %[c13], a[52:55], a[84:87], %[c13]
ds_read_b64_tr_b16 a[2:3], v48
v_mfma_f32_16x16x32_bf16 %[c21], a[52:55], a[88:91], %[c21]
v_mfma_f32_16x16x32_bf16 %[c29], a[52:55], a[92:95], %[c29]
s_waitcnt lgkmcnt(4)
v_mfma_f32_16x16x32_bf16 %[c6], a[56:59], a[80:83], %[c6]
ds_read_b128 a[76:79], v37 offset:3072
ds_read_b64_tr_b16 a[4:5], v47
v_mfma_f32_16x16x32_bf16 %[c14], a[56:59], a[84:87], %[c14]
ds_read_b64_tr_b16 a[6:7], v49
v_mfma_f32_16x16x32_bf16 %[c22], a[56:59], a[88:91], %[c22]
v_mfma_f32_16x16x32_bf16 %[c30], a[56:59], a[92:95], %[c30]
v_mfma_f32_16x16x32_bf16 %[c7], a[60:63], a[80:83], %[c7]
ds_read_b64_tr_b16 a[8:9], v46 offset:512
ds_read_b64_tr_b16 a[10:11], v48 offset:512
v_mfma_f32_16x16x32_bf16 %[c15], a[60:63], a[84:87], %[c15]
v_mfma_f32_16x16x32_bf16 %[c23], a[60:63], a[88:91], %[c23]
v_mfma_f32_16x16x32_bf16 %[c31], a[60:63], a[92:95], %[c31]
s_waitcnt lgkmcnt(2)
v_mfma_f32_16x16x32_bf16 %[c0], a[0:3], a[64:67], %[c0]
ds_read_b64_tr_b16 a[12:13], v47 offset:512
ds_read_b64_tr_b16 a[14:15], v49 offset:512
v_mfma_f32_16x16x32_bf16 %[c8], a[0:3], a[68:71], %[c8]
v_mfma_f32_16x16x32_bf16 %[c16], a[0:3], a[72:75], %[c16]
v_mfma_f32_16x16x32_bf16 %[c24], a[0:3], a[76:79], %[c24]
v_mfma_f32_16x16x32_bf16 %[c1], a[4:7], a[64:67], %[c1]
ds_read_b64_tr_b16 a[16:17], v46 offset:1024
ds_read_b64_tr_b16 a[18:19], v48 offset:1024
v_mfma_f32_16x16x32_bf16 %[c9], a[4:7], a[68:71], %[c9]
v_mfma_f32_16x16x32_bf16 %[c17], a[4:7], a[72:75], %[c17]
v_mfma_f32_16x16x32_bf16 %[c25], a[4:7], a[76:79], %[c25]
s_waitcnt lgkmcnt(2)
v_mfma_f32_16x16x32_bf16 %[c2], a[8:11], a[64:67], %[c2]
ds_read_b64_tr_b16 a[20:21], v47 offset:1024
ds_read_b64_tr_b16 a[22:23], v49 offset:1024
v_mfma_f32_16x16x32_bf16 %[c10], a[8:11], a[68:71], %[c10]
v_mfma_f32_16x16x32_bf16 %[c18], a[8:11], a[72:75], %[c18]
s_waitcnt vmcnt(12)
s_barrier
v_mfma_f32_16x16x32_bf16 %[c26], a[8:11], a[76:79], %[c26]
v_mfma_f32_16x16x32_bf16 %[c3], a[12:15], a[64:67], %[c3]
ds_read_b64_tr_b16 a[24:25], v46 offset:1536
ds_read_b64_tr_b16 a[26:27], v48 offset:1536
v_mfma_f32_16x16x32_bf16 %[c11], a[12:15], a[68:71], %[c11]
ds_read_b128 a[80:83], v37 offset:24576
v_mfma_f32_16x16x32_bf16 %[c19], a[12:15], a[72:75], %[c19]
v_mfma_f32_16x16x32_bf16 %[c27], a[12:15], a[76:79], %[c27]
s_waitcnt lgkmcnt(3)
v_mfma_f32_16x16x32_bf16 %[c4], a[16:19], a[64:67], %[c4]
ds_read_b64_tr_b16 a[28:29], v47 offset:1536
ds_read_b64_tr_b16 a[30:31], v49 offset:1536
v_mfma_f32_16x16x32_bf16 %[c12], a[16:19], a[68:71], %[c12]
ds_read_b128 a[84:87], v37 offset:25600
v_mfma_f32_16x16x32_bf16 %[c20], a[16:19], a[72:75], %[c20]
v_mfma_f32_16x16x32_bf16 %[c28], a[16:19], a[76:79], %[c28]
v_mfma_f32_16x16x32_bf16 %[c5], a[20:23], a[64:67], %[c5]
ds_read_b128 a[88:91], v37 offset:26624
ds_read_b64_tr_b16 a[32:33], v46 offset:24576
v_mfma_f32_16x16x32_bf16 %[c13], a[20:23], a[68:71], %[c13]
ds_read_b64_tr_b16 a[34:35], v48 offset:24576
v_mfma_f32_16x16x32_bf16 %[c21], a[20:23], a[72:75], %[c21]
v_mfma_f32_16x16x32_bf16 %[c29], a[20:23], a[76:79], %[c29]
s_waitcnt lgkmcnt(4)
v_mfma_f32_16x16x32_bf16 %[c6], a[24:27], a[64:67], %[c6]
ds_read_b128 a[92:95], v37 offset:27648
ds_read_b64_tr_b16 a[36:37], v47 offset:24576
v_mfma_f32_16x16x32_bf16 %[c14], a[24:27], a[68:71], %[c14]
ds_read_b64_tr_b16 a[38:39], v49 offset:24576
v_mfma_f32_16x16x32_bf16 %[c22], a[24:27], a[72:75], %[c22]
v_mfma_f32_16x16x32_bf16 %[c30], a[24:27], a[76:79], %[c30]
v_mfma_f32_16x16x32_bf16 %[c7], a[28:31], a[64:67], %[c7]
ds_read_b64_tr_b16 a[40:41], v46 offset:25088
ds_read_b64_tr_b16 a[42:43], v48 offset:25088
v_mfma_f32_16x16x32_bf16 %[c15], a[28:31], a[68:71], %[c15]
v_mfma_f32_16x16x32_bf16 %[c23], a[28:31], a[72:75], %[c23]
v_mfma_f32_16x16x32_bf16 %[c31], a[28:31], a[76:79], %[c31]
s_waitcnt lgkmcnt(2)
v_mfma_f32_16x16x32_bf16 %[c0], a[32:35], a[80:83], %[c0]
ds_read_b64_tr_b16 a[44:45], v47 offset:25088
ds_read_b64_tr_b16 a[46:47], v49 offset:25088
v_mfma_f32_16x16x32_bf16 %[c8], a[32:35], a[84:87], %[c8]
v_mfma_f32_16x16x32_bf16 %[c16], a[32:35], a[88:91], %[c16]
v_mfma_f32_16x16x32_bf16 %[c24], a[32:35], a[92:95], %[c24]
v_mfma_f32_16x16x32_bf16 %[c1], a[36:39], a[80:83], %[c1]
ds_read_b64_tr_b16 a[48:49], v46 offset:25600
ds_read_b64_tr_b16 a[50:51], v48 offset:25600
v_mfma_f32_16x16x32_bf16 %[c9], a[36:39], a[84:87], %[c9]
v_mfma_f32_16x16x32_bf16 %[c17], a[36:39], a[88:91], %[c17]
v_mfma_f32_16x16x32_bf16 %[c25], a[36:39], a[92:95], %[c25]
s_waitcnt lgkmcnt(2)
v_mfma_f32_16x16x32_bf16 %[c2], a[40:43], a[80:83], %[c2]
ds_read_b64_tr_b16 a[52:53], v47 offset:25600
ds_read_b64_tr_b16 a[54:55], v49 offset:25600
)ASM"
R"ASM(v_mfma_f32_16x16x32_bf16 %[c10], a[40:43], a[84:87], %[c10]
v_mfma_f32_16x16x32_bf16 %[c18], a[40:43], a[88:91], %[c18]
s_waitcnt vmcnt(6)
s_barrier
v_mfma_f32_16x16x32_bf16 %[c26], a[40:43], a[92:95], %[c26]
v_mfma_f32_16x16x32_bf16 %[c3], a[44:47], a[80:83], %[c3]
ds_read_b64_tr_b16 a[56:57], v46 offset:26112
ds_read_b64_tr_b16 a[58:59], v48 offset:26112
v_mfma_f32_16x16x32_bf16 %[c11], a[44:47], a[84:87], %[c11]
ds_read_b128 a[64:67], v35
v_mfma_f32_16x16x32_bf16 %[c19], a[44:47], a[88:91], %[c19]
v_mfma_f32_16x16x32_bf16 %[c27], a[44:47], a[92:95], %[c27]
s_waitcnt lgkmcnt(3)
v_mfma_f32_16x16x32_bf16 %[c4], a[48:51], a[80:83], %[c4]
ds_read_b64_tr_b16 a[60:61], v47 offset:26112
ds_read_b64_tr_b16 a[62:63], v49 offset:26112
v_mfma_f32_16x16x32_bf16 %[c12], a[48:51], a[84:87], %[c12]
ds_read_b128 a[68:71], v35 offset:1024
v_mfma_f32_16x16x32_bf16 %[c20], a[48:51], a[88:91], %[c20]
v_mfma_f32_16x16x32_bf16 %[c28], a[48:51], a[92:95], %[c28]
v_mfma_f32_16x16x32_bf16 %[c5], a[52:55], a[80:83], %[c5]
ds_read_b128 a[72:75], v35 offset:2048
ds_read_b64_tr_b16 a[0:1], v38
v_mfma_f32_16x16x32_bf16 %[c13], a[52:55], a[84:87], %[c13]
ds_read_b64_tr_b16 a[2:3], v40
v_mfma_f32_16x16x32_bf16 %[c21], a[52:55], a[88:91], %[c21]
v_mfma_f32_16x16x32_bf16 %[c29], a[52:55], a[92:95], %[c29]
s_waitcnt lgkmcnt(4)
v_mfma_f32_16x16x32_bf16 %[c6], a[56:59], a[80:83], %[c6]
ds_read_b128 a[76:79], v35 offset:3072
ds_read_b64_tr_b16 a[4:5], v39
v_mfma_f32_16x16x32_bf16 %[c14], a[56:59], a[84:87], %[c14]
ds_read_b64_tr_b16 a[6:7], v41
v_mfma_f32_16x16x32_bf16 %[c22], a[56:59], a[88:91], %[c22]
v_mfma_f32_16x16x32_bf16 %[c30], a[56:59], a[92:95], %[c30]
v_mfma_f32_16x16x32_bf16 %[c7], a[60:63], a[80:83], %[c7]
ds_read_b64_tr_b16 a[8:9], v38 offset:512
ds_read_b64_tr_b16 a[10:11], v40 offset:512
v_mfma_f32_16x16x32_bf16 %[c15], a[60:63], a[84:87], %[c15]
v_mfma_f32_16x16x32_bf16 %[c23], a[60:63], a[88:91], %[c23]
v_mfma_f32_16x16x32_bf16 %[c31], a[60:63], a[92:95], %[c31]
s_waitcnt lgkmcnt(2)
v_mfma_f32_16x16x32_bf16 %[c0], a[0:3], a[64:67], %[c0]
ds_read_b64_tr_b16 a[12:13], v39 offset:512
ds_read_b64_tr_b16 a[14:15], v41 offset:512
v_mfma_f32_16x16x32_bf16 %[c8], a[0:3], a[68:71], %[c8]
v_mfma_f32_16x16x32_bf16 %[c16], a[0:3], a[72:75], %[c16]
v_mfma_f32_16x16x32_bf16 %[c24], a[0:3], a[76:79], %[c24]
v_mfma_f32_16x16x32_bf16 %[c1], a[4:7], a[64:67], %[c1]
ds_read_b64_tr_b16 a[16:17], v38 offset:1024
ds_read_b64_tr_b16 a[18:19], v40 offset:1024
v_mfma_f32_16x16x32_bf16 %[c9], a[4:7], a[68:71], %[c9]
v_mfma_f32_16x16x32_bf16 %[c17], a[4:7], a[72:75], %[c17]
v_mfma_f32_16x16x32_bf16 %[c25], a[4:7], a[76:79], %[c25]
s_waitcnt lgkmcnt(2)
v_mfma_f32_16x16x32_bf16 %[c2], a[8:11], a[64:67], %[c2]
ds_read_b64_tr_b16 a[20:21], v39 offset:1024
ds_read_b64_tr_b16 a[22:23], v41 offset:1024
v_mfma_f32_16x16x32_bf16 %[c10], a[8:11], a[68:71], %[c10]
v_mfma_f32_16x16x32_bf16 %[c18], a[8:11], a[72:75], %[c18]
s_waitcnt vmcnt(0)
s_barrier
v_mfma_f32_16x16x32_bf16 %[c26], a[8:11], a[76:79], %[c26]
v_mfma_f32_16x16x32_bf16 %[c3], a[12:15], a[64:67], %[c3]
ds_read_b64_tr_b16 a[24:25], v38 offset:1536
ds_read_b64_tr_b16 a[26:27], v40 offset:1536
v_mfma_f32_16x16x32_bf16 %[c11], a[12:15], a[68:71], %[c11]
ds_read_b128 a[80:83], v35 offset:24576
v_mfma_f32_16x16x32_bf16 %[c19], a[12:15], a[72:75], %[c19]
v_mfma_f32_16x16x32_bf16 %[c27], a[12:15], a[76:79], %[c27]
s_waitcnt lgkmcnt(3)
v_mfma_f32_16x16x32_bf16 %[c4], a[16:19], a[64:67], %[c4]
ds_read_b64_tr_b16 a[28:29], v39 offset:1536
ds_read_b64_tr_b16 a[30:31], v41 offset:1536
v_mfma_f32_16x16x32_bf16 %[c12], a[16:19], a[68:71], %[c12]
ds_read_b128 a[84:87], v35 offset:25600
v_mfma_f32_16x16x32_bf16 %[c20], a[16:19], a[72:75], %[c20]
v_mfma_f32_16x16x32_bf16 %[c28], a[16:19], a[76:79], %[c28]
v_mfma_f32_16x16x32_bf16 %[c5], a[20:23], a[64:67], %[c5]
ds_read_b128 a[88:91], v35 offset:26624
ds_read_b64_tr_b16 a[32:33], v38 offset:24576
v_mfma_f32_16x16x32_bf16 %[c13], a[20:23], a[68:71], %[c13]
ds_read_b64_tr_b16 a[34:35], v40 offset:24576
v_mfma_f32_16x16x32_bf16 %[c21], a[20:23], a[72:75], %[c21]
v_mfma_f32_16x16x32_bf16 %[c29], a[20:23], a[76:79], %[c29]
s_waitcnt lgkmcnt(4)
v_mfma_f32_16x16x32_bf16 %[c6], a[24:27], a[64:67], %[c6]
ds_read_b128 a[92:95], v35 offset:27648
ds_read_b64_tr_b16 a[36:37], v39 offset:24576
v_mfma_f32_16x16x32_bf16 %[c14], a[24:27], a[68:71], %[c14]
ds_read_b64_tr_b16 a[38:39], v41 offset:24576
v_mfma_f32_16x16x32_bf16 %[c22], a[24:27], a[72:75], %[c22]
v_mfma_f32_16x16x32_bf16 %[c30], a[24:27], a[76:79], %[c30]
v_mfma_f32_16x16x32_bf16 %[c7], a[28:31], a[64:67], %[c7]
ds_read_b64_tr_b16 a[40:41], v38 offset:25088
ds_read_b64_tr_b16 a[42:43], v40 offset:25088
v_mfma_f32_16x16x32_bf16 %[c15], a[28:31], a[68:71], %[c15]
v_mfma_f32_16x16x32_bf16 %[c23], a[28:31], a[72:75], %[c23]
v_mfma_f32_16x16x32_bf16 %[c31], a[28:31], a[76:79], %[c31]
s_waitcnt lgkmcnt(2)
v_mfma_f32_16x16x32_bf16 %[c0], a[32:35], a[80:83], %[c0]
ds_read_b64_tr_b16 a[44:45], v39 offset:25088
ds_read_b64_tr_b16 a[46:47], v41 offset:25088
v_mfma_f32_16x16x32_bf16 %[c8], a[32:35], a[84:87], %[c8]
v_mfma_f32_16x16x32_bf16 %[c16], a[32:35], a[88:91], %[c16]
v_mfma_f32_16x16x32_bf16 %[c24], a[32:35], a[92:95], %[c24]
v_mfma_f32_16x16x32_bf16 %[c1], a[36:39], a[80:83], %[c1]
ds_read_b64_tr_b16 a[48:49], v38 offset:25600
ds_read_b64_tr_b16 a[50:51], v40 offset:25600
v_mfma_f32_16x16x32_bf16 %[c9], a[36:39], a[84:87], %[c9]
v_mfma_f32_16x16x32_bf16 %[c17], a[36:39], a[88:91], %[c17]
v_mfma_f32_16x16x32_bf16 %[c25], a[36:39], a[92:95], %[c25]
s_waitcnt lgkmcnt(2)
v_mfma_f32_16x16x32_bf16 %[c2], a[40:43], a[80:83], %[c2]
ds_read_b64_tr_b16 a[52:53], v39 offset:25600
ds_read_b64_tr_b16 a[54:55], v41 offset:25600
v_mfma_f32_16x16x32_bf16 %[c10], a[40:43], a[84:87], %[c10]
v_mfma_f32_16x16x32_bf16 %[c18], a[40:43], a[88:91], %[c18]
v_mfma_f32_16x16x32_bf16 %[c26], a[40:43], a[92:95], %[c26]
v_mfma_f32_16x16x32_bf16 %[c3], a[44:47], a[80:83], %[c3]
ds_read_b64_tr_b16 a[56:57], v38 offset:26112
ds_read_b64_tr_b16 a[58:59], v40 offset:26112
v_mfma_f32_16x16x32_bf16 %[c11], a[44:47], a[84:87], %[c11]
v_mfma_f32_16x16x32_bf16 %[c19], a[44:47], a[88:91], %[c19]
v_mfma_f32_16x16x32_bf16 %[c27], a[44:47], a[92:95], %[c27]
s_waitcnt lgkmcnt(2)
v_mfma_f32_16x16x32_bf16 %[c4], a[48:51], a[80:83], %[c4]
ds_read_b64_tr_b16 a[60:61], v39 offset:26112
ds_read_b64_tr_b16 a[62:63], v41 offset:26112
v_mfma_f32_16x16x32_bf16 %[c12], a[48:51], a[84:87], %[c12]
v_mfma_f32_16x16x32_bf16 %[c20], a[48:51], a[88:91], %[c20]
v_mfma_f32_16x16x32_bf16 %[c28], a[48:51], a[92:95], %[c28]
v_mfma_f32_16x16x32_bf16 %[c5], a[52:55], a[80:83], %[c5]
v_mfma_f32_16x16x32_bf16 %[c13], a[52:55], a[84:87], %[c13]
v_mfma_f32_16x16x32_bf16 %[c21], a[52:55], a[88:91], %[c21]
v_mfma_f32_16x16x32_bf16 %[c29], a[52:55], a[92:95], %[c29]
s_waitcnt lgkmcnt(0)
v_mfma_f32_16x16x32_bf16 %[c6], a[56:59], a[80:83], %[c6]
v_mfma_f32_16x16x32_bf16 %[c14], a[56:59], a[84:87], %[c14]
v_mfma_f32_16x16x32_bf16 %[c22], a[56:59], a[88:91], %[c22]
v_mfma_f32_16x16x32_bf16 %[c30], a[56:59], a[92:95], %[c30]
v_mfma_f32_16x16x32_bf16 %[c7], a[60:63], a[80:83], %[c7]
v_mfma_f32_16x16x32_bf16 %[c15], a[60:63], a[84:87], %[c15]
v_mfma_f32_16x16x32_bf16 %[c23], a[60:63], a[88:91], %[c23]
v_mfma_f32_16x16x32_bf16 %[c31], a[60:63], a[92:95], %[c31]
s_nop 7
)ASM"
                : ACC32_OUT
                : [x0] "s"(LO32(xb)), [x1] "s"(HI16(xb)), [xn] "i"(20 * S * 64), [w0] "s"(LO32(wb)), [w1] "s"(HI16(wb)), [wn] "i"(M * 2 * FH * 2 + 896), [w] "s"(w), [lds] "s"((unsigned)(size_t)lds),
                  [xo] "s"((rb * 128 + 32 * w) * 64), [wo] "s"(ct * 256 + 8 * w * (2 * FH * 2)), [wo1] "s"((FH - 32 + ct * 128) * 2 + 8 * w * (2 * FH * 2)), [xadv] "i"(S * 64), [wadv] "i"(32 * 2 * FH * 2), [ws] "i"(2 * FH * 2),
                  [tag0] "s"((unsigned)tag), [tag1] "s"((unsigned)(tag >> 32)), [fdef] "s"(8 * (F_KV + bid)), [flags] "s"(fb), [dbg] "s"(ws + OFF_DBG + 262144 + (bid * 4 + w) * 64)
                :
"v32", "v33", "v34", "v35", "v36", "v37", "v38", "v39", "v40", "v41", "v42", "v43", "v44", "v45", "v46", "v47", "v48", "v49", "v50", "v51", "v52", "v53", "v54", "v55", "v56", "v57", "v58", "v59", "v60", "v61", "v62", "v63", "v64", "v65", "v66", "v67", "v68", "v69", "v70", "v71", "a0", "a1", "a2", "a3", "a4", "a5", "a6", "a7", "a8", "a9", "a10", "a11", "a12", "a13", "a14", "a15", "a16", "a17", "a18", "a19", "a20", "a21", "a22", "a23", "a24", "a25", "a26", "a27", "a28", "a29", "a30", "a31", "a32", "a33", "a34", "a35", "a36", "a37", "a38", "a39", "a40", "a41", "a42", "a43", "a44", "a45", "a46", "a47", "a48", "a49", "a50", "a51", "a52", "a53", "a54", "a55", "a56", "a57", "a58", "a59", "a60", "a61", "a62", "a63", "a64", "a65", "a66", "a67", "a68", "a69", "a70", "a71", "a72", "a73", "a74", "a75", "a76", "a77", "a78", "a79", "a80", "a81", "a82", "a83", "a84", "a85", "a86", "a87", "a88", "a89", "a90", "a91", "a92", "a93", "a94", "a95", "s36", "s37", "s38", "s39", "s40", "s41", "s42", "s43", "s44", "s45", "s46", "s47", "s48", "s49", "s50", "s51", "s52", "s53", "s54", "s55", "s56", "s57", "s58", "s59", "s60", "s61", "s62", "s63", "s64", "s65", "s66", "s67", "s68", "s69", "s70", "s71", "s72", "s73", "vcc", "scc", "memory"
            );
            CST(1);
            SYNC();
            const int row0 = rb * 128 + 64 * wr + r16;
#pragma unroll
            for (int k = 0; k < 32; ++k) c[k] = gelu4(c[k]);
            CST(2);
            if (wc == 0) {
                q_pre(ap->wq, ws, lds, w, h, bs, bl);
#pragma unroll
                for (int m = 0; m < 4; ++m)
#pragma unroll
                    for (int G = 0; G < 4; ++G) { const f32x4 p = c[8 * m + 2 * G], b = c[8 * m + 2 * G + 1];
                        st16_wt(wsr, (unsigned)OFF_Z1 + ((row0 + 16 * m) * FP + ct * 128 + 32 * G + 8 * fq) * 2, (u32x4){cvtpk(p[0], p[1]), cvtpk(p[2], p[3]), cvtpk(b[0], b[1]), cvtpk(b[2], b[3])}); }
            } else {
                const bool full = ct != 0;
                float mu[4];
#pragma unroll
                for (int m = 0; m < 4; ++m) {
                    f32x4 s4 = (c[8 * m + 6] + c[8 * m + 7]) + (c[8 * m + 2] + c[8 * m + 3]) + (c[8 * m + 4] + c[8 * m + 5]); if (full) s4 += c[8 * m] + c[8 * m + 1];
                    float s = (s4[0] + s4[1]) + (s4[2] + s4[3]);
                    s += xor_lane<16>(s); s = xor32_add(s);
                    mu[m] = s * (full ? 1.f / 128.f : 1.f / 96.f);
                    f32x4 q4 = {0.f, 0.f, 0.f, 0.f};
#pragma unroll
                    for (int n = 0; n < 8; ++n) if (n >= 2 || full) { const f32x4 d = c[8 * m + n] - mu[m]; q4 += d * d; }
                    float q = (q4[0] + q4[1]) + (q4[2] + q4[3]);
                    q += xor_lane<16>(q); q = xor32_add(q);
                    st16_wt(wsr, fq == 0 ? (unsigned)OFF_Z2S + ((rb * 7 + ct) * 128 + 64 * wr + 16 * m + r16) * 16 : 0xfffffff0u, (u32x4){__float_as_uint(mu[m]), tg, __float_as_uint(q), tg});
                }
                CSTW(3, 64);
                u32x4 g0[4], g1[4];
                for (unsigned spins = 0; spins < (1u << 16); ++spins) {
                    bool ok = true;
#pragma unroll
                    for (int m = 0; m < 4; ++m) { const unsigned o = (unsigned)OFF_Z2S + (rb * 7 * 128 + 64 * wr + 16 * m + r16) * 16;
                        g0[m] = __builtin_bit_cast(u32x4, __builtin_amdgcn_raw_buffer_load_b128(wsr, o + fq * 2048, 0, 16));
                        g1[m] = __builtin_bit_cast(u32x4, __builtin_amdgcn_raw_buffer_load_b128(wsr, o + (fq < 3 ? fq + 4 : fq) * 2048, 0, 16));
                        ok = ok && g0[m][1] == tg && g0[m][3] == tg && g1[m][1] == tg && g1[m][3] == tg; }
                    if (__all(ok)) break;
                    __builtin_amdgcn_s_sleep(4);
                }
                CSTW(4, 64);
                q_pre(ap->wq, ws, lds, w, h, bs, bl);
#pragma unroll
                for (int m = 0; m < 4; ++m) {
                    const float ma = __uint_as_float(g0[m][0]), mb = __uint_as_float(g1[m][0]), na = fq == 0 ? 96.f : 128.f, nb = fq < 3 ? 128.f : 0.f;
                    float t = na * ma + nb * mb; t += xor_lane<16>(t); t = xor32_add(t);
                    const float mean = t * (1.f / FH), da = ma - mean, db = mb - mean;
                    float q = __uint_as_float(g0[m][2]) + na * da * da + (fq < 3 ? __uint_as_float(g1[m][2]) + nb * db * db : 0.f);
                    q += xor_lane<16>(q); q = xor32_add(q);
                    const float rs = 1.f / sqrtf(q * (1.f / FH) + 1e-5f);
                    const int row = row0 + 16 * m, rr = row & 31;
#pragma unroll
                    for (int G = 0; G < 4; ++G) {
                        f32x4 p = (c[8 * m + 2 * G] - mean) * rs, b = (c[8 * m + 2 * G + 1] - mean) * rs;
#pragma unroll
                        for (int r = 0; r < 4; ++r) { auto t2 = __builtin_amdgcn_permlane16_swap(__float_as_uint(p[r]), __float_as_uint(b[r]), false, false); p[r] = __uint_as_float(t2[0]); b[r] = __uint_as_float(t2[1]); }
                        const int ch = 4 * G + 2 * (fq & 1) + (fq >> 1);
                        const bool pad = ct == 0 && G == 0;
                        const u32x4 v = pad ? (u32x4){0, 0, 0, 0} : (u32x4){cvtpk(p[0], p[1]), cvtpk(p[2], p[3]), cvtpk(b[0], b[1]), cvtpk(b[2], b[3])};
                        st16_wt(wsr, (unsigned)OFF_Z2I + ((row >> 5) * 7 + ct) * 8192 + 2048 * (rr >> 3) + 512 * (ch >> 2) + 64 * (rr & 7) + 16 * ((ch & 3) ^ ((rr >> 2) & 3)), v);
                    }
                }
            }
            CST(7); CSTW(5, 64);
        }
    }
    {
        PHASE_VARS();
        const unsigned pk = 8u * ((lane < 8 && 2 * lane <= bl) ? F_KV + (h >> 2) + 8 * ((h & 3) + 4 * lane) : F_KV + bid); const char* fb = ws + OFF_CTL;
        const int Ts = 2 * (bs + 1), T = 2 * (bl + 1);
        const char* kvb = ws + OFF_KV + (size_t)h * 1048576; const char* xb = ws + OFF_XNI; const char* wb = (const char*)ap->wq - 896;
        unsigned flag;
        asm volatile(
R"ASM(s_mov_b32 s36, %[kv0]
s_mov_b32 s37, %[kv1]
s_mov_b32 s38, %[kvn]
s_mov_b32 s39, 0x20000
s_mov_b32 s43, %[w]
s_lshl_b32 s50, s43, 12
s_add_u32 s42, %[lds], s50
s_add_u32 s40, s50, 0x4000
s_add_u32 s41, s40, 0x4000
s_mov_b32 s44, %[ud0]
s_mov_b32 s45, %[ul0]
s_mov_b32 s46, %[ts1]
s_mov_b32 s47, %[t1]
s_mov_b32 s48, 1
s_sub_i32 s49, s43, 1
v_mbcnt_lo_u32_b32 v240, -1, 0
v_mbcnt_hi_u32_b32 v240, -1, v240
v_lshlrev_b32_e32 v238, 4, v240
v_and_b32_e32 v241, 15, v240
v_lshrrev_b32_e32 v242, 4, v240
s_mov_b32 s64, %[xi0]
s_mov_b32 s65, %[xi1]
s_mov_b32 s66, %[xin]
s_mov_b32 s67, 0x20000
s_mov_b32 s68, %[wq0]
s_mov_b32 s69, %[wq1]
s_mov_b32 s70, %[wqn]
s_mov_b32 s71, 0x20000
s_mov_b32 s72, %[xo0]
s_mov_b32 s73, %[xo1]
s_mov_b32 s78, %[tag0]
s_mov_b32 s79, %[tag1]
s_lshl_b32 s76, s43, 16
s_add_u32 s74, %[wqo], s76
s_lshl_b32 s76, s43, 11
s_add_u32 s75, %[lds], s76
v_lshrrev_b32_e32 v32, 4, v240
v_and_b32_e32 v32, 1, v32
s_lshl_b32 s76, s43, 1
v_add_u32_e32 v32, s76, v32
v_and_b32_e32 v32, 3, v32
v_and_b32_e32 v33, 3, v240
v_xor_b32_e32 v32, v32, v33
v_lshrrev_b32_e32 v33, 5, v240
v_lshl_add_u32 v32, v33, 2, v32
v_lshlrev_b32_e32 v32, 4, v32
v_lshrrev_b32_e32 v33, 2, v240
v_and_b32_e32 v33, 7, v33
v_lshl_add_u32 v177, v33, 13, v32
v_add_u32_e32 v176, 896, v177
s_add_u32 s72, s72, 786432
s_add_u32 s73, s73, 786432
s_add_u32 s74, s74, 1572864
v_lshrrev_b32_e32 v32, 2, v241
v_sub_u32_e32 v32, 4, v32
v_and_b32_e32 v32, 3, v32
v_xor_b32_e32 v32, v32, v242
v_lshlrev_b32_e32 v32, 4, v32
v_lshl_add_u32 v32, v241, 6, v32
v_add_u32_e32 v32, s42, v32
v_add_u32_e32 v160, 114688, v32
v_add_u32_e32 v161, 0, v32
v_add_u32_e32 v162, 65536, v32
v_lshrrev_b32_e32 v34, 2, v241
v_and_b32_e32 v35, 3, v240
v_lshlrev_b32_e32 v32, 1, v242
v_add_u32_e32 v32, 0, v32
v_and_b32_e32 v32, 3, v32
v_lshrrev_b32_e32 v33, 1, v35
v_or_b32_e32 v33, 0, v33
v_xor_b32_e32 v32, v32, v33
v_lshlrev_b32_e32 v32, 4, v32
v_and_b32_e32 v33, 1, v35
v_lshl_add_u32 v32, v33, 3, v32
v_lshl_add_u32 v32, v242, 11, v32
v_add_u32_e32 v33, 0, v34
v_lshl_add_u32 v32, v33, 6, v32
v_add_u32_e32 v32, %[lds], v32
v_add_u32_e32 v164, 131072, v32
v_add_u32_e32 v168, 16384, v32
v_add_u32_e32 v172, 81920, v32
v_lshlrev_b32_e32 v32, 1, v242
v_add_u32_e32 v32, 0, v32
v_and_b32_e32 v32, 3, v32
v_lshrrev_b32_e32 v33, 1, v35
v_or_b32_e32 v33, 2, v33
v_xor_b32_e32 v32, v32, v33
v_lshlrev_b32_e32 v32, 4, v32
v_and_b32_e32 v33, 1, v35
v_lshl_add_u32 v32, v33, 3, v32
v_lshl_add_u32 v32, v242, 11, v32
v_add_u32_e32 v33, 0, v34
v_lshl_add_u32 v32, v33, 6, v32
v_add_u32_e32 v32, %[lds], v32
v_add_u32_e32 v165, 131072, v32
v_add_u32_e32 v169, 16384, v32
v_add_u32_e32 v173, 81920, v32
v_lshlrev_b32_e32 v32, 1, v242
v_add_u32_e32 v32, 1, v32
v_and_b32_e32 v32, 3, v32
v_lshrrev_b32_e32 v33, 1, v35
v_or_b32_e32 v33, 0, v33
v_xor_b32_e32 v32, v32, v33
v_lshlrev_b32_e32 v32, 4, v32
v_and_b32_e32 v33, 1, v35
v_lshl_add_u32 v32, v33, 3, v32
v_lshl_add_u32 v32, v242, 11, v32
v_add_u32_e32 v33, 4, v34
v_lshl_add_u32 v32, v33, 6, v32
v_add_u32_e32 v32, %[lds], v32
v_add_u32_e32 v166, 131072, v32
v_add_u32_e32 v170, 16384, v32
v_add_u32_e32 v174, 81920, v32
v_lshlrev_b32_e32 v32, 1, v242
v_add_u32_e32 v32, 1, v32
v_and_b32_e32 v32, 3, v32
v_lshrrev_b32_e32 v33, 1, v35
v_or_b32_e32 v33, 2, v33
v_xor_b32_e32 v32, v32, v33
v_lshlrev_b32_e32 v32, 4, v32
v_and_b32_e32 v33, 1, v35
v_lshl_add_u32 v32, v33, 3, v32
v_lshl_add_u32 v32, v242, 11, v32
v_add_u32_e32 v33, 4, v34
v_lshl_add_u32 v32, v33, 6, v32
v_add_u32_e32 v32, %[lds], v32
v_add_u32_e32 v167, 131072, v32
v_add_u32_e32 v171, 16384, v32
v_add_u32_e32 v175, 81920, v32
v_lshrrev_b32_e32 v244, 2, v241
v_sub_u32_e32 v244, 4, v244
v_and_b32_e32 v244, 3, v244
v_xor_b32_e32 v244, v244, v242
v_lshlrev_b32_e32 v244, 4, v244
v_lshl_add_u32 v244, v241, 6, v244
v_add_u32_e32 v224, %[lds], v244
v_add_u32_e32 v228, 0x10000, v224
v_mov_b32_e32 v239, 0xff800000
v_mov_b32_e32 v232, 0
v_mov_b32_e32 v233, 0
v_mov_b32_e32 v234, 0
v_mov_b32_e32 v235, 0
v_mov_b32_e32 v248, 0x3f803f80
v_mov_b32_e32 v249, 0x3f803f80
v_mov_b32_e32 v250, 0x3f803f80
v_mov_b32_e32 v251, 0x3f803f80
s_mov_b32 s56, 0xf0000
s_mov_b32 s57, 0xfff00ff
s_mov_b32 s58, 0x1f0001
s_mov_b32 s59, 0x1fff01ff
s_mov_b32 s60, 0x3f0003
s_mov_b32 s61, 0x3fff03ff
s_mov_b32 s62, 0x7f0007
s_mov_b32 s63, 0x7fff07ff
s_waitcnt vmcnt(46)
s_barrier
ds_read_b128 a[192:195], v160
ds_read_b128 a[196:199], v160 offset:1024
ds_read_b128 a[200:203], v160 offset:2048
ds_read_b64_tr_b16 a[128:129], v164
ds_read_b64_tr_b16 a[130:131], v166
ds_read_b128 a[204:207], v160 offset:3072
ds_read_b64_tr_b16 a[132:133], v165
ds_read_b64_tr_b16 a[134:135], v167
ds_read_b64_tr_b16 a[136:137], v164 offset:512
ds_read_b64_tr_b16 a[138:139], v166 offset:512
s_waitcnt lgkmcnt(2)
v_mfma_f32_16x16x32_bf16 v[32:35], a[128:131], a[192:195], 0
ds_read_b64_tr_b16 a[140:141], v165 offset:512
ds_read_b64_tr_b16 a[142:143], v167 offset:512
v_mfma_f32_16x16x32_bf16 v[36:39], a[128:131], a[196:199], 0
v_mfma_f32_16x16x32_bf16 v[40:43], a[128:131], a[200:203], 0
v_mfma_f32_16x16x32_bf16 v[44:47], a[128:131], a[204:207], 0
v_mfma_f32_16x16x32_bf16 v[48:51], a[132:135], a[192:195], 0
ds_read_b64_tr_b16 a[144:145], v164 offset:1024
ds_read_b64_tr_b16 a[146:147], v166 offset:1024
v_mfma_f32_16x16x32_bf16 v[52:55], a[132:135], a[196:199], 0
v_mfma_f32_16x16x32_bf16 v[56:59], a[132:135], a[200:203], 0
v_mfma_f32_16x16x32_bf16 v[60:63], a[132:135], a[204:207], 0
s_waitcnt lgkmcnt(2)
v_mfma_f32_16x16x32_bf16 v[64:67], a[136:139], a[192:195], 0
ds_read_b64_tr_b16 a[148:149], v165 offset:1024
ds_read_b64_tr_b16 a[150:151], v167 offset:1024
v_mfma_f32_16x16x32_bf16 v[68:71], a[136:139], a[196:199], 0
v_mfma_f32_16x16x32_bf16 v[72:75], a[136:139], a[200:203], 0
s_waitcnt vmcnt(40)
s_barrier
v_mfma_f32_16x16x32_bf16 v[76:79], a[136:139], a[204:207], 0
v_mfma_f32_16x16x32_bf16 v[80:83], a[140:143], a[192:195], 0
ds_read_b64_tr_b16 a[152:153], v164 offset:1536
ds_read_b64_tr_b16 a[154:155], v166 offset:1536
v_mfma_f32_16x16x32_bf16 v[84:87], a[140:143], a[196:199], 0
ds_read_b128 a[208:211], v160 offset:24576
v_mfma_f32_16x16x32_bf16 v[88:91], a[140:143], a[200:203], 0
v_mfma_f32_16x16x32_bf16 v[92:95], a[140:143], a[204:207], 0
s_waitcnt lgkmcnt(3)
v_mfma_f32_16x16x32_bf16 v[96:99], a[144:147], a[192:195], 0
ds_read_b64_tr_b16 a[156:157], v165 offset:1536
ds_read_b64_tr_b16 a[158:159], v167 offset:1536
v_mfma_f32_16x16x32_bf16 v[100:103], a[144:147], a[196:199], 0
ds_read_b128 a[212:215], v160 offset:25600
v_mfma_f32_16x16x32_bf16 v[104:107], a[144:147], a[200:203], 0
v_mfma_f32_16x16x32_bf16 v[108:111], a[144:147], a[204:207], 0
v_mfma_f32_16x16x32_bf16 v[112:115], a[148:151], a[192:195], 0
ds_read_b128 a[216:219], v160 offset:26624
ds_read_b64_tr_b16 a[160:161], v164 offset:24576
v_mfma_f32_16x16x32_bf16 v[116:119], a[148:151], a[196:199], 0
ds_read_b64_tr_b16 a[162:163], v166 offset:24576
v_mfma_f32_16x16x32_bf16 v[120:123], a[148:151], a[200:203], 0
v_mfma_f32_16x16x32_bf16 v[124:127], a[148:151], a[204:207], 0
s_waitcnt lgkmcnt(4)
v_mfma_f32_16x16x32_bf16 v[128:131], a[152:155], a[192:195], 0
ds_read_b128 a[220:223], v160 offset:27648
ds_read_b64_tr_b16 a[164:165], v165 offset:24576
v_mfma_f32_16x16x32_bf16 v[132:135], a[152:155], a[196:199], 0
ds_read_b64_tr_b16 a[166:167], v167 offset:24576
v_mfma_f32_16x16x32_bf16 v[136:139], a[152:155], a[200:203], 0
v_mfma_f32_16x16x32_bf16 v[140:143], a[152:155], a[204:207], 0
v_mfma_f32_16x16x32_bf16 v[144:147], a[156:159], a[192:195], 0
ds_read_b64_tr_b16 a[168:169], v164 offset:25088
ds_read_b64_tr_b16 a[170:171], v166 offset:25088
v_mfma_f32_16x16x32_bf16 v[148:151], a[156:159], a[196:199], 0
v_mfma_f32_16x16x32_bf16 v[152:155], a[156:159], a[200:203], 0
v_mfma_f32_16x16x32_bf16 v[156:159], a[156:159], a[204:207], 0
s_waitcnt lgkmcnt(2)
v_mfma_f32_16x16x32_bf16 v[32:35], a[160:163], a[208:211], v[32:35]
ds_read_b64_tr_b16 a[172:173], v165 offset:25088
ds_read_b64_tr_b16 a[174:175], v167 offset:25088
v_accvgpr_write_b32 a0, 0
v_mfma_f32_16x16x32_bf16 v[36:39], a[160:163], a[212:215], v[36:39]
v_mfma_f32_16x16x32_bf16 v[40:43], a[160:163], a[216:219], v[40:43]
v_mfma_f32_16x16x32_bf16 v[44:47], a[160:163], a[220:223], v[44:47]
v_accvgpr_write_b32 a1, 0
v_mfma_f32_16x16x32_bf16 v[48:51], a[164:167], a[208:211], v[48:51]
ds_read_b64_tr_b16 a[176:177], v164 offset:25600
ds_read_b64_tr_b16 a[178:179], v166 offset:25600
v_mfma_f32_16x16x32_bf16 v[52:55], a[164:167], a[212:215], v[52:55]
v_mfma_f32_16x16x32_bf16 v[56:59], a[164:167], a[216:219], v[56:59]
v_accvgpr_write_b32 a2, 0
v_mfma_f32_16x16x32_bf16 v[60:63], a[164:167], a[220:223], v[60:63]
s_waitcnt lgkmcnt(2)
v_mfma_f32_16x16x32_bf16 v[64:67], a[168:171], a[208:211], v[64:67]
ds_read_b64_tr_b16 a[180:181], v165 offset:25600
ds_read_b64_tr_b16 a[182:183], v167 offset:25600
v_mfma_f32_16x16x32_bf16 v[68:71], a[168:171], a[212:215], v[68:71]
v_accvgpr_write_b32 a3, 0
v_mfma_f32_16x16x32_bf16 v[72:75], a[168:171], a[216:219], v[72:75]
s_waitcnt vmcnt(34)
s_barrier
v_mfma_f32_16x16x32_bf16 v[76:79], a[168:171], a[220:223], v[76:79]
s_add_u32 m0, s42, 114688
v_mfma_f32_16x16x32_bf16 v[80:83], a[172:175], a[208:211], v[80:83]
ds_read_b64_tr_b16 a[184:185], v164 offset:26112
ds_read_b64_tr_b16 a[186:187], v166 offset:26112
v_accvgpr_write_b32 a4, 0
v_mfma_f32_16x16x32_bf16 v[84:87], a[172:175], a[212:215], v[84:87]
ds_read_b128 a[192:195], v161
buffer_load_dwordx4 v238, s[64:67], s72 offen sc1 lds
v_mfma_f32_16x16x32_bf16 v[88:91], a[172:175], a[216:219], v[88:91]
v_mfma_f32_16x16x32_bf16 v[92:95], a[172:175], a[220:223], v[92:95]
v_accvgpr_write_b32 a5, 0
s_waitcnt lgkmcnt(3)
v_mfma_f32_16x16x32_bf16 v[96:99], a[176:179], a[208:211], v[96:99]
ds_read_b64_tr_b16 a[188:189], v165 offset:26112
ds_read_b64_tr_b16 a[190:191], v167 offset:26112
buffer_load_dwordx4 v238, s[64:67], s72 offen offset:1024 sc1 lds
v_mfma_f32_16x16x32_bf16 v[100:103], a[176:179], a[212:215], v[100:103]
ds_read_b128 a[196:199], v161 offset:1024
v_mfma_f32_16x16x32_bf16 v[104:107], a[176:179], a[216:219], v[104:107]
s_add_u32 m0, s42, 116736
v_accvgpr_write_b32 a6, 0
v_mfma_f32_16x16x32_bf16 v[108:111], a[176:179], a[220:223], v[108:111]
v_mfma_f32_16x16x32_bf16 v[112:115], a[180:183], a[208:211], v[112:115]
ds_read_b128 a[200:203], v161 offset:2048
ds_read_b64_tr_b16 a[128:129], v168
v_mfma_f32_16x16x32_bf16 v[116:119], a[180:183], a[212:215], v[116:119]
ds_read_b64_tr_b16 a[130:131], v170
buffer_load_dwordx4 v238, s[64:67], s73 offen sc1 lds
v_accvgpr_write_b32 a7, 0
v_mfma_f32_16x16x32_bf16 v[120:123], a[180:183], a[216:219], v[120:123]
v_mfma_f32_16x16x32_bf16 v[124:127], a[180:183], a[220:223], v[124:127]
buffer_load_dwordx4 v238, s[64:67], s73 offen offset:1024 sc1 lds
s_waitcnt lgkmcnt(4)
v_mfma_f32_16x16x32_bf16 v[128:131], a[184:187], a[208:211], v[128:131]
ds_read_b128 a[204:207], v161 offset:3072
ds_read_b64_tr_b16 a[132:133], v169
v_accvgpr_write_b32 a8, 0
v_mfma_f32_16x16x32_bf16 v[132:135], a[184:187], a[212:215], v[132:135]
ds_read_b64_tr_b16 a[134:135], v171
v_mfma_f32_16x16x32_bf16 v[136:139], a[184:187], a[216:219], v[136:139]
s_add_u32 m0, s75, 131072
v_mfma_f32_16x16x32_bf16 v[140:143], a[184:187], a[220:223], v[140:143]
v_accvgpr_write_b32 a9, 0
v_mfma_f32_16x16x32_bf16 v[144:147], a[188:191], a[208:211], v[144:147]
ds_read_b64_tr_b16 a[136:137], v168 offset:512
ds_read_b64_tr_b16 a[138:139], v170 offset:512
v_mfma_f32_16x16x32_bf16 v[148:151], a[188:191], a[212:215], v[148:151]
buffer_load_dwordx4 v176, s[68:71], s74 offen lds
v_mfma_f32_16x16x32_bf16 v[152:155], a[188:191], a[216:219], v[152:155]
v_accvgpr_write_b32 a10, 0
v_mfma_f32_16x16x32_bf16 v[156:159], a[188:191], a[220:223], v[156:159]
buffer_load_dwordx4 v177, s[68:71], s74 offen offset:1024 lds
s_waitcnt lgkmcnt(2)
v_mfma_f32_16x16x32_bf16 v[32:35], a[128:131], a[192:195], v[32:35]
ds_read_b64_tr_b16 a[140:141], v169 offset:512
ds_read_b64_tr_b16 a[142:143], v171 offset:512
v_mfma_f32_16x16x32_bf16 v[36:39], a[128:131], a[196:199], v[36:39]
v_accvgpr_write_b32 a11, 0
v_mfma_f32_16x16x32_bf16 v[40:43], a[128:131], a[200:203], v[40:43]
)ASM"
R"ASM(s_add_u32 s72, s72, 0x20000
v_mfma_f32_16x16x32_bf16 v[44:47], a[128:131], a[204:207], v[44:47]
v_mfma_f32_16x16x32_bf16 v[48:51], a[132:135], a[192:195], v[48:51]
s_add_u32 s73, s73, 0x20000
ds_read_b64_tr_b16 a[144:145], v168 offset:1024
ds_read_b64_tr_b16 a[146:147], v170 offset:1024
v_accvgpr_write_b32 a12, 0
v_mfma_f32_16x16x32_bf16 v[52:55], a[132:135], a[196:199], v[52:55]
v_mfma_f32_16x16x32_bf16 v[56:59], a[132:135], a[200:203], v[56:59]
v_mfma_f32_16x16x32_bf16 v[60:63], a[132:135], a[204:207], v[60:63]
s_add_u32 s74, s74, 262144
v_accvgpr_write_b32 a13, 0
s_waitcnt lgkmcnt(2)
v_mfma_f32_16x16x32_bf16 v[64:67], a[136:139], a[192:195], v[64:67]
ds_read_b64_tr_b16 a[148:149], v169 offset:1024
ds_read_b64_tr_b16 a[150:151], v171 offset:1024
v_mfma_f32_16x16x32_bf16 v[68:71], a[136:139], a[196:199], v[68:71]
v_mfma_f32_16x16x32_bf16 v[72:75], a[136:139], a[200:203], v[72:75]
s_waitcnt vmcnt(34)
s_barrier
v_accvgpr_write_b32 a14, 0
v_mfma_f32_16x16x32_bf16 v[76:79], a[136:139], a[204:207], v[76:79]
s_add_u32 m0, s42, 139264
v_mfma_f32_16x16x32_bf16 v[80:83], a[140:143], a[192:195], v[80:83]
ds_read_b64_tr_b16 a[152:153], v168 offset:1536
ds_read_b64_tr_b16 a[154:155], v170 offset:1536
v_mfma_f32_16x16x32_bf16 v[84:87], a[140:143], a[196:199], v[84:87]
ds_read_b128 a[208:211], v161 offset:24576
buffer_load_dwordx4 v238, s[64:67], s72 offen sc1 lds
v_accvgpr_write_b32 a15, 0
v_mfma_f32_16x16x32_bf16 v[88:91], a[140:143], a[200:203], v[88:91]
v_mfma_f32_16x16x32_bf16 v[92:95], a[140:143], a[204:207], v[92:95]
s_waitcnt lgkmcnt(3)
v_mfma_f32_16x16x32_bf16 v[96:99], a[144:147], a[192:195], v[96:99]
ds_read_b64_tr_b16 a[156:157], v169 offset:1536
ds_read_b64_tr_b16 a[158:159], v171 offset:1536
buffer_load_dwordx4 v238, s[64:67], s72 offen offset:1024 sc1 lds
v_accvgpr_write_b32 a16, 0
v_mfma_f32_16x16x32_bf16 v[100:103], a[144:147], a[196:199], v[100:103]
ds_read_b128 a[212:215], v161 offset:25600
v_mfma_f32_16x16x32_bf16 v[104:107], a[144:147], a[200:203], v[104:107]
s_add_u32 m0, s42, 141312
v_mfma_f32_16x16x32_bf16 v[108:111], a[144:147], a[204:207], v[108:111]
v_accvgpr_write_b32 a17, 0
v_mfma_f32_16x16x32_bf16 v[112:115], a[148:151], a[192:195], v[112:115]
ds_read_b128 a[216:219], v161 offset:26624
ds_read_b64_tr_b16 a[160:161], v168 offset:24576
v_mfma_f32_16x16x32_bf16 v[116:119], a[148:151], a[196:199], v[116:119]
ds_read_b64_tr_b16 a[162:163], v170 offset:24576
buffer_load_dwordx4 v238, s[64:67], s73 offen sc1 lds
v_mfma_f32_16x16x32_bf16 v[120:123], a[148:151], a[200:203], v[120:123]
v_accvgpr_write_b32 a18, 0
v_mfma_f32_16x16x32_bf16 v[124:127], a[148:151], a[204:207], v[124:127]
buffer_load_dwordx4 v238, s[64:67], s73 offen offset:1024 sc1 lds
s_waitcnt lgkmcnt(4)
v_mfma_f32_16x16x32_bf16 v[128:131], a[152:155], a[192:195], v[128:131]
ds_read_b128 a[220:223], v161 offset:27648
ds_read_b64_tr_b16 a[164:165], v169 offset:24576
v_mfma_f32_16x16x32_bf16 v[132:135], a[152:155], a[196:199], v[132:135]
ds_read_b64_tr_b16 a[166:167], v171 offset:24576
v_accvgpr_write_b32 a19, 0
v_mfma_f32_16x16x32_bf16 v[136:139], a[152:155], a[200:203], v[136:139]
s_add_u32 m0, s75, 155648
v_mfma_f32_16x16x32_bf16 v[140:143], a[152:155], a[204:207], v[140:143]
v_mfma_f32_16x16x32_bf16 v[144:147], a[156:159], a[192:195], v[144:147]
ds_read_b64_tr_b16 a[168:169], v168 offset:25088
ds_read_b64_tr_b16 a[170:171], v170 offset:25088
v_accvgpr_write_b32 a20, 0
v_mfma_f32_16x16x32_bf16 v[148:151], a[156:159], a[196:199], v[148:151]
buffer_load_dwordx4 v176, s[68:71], s74 offen lds
v_mfma_f32_16x16x32_bf16 v[152:155], a[156:159], a[200:203], v[152:155]
v_mfma_f32_16x16x32_bf16 v[156:159], a[156:159], a[204:207], v[156:159]
buffer_load_dwordx4 v177, s[68:71], s74 offen offset:1024 lds
v_accvgpr_write_b32 a21, 0
s_waitcnt lgkmcnt(2)
v_mfma_f32_16x16x32_bf16 v[32:35], a[160:163], a[208:211], v[32:35]
ds_read_b64_tr_b16 a[172:173], v169 offset:25088
ds_read_b64_tr_b16 a[174:175], v171 offset:25088
v_mfma_f32_16x16x32_bf16 v[36:39], a[160:163], a[212:215], v[36:39]
v_mfma_f32_16x16x32_bf16 v[40:43], a[160:163], a[216:219], v[40:43]
s_add_u32 s72, s72, 0x20000
v_accvgpr_write_b32 a22, 0
v_mfma_f32_16x16x32_bf16 v[44:47], a[160:163], a[220:223], v[44:47]
v_mfma_f32_16x16x32_bf16 v[48:51], a[164:167], a[208:211], v[48:51]
s_add_u32 s73, s73, 0x20000
ds_read_b64_tr_b16 a[176:177], v168 offset:25600
ds_read_b64_tr_b16 a[178:179], v170 offset:25600
v_mfma_f32_16x16x32_bf16 v[52:55], a[164:167], a[212:215], v[52:55]
v_accvgpr_write_b32 a23, 0
v_mfma_f32_16x16x32_bf16 v[56:59], a[164:167], a[216:219], v[56:59]
v_mfma_f32_16x16x32_bf16 v[60:63], a[164:167], a[220:223], v[60:63]
s_add_u32 s74, s74, 262144
s_waitcnt lgkmcnt(2)
v_mfma_f32_16x16x32_bf16 v[64:67], a[168:171], a[208:211], v[64:67]
ds_read_b64_tr_b16 a[180:181], v169 offset:25600
ds_read_b64_tr_b16 a[182:183], v171 offset:25600
v_accvgpr_write_b32 a24, 0
v_mfma_f32_16x16x32_bf16 v[68:71], a[168:171], a[212:215], v[68:71]
v_mfma_f32_16x16x32_bf16 v[72:75], a[168:171], a[216:219], v[72:75]
s_waitcnt vmcnt(34)
s_barrier
v_mfma_f32_16x16x32_bf16 v[76:79], a[168:171], a[220:223], v[76:79]
s_add_u32 m0, s42, 0
v_accvgpr_write_b32 a25, 0
v_mfma_f32_16x16x32_bf16 v[80:83], a[172:175], a[208:211], v[80:83]
ds_read_b64_tr_b16 a[184:185], v168 offset:26112
ds_read_b64_tr_b16 a[186:187], v170 offset:26112
v_mfma_f32_16x16x32_bf16 v[84:87], a[172:175], a[212:215], v[84:87]
ds_read_b128 a[192:195], v162
buffer_load_dwordx4 v238, s[64:67], s72 offen sc1 lds
v_mfma_f32_16x16x32_bf16 v[88:91], a[172:175], a[216:219], v[88:91]
v_accvgpr_write_b32 a26, 0
v_mfma_f32_16x16x32_bf16 v[92:95], a[172:175], a[220:223], v[92:95]
s_waitcnt lgkmcnt(3)
v_mfma_f32_16x16x32_bf16 v[96:99], a[176:179], a[208:211], v[96:99]
ds_read_b64_tr_b16 a[188:189], v169 offset:26112
ds_read_b64_tr_b16 a[190:191], v171 offset:26112
buffer_load_dwordx4 v238, s[64:67], s72 offen offset:1024 sc1 lds
v_mfma_f32_16x16x32_bf16 v[100:103], a[176:179], a[212:215], v[100:103]
ds_read_b128 a[196:199], v162 offset:1024
v_accvgpr_write_b32 a27, 0
v_mfma_f32_16x16x32_bf16 v[104:107], a[176:179], a[216:219], v[104:107]
s_add_u32 m0, s42, 2048
v_mfma_f32_16x16x32_bf16 v[108:111], a[176:179], a[220:223], v[108:111]
v_mfma_f32_16x16x32_bf16 v[112:115], a[180:183], a[208:211], v[112:115]
ds_read_b128 a[200:203], v162 offset:2048
ds_read_b64_tr_b16 a[128:129], v172
v_accvgpr_write_b32 a28, 0
v_mfma_f32_16x16x32_bf16 v[116:119], a[180:183], a[212:215], v[116:119]
ds_read_b64_tr_b16 a[130:131], v174
buffer_load_dwordx4 v238, s[64:67], s73 offen sc1 lds
v_mfma_f32_16x16x32_bf16 v[120:123], a[180:183], a[216:219], v[120:123]
v_mfma_f32_16x16x32_bf16 v[124:127], a[180:183], a[220:223], v[124:127]
buffer_load_dwordx4 v238, s[64:67], s73 offen offset:1024 sc1 lds
v_accvgpr_write_b32 a29, 0
s_waitcnt lgkmcnt(4)
v_mfma_f32_16x16x32_bf16 v[128:131], a[184:187], a[208:211], v[128:131]
ds_read_b128 a[204:207], v162 offset:3072
ds_read_b64_tr_b16 a[132:133], v173
v_mfma_f32_16x16x32_bf16 v[132:135], a[184:187], a[212:215], v[132:135]
ds_read_b64_tr_b16 a[134:135], v175
v_mfma_f32_16x16x32_bf16 v[136:139], a[184:187], a[216:219], v[136:139]
s_add_u32 m0, s75, 16384
v_accvgpr_write_b32 a30, 0
v_mfma_f32_16x16x32_bf16 v[140:143], a[184:187], a[220:223], v[140:143]
v_mfma_f32_16x16x32_bf16 v[144:147], a[188:191], a[208:211], v[144:147]
ds_read_b64_tr_b16 a[136:137], v172 offset:512
ds_read_b64_tr_b16 a[138:139], v174 offset:512
v_mfma_f32_16x16x32_bf16 v[148:151], a[188:191], a[212:215], v[148:151]
buffer_load_dwordx4 v176, s[68:71], s74 offen lds
v_accvgpr_write_b32 a31, 0
v_mfma_f32_16x16x32_bf16 v[152:155], a[188:191], a[216:219], v[152:155]
v_mfma_f32_16x16x32_bf16 v[156:159], a[188:191], a[220:223], v[156:159]
buffer_load_dwordx4 v177, s[68:71], s74 offen offset:1024 lds
s_waitcnt lgkmcnt(2)
v_mfma_f32_16x16x32_bf16 v[32:35], a[128:131], a[192:195], v[32:35]
ds_read_b64_tr_b16 a[140:141], v173 offset:512
ds_read_b64_tr_b16 a[142:143], v175 offset:512
v_accvgpr_write_b32 a32, 0
v_mfma_f32_16x16x32_bf16 v[36:39], a[128:131], a[196:199], v[36:39]
v_mfma_f32_16x16x32_bf16 v[40:43], a[128:131], a[200:203], v[40:43]
s_add_u32 s72, s72, 0x20000
v_mfma_f32_16x16x32_bf16 v[44:47], a[128:131], a[204:207], v[44:47]
v_accvgpr_write_b32 a33, 0
v_mfma_f32_16x16x32_bf16 v[48:51], a[132:135], a[192:195], v[48:51]
s_add_u32 s73, s73, 0x20000
ds_read_b64_tr_b16 a[144:145], v172 offset:1024
ds_read_b64_tr_b16 a[146:147], v174 offset:1024
v_mfma_f32_16x16x32_bf16 v[52:55], a[132:135], a[196:199], v[52:55]
v_mfma_f32_16x16x32_bf16 v[56:59], a[132:135], a[200:203], v[56:59]
v_accvgpr_write_b32 a34, 0
v_mfma_f32_16x16x32_bf16 v[60:63], a[132:135], a[204:207], v[60:63]
s_add_u32 s74, s74, 262144
s_waitcnt lgkmcnt(2)
v_mfma_f32_16x16x32_bf16 v[64:67], a[136:139], a[192:195], v[64:67]
ds_read_b64_tr_b16 a[148:149], v173 offset:1024
ds_read_b64_tr_b16 a[150:151], v175 offset:1024
v_mfma_f32_16x16x32_bf16 v[68:71], a[136:139], a[196:199], v[68:71]
v_accvgpr_write_b32 a35, 0
v_mfma_f32_16x16x32_bf16 v[72:75], a[136:139], a[200:203], v[72:75]
s_waitcnt vmcnt(34)
s_barrier
v_mfma_f32_16x16x32_bf16 v[76:79], a[136:139], a[204:207], v[76:79]
s_add_u32 m0, s42, 24576
v_mfma_f32_16x16x32_bf16 v[80:83], a[140:143], a[192:195], v[80:83]
ds_read_b64_tr_b16 a[152:153], v172 offset:1536
ds_read_b64_tr_b16 a[154:155], v174 offset:1536
v_accvgpr_write_b32 a36, 0
v_mfma_f32_16x16x32_bf16 v[84:87], a[140:143], a[196:199], v[84:87]
ds_read_b128 a[208:211], v162 offset:24576
buffer_load_dwordx4 v238, s[64:67], s72 offen sc1 lds
v_mfma_f32_16x16x32_bf16 v[88:91], a[140:143], a[200:203], v[88:91]
v_mfma_f32_16x16x32_bf16 v[92:95], a[140:143], a[204:207], v[92:95]
v_accvgpr_write_b32 a37, 0
s_waitcnt lgkmcnt(3)
v_mfma_f32_16x16x32_bf16 v[96:99], a[144:147], a[192:195], v[96:99]
ds_read_b64_tr_b16 a[156:157], v173 offset:1536
ds_read_b64_tr_b16 a[158:159], v175 offset:1536
buffer_load_dwordx4 v238, s[64:67], s72 offen offset:1024 sc1 lds
v_mfma_f32_16x16x32_bf16 v[100:103], a[144:147], a[196:199], v[100:103]
ds_read_b128 a[212:215], v162 offset:25600
v_mfma_f32_16x16x32_bf16 v[104:107], a[144:147], a[200:203], v[104:107]
s_add_u32 m0, s42, 26624
v_accvgpr_write_b32 a38, 0
v_mfma_f32_16x16x32_bf16 v[108:111], a[144:147], a[204:207], v[108:111]
v_mfma_f32_16x16x32_bf16 v[112:115], a[148:151], a[192:195], v[112:115]
ds_read_b128 a[216:219], v162 offset:26624
ds_read_b64_tr_b16 a[160:161], v172 offset:24576
v_mfma_f32_16x16x32_bf16 v[116:119], a[148:151], a[196:199], v[116:119]
ds_read_b64_tr_b16 a[162:163], v174 offset:24576
buffer_load_dwordx4 v238, s[64:67], s73 offen sc1 lds
v_accvgpr_write_b32 a39, 0
v_mfma_f32_16x16x32_bf16 v[120:123], a[148:151], a[200:203], v[120:123]
v_mfma_f32_16x16x32_bf16 v[124:127], a[148:151], a[204:207], v[124:127]
buffer_load_dwordx4 v238, s[64:67], s73 offen offset:1024 sc1 lds
s_waitcnt lgkmcnt(4)
v_mfma_f32_16x16x32_bf16 v[128:131], a[152:155], a[192:195], v[128:131]
ds_read_b128 a[220:223], v162 offset:27648
ds_read_b64_tr_b16 a[164:165], v173 offset:24576
v_accvgpr_write_b32 a40, 0
v_mfma_f32_16x16x32_bf16 v[132:135], a[152:155], a[196:199], v[132:135]
ds_read_b64_tr_b16 a[166:167], v175 offset:24576
v_mfma_f32_16x16x32_bf16 v[136:139], a[152:155], a[200:203], v[136:139]
s_add_u32 m0, s75, 40960
v_mfma_f32_16x16x32_bf16 v[140:143], a[152:155], a[204:207], v[140:143]
v_accvgpr_write_b32 a41, 0
v_mfma_f32_16x16x32_bf16 v[144:147], a[156:159], a[192:195], v[144:147]
ds_read_b64_tr_b16 a[168:169], v172 offset:25088
ds_read_b64_tr_b16 a[170:171], v174 offset:25088
v_mfma_f32_16x16x32_bf16 v[148:151], a[156:159], a[196:199], v[148:151]
buffer_load_dwordx4 v176, s[68:71], s74 offen lds
v_mfma_f32_16x16x32_bf16 v[152:155], a[156:159], a[200:203], v[152:155]
v_accvgpr_write_b32 a42, 0
v_mfma_f32_16x16x32_bf16 v[156:159], a[156:159], a[204:207], v[156:159]
buffer_load_dwordx4 v177, s[68:71], s74 offen offset:1024 lds
s_waitcnt lgkmcnt(2)
v_mfma_f32_16x16x32_bf16 v[32:35], a[160:163], a[208:211], v[32:35]
ds_read_b64_tr_b16 a[172:173], v173 offset:25088
)ASM"
R"ASM(ds_read_b64_tr_b16 a[174:175], v175 offset:25088
v_mfma_f32_16x16x32_bf16 v[36:39], a[160:163], a[212:215], v[36:39]
v_accvgpr_write_b32 a43, 0
v_mfma_f32_16x16x32_bf16 v[40:43], a[160:163], a[216:219], v[40:43]
s_add_u32 s72, s72, 0x20000
v_mfma_f32_16x16x32_bf16 v[44:47], a[160:163], a[220:223], v[44:47]
v_mfma_f32_16x16x32_bf16 v[48:51], a[164:167], a[208:211], v[48:51]
s_add_u32 s73, s73, 0x20000
ds_read_b64_tr_b16 a[176:177], v172 offset:25600
ds_read_b64_tr_b16 a[178:179], v174 offset:25600
v_accvgpr_write_b32 a44, 0
v_mfma_f32_16x16x32_bf16 v[52:55], a[164:167], a[212:215], v[52:55]
v_mfma_f32_16x16x32_bf16 v[56:59], a[164:167], a[216:219], v[56:59]
v_mfma_f32_16x16x32_bf16 v[60:63], a[164:167], a[220:223], v[60:63]
s_add_u32 s74, s74, 262144
v_accvgpr_write_b32 a45, 0
s_waitcnt lgkmcnt(2)
v_mfma_f32_16x16x32_bf16 v[64:67], a[168:171], a[208:211], v[64:67]
ds_read_b64_tr_b16 a[180:181], v173 offset:25600
ds_read_b64_tr_b16 a[182:183], v175 offset:25600
v_mfma_f32_16x16x32_bf16 v[68:71], a[168:171], a[212:215], v[68:71]
v_mfma_f32_16x16x32_bf16 v[72:75], a[168:171], a[216:219], v[72:75]
s_waitcnt vmcnt(18)
s_barrier
v_accvgpr_write_b32 a46, 0
v_mfma_f32_16x16x32_bf16 v[76:79], a[168:171], a[220:223], v[76:79]
s_add_u32 m0, s42, 65536
v_mfma_f32_16x16x32_bf16 v[80:83], a[172:175], a[208:211], v[80:83]
ds_read_b64_tr_b16 a[184:185], v172 offset:26112
ds_read_b64_tr_b16 a[186:187], v174 offset:26112
v_mfma_f32_16x16x32_bf16 v[84:87], a[172:175], a[212:215], v[84:87]
ds_read_b128 a[192:195], v160
buffer_load_dwordx4 v238, s[64:67], s72 offen sc1 lds
v_accvgpr_write_b32 a47, 0
v_mfma_f32_16x16x32_bf16 v[88:91], a[172:175], a[216:219], v[88:91]
v_mfma_f32_16x16x32_bf16 v[92:95], a[172:175], a[220:223], v[92:95]
s_waitcnt lgkmcnt(3)
v_mfma_f32_16x16x32_bf16 v[96:99], a[176:179], a[208:211], v[96:99]
ds_read_b64_tr_b16 a[188:189], v173 offset:26112
ds_read_b64_tr_b16 a[190:191], v175 offset:26112
buffer_load_dwordx4 v238, s[64:67], s72 offen offset:1024 sc1 lds
v_accvgpr_write_b32 a48, 0
v_mfma_f32_16x16x32_bf16 v[100:103], a[176:179], a[212:215], v[100:103]
ds_read_b128 a[196:199], v160 offset:1024
v_mfma_f32_16x16x32_bf16 v[104:107], a[176:179], a[216:219], v[104:107]
s_add_u32 m0, s42, 67584
v_mfma_f32_16x16x32_bf16 v[108:111], a[176:179], a[220:223], v[108:111]
v_accvgpr_write_b32 a49, 0
v_mfma_f32_16x16x32_bf16 v[112:115], a[180:183], a[208:211], v[112:115]
ds_read_b128 a[200:203], v160 offset:2048
ds_read_b64_tr_b16 a[128:129], v164
v_mfma_f32_16x16x32_bf16 v[116:119], a[180:183], a[212:215], v[116:119]
ds_read_b64_tr_b16 a[130:131], v166
buffer_load_dwordx4 v238, s[64:67], s73 offen sc1 lds
v_mfma_f32_16x16x32_bf16 v[120:123], a[180:183], a[216:219], v[120:123]
v_accvgpr_write_b32 a50, 0
v_mfma_f32_16x16x32_bf16 v[124:127], a[180:183], a[220:223], v[124:127]
buffer_load_dwordx4 v238, s[64:67], s73 offen offset:1024 sc1 lds
s_waitcnt lgkmcnt(4)
v_mfma_f32_16x16x32_bf16 v[128:131], a[184:187], a[208:211], v[128:131]
ds_read_b128 a[204:207], v160 offset:3072
ds_read_b64_tr_b16 a[132:133], v165
v_mfma_f32_16x16x32_bf16 v[132:135], a[184:187], a[212:215], v[132:135]
ds_read_b64_tr_b16 a[134:135], v167
v_accvgpr_write_b32 a51, 0
v_mfma_f32_16x16x32_bf16 v[136:139], a[184:187], a[216:219], v[136:139]
s_add_u32 m0, s75, 81920
v_mfma_f32_16x16x32_bf16 v[140:143], a[184:187], a[220:223], v[140:143]
v_mfma_f32_16x16x32_bf16 v[144:147], a[188:191], a[208:211], v[144:147]
ds_read_b64_tr_b16 a[136:137], v164 offset:512
ds_read_b64_tr_b16 a[138:139], v166 offset:512
v_accvgpr_write_b32 a52, 0
v_mfma_f32_16x16x32_bf16 v[148:151], a[188:191], a[212:215], v[148:151]
buffer_load_dwordx4 v176, s[68:71], s74 offen lds
v_mfma_f32_16x16x32_bf16 v[152:155], a[188:191], a[216:219], v[152:155]
v_mfma_f32_16x16x32_bf16 v[156:159], a[188:191], a[220:223], v[156:159]
buffer_load_dwordx4 v177, s[68:71], s74 offen offset:1024 lds
v_accvgpr_write_b32 a53, 0
s_waitcnt lgkmcnt(2)
v_mfma_f32_16x16x32_bf16 v[32:35], a[128:131], a[192:195], v[32:35]
ds_read_b64_tr_b16 a[140:141], v165 offset:512
ds_read_b64_tr_b16 a[142:143], v167 offset:512
v_mfma_f32_16x16x32_bf16 v[36:39], a[128:131], a[196:199], v[36:39]
v_mfma_f32_16x16x32_bf16 v[40:43], a[128:131], a[200:203], v[40:43]
s_add_u32 s72, s72, 0x20000
v_accvgpr_write_b32 a54, 0
v_mfma_f32_16x16x32_bf16 v[44:47], a[128:131], a[204:207], v[44:47]
v_mfma_f32_16x16x32_bf16 v[48:51], a[132:135], a[192:195], v[48:51]
s_add_u32 s73, s73, 0x20000
ds_read_b64_tr_b16 a[144:145], v164 offset:1024
ds_read_b64_tr_b16 a[146:147], v166 offset:1024
v_mfma_f32_16x16x32_bf16 v[52:55], a[132:135], a[196:199], v[52:55]
v_accvgpr_write_b32 a55, 0
v_mfma_f32_16x16x32_bf16 v[56:59], a[132:135], a[200:203], v[56:59]
v_mfma_f32_16x16x32_bf16 v[60:63], a[132:135], a[204:207], v[60:63]
s_add_u32 s74, s74, 262144
s_waitcnt lgkmcnt(2)
v_mfma_f32_16x16x32_bf16 v[64:67], a[136:139], a[192:195], v[64:67]
ds_read_b64_tr_b16 a[148:149], v165 offset:1024
ds_read_b64_tr_b16 a[150:151], v167 offset:1024
v_accvgpr_write_b32 a56, 0
v_mfma_f32_16x16x32_bf16 v[68:71], a[136:139], a[196:199], v[68:71]
v_mfma_f32_16x16x32_bf16 v[72:75], a[136:139], a[200:203], v[72:75]
s_waitcnt vmcnt(18)
s_barrier
v_mfma_f32_16x16x32_bf16 v[76:79], a[136:139], a[204:207], v[76:79]
s_add_u32 m0, s42, 90112
v_accvgpr_write_b32 a57, 0
v_mfma_f32_16x16x32_bf16 v[80:83], a[140:143], a[192:195], v[80:83]
ds_read_b64_tr_b16 a[152:153], v164 offset:1536
ds_read_b64_tr_b16 a[154:155], v166 offset:1536
v_mfma_f32_16x16x32_bf16 v[84:87], a[140:143], a[196:199], v[84:87]
ds_read_b128 a[208:211], v160 offset:24576
buffer_load_dwordx4 v238, s[64:67], s72 offen sc1 lds
v_mfma_f32_16x16x32_bf16 v[88:91], a[140:143], a[200:203], v[88:91]
v_accvgpr_write_b32 a58, 0
v_mfma_f32_16x16x32_bf16 v[92:95], a[140:143], a[204:207], v[92:95]
s_waitcnt lgkmcnt(3)
v_mfma_f32_16x16x32_bf16 v[96:99], a[144:147], a[192:195], v[96:99]
ds_read_b64_tr_b16 a[156:157], v165 offset:1536
ds_read_b64_tr_b16 a[158:159], v167 offset:1536
buffer_load_dwordx4 v238, s[64:67], s72 offen offset:1024 sc1 lds
v_mfma_f32_16x16x32_bf16 v[100:103], a[144:147], a[196:199], v[100:103]
ds_read_b128 a[212:215], v160 offset:25600
v_accvgpr_write_b32 a59, 0
v_mfma_f32_16x16x32_bf16 v[104:107], a[144:147], a[200:203], v[104:107]
s_add_u32 m0, s42, 92160
v_mfma_f32_16x16x32_bf16 v[108:111], a[144:147], a[204:207], v[108:111]
v_mfma_f32_16x16x32_bf16 v[112:115], a[148:151], a[192:195], v[112:115]
ds_read_b128 a[216:219], v160 offset:26624
ds_read_b64_tr_b16 a[160:161], v164 offset:24576
v_accvgpr_write_b32 a60, 0
v_mfma_f32_16x16x32_bf16 v[116:119], a[148:151], a[196:199], v[116:119]
ds_read_b64_tr_b16 a[162:163], v166 offset:24576
buffer_load_dwordx4 v238, s[64:67], s73 offen sc1 lds
v_mfma_f32_16x16x32_bf16 v[120:123], a[148:151], a[200:203], v[120:123]
v_mfma_f32_16x16x32_bf16 v[124:127], a[148:151], a[204:207], v[124:127]
buffer_load_dwordx4 v238, s[64:67], s73 offen offset:1024 sc1 lds
v_accvgpr_write_b32 a61, 0
s_waitcnt lgkmcnt(4)
v_mfma_f32_16x16x32_bf16 v[128:131], a[152:155], a[192:195], v[128:131]
ds_read_b128 a[220:223], v160 offset:27648
ds_read_b64_tr_b16 a[164:165], v165 offset:24576
v_mfma_f32_16x16x32_bf16 v[132:135], a[152:155], a[196:199], v[132:135]
ds_read_b64_tr_b16 a[166:167], v167 offset:24576
v_mfma_f32_16x16x32_bf16 v[136:139], a[152:155], a[200:203], v[136:139]
s_add_u32 m0, s75, 106496
v_accvgpr_write_b32 a62, 0
v_mfma_f32_16x16x32_bf16 v[140:143], a[152:155], a[204:207], v[140:143]
v_mfma_f32_16x16x32_bf16 v[144:147], a[156:159], a[192:195], v[144:147]
ds_read_b64_tr_b16 a[168:169], v164 offset:25088
ds_read_b64_tr_b16 a[170:171], v166 offset:25088
v_mfma_f32_16x16x32_bf16 v[148:151], a[156:159], a[196:199], v[148:151]
buffer_load_dwordx4 v176, s[68:71], s74 offen lds
v_accvgpr_write_b32 a63, 0
v_mfma_f32_16x16x32_bf16 v[152:155], a[156:159], a[200:203], v[152:155]
v_mfma_f32_16x16x32_bf16 v[156:159], a[156:159], a[204:207], v[156:159]
buffer_load_dwordx4 v177, s[68:71], s74 offen offset:1024 lds
s_waitcnt lgkmcnt(2)
v_mfma_f32_16x16x32_bf16 v[32:35], a[160:163], a[208:211], v[32:35]
ds_read_b64_tr_b16 a[172:173], v165 offset:25088
ds_read_b64_tr_b16 a[174:175], v167 offset:25088
v_accvgpr_write_b32 a64, 0
v_mfma_f32_16x16x32_bf16 v[36:39], a[160:163], a[212:215], v[36:39]
v_mfma_f32_16x16x32_bf16 v[40:43], a[160:163], a[216:219], v[40:43]
s_add_u32 s72, s72, 0x20000
v_mfma_f32_16x16x32_bf16 v[44:47], a[160:163], a[220:223], v[44:47]
v_accvgpr_write_b32 a65, 0
v_mfma_f32_16x16x32_bf16 v[48:51], a[164:167], a[208:211], v[48:51]
s_add_u32 s73, s73, 0x20000
ds_read_b64_tr_b16 a[176:177], v164 offset:25600
ds_read_b64_tr_b16 a[178:179], v166 offset:25600
v_mfma_f32_16x16x32_bf16 v[52:55], a[164:167], a[212:215], v[52:55]
v_mfma_f32_16x16x32_bf16 v[56:59], a[164:167], a[216:219], v[56:59]
v_accvgpr_write_b32 a66, 0
v_mfma_f32_16x16x32_bf16 v[60:63], a[164:167], a[220:223], v[60:63]
s_add_u32 s74, s74, 262144
s_waitcnt lgkmcnt(2)
v_mfma_f32_16x16x32_bf16 v[64:67], a[168:171], a[208:211], v[64:67]
ds_read_b64_tr_b16 a[180:181], v165 offset:25600
ds_read_b64_tr_b16 a[182:183], v167 offset:25600
v_mfma_f32_16x16x32_bf16 v[68:71], a[168:171], a[212:215], v[68:71]
v_accvgpr_write_b32 a67, 0
v_mfma_f32_16x16x32_bf16 v[72:75], a[168:171], a[216:219], v[72:75]
s_waitcnt vmcnt(18)
s_barrier
v_mfma_f32_16x16x32_bf16 v[76:79], a[168:171], a[220:223], v[76:79]
s_add_u32 m0, s42, 114688
v_mfma_f32_16x16x32_bf16 v[80:83], a[172:175], a[208:211], v[80:83]
ds_read_b64_tr_b16 a[184:185], v164 offset:26112
ds_read_b64_tr_b16 a[186:187], v166 offset:26112
v_accvgpr_write_b32 a68, 0
v_mfma_f32_16x16x32_bf16 v[84:87], a[172:175], a[212:215], v[84:87]
ds_read_b128 a[192:195], v161
buffer_load_dwordx4 v238, s[64:67], s72 offen sc1 lds
v_mfma_f32_16x16x32_bf16 v[88:91], a[172:175], a[216:219], v[88:91]
v_mfma_f32_16x16x32_bf16 v[92:95], a[172:175], a[220:223], v[92:95]
v_accvgpr_write_b32 a69, 0
s_waitcnt lgkmcnt(3)
v_mfma_f32_16x16x32_bf16 v[96:99], a[176:179], a[208:211], v[96:99]
ds_read_b64_tr_b16 a[188:189], v165 offset:26112
ds_read_b64_tr_b16 a[190:191], v167 offset:26112
buffer_load_dwordx4 v238, s[64:67], s72 offen offset:1024 sc1 lds
v_mfma_f32_16x16x32_bf16 v[100:103], a[176:179], a[212:215], v[100:103]
ds_read_b128 a[196:199], v161 offset:1024
v_mfma_f32_16x16x32_bf16 v[104:107], a[176:179], a[216:219], v[104:107]
s_add_u32 m0, s42, 116736
v_accvgpr_write_b32 a70, 0
v_mfma_f32_16x16x32_bf16 v[108:111], a[176:179], a[220:223], v[108:111]
v_mfma_f32_16x16x32_bf16 v[112:115], a[180:183], a[208:211], v[112:115]
ds_read_b128 a[200:203], v161 offset:2048
ds_read_b64_tr_b16 a[128:129], v168
v_mfma_f32_16x16x32_bf16 v[116:119], a[180:183], a[212:215], v[116:119]
ds_read_b64_tr_b16 a[130:131], v170
buffer_load_dwordx4 v238, s[64:67], s73 offen sc1 lds
v_accvgpr_write_b32 a71, 0
v_mfma_f32_16x16x32_bf16 v[120:123], a[180:183], a[216:219], v[120:123]
v_mfma_f32_16x16x32_bf16 v[124:127], a[180:183], a[220:223], v[124:127]
buffer_load_dwordx4 v238, s[64:67], s73 offen offset:1024 sc1 lds
s_waitcnt lgkmcnt(4)
v_mfma_f32_16x16x32_bf16 v[128:131], a[184:187], a[208:211], v[128:131]
ds_read_b128 a[204:207], v161 offset:3072
ds_read_b64_tr_b16 a[132:133], v169
v_accvgpr_write_b32 a72, 0
v_mfma_f32_16x16x32_bf16 v[132:135], a[184:187], a[212:215], v[132:135]
ds_read_b64_tr_b16 a[134:135], v171
v_mfma_f32_16x16x32_bf16 v[136:139], a[184:187], a[216:219], v[136:139]
s_add_u32 m0, s75, 131072
v_mfma_f32_16x16x32_bf16 v[140:143], a[184:187], a[220:223], v[140:143]
v_accvgpr_write_b32 a73, 0
v_mfma_f32_16x16x32_bf16 v[144:147], a[188:191], a[208:211], v[144:147]
ds_read_b64_tr_b16 a[136:137], v168 offset:512
ds_read_b64_tr_b16 a[138:139], v170 offset:512
v_mfma_f32_16x16x32_bf16 v[148:151], a[188:191], a[212:215], v[148:151]
buffer_load_dwordx4 v176, s[68:71], s74 offen lds
v_mfma_f32_16x16x32_bf16 v[152:155], a[188:191], a[216:219], v[152:155]
v_accvgpr_write_b32 a74, 0
v_mfma_f32_16x16x32_bf16 v[156:159], a[188:191], a[220:223], v[156:159]
buffer_load_dwordx4 v177, s[68:71], s74 offen offset:1024 lds
)ASM"
R"ASM(s_waitcnt lgkmcnt(2)
v_mfma_f32_16x16x32_bf16 v[32:35], a[128:131], a[192:195], v[32:35]
ds_read_b64_tr_b16 a[140:141], v169 offset:512
ds_read_b64_tr_b16 a[142:143], v171 offset:512
v_mfma_f32_16x16x32_bf16 v[36:39], a[128:131], a[196:199], v[36:39]
v_accvgpr_write_b32 a75, 0
v_mfma_f32_16x16x32_bf16 v[40:43], a[128:131], a[200:203], v[40:43]
s_add_u32 s72, s72, 0x20000
v_mfma_f32_16x16x32_bf16 v[44:47], a[128:131], a[204:207], v[44:47]
v_mfma_f32_16x16x32_bf16 v[48:51], a[132:135], a[192:195], v[48:51]
s_add_u32 s73, s73, 0x20000
ds_read_b64_tr_b16 a[144:145], v168 offset:1024
ds_read_b64_tr_b16 a[146:147], v170 offset:1024
v_accvgpr_write_b32 a76, 0
v_mfma_f32_16x16x32_bf16 v[52:55], a[132:135], a[196:199], v[52:55]
v_mfma_f32_16x16x32_bf16 v[56:59], a[132:135], a[200:203], v[56:59]
v_mfma_f32_16x16x32_bf16 v[60:63], a[132:135], a[204:207], v[60:63]
s_add_u32 s74, s74, 262144
v_accvgpr_write_b32 a77, 0
s_waitcnt lgkmcnt(2)
v_mfma_f32_16x16x32_bf16 v[64:67], a[136:139], a[192:195], v[64:67]
ds_read_b64_tr_b16 a[148:149], v169 offset:1024
ds_read_b64_tr_b16 a[150:151], v171 offset:1024
v_mfma_f32_16x16x32_bf16 v[68:71], a[136:139], a[196:199], v[68:71]
v_mfma_f32_16x16x32_bf16 v[72:75], a[136:139], a[200:203], v[72:75]
s_waitcnt vmcnt(18)
s_barrier
s_cmp_lg_u32 s43, 0
s_cbranch_scc1 L_fs1_%=
v_mov_b32 v200, s78
v_mov_b32 v201, s79
s_mov_b64 exec, 1
v_mov_b32 v202, %[fkv]
global_store_dwordx2 v202, v[200:201], %[flags] sc1
v_mov_b32 v203, %[fd1]
global_store_dwordx2 v203, v[200:201], %[flags] sc1
v_mov_b32 v204, %[fd2]
global_store_dwordx2 v204, v[200:201], %[flags] sc1
s_mov_b64 exec, -1
L_fs1_%=:
v_accvgpr_write_b32 a78, 0
v_mfma_f32_16x16x32_bf16 v[76:79], a[136:139], a[204:207], v[76:79]
s_add_u32 m0, s42, 139264
v_mfma_f32_16x16x32_bf16 v[80:83], a[140:143], a[192:195], v[80:83]
ds_read_b64_tr_b16 a[152:153], v168 offset:1536
ds_read_b64_tr_b16 a[154:155], v170 offset:1536
v_mfma_f32_16x16x32_bf16 v[84:87], a[140:143], a[196:199], v[84:87]
ds_read_b128 a[208:211], v161 offset:24576
buffer_load_dwordx4 v238, s[64:67], s72 offen sc1 lds
v_accvgpr_write_b32 a79, 0
v_mfma_f32_16x16x32_bf16 v[88:91], a[140:143], a[200:203], v[88:91]
v_mfma_f32_16x16x32_bf16 v[92:95], a[140:143], a[204:207], v[92:95]
s_waitcnt lgkmcnt(3)
v_mfma_f32_16x16x32_bf16 v[96:99], a[144:147], a[192:195], v[96:99]
ds_read_b64_tr_b16 a[156:157], v169 offset:1536
ds_read_b64_tr_b16 a[158:159], v171 offset:1536
buffer_load_dwordx4 v238, s[64:67], s72 offen offset:1024 sc1 lds
v_accvgpr_write_b32 a80, 0
v_mfma_f32_16x16x32_bf16 v[100:103], a[144:147], a[196:199], v[100:103]
ds_read_b128 a[212:215], v161 offset:25600
v_mfma_f32_16x16x32_bf16 v[104:107], a[144:147], a[200:203], v[104:107]
s_add_u32 m0, s42, 141312
v_mfma_f32_16x16x32_bf16 v[108:111], a[144:147], a[204:207], v[108:111]
v_accvgpr_write_b32 a81, 0
v_mfma_f32_16x16x32_bf16 v[112:115], a[148:151], a[192:195], v[112:115]
ds_read_b128 a[216:219], v161 offset:26624
ds_read_b64_tr_b16 a[160:161], v168 offset:24576
v_mfma_f32_16x16x32_bf16 v[116:119], a[148:151], a[196:199], v[116:119]
ds_read_b64_tr_b16 a[162:163], v170 offset:24576
buffer_load_dwordx4 v238, s[64:67], s73 offen sc1 lds
v_mfma_f32_16x16x32_bf16 v[120:123], a[148:151], a[200:203], v[120:123]
v_accvgpr_write_b32 a82, 0
v_mfma_f32_16x16x32_bf16 v[124:127], a[148:151], a[204:207], v[124:127]
buffer_load_dwordx4 v238, s[64:67], s73 offen offset:1024 sc1 lds
s_waitcnt lgkmcnt(4)
v_mfma_f32_16x16x32_bf16 v[128:131], a[152:155], a[192:195], v[128:131]
ds_read_b128 a[220:223], v161 offset:27648
ds_read_b64_tr_b16 a[164:165], v169 offset:24576
v_mfma_f32_16x16x32_bf16 v[132:135], a[152:155], a[196:199], v[132:135]
ds_read_b64_tr_b16 a[166:167], v171 offset:24576
v_accvgpr_write_b32 a83, 0
v_mfma_f32_16x16x32_bf16 v[136:139], a[152:155], a[200:203], v[136:139]
s_add_u32 m0, s75, 155648
v_mfma_f32_16x16x32_bf16 v[140:143], a[152:155], a[204:207], v[140:143]
v_mfma_f32_16x16x32_bf16 v[144:147], a[156:159], a[192:195], v[144:147]
ds_read_b64_tr_b16 a[168:169], v168 offset:25088
ds_read_b64_tr_b16 a[170:171], v170 offset:25088
v_accvgpr_write_b32 a84, 0
v_mfma_f32_16x16x32_bf16 v[148:151], a[156:159], a[196:199], v[148:151]
buffer_load_dwordx4 v176, s[68:71], s74 offen lds
v_mfma_f32_16x16x32_bf16 v[152:155], a[156:159], a[200:203], v[152:155]
v_mfma_f32_16x16x32_bf16 v[156:159], a[156:159], a[204:207], v[156:159]
buffer_load_dwordx4 v177, s[68:71], s74 offen offset:1024 lds
v_accvgpr_write_b32 a85, 0
s_waitcnt lgkmcnt(2)
v_mfma_f32_16x16x32_bf16 v[32:35], a[160:163], a[208:211], v[32:35]
ds_read_b64_tr_b16 a[172:173], v169 offset:25088
ds_read_b64_tr_b16 a[174:175], v171 offset:25088
v_mfma_f32_16x16x32_bf16 v[36:39], a[160:163], a[212:215], v[36:39]
v_mfma_f32_16x16x32_bf16 v[40:43], a[160:163], a[216:219], v[40:43]
s_add_u32 s72, s72, 0x20000
v_accvgpr_write_b32 a86, 0
v_mfma_f32_16x16x32_bf16 v[44:47], a[160:163], a[220:223], v[44:47]
v_mfma_f32_16x16x32_bf16 v[48:51], a[164:167], a[208:211], v[48:51]
s_add_u32 s73, s73, 0x20000
ds_read_b64_tr_b16 a[176:177], v168 offset:25600
ds_read_b64_tr_b16 a[178:179], v170 offset:25600
v_mfma_f32_16x16x32_bf16 v[52:55], a[164:167], a[212:215], v[52:55]
v_accvgpr_write_b32 a87, 0
v_mfma_f32_16x16x32_bf16 v[56:59], a[164:167], a[216:219], v[56:59]
v_mfma_f32_16x16x32_bf16 v[60:63], a[164:167], a[220:223], v[60:63]
s_add_u32 s74, s74, 262144
s_waitcnt lgkmcnt(2)
v_mfma_f32_16x16x32_bf16 v[64:67], a[168:171], a[208:211], v[64:67]
ds_read_b64_tr_b16 a[180:181], v169 offset:25600
ds_read_b64_tr_b16 a[182:183], v171 offset:25600
v_accvgpr_write_b32 a88, 0
v_mfma_f32_16x16x32_bf16 v[68:71], a[168:171], a[212:215], v[68:71]
v_mfma_f32_16x16x32_bf16 v[72:75], a[168:171], a[216:219], v[72:75]
s_waitcnt vmcnt(18)
s_barrier
v_mfma_f32_16x16x32_bf16 v[76:79], a[168:171], a[220:223], v[76:79]
s_add_u32 m0, s42, 0
v_accvgpr_write_b32 a89, 0
v_mfma_f32_16x16x32_bf16 v[80:83], a[172:175], a[208:211], v[80:83]
ds_read_b64_tr_b16 a[184:185], v168 offset:26112
ds_read_b64_tr_b16 a[186:187], v170 offset:26112
v_mfma_f32_16x16x32_bf16 v[84:87], a[172:175], a[212:215], v[84:87]
ds_read_b128 a[192:195], v162
buffer_load_dwordx4 v238, s[64:67], s72 offen sc1 lds
v_mfma_f32_16x16x32_bf16 v[88:91], a[172:175], a[216:219], v[88:91]
v_accvgpr_write_b32 a90, 0
v_mfma_f32_16x16x32_bf16 v[92:95], a[172:175], a[220:223], v[92:95]
s_waitcnt lgkmcnt(3)
v_mfma_f32_16x16x32_bf16 v[96:99], a[176:179], a[208:211], v[96:99]
ds_read_b64_tr_b16 a[188:189], v169 offset:26112
ds_read_b64_tr_b16 a[190:191], v171 offset:26112
buffer_load_dwordx4 v238, s[64:67], s72 offen offset:1024 sc1 lds
v_mfma_f32_16x16x32_bf16 v[100:103], a[176:179], a[212:215], v[100:103]
ds_read_b128 a[196:199], v162 offset:1024
v_accvgpr_write_b32 a91, 0
v_mfma_f32_16x16x32_bf16 v[104:107], a[176:179], a[216:219], v[104:107]
s_add_u32 m0, s42, 2048
v_mfma_f32_16x16x32_bf16 v[108:111], a[176:179], a[220:223], v[108:111]
v_mfma_f32_16x16x32_bf16 v[112:115], a[180:183], a[208:211], v[112:115]
ds_read_b128 a[200:203], v162 offset:2048
ds_read_b64_tr_b16 a[128:129], v172
v_accvgpr_write_b32 a92, 0
v_mfma_f32_16x16x32_bf16 v[116:119], a[180:183], a[212:215], v[116:119]
ds_read_b64_tr_b16 a[130:131], v174
buffer_load_dwordx4 v238, s[64:67], s73 offen sc1 lds
v_mfma_f32_16x16x32_bf16 v[120:123], a[180:183], a[216:219], v[120:123]
v_mfma_f32_16x16x32_bf16 v[124:127], a[180:183], a[220:223], v[124:127]
buffer_load_dwordx4 v238, s[64:67], s73 offen offset:1024 sc1 lds
v_accvgpr_write_b32 a93, 0
s_waitcnt lgkmcnt(4)
v_mfma_f32_16x16x32_bf16 v[128:131], a[184:187], a[208:211], v[128:131]
ds_read_b128 a[204:207], v162 offset:3072
ds_read_b64_tr_b16 a[132:133], v173
v_mfma_f32_16x16x32_bf16 v[132:135], a[184:187], a[212:215], v[132:135]
ds_read_b64_tr_b16 a[134:135], v175
v_mfma_f32_16x16x32_bf16 v[136:139], a[184:187], a[216:219], v[136:139]
s_add_u32 m0, s75, 16384
v_accvgpr_write_b32 a94, 0
v_mfma_f32_16x16x32_bf16 v[140:143], a[184:187], a[220:223], v[140:143]
v_mfma_f32_16x16x32_bf16 v[144:147], a[188:191], a[208:211], v[144:147]
ds_read_b64_tr_b16 a[136:137], v172 offset:512
ds_read_b64_tr_b16 a[138:139], v174 offset:512
v_mfma_f32_16x16x32_bf16 v[148:151], a[188:191], a[212:215], v[148:151]
buffer_load_dwordx4 v176, s[68:71], s74 offen lds
v_accvgpr_write_b32 a95, 0
v_mfma_f32_16x16x32_bf16 v[152:155], a[188:191], a[216:219], v[152:155]
v_mfma_f32_16x16x32_bf16 v[156:159], a[188:191], a[220:223], v[156:159]
buffer_load_dwordx4 v177, s[68:71], s74 offen offset:1024 lds
s_waitcnt lgkmcnt(2)
v_mfma_f32_16x16x32_bf16 v[32:35], a[128:131], a[192:195], v[32:35]
ds_read_b64_tr_b16 a[140:141], v173 offset:512
ds_read_b64_tr_b16 a[142:143], v175 offset:512
v_accvgpr_write_b32 a96, 0
v_mfma_f32_16x16x32_bf16 v[36:39], a[128:131], a[196:199], v[36:39]
v_mfma_f32_16x16x32_bf16 v[40:43], a[128:131], a[200:203], v[40:43]
s_add_u32 s72, s72, 0x20000
v_mfma_f32_16x16x32_bf16 v[44:47], a[128:131], a[204:207], v[44:47]
v_accvgpr_write_b32 a97, 0
v_mfma_f32_16x16x32_bf16 v[48:51], a[132:135], a[192:195], v[48:51]
s_add_u32 s73, s73, 0x20000
ds_read_b64_tr_b16 a[144:145], v172 offset:1024
ds_read_b64_tr_b16 a[146:147], v174 offset:1024
v_mfma_f32_16x16x32_bf16 v[52:55], a[132:135], a[196:199], v[52:55]
v_mfma_f32_16x16x32_bf16 v[56:59], a[132:135], a[200:203], v[56:59]
v_accvgpr_write_b32 a98, 0
v_mfma_f32_16x16x32_bf16 v[60:63], a[132:135], a[204:207], v[60:63]
s_add_u32 s74, s74, 262144
s_waitcnt lgkmcnt(2)
v_mfma_f32_16x16x32_bf16 v[64:67], a[136:139], a[192:195], v[64:67]
ds_read_b64_tr_b16 a[148:149], v173 offset:1024
ds_read_b64_tr_b16 a[150:151], v175 offset:1024
v_mfma_f32_16x16x32_bf16 v[68:71], a[136:139], a[196:199], v[68:71]
v_accvgpr_write_b32 a99, 0
v_mfma_f32_16x16x32_bf16 v[72:75], a[136:139], a[200:203], v[72:75]
s_waitcnt vmcnt(18)
s_barrier
v_mfma_f32_16x16x32_bf16 v[76:79], a[136:139], a[204:207], v[76:79]
s_add_u32 m0, s42, 24576
v_mfma_f32_16x16x32_bf16 v[80:83], a[140:143], a[192:195], v[80:83]
ds_read_b64_tr_b16 a[152:153], v172 offset:1536
ds_read_b64_tr_b16 a[154:155], v174 offset:1536
v_accvgpr_write_b32 a100, 0
v_mfma_f32_16x16x32_bf16 v[84:87], a[140:143], a[196:199], v[84:87]
ds_read_b128 a[208:211], v162 offset:24576
buffer_load_dwordx4 v238, s[64:67], s72 offen sc1 lds
v_mfma_f32_16x16x32_bf16 v[88:91], a[140:143], a[200:203], v[88:91]
v_mfma_f32_16x16x32_bf16 v[92:95], a[140:143], a[204:207], v[92:95]
v_accvgpr_write_b32 a101, 0
s_waitcnt lgkmcnt(3)
v_mfma_f32_16x16x32_bf16 v[96:99], a[144:147], a[192:195], v[96:99]
ds_read_b64_tr_b16 a[156:157], v173 offset:1536
ds_read_b64_tr_b16 a[158:159], v175 offset:1536
buffer_load_dwordx4 v238, s[64:67], s72 offen offset:1024 sc1 lds
v_mfma_f32_16x16x32_bf16 v[100:103], a[144:147], a[196:199], v[100:103]
ds_read_b128 a[212:215], v162 offset:25600
v_mfma_f32_16x16x32_bf16 v[104:107], a[144:147], a[200:203], v[104:107]
s_add_u32 m0, s42, 26624
v_accvgpr_write_b32 a102, 0
v_mfma_f32_16x16x32_bf16 v[108:111], a[144:147], a[204:207], v[108:111]
v_mfma_f32_16x16x32_bf16 v[112:115], a[148:151], a[192:195], v[112:115]
ds_read_b128 a[216:219], v162 offset:26624
ds_read_b64_tr_b16 a[160:161], v172 offset:24576
v_mfma_f32_16x16x32_bf16 v[116:119], a[148:151], a[196:199], v[116:119]
ds_read_b64_tr_b16 a[162:163], v174 offset:24576
buffer_load_dwordx4 v238, s[64:67], s73 offen sc1 lds
v_accvgpr_write_b32 a103, 0
v_mfma_f32_16x16x32_bf16 v[120:123], a[148:151], a[200:203], v[120:123]
v_mfma_f32_16x16x32_bf16 v[124:127], a[148:151], a[204:207], v[124:127]
buffer_load_dwordx4 v238, s[64:67], s73 offen offset:1024 sc1 lds
s_waitcnt lgkmcnt(4)
v_mfma_f32_16x16x32_bf16 v[128:131], a[152:155], a[192:195], v[128:131]
ds_read_b128 a[220:223], v162 offset:27648
ds_read_b64_tr_b16 a[164:165], v173 offset:24576
v_accvgpr_write_b32 a104, 0
v_mfma_f32_16x16x32_bf16 v[132:135], a[152:155], a[196:199], v[132:135]
ds_read_b64_tr_b16 a[166:167], v175 offset:24576
v_mfma_f32_16x16x32_bf16 v[136:139], a[152:155], a[200:203], v[136:139]
s_add_u32 m0, s75, 40960
v_mfma_f32_16x16x32_bf16 v[140:143], a[152:155], a[204:207], v[140:143]
)ASM"
R"ASM(v_accvgpr_write_b32 a105, 0
v_mfma_f32_16x16x32_bf16 v[144:147], a[156:159], a[192:195], v[144:147]
ds_read_b64_tr_b16 a[168:169], v172 offset:25088
ds_read_b64_tr_b16 a[170:171], v174 offset:25088
v_mfma_f32_16x16x32_bf16 v[148:151], a[156:159], a[196:199], v[148:151]
buffer_load_dwordx4 v176, s[68:71], s74 offen lds
v_mfma_f32_16x16x32_bf16 v[152:155], a[156:159], a[200:203], v[152:155]
v_accvgpr_write_b32 a106, 0
v_mfma_f32_16x16x32_bf16 v[156:159], a[156:159], a[204:207], v[156:159]
buffer_load_dwordx4 v177, s[68:71], s74 offen offset:1024 lds
s_waitcnt lgkmcnt(2)
v_mfma_f32_16x16x32_bf16 v[32:35], a[160:163], a[208:211], v[32:35]
ds_read_b64_tr_b16 a[172:173], v173 offset:25088
ds_read_b64_tr_b16 a[174:175], v175 offset:25088
v_mfma_f32_16x16x32_bf16 v[36:39], a[160:163], a[212:215], v[36:39]
v_accvgpr_write_b32 a107, 0
v_mfma_f32_16x16x32_bf16 v[40:43], a[160:163], a[216:219], v[40:43]
s_add_u32 s72, s72, 0x20000
v_mfma_f32_16x16x32_bf16 v[44:47], a[160:163], a[220:223], v[44:47]
v_mfma_f32_16x16x32_bf16 v[48:51], a[164:167], a[208:211], v[48:51]
s_add_u32 s73, s73, 0x20000
ds_read_b64_tr_b16 a[176:177], v172 offset:25600
ds_read_b64_tr_b16 a[178:179], v174 offset:25600
v_accvgpr_write_b32 a108, 0
v_mfma_f32_16x16x32_bf16 v[52:55], a[164:167], a[212:215], v[52:55]
v_mfma_f32_16x16x32_bf16 v[56:59], a[164:167], a[216:219], v[56:59]
v_mfma_f32_16x16x32_bf16 v[60:63], a[164:167], a[220:223], v[60:63]
s_add_u32 s74, s74, 262144
v_accvgpr_write_b32 a109, 0
s_waitcnt lgkmcnt(2)
v_mfma_f32_16x16x32_bf16 v[64:67], a[168:171], a[208:211], v[64:67]
ds_read_b64_tr_b16 a[180:181], v173 offset:25600
ds_read_b64_tr_b16 a[182:183], v175 offset:25600
v_mfma_f32_16x16x32_bf16 v[68:71], a[168:171], a[212:215], v[68:71]
v_mfma_f32_16x16x32_bf16 v[72:75], a[168:171], a[216:219], v[72:75]
s_waitcnt vmcnt(18)
s_barrier
v_accvgpr_write_b32 a110, 0
v_mfma_f32_16x16x32_bf16 v[76:79], a[168:171], a[220:223], v[76:79]
s_add_u32 m0, s42, 65536
v_mfma_f32_16x16x32_bf16 v[80:83], a[172:175], a[208:211], v[80:83]
ds_read_b64_tr_b16 a[184:185], v172 offset:26112
ds_read_b64_tr_b16 a[186:187], v174 offset:26112
v_mfma_f32_16x16x32_bf16 v[84:87], a[172:175], a[212:215], v[84:87]
ds_read_b128 a[192:195], v160
buffer_load_dwordx4 v238, s[64:67], s72 offen sc1 lds
v_accvgpr_write_b32 a111, 0
v_mfma_f32_16x16x32_bf16 v[88:91], a[172:175], a[216:219], v[88:91]
v_mfma_f32_16x16x32_bf16 v[92:95], a[172:175], a[220:223], v[92:95]
s_waitcnt lgkmcnt(3)
v_mfma_f32_16x16x32_bf16 v[96:99], a[176:179], a[208:211], v[96:99]
ds_read_b64_tr_b16 a[188:189], v173 offset:26112
ds_read_b64_tr_b16 a[190:191], v175 offset:26112
buffer_load_dwordx4 v238, s[64:67], s72 offen offset:1024 sc1 lds
v_accvgpr_write_b32 a112, 0
v_mfma_f32_16x16x32_bf16 v[100:103], a[176:179], a[212:215], v[100:103]
ds_read_b128 a[196:199], v160 offset:1024
v_mfma_f32_16x16x32_bf16 v[104:107], a[176:179], a[216:219], v[104:107]
s_add_u32 m0, s42, 67584
v_mfma_f32_16x16x32_bf16 v[108:111], a[176:179], a[220:223], v[108:111]
v_accvgpr_write_b32 a113, 0
v_mfma_f32_16x16x32_bf16 v[112:115], a[180:183], a[208:211], v[112:115]
ds_read_b128 a[200:203], v160 offset:2048
ds_read_b64_tr_b16 a[128:129], v164
v_mfma_f32_16x16x32_bf16 v[116:119], a[180:183], a[212:215], v[116:119]
ds_read_b64_tr_b16 a[130:131], v166
buffer_load_dwordx4 v238, s[64:67], s73 offen sc1 lds
v_mfma_f32_16x16x32_bf16 v[120:123], a[180:183], a[216:219], v[120:123]
v_accvgpr_write_b32 a114, 0
v_mfma_f32_16x16x32_bf16 v[124:127], a[180:183], a[220:223], v[124:127]
buffer_load_dwordx4 v238, s[64:67], s73 offen offset:1024 sc1 lds
s_waitcnt lgkmcnt(4)
v_mfma_f32_16x16x32_bf16 v[128:131], a[184:187], a[208:211], v[128:131]
ds_read_b128 a[204:207], v160 offset:3072
ds_read_b64_tr_b16 a[132:133], v165
v_mfma_f32_16x16x32_bf16 v[132:135], a[184:187], a[212:215], v[132:135]
ds_read_b64_tr_b16 a[134:135], v167
v_accvgpr_write_b32 a115, 0
v_mfma_f32_16x16x32_bf16 v[136:139], a[184:187], a[216:219], v[136:139]
s_add_u32 m0, s75, 81920
v_mfma_f32_16x16x32_bf16 v[140:143], a[184:187], a[220:223], v[140:143]
v_mfma_f32_16x16x32_bf16 v[144:147], a[188:191], a[208:211], v[144:147]
ds_read_b64_tr_b16 a[136:137], v164 offset:512
ds_read_b64_tr_b16 a[138:139], v166 offset:512
v_accvgpr_write_b32 a116, 0
v_mfma_f32_16x16x32_bf16 v[148:151], a[188:191], a[212:215], v[148:151]
buffer_load_dwordx4 v176, s[68:71], s74 offen lds
v_mfma_f32_16x16x32_bf16 v[152:155], a[188:191], a[216:219], v[152:155]
v_mfma_f32_16x16x32_bf16 v[156:159], a[188:191], a[220:223], v[156:159]
buffer_load_dwordx4 v177, s[68:71], s74 offen offset:1024 lds
v_accvgpr_write_b32 a117, 0
s_waitcnt lgkmcnt(2)
v_mfma_f32_16x16x32_bf16 v[32:35], a[128:131], a[192:195], v[32:35]
ds_read_b64_tr_b16 a[140:141], v165 offset:512
ds_read_b64_tr_b16 a[142:143], v167 offset:512
v_mfma_f32_16x16x32_bf16 v[36:39], a[128:131], a[196:199], v[36:39]
v_mfma_f32_16x16x32_bf16 v[40:43], a[128:131], a[200:203], v[40:43]
s_add_u32 s72, s72, 0x20000
v_accvgpr_write_b32 a118, 0
v_mfma_f32_16x16x32_bf16 v[44:47], a[128:131], a[204:207], v[44:47]
v_mfma_f32_16x16x32_bf16 v[48:51], a[132:135], a[192:195], v[48:51]
s_add_u32 s73, s73, 0x20000
ds_read_b64_tr_b16 a[144:145], v164 offset:1024
ds_read_b64_tr_b16 a[146:147], v166 offset:1024
v_mfma_f32_16x16x32_bf16 v[52:55], a[132:135], a[196:199], v[52:55]
v_accvgpr_write_b32 a119, 0
v_mfma_f32_16x16x32_bf16 v[56:59], a[132:135], a[200:203], v[56:59]
v_mfma_f32_16x16x32_bf16 v[60:63], a[132:135], a[204:207], v[60:63]
s_add_u32 s74, s74, 262144
s_waitcnt lgkmcnt(2)
v_mfma_f32_16x16x32_bf16 v[64:67], a[136:139], a[192:195], v[64:67]
ds_read_b64_tr_b16 a[148:149], v165 offset:1024
ds_read_b64_tr_b16 a[150:151], v167 offset:1024
v_accvgpr_write_b32 a120, 0
v_mfma_f32_16x16x32_bf16 v[68:71], a[136:139], a[196:199], v[68:71]
v_mfma_f32_16x16x32_bf16 v[72:75], a[136:139], a[200:203], v[72:75]
s_waitcnt vmcnt(18)
s_barrier
v_mfma_f32_16x16x32_bf16 v[76:79], a[136:139], a[204:207], v[76:79]
s_add_u32 m0, s42, 90112
v_accvgpr_write_b32 a121, 0
v_mfma_f32_16x16x32_bf16 v[80:83], a[140:143], a[192:195], v[80:83]
ds_read_b64_tr_b16 a[152:153], v164 offset:1536
ds_read_b64_tr_b16 a[154:155], v166 offset:1536
v_mfma_f32_16x16x32_bf16 v[84:87], a[140:143], a[196:199], v[84:87]
ds_read_b128 a[208:211], v160 offset:24576
buffer_load_dwordx4 v238, s[64:67], s72 offen sc1 lds
v_mfma_f32_16x16x32_bf16 v[88:91], a[140:143], a[200:203], v[88:91]
v_accvgpr_write_b32 a122, 0
v_mfma_f32_16x16x32_bf16 v[92:95], a[140:143], a[204:207], v[92:95]
s_waitcnt lgkmcnt(3)
v_mfma_f32_16x16x32_bf16 v[96:99], a[144:147], a[192:195], v[96:99]
ds_read_b64_tr_b16 a[156:157], v165 offset:1536
ds_read_b64_tr_b16 a[158:159], v167 offset:1536
buffer_load_dwordx4 v238, s[64:67], s72 offen offset:1024 sc1 lds
v_mfma_f32_16x16x32_bf16 v[100:103], a[144:147], a[196:199], v[100:103]
ds_read_b128 a[212:215], v160 offset:25600
v_accvgpr_write_b32 a123, 0
v_mfma_f32_16x16x32_bf16 v[104:107], a[144:147], a[200:203], v[104:107]
s_add_u32 m0, s42, 92160
v_mfma_f32_16x16x32_bf16 v[108:111], a[144:147], a[204:207], v[108:111]
v_mfma_f32_16x16x32_bf16 v[112:115], a[148:151], a[192:195], v[112:115]
ds_read_b128 a[216:219], v160 offset:26624
ds_read_b64_tr_b16 a[160:161], v164 offset:24576
v_accvgpr_write_b32 a124, 0
v_mfma_f32_16x16x32_bf16 v[116:119], a[148:151], a[196:199], v[116:119]
ds_read_b64_tr_b16 a[162:163], v166 offset:24576
buffer_load_dwordx4 v238, s[64:67], s73 offen sc1 lds
v_mfma_f32_16x16x32_bf16 v[120:123], a[148:151], a[200:203], v[120:123]
v_mfma_f32_16x16x32_bf16 v[124:127], a[148:151], a[204:207], v[124:127]
buffer_load_dwordx4 v238, s[64:67], s73 offen offset:1024 sc1 lds
v_accvgpr_write_b32 a125, 0
s_waitcnt lgkmcnt(4)
v_mfma_f32_16x16x32_bf16 v[128:131], a[152:155], a[192:195], v[128:131]
ds_read_b128 a[220:223], v160 offset:27648
ds_read_b64_tr_b16 a[164:165], v165 offset:24576
v_mfma_f32_16x16x32_bf16 v[132:135], a[152:155], a[196:199], v[132:135]
ds_read_b64_tr_b16 a[166:167], v167 offset:24576
v_mfma_f32_16x16x32_bf16 v[136:139], a[152:155], a[200:203], v[136:139]
s_add_u32 m0, s75, 106496
v_accvgpr_write_b32 a126, 0
v_mfma_f32_16x16x32_bf16 v[140:143], a[152:155], a[204:207], v[140:143]
v_mfma_f32_16x16x32_bf16 v[144:147], a[156:159], a[192:195], v[144:147]
ds_read_b64_tr_b16 a[168:169], v164 offset:25088
ds_read_b64_tr_b16 a[170:171], v166 offset:25088
v_mfma_f32_16x16x32_bf16 v[148:151], a[156:159], a[196:199], v[148:151]
buffer_load_dwordx4 v176, s[68:71], s74 offen lds
v_accvgpr_write_b32 a127, 0
v_mfma_f32_16x16x32_bf16 v[152:155], a[156:159], a[200:203], v[152:155]
v_mfma_f32_16x16x32_bf16 v[156:159], a[156:159], a[204:207], v[156:159]
buffer_load_dwordx4 v177, s[68:71], s74 offen offset:1024 lds
s_waitcnt lgkmcnt(2)
v_mfma_f32_16x16x32_bf16 v[32:35], a[160:163], a[208:211], v[32:35]
ds_read_b64_tr_b16 a[172:173], v165 offset:25088
ds_read_b64_tr_b16 a[174:175], v167 offset:25088
v_mfma_f32_16x16x32_bf16 v[36:39], a[160:163], a[212:215], v[36:39]
v_mfma_f32_16x16x32_bf16 v[40:43], a[160:163], a[216:219], v[40:43]
s_add_u32 s72, s72, 0x20000
v_mfma_f32_16x16x32_bf16 v[44:47], a[160:163], a[220:223], v[44:47]
v_mfma_f32_16x16x32_bf16 v[48:51], a[164:167], a[208:211], v[48:51]
s_add_u32 s73, s73, 0x20000
ds_read_b64_tr_b16 a[176:177], v164 offset:25600
ds_read_b64_tr_b16 a[178:179], v166 offset:25600
v_mfma_f32_16x16x32_bf16 v[52:55], a[164:167], a[212:215], v[52:55]
v_mfma_f32_16x16x32_bf16 v[56:59], a[164:167], a[216:219], v[56:59]
v_mfma_f32_16x16x32_bf16 v[60:63], a[164:167], a[220:223], v[60:63]
s_add_u32 s74, s74, 262144
s_waitcnt lgkmcnt(2)
v_mfma_f32_16x16x32_bf16 v[64:67], a[168:171], a[208:211], v[64:67]
ds_read_b64_tr_b16 a[180:181], v165 offset:25600
ds_read_b64_tr_b16 a[182:183], v167 offset:25600
v_mfma_f32_16x16x32_bf16 v[68:71], a[168:171], a[212:215], v[68:71]
v_mfma_f32_16x16x32_bf16 v[72:75], a[168:171], a[216:219], v[72:75]
s_waitcnt vmcnt(18)
s_barrier
v_mfma_f32_16x16x32_bf16 v[76:79], a[168:171], a[220:223], v[76:79]
s_add_u32 m0, s42, 114688
v_mfma_f32_16x16x32_bf16 v[80:83], a[172:175], a[208:211], v[80:83]
ds_read_b64_tr_b16 a[184:185], v164 offset:26112
ds_read_b64_tr_b16 a[186:187], v166 offset:26112
v_mfma_f32_16x16x32_bf16 v[84:87], a[172:175], a[212:215], v[84:87]
ds_read_b128 a[192:195], v161
buffer_load_dwordx4 v238, s[64:67], s72 offen sc1 lds
v_mfma_f32_16x16x32_bf16 v[88:91], a[172:175], a[216:219], v[88:91]
v_mfma_f32_16x16x32_bf16 v[92:95], a[172:175], a[220:223], v[92:95]
s_waitcnt lgkmcnt(3)
v_mfma_f32_16x16x32_bf16 v[96:99], a[176:179], a[208:211], v[96:99]
ds_read_b64_tr_b16 a[188:189], v165 offset:26112
ds_read_b64_tr_b16 a[190:191], v167 offset:26112
buffer_load_dwordx4 v238, s[64:67], s72 offen offset:1024 sc1 lds
v_mfma_f32_16x16x32_bf16 v[100:103], a[176:179], a[212:215], v[100:103]
ds_read_b128 a[196:199], v161 offset:1024
v_mfma_f32_16x16x32_bf16 v[104:107], a[176:179], a[216:219], v[104:107]
s_add_u32 m0, s42, 116736
v_mfma_f32_16x16x32_bf16 v[108:111], a[176:179], a[220:223], v[108:111]
v_mfma_f32_16x16x32_bf16 v[112:115], a[180:183], a[208:211], v[112:115]
ds_read_b128 a[200:203], v161 offset:2048
ds_read_b64_tr_b16 a[128:129], v168
v_mfma_f32_16x16x32_bf16 v[116:119], a[180:183], a[212:215], v[116:119]
ds_read_b64_tr_b16 a[130:131], v170
buffer_load_dwordx4 v238, s[64:67], s73 offen sc1 lds
v_mfma_f32_16x16x32_bf16 v[120:123], a[180:183], a[216:219], v[120:123]
v_mfma_f32_16x16x32_bf16 v[124:127], a[180:183], a[220:223], v[124:127]
buffer_load_dwordx4 v238, s[64:67], s73 offen offset:1024 sc1 lds
s_waitcnt lgkmcnt(4)
v_mfma_f32_16x16x32_bf16 v[128:131], a[184:187], a[208:211], v[128:131]
ds_read_b128 a[204:207], v161 offset:3072
ds_read_b64_tr_b16 a[132:133], v169
v_mfma_f32_16x16x32_bf16 v[132:135], a[184:187], a[212:215], v[132:135]
ds_read_b64_tr_b16 a[134:135], v171
v_mfma_f32_16x16x32_bf16 v[136:139], a[184:187], a[216:219], v[136:139]
s_add_u32 m0, s75, 131072
v_mfma_f32_16x16x32_bf16 v[140:143], a[184:187], a[220:223], v[140:143]
v_mfma_f32_16x16x32_bf16 v[144:147], a[188:191], a[208:211], v[144:147]
)ASM"
R"ASM(ds_read_b64_tr_b16 a[136:137], v168 offset:512
ds_read_b64_tr_b16 a[138:139], v170 offset:512
v_mfma_f32_16x16x32_bf16 v[148:151], a[188:191], a[212:215], v[148:151]
buffer_load_dwordx4 v176, s[68:71], s74 offen lds
v_mfma_f32_16x16x32_bf16 v[152:155], a[188:191], a[216:219], v[152:155]
v_mfma_f32_16x16x32_bf16 v[156:159], a[188:191], a[220:223], v[156:159]
buffer_load_dwordx4 v177, s[68:71], s74 offen offset:1024 lds
s_waitcnt lgkmcnt(2)
v_mfma_f32_16x16x32_bf16 v[32:35], a[128:131], a[192:195], v[32:35]
ds_read_b64_tr_b16 a[140:141], v169 offset:512
ds_read_b64_tr_b16 a[142:143], v171 offset:512
v_mfma_f32_16x16x32_bf16 v[36:39], a[128:131], a[196:199], v[36:39]
v_mfma_f32_16x16x32_bf16 v[40:43], a[128:131], a[200:203], v[40:43]
s_add_u32 s72, s72, 0x20000
v_mfma_f32_16x16x32_bf16 v[44:47], a[128:131], a[204:207], v[44:47]
v_mfma_f32_16x16x32_bf16 v[48:51], a[132:135], a[192:195], v[48:51]
s_add_u32 s73, s73, 0x20000
ds_read_b64_tr_b16 a[144:145], v168 offset:1024
ds_read_b64_tr_b16 a[146:147], v170 offset:1024
v_mfma_f32_16x16x32_bf16 v[52:55], a[132:135], a[196:199], v[52:55]
v_mfma_f32_16x16x32_bf16 v[56:59], a[132:135], a[200:203], v[56:59]
v_mfma_f32_16x16x32_bf16 v[60:63], a[132:135], a[204:207], v[60:63]
s_add_u32 s74, s74, 262144
s_waitcnt lgkmcnt(2)
v_mfma_f32_16x16x32_bf16 v[64:67], a[136:139], a[192:195], v[64:67]
ds_read_b64_tr_b16 a[148:149], v169 offset:1024
ds_read_b64_tr_b16 a[150:151], v171 offset:1024
v_mfma_f32_16x16x32_bf16 v[68:71], a[136:139], a[196:199], v[68:71]
v_mfma_f32_16x16x32_bf16 v[72:75], a[136:139], a[200:203], v[72:75]
s_waitcnt vmcnt(18)
s_barrier
global_load_dwordx2 v[206:207], %[pk], %[flags] sc1
v_mfma_f32_16x16x32_bf16 v[76:79], a[136:139], a[204:207], v[76:79]
s_add_u32 m0, s42, 139264
v_mfma_f32_16x16x32_bf16 v[80:83], a[140:143], a[192:195], v[80:83]
ds_read_b64_tr_b16 a[152:153], v168 offset:1536
ds_read_b64_tr_b16 a[154:155], v170 offset:1536
v_mfma_f32_16x16x32_bf16 v[84:87], a[140:143], a[196:199], v[84:87]
ds_read_b128 a[208:211], v161 offset:24576
buffer_load_dwordx4 v238, s[64:67], s72 offen sc1 lds
v_mfma_f32_16x16x32_bf16 v[88:91], a[140:143], a[200:203], v[88:91]
v_mfma_f32_16x16x32_bf16 v[92:95], a[140:143], a[204:207], v[92:95]
s_waitcnt lgkmcnt(3)
v_mfma_f32_16x16x32_bf16 v[96:99], a[144:147], a[192:195], v[96:99]
ds_read_b64_tr_b16 a[156:157], v169 offset:1536
ds_read_b64_tr_b16 a[158:159], v171 offset:1536
buffer_load_dwordx4 v238, s[64:67], s72 offen offset:1024 sc1 lds
v_mfma_f32_16x16x32_bf16 v[100:103], a[144:147], a[196:199], v[100:103]
ds_read_b128 a[212:215], v161 offset:25600
v_mfma_f32_16x16x32_bf16 v[104:107], a[144:147], a[200:203], v[104:107]
s_add_u32 m0, s42, 141312
v_mfma_f32_16x16x32_bf16 v[108:111], a[144:147], a[204:207], v[108:111]
v_mfma_f32_16x16x32_bf16 v[112:115], a[148:151], a[192:195], v[112:115]
ds_read_b128 a[216:219], v161 offset:26624
ds_read_b64_tr_b16 a[160:161], v168 offset:24576
v_mfma_f32_16x16x32_bf16 v[116:119], a[148:151], a[196:199], v[116:119]
ds_read_b64_tr_b16 a[162:163], v170 offset:24576
buffer_load_dwordx4 v238, s[64:67], s73 offen sc1 lds
v_mfma_f32_16x16x32_bf16 v[120:123], a[148:151], a[200:203], v[120:123]
v_mfma_f32_16x16x32_bf16 v[124:127], a[148:151], a[204:207], v[124:127]
buffer_load_dwordx4 v238, s[64:67], s73 offen offset:1024 sc1 lds
s_waitcnt lgkmcnt(4)
v_mfma_f32_16x16x32_bf16 v[128:131], a[152:155], a[192:195], v[128:131]
ds_read_b128 a[220:223], v161 offset:27648
ds_read_b64_tr_b16 a[164:165], v169 offset:24576
v_mfma_f32_16x16x32_bf16 v[132:135], a[152:155], a[196:199], v[132:135]
ds_read_b64_tr_b16 a[166:167], v171 offset:24576
v_mfma_f32_16x16x32_bf16 v[136:139], a[152:155], a[200:203], v[136:139]
s_add_u32 m0, s75, 155648
v_mfma_f32_16x16x32_bf16 v[140:143], a[152:155], a[204:207], v[140:143]
v_mfma_f32_16x16x32_bf16 v[144:147], a[156:159], a[192:195], v[144:147]
ds_read_b64_tr_b16 a[168:169], v168 offset:25088
ds_read_b64_tr_b16 a[170:171], v170 offset:25088
v_mfma_f32_16x16x32_bf16 v[148:151], a[156:159], a[196:199], v[148:151]
buffer_load_dwordx4 v176, s[68:71], s74 offen lds
v_mfma_f32_16x16x32_bf16 v[152:155], a[156:159], a[200:203], v[152:155]
v_mfma_f32_16x16x32_bf16 v[156:159], a[156:159], a[204:207], v[156:159]
buffer_load_dwordx4 v177, s[68:71], s74 offen offset:1024 lds
s_waitcnt lgkmcnt(2)
v_mfma_f32_16x16x32_bf16 v[32:35], a[160:163], a[208:211], v[32:35]
ds_read_b64_tr_b16 a[172:173], v169 offset:25088
ds_read_b64_tr_b16 a[174:175], v171 offset:25088
v_mfma_f32_16x16x32_bf16 v[36:39], a[160:163], a[212:215], v[36:39]
v_mfma_f32_16x16x32_bf16 v[40:43], a[160:163], a[216:219], v[40:43]
s_add_u32 s72, s72, 0x20000
v_mfma_f32_16x16x32_bf16 v[44:47], a[160:163], a[220:223], v[44:47]
v_mfma_f32_16x16x32_bf16 v[48:51], a[164:167], a[208:211], v[48:51]
s_add_u32 s73, s73, 0x20000
ds_read_b64_tr_b16 a[176:177], v168 offset:25600
ds_read_b64_tr_b16 a[178:179], v170 offset:25600
v_mfma_f32_16x16x32_bf16 v[52:55], a[164:167], a[212:215], v[52:55]
v_mfma_f32_16x16x32_bf16 v[56:59], a[164:167], a[216:219], v[56:59]
v_mfma_f32_16x16x32_bf16 v[60:63], a[164:167], a[220:223], v[60:63]
s_add_u32 s74, s74, 262144
s_waitcnt lgkmcnt(2)
v_mfma_f32_16x16x32_bf16 v[64:67], a[168:171], a[208:211], v[64:67]
ds_read_b64_tr_b16 a[180:181], v169 offset:25600
ds_read_b64_tr_b16 a[182:183], v171 offset:25600
v_mfma_f32_16x16x32_bf16 v[68:71], a[168:171], a[212:215], v[68:71]
v_mfma_f32_16x16x32_bf16 v[72:75], a[168:171], a[216:219], v[72:75]
s_waitcnt vmcnt(19)
s_barrier
s_waitcnt vmcnt(6)
v_cmp_ne_u64_e32 vcc, s[78:79], v[206:207]
s_nop 4
s_cmp_lg_u64 vcc, 0
s_cbranch_scc0 L_pok2_%=
s_mov_b32 s80, 0x10000
L_plp2_%=:
s_sleep 2
global_load_dwordx2 v[206:207], %[pk], %[flags] sc1
s_waitcnt vmcnt(0)
v_cmp_ne_u64_e32 vcc, s[78:79], v[206:207]
s_nop 4
s_cmp_lg_u64 vcc, 0
s_cbranch_scc0 L_pok2_%=
s_sub_u32 s80, s80, 1
s_cmp_lg_u32 s80, 0
s_cbranch_scc1 L_plp2_%=
L_pok2_%=:
v_mfma_f32_16x16x32_bf16 v[76:79], a[168:171], a[220:223], v[76:79]
v_mfma_f32_16x16x32_bf16 v[80:83], a[172:175], a[208:211], v[80:83]
ds_read_b64_tr_b16 a[184:185], v168 offset:26112
ds_read_b64_tr_b16 a[186:187], v170 offset:26112
v_mfma_f32_16x16x32_bf16 v[84:87], a[172:175], a[212:215], v[84:87]
ds_read_b128 a[192:195], v162
v_mfma_f32_16x16x32_bf16 v[88:91], a[172:175], a[216:219], v[88:91]
v_mfma_f32_16x16x32_bf16 v[92:95], a[172:175], a[220:223], v[92:95]
s_waitcnt lgkmcnt(3)
v_mfma_f32_16x16x32_bf16 v[96:99], a[176:179], a[208:211], v[96:99]
ds_read_b64_tr_b16 a[188:189], v169 offset:26112
ds_read_b64_tr_b16 a[190:191], v171 offset:26112
v_mfma_f32_16x16x32_bf16 v[100:103], a[176:179], a[212:215], v[100:103]
ds_read_b128 a[196:199], v162 offset:1024
v_mfma_f32_16x16x32_bf16 v[104:107], a[176:179], a[216:219], v[104:107]
v_mfma_f32_16x16x32_bf16 v[108:111], a[176:179], a[220:223], v[108:111]
v_mfma_f32_16x16x32_bf16 v[112:115], a[180:183], a[208:211], v[112:115]
ds_read_b128 a[200:203], v162 offset:2048
ds_read_b64_tr_b16 a[128:129], v172
v_mfma_f32_16x16x32_bf16 v[116:119], a[180:183], a[212:215], v[116:119]
ds_read_b64_tr_b16 a[130:131], v174
v_mfma_f32_16x16x32_bf16 v[120:123], a[180:183], a[216:219], v[120:123]
v_mfma_f32_16x16x32_bf16 v[124:127], a[180:183], a[220:223], v[124:127]
s_waitcnt lgkmcnt(4)
v_mfma_f32_16x16x32_bf16 v[128:131], a[184:187], a[208:211], v[128:131]
ds_read_b128 a[204:207], v162 offset:3072
ds_read_b64_tr_b16 a[132:133], v173
v_mfma_f32_16x16x32_bf16 v[132:135], a[184:187], a[212:215], v[132:135]
ds_read_b64_tr_b16 a[134:135], v175
v_mfma_f32_16x16x32_bf16 v[136:139], a[184:187], a[216:219], v[136:139]
v_mfma_f32_16x16x32_bf16 v[140:143], a[184:187], a[220:223], v[140:143]
v_mfma_f32_16x16x32_bf16 v[144:147], a[188:191], a[208:211], v[144:147]
ds_read_b64_tr_b16 a[136:137], v172 offset:512
ds_read_b64_tr_b16 a[138:139], v174 offset:512
v_mfma_f32_16x16x32_bf16 v[148:151], a[188:191], a[212:215], v[148:151]
v_mfma_f32_16x16x32_bf16 v[152:155], a[188:191], a[216:219], v[152:155]
v_mfma_f32_16x16x32_bf16 v[156:159], a[188:191], a[220:223], v[156:159]
s_waitcnt lgkmcnt(2)
v_mfma_f32_16x16x32_bf16 v[32:35], a[128:131], a[192:195], v[32:35]
ds_read_b64_tr_b16 a[140:141], v173 offset:512
ds_read_b64_tr_b16 a[142:143], v175 offset:512
v_mfma_f32_16x16x32_bf16 v[36:39], a[128:131], a[196:199], v[36:39]
v_mfma_f32_16x16x32_bf16 v[40:43], a[128:131], a[200:203], v[40:43]
v_mfma_f32_16x16x32_bf16 v[44:47], a[128:131], a[204:207], v[44:47]
v_mfma_f32_16x16x32_bf16 v[48:51], a[132:135], a[192:195], v[48:51]
ds_read_b64_tr_b16 a[144:145], v172 offset:1024
ds_read_b64_tr_b16 a[146:147], v174 offset:1024
v_mfma_f32_16x16x32_bf16 v[52:55], a[132:135], a[196:199], v[52:55]
v_mfma_f32_16x16x32_bf16 v[56:59], a[132:135], a[200:203], v[56:59]
v_mfma_f32_16x16x32_bf16 v[60:63], a[132:135], a[204:207], v[60:63]
s_waitcnt lgkmcnt(2)
v_mfma_f32_16x16x32_bf16 v[64:67], a[136:139], a[192:195], v[64:67]
ds_read_b64_tr_b16 a[148:149], v173 offset:1024
ds_read_b64_tr_b16 a[150:151], v175 offset:1024
v_mfma_f32_16x16x32_bf16 v[68:71], a[136:139], a[196:199], v[68:71]
v_mfma_f32_16x16x32_bf16 v[72:75], a[136:139], a[200:203], v[72:75]
s_barrier
v_mfma_f32_16x16x32_bf16 v[76:79], a[136:139], a[204:207], v[76:79]
s_add_u32 m0, s42, 0
v_mfma_f32_16x16x32_bf16 v[80:83], a[140:143], a[192:195], v[80:83]
ds_read_b64_tr_b16 a[152:153], v172 offset:1536
ds_read_b64_tr_b16 a[154:155], v174 offset:1536
buffer_load_dwordx4 v238, s[36:39], s50 offen sc1 lds
v_mfma_f32_16x16x32_bf16 v[84:87], a[140:143], a[196:199], v[84:87]
ds_read_b128 a[208:211], v162 offset:24576
buffer_load_dwordx4 v238, s[36:39], s50 offen offset:1024 sc1 lds
v_mfma_f32_16x16x32_bf16 v[88:91], a[140:143], a[200:203], v[88:91]
buffer_load_dwordx4 v238, s[36:39], s50 offen offset:2048 sc1 lds
v_mfma_f32_16x16x32_bf16 v[92:95], a[140:143], a[204:207], v[92:95]
buffer_load_dwordx4 v238, s[36:39], s50 offen offset:3072 sc1 lds
s_waitcnt lgkmcnt(3)
v_mfma_f32_16x16x32_bf16 v[96:99], a[144:147], a[192:195], v[96:99]
s_add_u32 s50, s50, 0x8000
ds_read_b64_tr_b16 a[156:157], v173 offset:1536
ds_read_b64_tr_b16 a[158:159], v175 offset:1536
v_mfma_f32_16x16x32_bf16 v[100:103], a[144:147], a[196:199], v[100:103]
s_add_u32 m0, s42, 16384
ds_read_b128 a[212:215], v162 offset:25600
v_mfma_f32_16x16x32_bf16 v[104:107], a[144:147], a[200:203], v[104:107]
buffer_load_dwordx4 v238, s[36:39], s50 offen sc1 lds
v_mfma_f32_16x16x32_bf16 v[108:111], a[144:147], a[204:207], v[108:111]
buffer_load_dwordx4 v238, s[36:39], s50 offen offset:1024 sc1 lds
v_mfma_f32_16x16x32_bf16 v[112:115], a[148:151], a[192:195], v[112:115]
ds_read_b128 a[216:219], v162 offset:26624
ds_read_b64_tr_b16 a[160:161], v172 offset:24576
buffer_load_dwordx4 v238, s[36:39], s50 offen offset:2048 sc1 lds
v_mfma_f32_16x16x32_bf16 v[116:119], a[148:151], a[196:199], v[116:119]
ds_read_b64_tr_b16 a[162:163], v174 offset:24576
buffer_load_dwordx4 v238, s[36:39], s50 offen offset:3072 sc1 lds
v_mfma_f32_16x16x32_bf16 v[120:123], a[148:151], a[200:203], v[120:123]
s_add_u32 s50, s50, 0x8000
v_mfma_f32_16x16x32_bf16 v[124:127], a[148:151], a[204:207], v[124:127]
s_add_u32 m0, s42, 32768
s_waitcnt lgkmcnt(4)
v_mfma_f32_16x16x32_bf16 v[128:131], a[152:155], a[192:195], v[128:131]
ds_read_b128 a[220:223], v162 offset:27648
ds_read_b64_tr_b16 a[164:165], v173 offset:24576
buffer_load_dwordx4 v238, s[36:39], s50 offen sc1 lds
v_mfma_f32_16x16x32_bf16 v[132:135], a[152:155], a[196:199], v[132:135]
ds_read_b64_tr_b16 a[166:167], v175 offset:24576
buffer_load_dwordx4 v238, s[36:39], s50 offen offset:1024 sc1 lds
v_mfma_f32_16x16x32_bf16 v[136:139], a[152:155], a[200:203], v[136:139]
buffer_load_dwordx4 v238, s[36:39], s50 offen offset:2048 sc1 lds
v_mfma_f32_16x16x32_bf16 v[140:143], a[152:155], a[204:207], v[140:143]
buffer_load_dwordx4 v238, s[36:39], s50 offen offset:3072 sc1 lds
v_mfma_f32_16x16x32_bf16 v[144:147], a[156:159], a[192:195], v[144:147]
s_add_u32 s50, s50, 0x8000
ds_read_b64_tr_b16 a[168:169], v172 offset:25088
ds_read_b64_tr_b16 a[170:171], v174 offset:25088
v_mfma_f32_16x16x32_bf16 v[148:151], a[156:159], a[196:199], v[148:151]
)ASM"
R"ASM(v_mfma_f32_16x16x32_bf16 v[152:155], a[156:159], a[200:203], v[152:155]
v_mfma_f32_16x16x32_bf16 v[156:159], a[156:159], a[204:207], v[156:159]
s_waitcnt lgkmcnt(2)
v_mfma_f32_16x16x32_bf16 v[32:35], a[160:163], a[208:211], v[32:35]
ds_read_b64_tr_b16 a[172:173], v173 offset:25088
ds_read_b64_tr_b16 a[174:175], v175 offset:25088
v_mfma_f32_16x16x32_bf16 v[36:39], a[160:163], a[212:215], v[36:39]
v_mfma_f32_16x16x32_bf16 v[40:43], a[160:163], a[216:219], v[40:43]
v_mfma_f32_16x16x32_bf16 v[44:47], a[160:163], a[220:223], v[44:47]
v_mfma_f32_16x16x32_bf16 v[48:51], a[164:167], a[208:211], v[48:51]
ds_read_b64_tr_b16 a[176:177], v172 offset:25600
ds_read_b64_tr_b16 a[178:179], v174 offset:25600
v_mfma_f32_16x16x32_bf16 v[52:55], a[164:167], a[212:215], v[52:55]
v_mfma_f32_16x16x32_bf16 v[56:59], a[164:167], a[216:219], v[56:59]
v_mfma_f32_16x16x32_bf16 v[60:63], a[164:167], a[220:223], v[60:63]
s_waitcnt lgkmcnt(2)
v_mfma_f32_16x16x32_bf16 v[64:67], a[168:171], a[208:211], v[64:67]
ds_read_b64_tr_b16 a[180:181], v173 offset:25600
ds_read_b64_tr_b16 a[182:183], v175 offset:25600
v_mfma_f32_16x16x32_bf16 v[68:71], a[168:171], a[212:215], v[68:71]
v_mfma_f32_16x16x32_bf16 v[72:75], a[168:171], a[216:219], v[72:75]
s_barrier
v_mfma_f32_16x16x32_bf16 v[76:79], a[168:171], a[220:223], v[76:79]
v_mfma_f32_16x16x32_bf16 v[80:83], a[172:175], a[208:211], v[80:83]
ds_read_b64_tr_b16 a[184:185], v172 offset:26112
ds_read_b64_tr_b16 a[186:187], v174 offset:26112
v_mfma_f32_16x16x32_bf16 v[84:87], a[172:175], a[212:215], v[84:87]
ds_read_b128 a[192:195], v160
v_mfma_f32_16x16x32_bf16 v[88:91], a[172:175], a[216:219], v[88:91]
v_mfma_f32_16x16x32_bf16 v[92:95], a[172:175], a[220:223], v[92:95]
s_waitcnt lgkmcnt(3)
v_mfma_f32_16x16x32_bf16 v[96:99], a[176:179], a[208:211], v[96:99]
ds_read_b64_tr_b16 a[188:189], v173 offset:26112
ds_read_b64_tr_b16 a[190:191], v175 offset:26112
v_mfma_f32_16x16x32_bf16 v[100:103], a[176:179], a[212:215], v[100:103]
ds_read_b128 a[196:199], v160 offset:1024
v_mfma_f32_16x16x32_bf16 v[104:107], a[176:179], a[216:219], v[104:107]
v_mfma_f32_16x16x32_bf16 v[108:111], a[176:179], a[220:223], v[108:111]
v_mfma_f32_16x16x32_bf16 v[112:115], a[180:183], a[208:211], v[112:115]
ds_read_b128 a[200:203], v160 offset:2048
ds_read_b64_tr_b16 a[128:129], v164
v_mfma_f32_16x16x32_bf16 v[116:119], a[180:183], a[212:215], v[116:119]
ds_read_b64_tr_b16 a[130:131], v166
v_mfma_f32_16x16x32_bf16 v[120:123], a[180:183], a[216:219], v[120:123]
v_mfma_f32_16x16x32_bf16 v[124:127], a[180:183], a[220:223], v[124:127]
s_waitcnt lgkmcnt(4)
v_mfma_f32_16x16x32_bf16 v[128:131], a[184:187], a[208:211], v[128:131]
ds_read_b128 a[204:207], v160 offset:3072
ds_read_b64_tr_b16 a[132:133], v165
v_mfma_f32_16x16x32_bf16 v[132:135], a[184:187], a[212:215], v[132:135]
ds_read_b64_tr_b16 a[134:135], v167
v_mfma_f32_16x16x32_bf16 v[136:139], a[184:187], a[216:219], v[136:139]
v_mfma_f32_16x16x32_bf16 v[140:143], a[184:187], a[220:223], v[140:143]
v_mfma_f32_16x16x32_bf16 v[144:147], a[188:191], a[208:211], v[144:147]
ds_read_b64_tr_b16 a[136:137], v164 offset:512
ds_read_b64_tr_b16 a[138:139], v166 offset:512
v_mfma_f32_16x16x32_bf16 v[148:151], a[188:191], a[212:215], v[148:151]
v_mfma_f32_16x16x32_bf16 v[152:155], a[188:191], a[216:219], v[152:155]
v_mfma_f32_16x16x32_bf16 v[156:159], a[188:191], a[220:223], v[156:159]
s_waitcnt lgkmcnt(2)
v_mfma_f32_16x16x32_bf16 v[32:35], a[128:131], a[192:195], v[32:35]
ds_read_b64_tr_b16 a[140:141], v165 offset:512
ds_read_b64_tr_b16 a[142:143], v167 offset:512
v_mfma_f32_16x16x32_bf16 v[36:39], a[128:131], a[196:199], v[36:39]
v_mfma_f32_16x16x32_bf16 v[40:43], a[128:131], a[200:203], v[40:43]
v_mfma_f32_16x16x32_bf16 v[44:47], a[128:131], a[204:207], v[44:47]
v_mfma_f32_16x16x32_bf16 v[48:51], a[132:135], a[192:195], v[48:51]
ds_read_b64_tr_b16 a[144:145], v164 offset:1024
ds_read_b64_tr_b16 a[146:147], v166 offset:1024
v_mfma_f32_16x16x32_bf16 v[52:55], a[132:135], a[196:199], v[52:55]
v_mfma_f32_16x16x32_bf16 v[56:59], a[132:135], a[200:203], v[56:59]
v_mfma_f32_16x16x32_bf16 v[60:63], a[132:135], a[204:207], v[60:63]
s_waitcnt lgkmcnt(2)
v_mfma_f32_16x16x32_bf16 v[64:67], a[136:139], a[192:195], v[64:67]
ds_read_b64_tr_b16 a[148:149], v165 offset:1024
ds_read_b64_tr_b16 a[150:151], v167 offset:1024
v_mfma_f32_16x16x32_bf16 v[68:71], a[136:139], a[196:199], v[68:71]
v_mfma_f32_16x16x32_bf16 v[72:75], a[136:139], a[200:203], v[72:75]
s_waitcnt vmcnt(12)
s_barrier
v_mfma_f32_16x16x32_bf16 v[76:79], a[136:139], a[204:207], v[76:79]
s_add_u32 m0, s42, 65536
v_mfma_f32_16x16x32_bf16 v[80:83], a[140:143], a[192:195], v[80:83]
ds_read_b64_tr_b16 a[152:153], v164 offset:1536
ds_read_b64_tr_b16 a[154:155], v166 offset:1536
buffer_load_dwordx4 v238, s[36:39], s40 offen sc1 lds
v_mfma_f32_16x16x32_bf16 v[84:87], a[140:143], a[196:199], v[84:87]
ds_read_b128 a[208:211], v160 offset:24576
buffer_load_dwordx4 v238, s[36:39], s40 offen offset:1024 sc1 lds
v_mfma_f32_16x16x32_bf16 v[88:91], a[140:143], a[200:203], v[88:91]
buffer_load_dwordx4 v238, s[36:39], s40 offen offset:2048 sc1 lds
v_mfma_f32_16x16x32_bf16 v[92:95], a[140:143], a[204:207], v[92:95]
buffer_load_dwordx4 v238, s[36:39], s40 offen offset:3072 sc1 lds
s_waitcnt lgkmcnt(3)
v_mfma_f32_16x16x32_bf16 v[96:99], a[144:147], a[192:195], v[96:99]
s_add_u32 s40, s40, 0x8000
ds_read_b64_tr_b16 a[156:157], v165 offset:1536
ds_read_b64_tr_b16 a[158:159], v167 offset:1536
v_mfma_f32_16x16x32_bf16 v[100:103], a[144:147], a[196:199], v[100:103]
s_add_u32 m0, s42, 81920
ds_read_b128 a[212:215], v160 offset:25600
v_mfma_f32_16x16x32_bf16 v[104:107], a[144:147], a[200:203], v[104:107]
buffer_load_dwordx4 v238, s[36:39], s40 offen sc1 lds
v_mfma_f32_16x16x32_bf16 v[108:111], a[144:147], a[204:207], v[108:111]
buffer_load_dwordx4 v238, s[36:39], s40 offen offset:1024 sc1 lds
v_mfma_f32_16x16x32_bf16 v[112:115], a[148:151], a[192:195], v[112:115]
ds_read_b128 a[216:219], v160 offset:26624
ds_read_b64_tr_b16 a[160:161], v164 offset:24576
buffer_load_dwordx4 v238, s[36:39], s40 offen offset:2048 sc1 lds
v_mfma_f32_16x16x32_bf16 v[116:119], a[148:151], a[196:199], v[116:119]
ds_read_b64_tr_b16 a[162:163], v166 offset:24576
buffer_load_dwordx4 v238, s[36:39], s40 offen offset:3072 sc1 lds
v_mfma_f32_16x16x32_bf16 v[120:123], a[148:151], a[200:203], v[120:123]
s_add_u32 s40, s40, 0x8000
v_mfma_f32_16x16x32_bf16 v[124:127], a[148:151], a[204:207], v[124:127]
s_waitcnt lgkmcnt(4)
v_mfma_f32_16x16x32_bf16 v[128:131], a[152:155], a[192:195], v[128:131]
ds_read_b128 a[220:223], v160 offset:27648
ds_read_b64_tr_b16 a[164:165], v165 offset:24576
v_mfma_f32_16x16x32_bf16 v[132:135], a[152:155], a[196:199], v[132:135]
ds_read_b64_tr_b16 a[166:167], v167 offset:24576
v_mfma_f32_16x16x32_bf16 v[136:139], a[152:155], a[200:203], v[136:139]
v_mfma_f32_16x16x32_bf16 v[140:143], a[152:155], a[204:207], v[140:143]
v_mfma_f32_16x16x32_bf16 v[144:147], a[156:159], a[192:195], v[144:147]
ds_read_b64_tr_b16 a[168:169], v164 offset:25088
ds_read_b64_tr_b16 a[170:171], v166 offset:25088
v_mfma_f32_16x16x32_bf16 v[148:151], a[156:159], a[196:199], v[148:151]
v_mfma_f32_16x16x32_bf16 v[152:155], a[156:159], a[200:203], v[152:155]
v_mfma_f32_16x16x32_bf16 v[156:159], a[156:159], a[204:207], v[156:159]
s_waitcnt lgkmcnt(2)
v_mfma_f32_16x16x32_bf16 v[32:35], a[160:163], a[208:211], v[32:35]
ds_read_b64_tr_b16 a[172:173], v165 offset:25088
ds_read_b64_tr_b16 a[174:175], v167 offset:25088
v_mfma_f32_16x16x32_bf16 v[36:39], a[160:163], a[212:215], v[36:39]
v_mfma_f32_16x16x32_bf16 v[40:43], a[160:163], a[216:219], v[40:43]
v_mfma_f32_16x16x32_bf16 v[44:47], a[160:163], a[220:223], v[44:47]
v_mfma_f32_16x16x32_bf16 v[48:51], a[164:167], a[208:211], v[48:51]
ds_read_b64_tr_b16 a[176:177], v164 offset:25600
ds_read_b64_tr_b16 a[178:179], v166 offset:25600
v_mfma_f32_16x16x32_bf16 v[52:55], a[164:167], a[212:215], v[52:55]
v_mfma_f32_16x16x32_bf16 v[56:59], a[164:167], a[216:219], v[56:59]
v_mfma_f32_16x16x32_bf16 v[60:63], a[164:167], a[220:223], v[60:63]
s_waitcnt lgkmcnt(2)
v_mfma_f32_16x16x32_bf16 v[64:67], a[168:171], a[208:211], v[64:67]
ds_read_b64_tr_b16 a[180:181], v165 offset:25600
ds_read_b64_tr_b16 a[182:183], v167 offset:25600
v_mfma_f32_16x16x32_bf16 v[68:71], a[168:171], a[212:215], v[68:71]
v_mfma_f32_16x16x32_bf16 v[72:75], a[168:171], a[216:219], v[72:75]
v_mfma_f32_16x16x32_bf16 v[76:79], a[168:171], a[220:223], v[76:79]
v_mfma_f32_16x16x32_bf16 v[80:83], a[172:175], a[208:211], v[80:83]
ds_read_b64_tr_b16 a[184:185], v164 offset:26112
ds_read_b64_tr_b16 a[186:187], v166 offset:26112
v_mfma_f32_16x16x32_bf16 v[84:87], a[172:175], a[212:215], v[84:87]
v_mfma_f32_16x16x32_bf16 v[88:91], a[172:175], a[216:219], v[88:91]
v_mfma_f32_16x16x32_bf16 v[92:95], a[172:175], a[220:223], v[92:95]
s_waitcnt lgkmcnt(2)
v_mfma_f32_16x16x32_bf16 v[96:99], a[176:179], a[208:211], v[96:99]
ds_read_b64_tr_b16 a[188:189], v165 offset:26112
ds_read_b64_tr_b16 a[190:191], v167 offset:26112
v_mfma_f32_16x16x32_bf16 v[100:103], a[176:179], a[212:215], v[100:103]
v_mfma_f32_16x16x32_bf16 v[104:107], a[176:179], a[216:219], v[104:107]
v_mfma_f32_16x16x32_bf16 v[108:111], a[176:179], a[220:223], v[108:111]
v_mfma_f32_16x16x32_bf16 v[112:115], a[180:183], a[208:211], v[112:115]
v_mfma_f32_16x16x32_bf16 v[116:119], a[180:183], a[212:215], v[116:119]
v_mfma_f32_16x16x32_bf16 v[120:123], a[180:183], a[216:219], v[120:123]
v_mfma_f32_16x16x32_bf16 v[124:127], a[180:183], a[220:223], v[124:127]
s_waitcnt lgkmcnt(0)
v_mfma_f32_16x16x32_bf16 v[128:131], a[184:187], a[208:211], v[128:131]
v_mfma_f32_16x16x32_bf16 v[132:135], a[184:187], a[212:215], v[132:135]
v_mfma_f32_16x16x32_bf16 v[136:139], a[184:187], a[216:219], v[136:139]
v_mfma_f32_16x16x32_bf16 v[140:143], a[184:187], a[220:223], v[140:143]
v_mfma_f32_16x16x32_bf16 v[144:147], a[188:191], a[208:211], v[144:147]
v_mfma_f32_16x16x32_bf16 v[148:151], a[188:191], a[212:215], v[148:151]
v_mfma_f32_16x16x32_bf16 v[152:155], a[188:191], a[216:219], v[152:155]
v_mfma_f32_16x16x32_bf16 v[156:159], a[188:191], a[220:223], v[156:159]
s_nop 7
v_cvt_pk_bf16_f32 v160, v32, v33
v_cvt_pk_bf16_f32 v161, v34, v35
v_cvt_pk_bf16_f32 v162, v48, v49
v_cvt_pk_bf16_f32 v163, v50, v51
v_cvt_pk_bf16_f32 v164, v64, v65
v_cvt_pk_bf16_f32 v165, v66, v67
v_cvt_pk_bf16_f32 v166, v80, v81
v_cvt_pk_bf16_f32 v167, v82, v83
v_cvt_pk_bf16_f32 v168, v96, v97
v_cvt_pk_bf16_f32 v169, v98, v99
v_cvt_pk_bf16_f32 v170, v112, v113
v_cvt_pk_bf16_f32 v171, v114, v115
v_cvt_pk_bf16_f32 v172, v128, v129
v_cvt_pk_bf16_f32 v173, v130, v131
v_cvt_pk_bf16_f32 v174, v144, v145
v_cvt_pk_bf16_f32 v175, v146, v147
v_cvt_pk_bf16_f32 v176, v36, v37
v_cvt_pk_bf16_f32 v177, v38, v39
v_cvt_pk_bf16_f32 v178, v52, v53
v_cvt_pk_bf16_f32 v179, v54, v55
v_cvt_pk_bf16_f32 v180, v68, v69
v_cvt_pk_bf16_f32 v181, v70, v71
v_cvt_pk_bf16_f32 v182, v84, v85
v_cvt_pk_bf16_f32 v183, v86, v87
v_cvt_pk_bf16_f32 v184, v100, v101
v_cvt_pk_bf16_f32 v185, v102, v103
v_cvt_pk_bf16_f32 v186, v116, v117
v_cvt_pk_bf16_f32 v187, v118, v119
v_cvt_pk_bf16_f32 v188, v132, v133
v_cvt_pk_bf16_f32 v189, v134, v135
v_cvt_pk_bf16_f32 v190, v148, v149
v_cvt_pk_bf16_f32 v191, v150, v151
v_cvt_pk_bf16_f32 v192, v40, v41
v_cvt_pk_bf16_f32 v193, v42, v43
v_cvt_pk_bf16_f32 v194, v56, v57
v_cvt_pk_bf16_f32 v195, v58, v59
v_cvt_pk_bf16_f32 v196, v72, v73
v_cvt_pk_bf16_f32 v197, v74, v75
v_cvt_pk_bf16_f32 v198, v88, v89
v_cvt_pk_bf16_f32 v199, v90, v91
v_cvt_pk_bf16_f32 v200, v104, v105
v_cvt_pk_bf16_f32 v201, v106, v107
v_cvt_pk_bf16_f32 v202, v120, v121
v_cvt_pk_bf16_f32 v203, v122, v123
v_cvt_pk_bf16_f32 v204, v136, v137
v_cvt_pk_bf16_f32 v205, v138, v139
v_cvt_pk_bf16_f32 v206, v152, v153
v_cvt_pk_bf16_f32 v207, v154, v155
v_cvt_pk_bf16_f32 v208, v44, v45
v_cvt_pk_bf16_f32 v209, v46, v47
v_cvt_pk_bf16_f32 v210, v60, v61
v_cvt_pk_bf16_f32 v211, v62, v63
v_cvt_pk_bf16_f32 v212, v76, v77
v_cvt_pk_bf16_f32 v213, v78, v79
v_cvt_pk_bf16_f32 v214, v92, v93
v_cvt_pk_bf16_f32 v215, v94, v95
v_cvt_pk_bf16_f32 v216, v108, v109
)ASM"
R"ASM(v_cvt_pk_bf16_f32 v217, v110, v111
v_cvt_pk_bf16_f32 v218, v124, v125
v_cvt_pk_bf16_f32 v219, v126, v127
v_cvt_pk_bf16_f32 v220, v140, v141
v_cvt_pk_bf16_f32 v221, v142, v143
v_cvt_pk_bf16_f32 v222, v156, v157
v_cvt_pk_bf16_f32 v223, v158, v159
v_mov_b32_e32 v144, 0
v_mov_b32_e32 v145, 0
v_mov_b32_e32 v146, 0
v_mov_b32_e32 v147, 0
v_mov_b32_e32 v148, 0
v_mov_b32_e32 v149, 0
v_mov_b32_e32 v150, 0
v_mov_b32_e32 v151, 0
v_mov_b32_e32 v152, 0
v_mov_b32_e32 v153, 0
v_mov_b32_e32 v154, 0
v_mov_b32_e32 v155, 0
v_mov_b32_e32 v156, 0
v_mov_b32_e32 v157, 0
v_mov_b32_e32 v158, 0
v_mov_b32_e32 v159, 0
s_add_u32 s41, s40, 0x4000
s_waitcnt vmcnt(4)
s_barrier
ds_read_b128 a[128:131], v224
ds_read_b128 a[132:135], v224 offset:4096
ds_read_b128 a[136:139], v224 offset:8192
ds_read_b128 a[140:143], v224 offset:12288
ds_read_b128 a[144:147], v224 offset:1024
ds_read_b128 a[148:151], v224 offset:5120
ds_read_b128 a[152:155], v224 offset:9216
ds_read_b128 a[156:159], v224 offset:13312
s_add_u32 m0, s42, 49152
s_waitcnt lgkmcnt(0)
v_mfma_f32_16x16x32_bf16 v[32:35], a[128:131], v[160:163], 0
v_mfma_f32_16x16x32_bf16 v[40:43], a[128:131], v[176:179], 0
buffer_load_dwordx4 v238, s[36:39], s41 offen sc1 lds
v_mfma_f32_16x16x32_bf16 v[48:51], a[128:131], v[192:195], 0
v_mfma_f32_16x16x32_bf16 v[56:59], a[128:131], v[208:211], 0
buffer_load_dwordx4 v238, s[36:39], s41 offen offset:1024 sc1 lds
v_mfma_f32_16x16x32_bf16 v[32:35], a[132:135], v[164:167], v[32:35]
v_mfma_f32_16x16x32_bf16 v[40:43], a[132:135], v[180:183], v[40:43]
buffer_load_dwordx4 v238, s[36:39], s41 offen offset:2048 sc1 lds
v_mfma_f32_16x16x32_bf16 v[48:51], a[132:135], v[196:199], v[48:51]
v_mfma_f32_16x16x32_bf16 v[56:59], a[132:135], v[212:215], v[56:59]
buffer_load_dwordx4 v238, s[36:39], s41 offen offset:3072 sc1 lds
v_mfma_f32_16x16x32_bf16 v[32:35], a[136:139], v[168:171], v[32:35]
v_mfma_f32_16x16x32_bf16 v[40:43], a[136:139], v[184:187], v[40:43]
s_add_u32 m0, s42, 98304
s_nop 0
v_mfma_f32_16x16x32_bf16 v[48:51], a[136:139], v[200:203], v[48:51]
v_mfma_f32_16x16x32_bf16 v[56:59], a[136:139], v[216:219], v[56:59]
buffer_load_dwordx4 v238, s[36:39], s40 offen sc1 lds
v_mfma_f32_16x16x32_bf16 v[32:35], a[140:143], v[172:175], v[32:35]
v_mfma_f32_16x16x32_bf16 v[40:43], a[140:143], v[188:191], v[40:43]
buffer_load_dwordx4 v238, s[36:39], s40 offen offset:1024 sc1 lds
v_mfma_f32_16x16x32_bf16 v[48:51], a[140:143], v[204:207], v[48:51]
v_mfma_f32_16x16x32_bf16 v[56:59], a[140:143], v[220:223], v[56:59]
buffer_load_dwordx4 v238, s[36:39], s40 offen offset:2048 sc1 lds
v_mfma_f32_16x16x32_bf16 v[36:39], a[144:147], v[160:163], 0
v_mfma_f32_16x16x32_bf16 v[44:47], a[144:147], v[176:179], 0
buffer_load_dwordx4 v238, s[36:39], s40 offen offset:3072 sc1 lds
v_mfma_f32_16x16x32_bf16 v[52:55], a[144:147], v[192:195], 0
v_mfma_f32_16x16x32_bf16 v[60:63], a[144:147], v[208:211], 0
s_add_u32 s41, s41, 0x8000
v_mfma_f32_16x16x32_bf16 v[36:39], a[148:151], v[164:167], v[36:39]
v_mfma_f32_16x16x32_bf16 v[44:47], a[148:151], v[180:183], v[44:47]
s_add_u32 s40, s40, 0x8000
v_mfma_f32_16x16x32_bf16 v[52:55], a[148:151], v[196:199], v[52:55]
v_mfma_f32_16x16x32_bf16 v[60:63], a[148:151], v[212:215], v[60:63]
v_mfma_f32_16x16x32_bf16 v[36:39], a[152:155], v[168:171], v[36:39]
v_mfma_f32_16x16x32_bf16 v[44:47], a[152:155], v[184:187], v[44:47]
v_mfma_f32_16x16x32_bf16 v[52:55], a[152:155], v[200:203], v[52:55]
v_mfma_f32_16x16x32_bf16 v[60:63], a[152:155], v[216:219], v[60:63]
v_mfma_f32_16x16x32_bf16 v[36:39], a[156:159], v[172:175], v[36:39]
v_mfma_f32_16x16x32_bf16 v[44:47], a[156:159], v[188:191], v[44:47]
v_mfma_f32_16x16x32_bf16 v[52:55], a[156:159], v[204:207], v[52:55]
v_mfma_f32_16x16x32_bf16 v[60:63], a[156:159], v[220:223], v[60:63]
s_nop 1
ds_read_b128 a[128:131], v224 offset:2048
ds_read_b128 a[132:135], v224 offset:6144
ds_read_b128 a[136:139], v224 offset:10240
ds_read_b128 a[140:143], v224 offset:14336
s_nop 6
s_cmp_ge_i32 s44, s43
s_cbranch_scc0 L_ms33_%=
s_cmp_eq_i32 s44, s43
s_cbranch_scc1 L_mt33_%=
v_mov_b32_e32 v48, v239
v_mov_b32_e32 v49, v239
v_mov_b32_e32 v50, v239
v_mov_b32_e32 v51, v239
v_mov_b32_e32 v56, v239
v_mov_b32_e32 v57, v239
v_mov_b32_e32 v58, v239
v_mov_b32_e32 v59, v239
v_mov_b32_e32 v52, v239
v_mov_b32_e32 v53, v239
v_mov_b32_e32 v54, v239
v_mov_b32_e32 v55, v239
v_mov_b32_e32 v60, v239
v_mov_b32_e32 v61, v239
v_mov_b32_e32 v62, v239
v_mov_b32_e32 v63, v239
s_branch L_ms33_%=
L_mt33_%=:
v_cndmask_b32_e64 v48, v48, v239, s[56:57]
v_cndmask_b32_e64 v49, v49, v239, s[58:59]
v_cndmask_b32_e64 v50, v50, v239, s[60:61]
v_cndmask_b32_e64 v51, v51, v239, s[62:63]
v_mov_b32_e32 v52, v239
v_mov_b32_e32 v53, v239
v_mov_b32_e32 v54, v239
v_mov_b32_e32 v55, v239
v_cndmask_b32_e64 v60, v60, v239, s[56:57]
v_cndmask_b32_e64 v61, v61, v239, s[58:59]
v_cndmask_b32_e64 v62, v62, v239, s[60:61]
v_cndmask_b32_e64 v63, v63, v239, s[62:63]
L_ms33_%=:
v_max3_f32 v240, v32, v33, v34
v_max3_f32 v240, v240, v35, v36
v_max3_f32 v240, v240, v37, v38
v_max_f32_e32 v240, v240, v39
v_max3_f32 v241, v40, v41, v42
v_max3_f32 v241, v241, v43, v44
v_max3_f32 v241, v241, v45, v46
v_max_f32_e32 v241, v241, v47
v_max3_f32 v242, v48, v49, v50
v_max3_f32 v242, v242, v51, v52
v_max3_f32 v242, v242, v53, v54
v_max_f32_e32 v242, v242, v55
v_max3_f32 v243, v56, v57, v58
v_max3_f32 v243, v243, v59, v60
v_max3_f32 v243, v243, v61, v62
v_max_f32_e32 v243, v243, v63
v_mov_b32_e32 v244, v240
v_mov_b32_e32 v245, v241
v_mov_b32_e32 v246, v242
v_mov_b32_e32 v247, v243
s_nop 1
v_permlane16_swap_b32_e32 v240, v244
v_permlane16_swap_b32_e32 v241, v245
v_permlane16_swap_b32_e32 v242, v246
v_permlane16_swap_b32_e32 v243, v247
v_max_f32_e32 v240, v240, v244
v_max_f32_e32 v241, v241, v245
v_max_f32_e32 v242, v242, v246
v_max_f32_e32 v243, v243, v247
v_mov_b32_e32 v244, v240
v_mov_b32_e32 v245, v241
v_mov_b32_e32 v246, v242
v_mov_b32_e32 v247, v243
s_nop 1
v_permlane32_swap_b32_e32 v240, v244
v_permlane32_swap_b32_e32 v241, v245
v_permlane32_swap_b32_e32 v242, v246
v_permlane32_swap_b32_e32 v243, v247
v_max_f32_e32 v240, v240, v244
v_max_f32_e32 v241, v241, v245
v_max_f32_e32 v242, v242, v246
v_max_f32_e32 v243, v243, v247
v_sub_f32_e32 v32, v32, v240
v_sub_f32_e32 v33, v33, v240
v_sub_f32_e32 v34, v34, v240
v_sub_f32_e32 v35, v35, v240
v_sub_f32_e32 v36, v36, v240
v_sub_f32_e32 v37, v37, v240
v_sub_f32_e32 v38, v38, v240
v_sub_f32_e32 v39, v39, v240
v_sub_f32_e32 v128, 0, v240
v_sub_f32_e32 v129, 0, v240
v_sub_f32_e32 v130, 0, v240
v_sub_f32_e32 v131, 0, v240
v_sub_f32_e32 v40, v40, v241
v_sub_f32_e32 v41, v41, v241
v_sub_f32_e32 v42, v42, v241
v_sub_f32_e32 v43, v43, v241
v_sub_f32_e32 v44, v44, v241
v_sub_f32_e32 v45, v45, v241
v_sub_f32_e32 v46, v46, v241
v_sub_f32_e32 v47, v47, v241
v_sub_f32_e32 v132, 0, v241
v_sub_f32_e32 v133, 0, v241
v_sub_f32_e32 v134, 0, v241
v_sub_f32_e32 v135, 0, v241
v_sub_f32_e32 v48, v48, v242
v_sub_f32_e32 v49, v49, v242
v_sub_f32_e32 v50, v50, v242
v_sub_f32_e32 v51, v51, v242
v_sub_f32_e32 v52, v52, v242
v_sub_f32_e32 v53, v53, v242
v_sub_f32_e32 v54, v54, v242
v_sub_f32_e32 v55, v55, v242
v_sub_f32_e32 v136, 0, v242
v_sub_f32_e32 v137, 0, v242
v_sub_f32_e32 v138, 0, v242
v_sub_f32_e32 v139, 0, v242
v_sub_f32_e32 v56, v56, v243
v_sub_f32_e32 v57, v57, v243
v_sub_f32_e32 v58, v58, v243
v_sub_f32_e32 v59, v59, v243
v_sub_f32_e32 v60, v60, v243
v_sub_f32_e32 v61, v61, v243
v_sub_f32_e32 v62, v62, v243
v_sub_f32_e32 v63, v63, v243
v_sub_f32_e32 v140, 0, v243
v_sub_f32_e32 v141, 0, v243
v_sub_f32_e32 v142, 0, v243
v_sub_f32_e32 v143, 0, v243
v_exp_f32_e32 v32, v32
s_nop 0
v_exp_f32_e32 v33, v33
s_nop 0
v_cvt_pk_bf16_f32 v96, v32, v33
v_exp_f32_e32 v34, v34
s_nop 0
v_exp_f32_e32 v35, v35
s_nop 0
v_cvt_pk_bf16_f32 v97, v34, v35
v_exp_f32_e32 v40, v40
s_nop 0
v_exp_f32_e32 v41, v41
s_nop 0
v_cvt_pk_bf16_f32 v100, v40, v41
v_exp_f32_e32 v42, v42
s_nop 0
v_exp_f32_e32 v43, v43
s_nop 0
v_cvt_pk_bf16_f32 v101, v42, v43
v_exp_f32_e32 v48, v48
s_nop 0
v_exp_f32_e32 v49, v49
s_nop 0
v_cvt_pk_bf16_f32 v104, v48, v49
v_exp_f32_e32 v50, v50
s_nop 0
v_exp_f32_e32 v51, v51
s_nop 0
v_cvt_pk_bf16_f32 v105, v50, v51
v_exp_f32_e32 v56, v56
s_nop 0
v_exp_f32_e32 v57, v57
s_nop 0
v_cvt_pk_bf16_f32 v108, v56, v57
v_exp_f32_e32 v58, v58
s_nop 0
v_exp_f32_e32 v59, v59
s_nop 0
v_cvt_pk_bf16_f32 v109, v58, v59
v_exp_f32_e32 v36, v36
s_nop 0
v_exp_f32_e32 v37, v37
s_nop 0
v_cvt_pk_bf16_f32 v98, v36, v37
v_exp_f32_e32 v38, v38
s_nop 0
v_exp_f32_e32 v39, v39
s_nop 0
v_cvt_pk_bf16_f32 v99, v38, v39
v_exp_f32_e32 v44, v44
s_nop 0
v_exp_f32_e32 v45, v45
s_nop 0
v_cvt_pk_bf16_f32 v102, v44, v45
v_exp_f32_e32 v46, v46
s_nop 0
v_exp_f32_e32 v47, v47
s_nop 0
v_cvt_pk_bf16_f32 v103, v46, v47
L_D0a_%=:
s_waitcnt lgkmcnt(2)
v_mfma_f32_16x16x32_bf16 v[64:67], a[128:131], v[160:163], v[128:131]
s_add_i32 s44, s44, 1
v_exp_f32_e32 v52, v52
v_mfma_f32_16x16x32_bf16 v[72:75], a[128:131], v[176:179], v[132:135]
ds_read_b128 a[144:147], v224 offset:3072
v_mfma_f32_16x16x32_bf16 v[80:83], a[128:131], v[192:195], v[136:139]
v_exp_f32_e32 v53, v53
v_mfma_f32_16x16x32_bf16 v[88:91], a[128:131], v[208:211], v[140:143]
v_cvt_pk_bf16_f32 v106, v52, v53
v_mfma_f32_16x16x32_bf16 v[64:67], a[132:135], v[164:167], v[64:67]
v_exp_f32_e32 v54, v54
v_mfma_f32_16x16x32_bf16 v[72:75], a[132:135], v[180:183], v[72:75]
ds_read_b128 a[148:151], v224 offset:7168
v_mfma_f32_16x16x32_bf16 v[80:83], a[132:135], v[196:199], v[80:83]
v_exp_f32_e32 v55, v55
v_mfma_f32_16x16x32_bf16 v[88:91], a[132:135], v[212:215], v[88:91]
v_cvt_pk_bf16_f32 v107, v54, v55
s_waitcnt lgkmcnt(2)
v_mfma_f32_16x16x32_bf16 v[64:67], a[136:139], v[168:171], v[64:67]
v_exp_f32_e32 v60, v60
v_mfma_f32_16x16x32_bf16 v[72:75], a[136:139], v[184:187], v[72:75]
ds_read_b128 a[152:155], v224 offset:11264
v_mfma_f32_16x16x32_bf16 v[80:83], a[136:139], v[200:203], v[80:83]
v_exp_f32_e32 v61, v61
v_mfma_f32_16x16x32_bf16 v[88:91], a[136:139], v[216:219], v[88:91]
v_cvt_pk_bf16_f32 v110, v60, v61
v_mfma_f32_16x16x32_bf16 v[64:67], a[140:143], v[172:175], v[64:67]
v_exp_f32_e32 v62, v62
v_mfma_f32_16x16x32_bf16 v[72:75], a[140:143], v[188:191], v[72:75]
ds_read_b128 a[156:159], v224 offset:15360
v_mfma_f32_16x16x32_bf16 v[80:83], a[140:143], v[204:207], v[80:83]
v_exp_f32_e32 v63, v63
v_mfma_f32_16x16x32_bf16 v[88:91], a[140:143], v[220:223], v[88:91]
v_cvt_pk_bf16_f32 v111, v62, v63
s_waitcnt lgkmcnt(2)
v_mfma_f32_16x16x32_bf16 v[68:71], a[144:147], v[160:163], v[128:131]
v_mfma_f32_16x16x32_bf16 v[76:79], a[144:147], v[176:179], v[132:135]
ds_read_b128 a[192:195], v228
v_mfma_f32_16x16x32_bf16 v[84:87], a[144:147], v[192:195], v[136:139]
v_mfma_f32_16x16x32_bf16 v[92:95], a[144:147], v[208:211], v[140:143]
s_cmp_ge_i32 s44, s43
s_cbranch_scc0 L_ms1_%=
s_cmp_eq_i32 s44, s43
s_cbranch_scc1 L_mt1_%=
v_mov_b32_e32 v80, v239
v_mov_b32_e32 v81, v239
v_mov_b32_e32 v82, v239
v_mov_b32_e32 v83, v239
v_mov_b32_e32 v88, v239
v_mov_b32_e32 v89, v239
v_mov_b32_e32 v90, v239
v_mov_b32_e32 v91, v239
s_branch L_ms1_%=
L_mt1_%=:
v_cndmask_b32_e64 v80, v80, v239, s[56:57]
v_cndmask_b32_e64 v81, v81, v239, s[58:59]
v_cndmask_b32_e64 v82, v82, v239, s[60:61]
v_cndmask_b32_e64 v83, v83, v239, s[62:63]
L_ms1_%=:
v_mfma_f32_16x16x32_bf16 v[68:71], a[148:151], v[164:167], v[68:71]
v_exp_f32_e32 v64, v64
v_mfma_f32_16x16x32_bf16 v[76:79], a[148:151], v[180:183], v[76:79]
ds_read_b128 a[196:199], v228 offset:1024
v_mfma_f32_16x16x32_bf16 v[84:87], a[148:151], v[196:199], v[84:87]
v_exp_f32_e32 v65, v65
v_mfma_f32_16x16x32_bf16 v[92:95], a[148:151], v[212:215], v[92:95]
v_cvt_pk_bf16_f32 v112, v64, v65
s_waitcnt lgkmcnt(2)
v_mfma_f32_16x16x32_bf16 v[68:71], a[152:155], v[168:171], v[68:71]
v_exp_f32_e32 v66, v66
v_mfma_f32_16x16x32_bf16 v[76:79], a[152:155], v[184:187], v[76:79]
ds_read_b128 a[200:203], v228 offset:2048
v_mfma_f32_16x16x32_bf16 v[84:87], a[152:155], v[200:203], v[84:87]
v_exp_f32_e32 v67, v67
v_mfma_f32_16x16x32_bf16 v[92:95], a[152:155], v[216:219], v[92:95]
v_cvt_pk_bf16_f32 v113, v66, v67
v_mfma_f32_16x16x32_bf16 v[68:71], a[156:159], v[172:175], v[68:71]
)ASM"
R"ASM(v_exp_f32_e32 v72, v72
v_mfma_f32_16x16x32_bf16 v[76:79], a[156:159], v[188:191], v[76:79]
ds_read_b128 a[204:207], v228 offset:3072
v_mfma_f32_16x16x32_bf16 v[84:87], a[156:159], v[204:207], v[84:87]
v_exp_f32_e32 v73, v73
v_mfma_f32_16x16x32_bf16 v[92:95], a[156:159], v[220:223], v[92:95]
v_cvt_pk_bf16_f32 v116, v72, v73
s_waitcnt lgkmcnt(2)
v_mfma_f32_16x16x32_bf16 a[0:3], a[192:195], v[96:99], a[0:3]
v_exp_f32_e32 v74, v74
v_mfma_f32_16x16x32_bf16 a[4:7], a[192:195], v[100:103], a[4:7]
ds_read_b128 a[208:211], v228 offset:4096
v_mfma_f32_16x16x32_bf16 a[8:11], a[192:195], v[104:107], a[8:11]
v_exp_f32_e32 v75, v75
v_mfma_f32_16x16x32_bf16 a[12:15], a[192:195], v[108:111], a[12:15]
v_cvt_pk_bf16_f32 v117, v74, v75
v_mfma_f32_16x16x32_bf16 a[16:19], a[196:199], v[96:99], a[16:19]
v_exp_f32_e32 v80, v80
v_mfma_f32_16x16x32_bf16 a[20:23], a[196:199], v[100:103], a[20:23]
ds_read_b128 a[212:215], v228 offset:5120
v_mfma_f32_16x16x32_bf16 a[24:27], a[196:199], v[104:107], a[24:27]
v_exp_f32_e32 v81, v81
v_mfma_f32_16x16x32_bf16 a[28:31], a[196:199], v[108:111], a[28:31]
v_cvt_pk_bf16_f32 v120, v80, v81
s_waitcnt lgkmcnt(2)
v_mfma_f32_16x16x32_bf16 a[32:35], a[200:203], v[96:99], a[32:35]
v_exp_f32_e32 v82, v82
v_mfma_f32_16x16x32_bf16 a[36:39], a[200:203], v[100:103], a[36:39]
ds_read_b128 a[216:219], v228 offset:6144
s_cmp_ge_i32 s44, s43
s_cbranch_scc0 L_ms2_%=
s_cmp_eq_i32 s44, s43
s_cbranch_scc1 L_mt2_%=
v_mov_b32_e32 v84, v239
v_mov_b32_e32 v85, v239
v_mov_b32_e32 v86, v239
v_mov_b32_e32 v87, v239
v_mov_b32_e32 v92, v239
v_mov_b32_e32 v93, v239
v_mov_b32_e32 v94, v239
v_mov_b32_e32 v95, v239
s_branch L_ms2_%=
L_mt2_%=:
v_mov_b32_e32 v84, v239
v_mov_b32_e32 v85, v239
v_mov_b32_e32 v86, v239
v_mov_b32_e32 v87, v239
v_cndmask_b32_e64 v92, v92, v239, s[56:57]
v_cndmask_b32_e64 v93, v93, v239, s[58:59]
v_cndmask_b32_e64 v94, v94, v239, s[60:61]
v_cndmask_b32_e64 v95, v95, v239, s[62:63]
L_ms2_%=:
v_mfma_f32_16x16x32_bf16 a[40:43], a[200:203], v[104:107], a[40:43]
v_exp_f32_e32 v83, v83
v_mfma_f32_16x16x32_bf16 a[44:47], a[200:203], v[108:111], a[44:47]
v_cvt_pk_bf16_f32 v121, v82, v83
v_mfma_f32_16x16x32_bf16 a[48:51], a[204:207], v[96:99], a[48:51]
v_exp_f32_e32 v88, v88
v_mfma_f32_16x16x32_bf16 a[52:55], a[204:207], v[100:103], a[52:55]
ds_read_b128 a[220:223], v228 offset:7168
v_mfma_f32_16x16x32_bf16 a[56:59], a[204:207], v[104:107], a[56:59]
v_exp_f32_e32 v89, v89
v_mfma_f32_16x16x32_bf16 a[60:63], a[204:207], v[108:111], a[60:63]
v_cvt_pk_bf16_f32 v124, v88, v89
s_waitcnt lgkmcnt(2)
v_mfma_f32_16x16x32_bf16 a[64:67], a[208:211], v[96:99], a[64:67]
s_waitcnt vmcnt(8)
s_barrier
v_exp_f32_e32 v90, v90
v_mfma_f32_16x16x32_bf16 a[68:71], a[208:211], v[100:103], a[68:71]
v_mfma_f32_16x16x32_bf16 a[72:75], a[208:211], v[104:107], a[72:75]
v_exp_f32_e32 v91, v91
v_mfma_f32_16x16x32_bf16 a[76:79], a[208:211], v[108:111], a[76:79]
v_cvt_pk_bf16_f32 v125, v90, v91
v_mfma_f32_16x16x32_bf16 a[80:83], a[212:215], v[96:99], a[80:83]
v_exp_f32_e32 v68, v68
v_mfma_f32_16x16x32_bf16 a[84:87], a[212:215], v[100:103], a[84:87]
ds_read_b128 a[128:131], v224 offset:16384
v_mfma_f32_16x16x32_bf16 a[88:91], a[212:215], v[104:107], a[88:91]
v_exp_f32_e32 v69, v69
v_mfma_f32_16x16x32_bf16 a[92:95], a[212:215], v[108:111], a[92:95]
v_cvt_pk_bf16_f32 v114, v68, v69
s_waitcnt lgkmcnt(1)
v_mfma_f32_16x16x32_bf16 a[96:99], a[216:219], v[96:99], a[96:99]
v_exp_f32_e32 v70, v70
v_mfma_f32_16x16x32_bf16 a[100:103], a[216:219], v[100:103], a[100:103]
ds_read_b128 a[132:135], v224 offset:20480
v_mfma_f32_16x16x32_bf16 a[104:107], a[216:219], v[104:107], a[104:107]
v_exp_f32_e32 v71, v71
v_mfma_f32_16x16x32_bf16 a[108:111], a[216:219], v[108:111], a[108:111]
v_cvt_pk_bf16_f32 v115, v70, v71
v_mfma_f32_16x16x32_bf16 a[112:115], a[220:223], v[96:99], a[112:115]
v_exp_f32_e32 v76, v76
v_mfma_f32_16x16x32_bf16 a[116:119], a[220:223], v[100:103], a[116:119]
ds_read_b128 a[136:139], v224 offset:24576
v_mfma_f32_16x16x32_bf16 a[120:123], a[220:223], v[104:107], a[120:123]
v_exp_f32_e32 v77, v77
v_mfma_f32_16x16x32_bf16 a[124:127], a[220:223], v[108:111], a[124:127]
v_cvt_pk_bf16_f32 v118, v76, v77
v_mfma_f32_16x16x32_bf16 v[144:147], v[248:251], v[96:99], v[144:147]
v_exp_f32_e32 v78, v78
v_mfma_f32_16x16x32_bf16 v[148:151], v[248:251], v[100:103], v[148:151]
ds_read_b128 a[140:143], v224 offset:28672
v_mfma_f32_16x16x32_bf16 v[152:155], v[248:251], v[104:107], v[152:155]
v_exp_f32_e32 v79, v79
v_mfma_f32_16x16x32_bf16 v[156:159], v[248:251], v[108:111], v[156:159]
v_cvt_pk_bf16_f32 v119, v78, v79
L_D0b_%=:
s_waitcnt lgkmcnt(2)
v_mfma_f32_16x16x32_bf16 v[32:35], a[128:131], v[160:163], v[128:131]
s_add_i32 s44, s44, 1
v_exp_f32_e32 v84, v84
v_mfma_f32_16x16x32_bf16 v[40:43], a[128:131], v[176:179], v[132:135]
s_add_u32 m0, s42, 0
ds_read_b128 a[144:147], v224 offset:17408
v_mfma_f32_16x16x32_bf16 v[48:51], a[128:131], v[192:195], v[136:139]
buffer_load_dwordx4 v238, s[36:39], s41 offen sc1 lds
v_exp_f32_e32 v85, v85
v_mfma_f32_16x16x32_bf16 v[56:59], a[128:131], v[208:211], v[140:143]
v_cvt_pk_bf16_f32 v122, v84, v85
v_mfma_f32_16x16x32_bf16 v[32:35], a[132:135], v[164:167], v[32:35]
v_exp_f32_e32 v86, v86
v_mfma_f32_16x16x32_bf16 v[40:43], a[132:135], v[180:183], v[40:43]
ds_read_b128 a[148:151], v224 offset:21504
v_mfma_f32_16x16x32_bf16 v[48:51], a[132:135], v[196:199], v[48:51]
v_exp_f32_e32 v87, v87
v_mfma_f32_16x16x32_bf16 v[56:59], a[132:135], v[212:215], v[56:59]
v_cvt_pk_bf16_f32 v123, v86, v87
s_waitcnt lgkmcnt(2)
v_mfma_f32_16x16x32_bf16 v[32:35], a[136:139], v[168:171], v[32:35]
v_exp_f32_e32 v92, v92
v_mfma_f32_16x16x32_bf16 v[40:43], a[136:139], v[184:187], v[40:43]
ds_read_b128 a[152:155], v224 offset:25600
v_mfma_f32_16x16x32_bf16 v[48:51], a[136:139], v[200:203], v[48:51]
buffer_load_dwordx4 v238, s[36:39], s41 offen offset:1024 sc1 lds
v_exp_f32_e32 v93, v93
v_mfma_f32_16x16x32_bf16 v[56:59], a[136:139], v[216:219], v[56:59]
v_cvt_pk_bf16_f32 v126, v92, v93
v_mfma_f32_16x16x32_bf16 v[32:35], a[140:143], v[172:175], v[32:35]
v_exp_f32_e32 v94, v94
v_mfma_f32_16x16x32_bf16 v[40:43], a[140:143], v[188:191], v[40:43]
ds_read_b128 a[156:159], v224 offset:29696
v_mfma_f32_16x16x32_bf16 v[48:51], a[140:143], v[204:207], v[48:51]
v_exp_f32_e32 v95, v95
v_mfma_f32_16x16x32_bf16 v[56:59], a[140:143], v[220:223], v[56:59]
v_cvt_pk_bf16_f32 v127, v94, v95
s_waitcnt lgkmcnt(2)
v_mfma_f32_16x16x32_bf16 v[36:39], a[144:147], v[160:163], v[128:131]
v_mfma_f32_16x16x32_bf16 v[44:47], a[144:147], v[176:179], v[132:135]
ds_read_b128 a[192:195], v228 offset:8192
v_mfma_f32_16x16x32_bf16 v[52:55], a[144:147], v[192:195], v[136:139]
buffer_load_dwordx4 v238, s[36:39], s41 offen offset:2048 sc1 lds
v_mfma_f32_16x16x32_bf16 v[60:63], a[144:147], v[208:211], v[140:143]
s_cmp_ge_i32 s44, s43
s_cbranch_scc0 L_ms3_%=
s_cmp_eq_i32 s44, s43
s_cbranch_scc1 L_mt3_%=
v_mov_b32_e32 v48, v239
v_mov_b32_e32 v49, v239
v_mov_b32_e32 v50, v239
v_mov_b32_e32 v51, v239
v_mov_b32_e32 v56, v239
v_mov_b32_e32 v57, v239
v_mov_b32_e32 v58, v239
v_mov_b32_e32 v59, v239
s_branch L_ms3_%=
L_mt3_%=:
v_cndmask_b32_e64 v48, v48, v239, s[56:57]
v_cndmask_b32_e64 v49, v49, v239, s[58:59]
v_cndmask_b32_e64 v50, v50, v239, s[60:61]
v_cndmask_b32_e64 v51, v51, v239, s[62:63]
L_ms3_%=:
v_mfma_f32_16x16x32_bf16 v[36:39], a[148:151], v[164:167], v[36:39]
v_exp_f32_e32 v32, v32
v_mfma_f32_16x16x32_bf16 v[44:47], a[148:151], v[180:183], v[44:47]
ds_read_b128 a[196:199], v228 offset:9216
v_mfma_f32_16x16x32_bf16 v[52:55], a[148:151], v[196:199], v[52:55]
v_exp_f32_e32 v33, v33
v_mfma_f32_16x16x32_bf16 v[60:63], a[148:151], v[212:215], v[60:63]
v_cvt_pk_bf16_f32 v96, v32, v33
s_waitcnt lgkmcnt(2)
v_mfma_f32_16x16x32_bf16 v[36:39], a[152:155], v[168:171], v[36:39]
v_exp_f32_e32 v34, v34
v_mfma_f32_16x16x32_bf16 v[44:47], a[152:155], v[184:187], v[44:47]
ds_read_b128 a[200:203], v228 offset:10240
v_mfma_f32_16x16x32_bf16 v[52:55], a[152:155], v[200:203], v[52:55]
buffer_load_dwordx4 v238, s[36:39], s41 offen offset:3072 sc1 lds
v_exp_f32_e32 v35, v35
v_mfma_f32_16x16x32_bf16 v[60:63], a[152:155], v[216:219], v[60:63]
v_cvt_pk_bf16_f32 v97, v34, v35
v_mfma_f32_16x16x32_bf16 v[36:39], a[156:159], v[172:175], v[36:39]
v_exp_f32_e32 v40, v40
v_mfma_f32_16x16x32_bf16 v[44:47], a[156:159], v[188:191], v[44:47]
ds_read_b128 a[204:207], v228 offset:11264
v_mfma_f32_16x16x32_bf16 v[52:55], a[156:159], v[204:207], v[52:55]
v_exp_f32_e32 v41, v41
v_mfma_f32_16x16x32_bf16 v[60:63], a[156:159], v[220:223], v[60:63]
v_cvt_pk_bf16_f32 v100, v40, v41
s_waitcnt lgkmcnt(2)
v_mfma_f32_16x16x32_bf16 a[0:3], a[192:195], v[112:115], a[0:3]
v_exp_f32_e32 v42, v42
v_mfma_f32_16x16x32_bf16 a[4:7], a[192:195], v[116:119], a[4:7]
s_add_u32 m0, s42, 114688
ds_read_b128 a[208:211], v228 offset:12288
v_mfma_f32_16x16x32_bf16 a[8:11], a[192:195], v[120:123], a[8:11]
buffer_load_dwordx4 v238, s[36:39], s40 offen sc1 lds
v_exp_f32_e32 v43, v43
v_mfma_f32_16x16x32_bf16 a[12:15], a[192:195], v[124:127], a[12:15]
v_cvt_pk_bf16_f32 v101, v42, v43
v_mfma_f32_16x16x32_bf16 a[16:19], a[196:199], v[112:115], a[16:19]
v_exp_f32_e32 v48, v48
v_mfma_f32_16x16x32_bf16 a[20:23], a[196:199], v[116:119], a[20:23]
ds_read_b128 a[212:215], v228 offset:13312
v_mfma_f32_16x16x32_bf16 a[24:27], a[196:199], v[120:123], a[24:27]
v_exp_f32_e32 v49, v49
v_mfma_f32_16x16x32_bf16 a[28:31], a[196:199], v[124:127], a[28:31]
v_cvt_pk_bf16_f32 v104, v48, v49
s_waitcnt lgkmcnt(2)
v_mfma_f32_16x16x32_bf16 a[32:35], a[200:203], v[112:115], a[32:35]
v_exp_f32_e32 v50, v50
v_mfma_f32_16x16x32_bf16 a[36:39], a[200:203], v[116:119], a[36:39]
ds_read_b128 a[216:219], v228 offset:14336
s_cmp_ge_i32 s44, s43
s_cbranch_scc0 L_ms4_%=
s_cmp_eq_i32 s44, s43
s_cbranch_scc1 L_mt4_%=
v_mov_b32_e32 v52, v239
v_mov_b32_e32 v53, v239
v_mov_b32_e32 v54, v239
v_mov_b32_e32 v55, v239
v_mov_b32_e32 v60, v239
v_mov_b32_e32 v61, v239
v_mov_b32_e32 v62, v239
v_mov_b32_e32 v63, v239
s_branch L_ms4_%=
L_mt4_%=:
v_mov_b32_e32 v52, v239
v_mov_b32_e32 v53, v239
v_mov_b32_e32 v54, v239
v_mov_b32_e32 v55, v239
v_cndmask_b32_e64 v60, v60, v239, s[56:57]
v_cndmask_b32_e64 v61, v61, v239, s[58:59]
v_cndmask_b32_e64 v62, v62, v239, s[60:61]
v_cndmask_b32_e64 v63, v63, v239, s[62:63]
L_ms4_%=:
v_mfma_f32_16x16x32_bf16 a[40:43], a[200:203], v[120:123], a[40:43]
buffer_load_dwordx4 v238, s[36:39], s40 offen offset:1024 sc1 lds
v_exp_f32_e32 v51, v51
v_mfma_f32_16x16x32_bf16 a[44:47], a[200:203], v[124:127], a[44:47]
v_cvt_pk_bf16_f32 v105, v50, v51
v_mfma_f32_16x16x32_bf16 a[48:51], a[204:207], v[112:115], a[48:51]
v_exp_f32_e32 v56, v56
v_mfma_f32_16x16x32_bf16 a[52:55], a[204:207], v[116:119], a[52:55]
ds_read_b128 a[220:223], v228 offset:15360
v_mfma_f32_16x16x32_bf16 a[56:59], a[204:207], v[120:123], a[56:59]
v_exp_f32_e32 v57, v57
v_mfma_f32_16x16x32_bf16 a[60:63], a[204:207], v[124:127], a[60:63]
v_cvt_pk_bf16_f32 v108, v56, v57
s_waitcnt lgkmcnt(2)
v_mfma_f32_16x16x32_bf16 a[64:67], a[208:211], v[112:115], a[64:67]
v_exp_f32_e32 v58, v58
v_mfma_f32_16x16x32_bf16 a[68:71], a[208:211], v[116:119], a[68:71]
v_mfma_f32_16x16x32_bf16 a[72:75], a[208:211], v[120:123], a[72:75]
buffer_load_dwordx4 v238, s[36:39], s40 offen offset:2048 sc1 lds
v_exp_f32_e32 v59, v59
v_mfma_f32_16x16x32_bf16 a[76:79], a[208:211], v[124:127], a[76:79]
v_cvt_pk_bf16_f32 v109, v58, v59
v_mfma_f32_16x16x32_bf16 a[80:83], a[212:215], v[112:115], a[80:83]
v_exp_f32_e32 v36, v36
v_mfma_f32_16x16x32_bf16 a[84:87], a[212:215], v[116:119], a[84:87]
ds_read_b128 a[128:131], v224 offset:18432
v_mfma_f32_16x16x32_bf16 a[88:91], a[212:215], v[120:123], a[88:91]
v_exp_f32_e32 v37, v37
v_mfma_f32_16x16x32_bf16 a[92:95], a[212:215], v[124:127], a[92:95]
v_cvt_pk_bf16_f32 v98, v36, v37
s_waitcnt lgkmcnt(1)
v_mfma_f32_16x16x32_bf16 a[96:99], a[216:219], v[112:115], a[96:99]
v_exp_f32_e32 v38, v38
v_mfma_f32_16x16x32_bf16 a[100:103], a[216:219], v[116:119], a[100:103]
ds_read_b128 a[132:135], v224 offset:22528
v_mfma_f32_16x16x32_bf16 a[104:107], a[216:219], v[120:123], a[104:107]
buffer_load_dwordx4 v238, s[36:39], s40 offen offset:3072 sc1 lds
)ASM"
R"ASM(v_exp_f32_e32 v39, v39
v_mfma_f32_16x16x32_bf16 a[108:111], a[216:219], v[124:127], a[108:111]
s_add_u32 s41, s41, 0x8000
v_cvt_pk_bf16_f32 v99, v38, v39
v_mfma_f32_16x16x32_bf16 a[112:115], a[220:223], v[112:115], a[112:115]
s_add_u32 s40, s40, 0x8000
v_exp_f32_e32 v44, v44
v_mfma_f32_16x16x32_bf16 a[116:119], a[220:223], v[116:119], a[116:119]
ds_read_b128 a[136:139], v224 offset:26624
v_mfma_f32_16x16x32_bf16 a[120:123], a[220:223], v[120:123], a[120:123]
v_exp_f32_e32 v45, v45
v_mfma_f32_16x16x32_bf16 a[124:127], a[220:223], v[124:127], a[124:127]
v_cvt_pk_bf16_f32 v102, v44, v45
v_mfma_f32_16x16x32_bf16 v[144:147], v[248:251], v[112:115], v[144:147]
v_exp_f32_e32 v46, v46
v_mfma_f32_16x16x32_bf16 v[148:151], v[248:251], v[116:119], v[148:151]
ds_read_b128 a[140:143], v224 offset:30720
v_mfma_f32_16x16x32_bf16 v[152:155], v[248:251], v[120:123], v[152:155]
v_exp_f32_e32 v47, v47
v_mfma_f32_16x16x32_bf16 v[156:159], v[248:251], v[124:127], v[156:159]
v_cvt_pk_bf16_f32 v103, v46, v47
L_D1a_%=:
s_cmp_eq_u32 s48, s46
s_cbranch_scc1 L_PRE1_%=
s_add_u32 s48, s48, 2
s_waitcnt lgkmcnt(2)
v_mfma_f32_16x16x32_bf16 v[64:67], a[128:131], v[160:163], v[128:131]
s_add_i32 s44, s44, 1
v_exp_f32_e32 v52, v52
v_mfma_f32_16x16x32_bf16 v[72:75], a[128:131], v[176:179], v[132:135]
ds_read_b128 a[144:147], v224 offset:19456
v_mfma_f32_16x16x32_bf16 v[80:83], a[128:131], v[192:195], v[136:139]
v_exp_f32_e32 v53, v53
v_mfma_f32_16x16x32_bf16 v[88:91], a[128:131], v[208:211], v[140:143]
v_cvt_pk_bf16_f32 v106, v52, v53
v_mfma_f32_16x16x32_bf16 v[64:67], a[132:135], v[164:167], v[64:67]
v_exp_f32_e32 v54, v54
v_mfma_f32_16x16x32_bf16 v[72:75], a[132:135], v[180:183], v[72:75]
ds_read_b128 a[148:151], v224 offset:23552
v_mfma_f32_16x16x32_bf16 v[80:83], a[132:135], v[196:199], v[80:83]
v_exp_f32_e32 v55, v55
v_mfma_f32_16x16x32_bf16 v[88:91], a[132:135], v[212:215], v[88:91]
v_cvt_pk_bf16_f32 v107, v54, v55
s_waitcnt lgkmcnt(2)
v_mfma_f32_16x16x32_bf16 v[64:67], a[136:139], v[168:171], v[64:67]
v_exp_f32_e32 v60, v60
v_mfma_f32_16x16x32_bf16 v[72:75], a[136:139], v[184:187], v[72:75]
ds_read_b128 a[152:155], v224 offset:27648
v_mfma_f32_16x16x32_bf16 v[80:83], a[136:139], v[200:203], v[80:83]
v_exp_f32_e32 v61, v61
v_mfma_f32_16x16x32_bf16 v[88:91], a[136:139], v[216:219], v[88:91]
v_cvt_pk_bf16_f32 v110, v60, v61
v_mfma_f32_16x16x32_bf16 v[64:67], a[140:143], v[172:175], v[64:67]
v_exp_f32_e32 v62, v62
v_mfma_f32_16x16x32_bf16 v[72:75], a[140:143], v[188:191], v[72:75]
ds_read_b128 a[156:159], v224 offset:31744
v_mfma_f32_16x16x32_bf16 v[80:83], a[140:143], v[204:207], v[80:83]
v_exp_f32_e32 v63, v63
v_mfma_f32_16x16x32_bf16 v[88:91], a[140:143], v[220:223], v[88:91]
v_cvt_pk_bf16_f32 v111, v62, v63
s_waitcnt lgkmcnt(2)
v_mfma_f32_16x16x32_bf16 v[68:71], a[144:147], v[160:163], v[128:131]
v_mfma_f32_16x16x32_bf16 v[76:79], a[144:147], v[176:179], v[132:135]
ds_read_b128 a[192:195], v228 offset:16384
v_mfma_f32_16x16x32_bf16 v[84:87], a[144:147], v[192:195], v[136:139]
v_mfma_f32_16x16x32_bf16 v[92:95], a[144:147], v[208:211], v[140:143]
s_cmp_ge_i32 s44, s43
s_cbranch_scc0 L_ms7_%=
s_cmp_eq_i32 s44, s43
s_cbranch_scc1 L_mt7_%=
v_mov_b32_e32 v80, v239
v_mov_b32_e32 v81, v239
v_mov_b32_e32 v82, v239
v_mov_b32_e32 v83, v239
v_mov_b32_e32 v88, v239
v_mov_b32_e32 v89, v239
v_mov_b32_e32 v90, v239
v_mov_b32_e32 v91, v239
s_branch L_ms7_%=
L_mt7_%=:
v_cndmask_b32_e64 v80, v80, v239, s[56:57]
v_cndmask_b32_e64 v81, v81, v239, s[58:59]
v_cndmask_b32_e64 v82, v82, v239, s[60:61]
v_cndmask_b32_e64 v83, v83, v239, s[62:63]
L_ms7_%=:
v_mfma_f32_16x16x32_bf16 v[68:71], a[148:151], v[164:167], v[68:71]
v_exp_f32_e32 v64, v64
v_mfma_f32_16x16x32_bf16 v[76:79], a[148:151], v[180:183], v[76:79]
ds_read_b128 a[196:199], v228 offset:17408
v_mfma_f32_16x16x32_bf16 v[84:87], a[148:151], v[196:199], v[84:87]
v_exp_f32_e32 v65, v65
v_mfma_f32_16x16x32_bf16 v[92:95], a[148:151], v[212:215], v[92:95]
v_cvt_pk_bf16_f32 v112, v64, v65
s_waitcnt lgkmcnt(2)
v_mfma_f32_16x16x32_bf16 v[68:71], a[152:155], v[168:171], v[68:71]
v_exp_f32_e32 v66, v66
v_mfma_f32_16x16x32_bf16 v[76:79], a[152:155], v[184:187], v[76:79]
ds_read_b128 a[200:203], v228 offset:18432
v_mfma_f32_16x16x32_bf16 v[84:87], a[152:155], v[200:203], v[84:87]
v_exp_f32_e32 v67, v67
v_mfma_f32_16x16x32_bf16 v[92:95], a[152:155], v[216:219], v[92:95]
v_cvt_pk_bf16_f32 v113, v66, v67
v_mfma_f32_16x16x32_bf16 v[68:71], a[156:159], v[172:175], v[68:71]
v_exp_f32_e32 v72, v72
v_mfma_f32_16x16x32_bf16 v[76:79], a[156:159], v[188:191], v[76:79]
ds_read_b128 a[204:207], v228 offset:19456
v_mfma_f32_16x16x32_bf16 v[84:87], a[156:159], v[204:207], v[84:87]
v_exp_f32_e32 v73, v73
v_mfma_f32_16x16x32_bf16 v[92:95], a[156:159], v[220:223], v[92:95]
v_cvt_pk_bf16_f32 v116, v72, v73
s_waitcnt lgkmcnt(2)
v_mfma_f32_16x16x32_bf16 a[0:3], a[192:195], v[96:99], a[0:3]
v_exp_f32_e32 v74, v74
v_mfma_f32_16x16x32_bf16 a[4:7], a[192:195], v[100:103], a[4:7]
ds_read_b128 a[208:211], v228 offset:20480
v_mfma_f32_16x16x32_bf16 a[8:11], a[192:195], v[104:107], a[8:11]
v_exp_f32_e32 v75, v75
v_mfma_f32_16x16x32_bf16 a[12:15], a[192:195], v[108:111], a[12:15]
v_cvt_pk_bf16_f32 v117, v74, v75
v_mfma_f32_16x16x32_bf16 a[16:19], a[196:199], v[96:99], a[16:19]
v_exp_f32_e32 v80, v80
v_mfma_f32_16x16x32_bf16 a[20:23], a[196:199], v[100:103], a[20:23]
ds_read_b128 a[212:215], v228 offset:21504
v_mfma_f32_16x16x32_bf16 a[24:27], a[196:199], v[104:107], a[24:27]
v_exp_f32_e32 v81, v81
v_mfma_f32_16x16x32_bf16 a[28:31], a[196:199], v[108:111], a[28:31]
v_cvt_pk_bf16_f32 v120, v80, v81
s_waitcnt lgkmcnt(2)
v_mfma_f32_16x16x32_bf16 a[32:35], a[200:203], v[96:99], a[32:35]
v_exp_f32_e32 v82, v82
v_mfma_f32_16x16x32_bf16 a[36:39], a[200:203], v[100:103], a[36:39]
ds_read_b128 a[216:219], v228 offset:22528
s_cmp_ge_i32 s44, s43
s_cbranch_scc0 L_ms8_%=
s_cmp_eq_i32 s44, s43
s_cbranch_scc1 L_mt8_%=
v_mov_b32_e32 v84, v239
v_mov_b32_e32 v85, v239
v_mov_b32_e32 v86, v239
v_mov_b32_e32 v87, v239
v_mov_b32_e32 v92, v239
v_mov_b32_e32 v93, v239
v_mov_b32_e32 v94, v239
v_mov_b32_e32 v95, v239
s_branch L_ms8_%=
L_mt8_%=:
v_mov_b32_e32 v84, v239
v_mov_b32_e32 v85, v239
v_mov_b32_e32 v86, v239
v_mov_b32_e32 v87, v239
v_cndmask_b32_e64 v92, v92, v239, s[56:57]
v_cndmask_b32_e64 v93, v93, v239, s[58:59]
v_cndmask_b32_e64 v94, v94, v239, s[60:61]
v_cndmask_b32_e64 v95, v95, v239, s[62:63]
L_ms8_%=:
v_mfma_f32_16x16x32_bf16 a[40:43], a[200:203], v[104:107], a[40:43]
v_exp_f32_e32 v83, v83
v_mfma_f32_16x16x32_bf16 a[44:47], a[200:203], v[108:111], a[44:47]
v_cvt_pk_bf16_f32 v121, v82, v83
v_mfma_f32_16x16x32_bf16 a[48:51], a[204:207], v[96:99], a[48:51]
v_exp_f32_e32 v88, v88
v_mfma_f32_16x16x32_bf16 a[52:55], a[204:207], v[100:103], a[52:55]
ds_read_b128 a[220:223], v228 offset:23552
v_mfma_f32_16x16x32_bf16 a[56:59], a[204:207], v[104:107], a[56:59]
v_exp_f32_e32 v89, v89
v_mfma_f32_16x16x32_bf16 a[60:63], a[204:207], v[108:111], a[60:63]
v_cvt_pk_bf16_f32 v124, v88, v89
s_waitcnt lgkmcnt(2)
v_mfma_f32_16x16x32_bf16 a[64:67], a[208:211], v[96:99], a[64:67]
s_waitcnt vmcnt(8)
s_barrier
v_exp_f32_e32 v90, v90
v_mfma_f32_16x16x32_bf16 a[68:71], a[208:211], v[100:103], a[68:71]
v_mfma_f32_16x16x32_bf16 a[72:75], a[208:211], v[104:107], a[72:75]
v_exp_f32_e32 v91, v91
v_mfma_f32_16x16x32_bf16 a[76:79], a[208:211], v[108:111], a[76:79]
v_cvt_pk_bf16_f32 v125, v90, v91
v_mfma_f32_16x16x32_bf16 a[80:83], a[212:215], v[96:99], a[80:83]
v_exp_f32_e32 v68, v68
v_mfma_f32_16x16x32_bf16 a[84:87], a[212:215], v[100:103], a[84:87]
ds_read_b128 a[128:131], v224 offset:32768
v_mfma_f32_16x16x32_bf16 a[88:91], a[212:215], v[104:107], a[88:91]
v_exp_f32_e32 v69, v69
v_mfma_f32_16x16x32_bf16 a[92:95], a[212:215], v[108:111], a[92:95]
v_cvt_pk_bf16_f32 v114, v68, v69
s_waitcnt lgkmcnt(1)
v_mfma_f32_16x16x32_bf16 a[96:99], a[216:219], v[96:99], a[96:99]
v_exp_f32_e32 v70, v70
v_mfma_f32_16x16x32_bf16 a[100:103], a[216:219], v[100:103], a[100:103]
ds_read_b128 a[132:135], v224 offset:36864
v_mfma_f32_16x16x32_bf16 a[104:107], a[216:219], v[104:107], a[104:107]
v_exp_f32_e32 v71, v71
v_mfma_f32_16x16x32_bf16 a[108:111], a[216:219], v[108:111], a[108:111]
v_cvt_pk_bf16_f32 v115, v70, v71
v_mfma_f32_16x16x32_bf16 a[112:115], a[220:223], v[96:99], a[112:115]
v_exp_f32_e32 v76, v76
v_mfma_f32_16x16x32_bf16 a[116:119], a[220:223], v[100:103], a[116:119]
ds_read_b128 a[136:139], v224 offset:40960
v_mfma_f32_16x16x32_bf16 a[120:123], a[220:223], v[104:107], a[120:123]
v_exp_f32_e32 v77, v77
v_mfma_f32_16x16x32_bf16 a[124:127], a[220:223], v[108:111], a[124:127]
v_cvt_pk_bf16_f32 v118, v76, v77
v_mfma_f32_16x16x32_bf16 v[144:147], v[248:251], v[96:99], v[144:147]
v_exp_f32_e32 v78, v78
v_mfma_f32_16x16x32_bf16 v[148:151], v[248:251], v[100:103], v[148:151]
ds_read_b128 a[140:143], v224 offset:45056
v_mfma_f32_16x16x32_bf16 v[152:155], v[248:251], v[104:107], v[152:155]
v_exp_f32_e32 v79, v79
v_mfma_f32_16x16x32_bf16 v[156:159], v[248:251], v[108:111], v[156:159]
v_cvt_pk_bf16_f32 v119, v78, v79
L_D1b_%=:
s_waitcnt lgkmcnt(2)
v_mfma_f32_16x16x32_bf16 v[32:35], a[128:131], v[160:163], v[128:131]
s_add_i32 s44, s44, 1
v_exp_f32_e32 v84, v84
v_mfma_f32_16x16x32_bf16 v[40:43], a[128:131], v[176:179], v[132:135]
s_add_u32 m0, s42, 16384
ds_read_b128 a[144:147], v224 offset:33792
v_mfma_f32_16x16x32_bf16 v[48:51], a[128:131], v[192:195], v[136:139]
buffer_load_dwordx4 v238, s[36:39], s41 offen sc1 lds
v_exp_f32_e32 v85, v85
v_mfma_f32_16x16x32_bf16 v[56:59], a[128:131], v[208:211], v[140:143]
v_cvt_pk_bf16_f32 v122, v84, v85
v_mfma_f32_16x16x32_bf16 v[32:35], a[132:135], v[164:167], v[32:35]
v_exp_f32_e32 v86, v86
v_mfma_f32_16x16x32_bf16 v[40:43], a[132:135], v[180:183], v[40:43]
ds_read_b128 a[148:151], v224 offset:37888
v_mfma_f32_16x16x32_bf16 v[48:51], a[132:135], v[196:199], v[48:51]
v_exp_f32_e32 v87, v87
v_mfma_f32_16x16x32_bf16 v[56:59], a[132:135], v[212:215], v[56:59]
v_cvt_pk_bf16_f32 v123, v86, v87
s_waitcnt lgkmcnt(2)
v_mfma_f32_16x16x32_bf16 v[32:35], a[136:139], v[168:171], v[32:35]
v_exp_f32_e32 v92, v92
v_mfma_f32_16x16x32_bf16 v[40:43], a[136:139], v[184:187], v[40:43]
ds_read_b128 a[152:155], v224 offset:41984
v_mfma_f32_16x16x32_bf16 v[48:51], a[136:139], v[200:203], v[48:51]
buffer_load_dwordx4 v238, s[36:39], s41 offen offset:1024 sc1 lds
v_exp_f32_e32 v93, v93
v_mfma_f32_16x16x32_bf16 v[56:59], a[136:139], v[216:219], v[56:59]
v_cvt_pk_bf16_f32 v126, v92, v93
v_mfma_f32_16x16x32_bf16 v[32:35], a[140:143], v[172:175], v[32:35]
v_exp_f32_e32 v94, v94
v_mfma_f32_16x16x32_bf16 v[40:43], a[140:143], v[188:191], v[40:43]
ds_read_b128 a[156:159], v224 offset:46080
v_mfma_f32_16x16x32_bf16 v[48:51], a[140:143], v[204:207], v[48:51]
v_exp_f32_e32 v95, v95
v_mfma_f32_16x16x32_bf16 v[56:59], a[140:143], v[220:223], v[56:59]
v_cvt_pk_bf16_f32 v127, v94, v95
s_waitcnt lgkmcnt(2)
v_mfma_f32_16x16x32_bf16 v[36:39], a[144:147], v[160:163], v[128:131]
v_mfma_f32_16x16x32_bf16 v[44:47], a[144:147], v[176:179], v[132:135]
ds_read_b128 a[192:195], v228 offset:24576
v_mfma_f32_16x16x32_bf16 v[52:55], a[144:147], v[192:195], v[136:139]
buffer_load_dwordx4 v238, s[36:39], s41 offen offset:2048 sc1 lds
v_mfma_f32_16x16x32_bf16 v[60:63], a[144:147], v[208:211], v[140:143]
s_cmp_ge_i32 s44, s43
s_cbranch_scc0 L_ms9_%=
s_cmp_eq_i32 s44, s43
s_cbranch_scc1 L_mt9_%=
v_mov_b32_e32 v48, v239
v_mov_b32_e32 v49, v239
v_mov_b32_e32 v50, v239
v_mov_b32_e32 v51, v239
v_mov_b32_e32 v56, v239
v_mov_b32_e32 v57, v239
v_mov_b32_e32 v58, v239
v_mov_b32_e32 v59, v239
s_branch L_ms9_%=
L_mt9_%=:
v_cndmask_b32_e64 v48, v48, v239, s[56:57]
v_cndmask_b32_e64 v49, v49, v239, s[58:59]
v_cndmask_b32_e64 v50, v50, v239, s[60:61]
v_cndmask_b32_e64 v51, v51, v239, s[62:63]
L_ms9_%=:
v_mfma_f32_16x16x32_bf16 v[36:39], a[148:151], v[164:167], v[36:39]
v_exp_f32_e32 v32, v32
v_mfma_f32_16x16x32_bf16 v[44:47], a[148:151], v[180:183], v[44:47]
ds_read_b128 a[196:199], v228 offset:25600
v_mfma_f32_16x16x32_bf16 v[52:55], a[148:151], v[196:199], v[52:55]
v_exp_f32_e32 v33, v33
v_mfma_f32_16x16x32_bf16 v[60:63], a[148:151], v[212:215], v[60:63]
)ASM"
R"ASM(v_cvt_pk_bf16_f32 v96, v32, v33
s_waitcnt lgkmcnt(2)
v_mfma_f32_16x16x32_bf16 v[36:39], a[152:155], v[168:171], v[36:39]
v_exp_f32_e32 v34, v34
v_mfma_f32_16x16x32_bf16 v[44:47], a[152:155], v[184:187], v[44:47]
ds_read_b128 a[200:203], v228 offset:26624
v_mfma_f32_16x16x32_bf16 v[52:55], a[152:155], v[200:203], v[52:55]
buffer_load_dwordx4 v238, s[36:39], s41 offen offset:3072 sc1 lds
v_exp_f32_e32 v35, v35
v_mfma_f32_16x16x32_bf16 v[60:63], a[152:155], v[216:219], v[60:63]
v_cvt_pk_bf16_f32 v97, v34, v35
v_mfma_f32_16x16x32_bf16 v[36:39], a[156:159], v[172:175], v[36:39]
v_exp_f32_e32 v40, v40
v_mfma_f32_16x16x32_bf16 v[44:47], a[156:159], v[188:191], v[44:47]
ds_read_b128 a[204:207], v228 offset:27648
v_mfma_f32_16x16x32_bf16 v[52:55], a[156:159], v[204:207], v[52:55]
v_exp_f32_e32 v41, v41
v_mfma_f32_16x16x32_bf16 v[60:63], a[156:159], v[220:223], v[60:63]
v_cvt_pk_bf16_f32 v100, v40, v41
s_waitcnt lgkmcnt(2)
v_mfma_f32_16x16x32_bf16 a[0:3], a[192:195], v[112:115], a[0:3]
v_exp_f32_e32 v42, v42
v_mfma_f32_16x16x32_bf16 a[4:7], a[192:195], v[116:119], a[4:7]
s_add_u32 m0, s42, 65536
ds_read_b128 a[208:211], v228 offset:28672
v_mfma_f32_16x16x32_bf16 a[8:11], a[192:195], v[120:123], a[8:11]
buffer_load_dwordx4 v238, s[36:39], s40 offen sc1 lds
v_exp_f32_e32 v43, v43
v_mfma_f32_16x16x32_bf16 a[12:15], a[192:195], v[124:127], a[12:15]
v_cvt_pk_bf16_f32 v101, v42, v43
v_mfma_f32_16x16x32_bf16 a[16:19], a[196:199], v[112:115], a[16:19]
v_exp_f32_e32 v48, v48
v_mfma_f32_16x16x32_bf16 a[20:23], a[196:199], v[116:119], a[20:23]
ds_read_b128 a[212:215], v228 offset:29696
v_mfma_f32_16x16x32_bf16 a[24:27], a[196:199], v[120:123], a[24:27]
v_exp_f32_e32 v49, v49
v_mfma_f32_16x16x32_bf16 a[28:31], a[196:199], v[124:127], a[28:31]
v_cvt_pk_bf16_f32 v104, v48, v49
s_waitcnt lgkmcnt(2)
v_mfma_f32_16x16x32_bf16 a[32:35], a[200:203], v[112:115], a[32:35]
v_exp_f32_e32 v50, v50
v_mfma_f32_16x16x32_bf16 a[36:39], a[200:203], v[116:119], a[36:39]
ds_read_b128 a[216:219], v228 offset:30720
s_cmp_ge_i32 s44, s43
s_cbranch_scc0 L_ms10_%=
s_cmp_eq_i32 s44, s43
s_cbranch_scc1 L_mt10_%=
v_mov_b32_e32 v52, v239
v_mov_b32_e32 v53, v239
v_mov_b32_e32 v54, v239
v_mov_b32_e32 v55, v239
v_mov_b32_e32 v60, v239
v_mov_b32_e32 v61, v239
v_mov_b32_e32 v62, v239
v_mov_b32_e32 v63, v239
s_branch L_ms10_%=
L_mt10_%=:
v_mov_b32_e32 v52, v239
v_mov_b32_e32 v53, v239
v_mov_b32_e32 v54, v239
v_mov_b32_e32 v55, v239
v_cndmask_b32_e64 v60, v60, v239, s[56:57]
v_cndmask_b32_e64 v61, v61, v239, s[58:59]
v_cndmask_b32_e64 v62, v62, v239, s[60:61]
v_cndmask_b32_e64 v63, v63, v239, s[62:63]
L_ms10_%=:
v_mfma_f32_16x16x32_bf16 a[40:43], a[200:203], v[120:123], a[40:43]
buffer_load_dwordx4 v238, s[36:39], s40 offen offset:1024 sc1 lds
v_exp_f32_e32 v51, v51
v_mfma_f32_16x16x32_bf16 a[44:47], a[200:203], v[124:127], a[44:47]
v_cvt_pk_bf16_f32 v105, v50, v51
v_mfma_f32_16x16x32_bf16 a[48:51], a[204:207], v[112:115], a[48:51]
v_exp_f32_e32 v56, v56
v_mfma_f32_16x16x32_bf16 a[52:55], a[204:207], v[116:119], a[52:55]
ds_read_b128 a[220:223], v228 offset:31744
v_mfma_f32_16x16x32_bf16 a[56:59], a[204:207], v[120:123], a[56:59]
v_exp_f32_e32 v57, v57
v_mfma_f32_16x16x32_bf16 a[60:63], a[204:207], v[124:127], a[60:63]
v_cvt_pk_bf16_f32 v108, v56, v57
s_waitcnt lgkmcnt(2)
v_mfma_f32_16x16x32_bf16 a[64:67], a[208:211], v[112:115], a[64:67]
v_exp_f32_e32 v58, v58
v_mfma_f32_16x16x32_bf16 a[68:71], a[208:211], v[116:119], a[68:71]
v_mfma_f32_16x16x32_bf16 a[72:75], a[208:211], v[120:123], a[72:75]
buffer_load_dwordx4 v238, s[36:39], s40 offen offset:2048 sc1 lds
v_exp_f32_e32 v59, v59
v_mfma_f32_16x16x32_bf16 a[76:79], a[208:211], v[124:127], a[76:79]
v_cvt_pk_bf16_f32 v109, v58, v59
v_mfma_f32_16x16x32_bf16 a[80:83], a[212:215], v[112:115], a[80:83]
v_exp_f32_e32 v36, v36
v_mfma_f32_16x16x32_bf16 a[84:87], a[212:215], v[116:119], a[84:87]
ds_read_b128 a[128:131], v224 offset:34816
v_mfma_f32_16x16x32_bf16 a[88:91], a[212:215], v[120:123], a[88:91]
v_exp_f32_e32 v37, v37
v_mfma_f32_16x16x32_bf16 a[92:95], a[212:215], v[124:127], a[92:95]
v_cvt_pk_bf16_f32 v98, v36, v37
s_waitcnt lgkmcnt(1)
v_mfma_f32_16x16x32_bf16 a[96:99], a[216:219], v[112:115], a[96:99]
v_exp_f32_e32 v38, v38
v_mfma_f32_16x16x32_bf16 a[100:103], a[216:219], v[116:119], a[100:103]
ds_read_b128 a[132:135], v224 offset:38912
v_mfma_f32_16x16x32_bf16 a[104:107], a[216:219], v[120:123], a[104:107]
buffer_load_dwordx4 v238, s[36:39], s40 offen offset:3072 sc1 lds
v_exp_f32_e32 v39, v39
v_mfma_f32_16x16x32_bf16 a[108:111], a[216:219], v[124:127], a[108:111]
s_add_u32 s41, s41, 0x8000
v_cvt_pk_bf16_f32 v99, v38, v39
v_mfma_f32_16x16x32_bf16 a[112:115], a[220:223], v[112:115], a[112:115]
s_add_u32 s40, s40, 0x8000
v_exp_f32_e32 v44, v44
v_mfma_f32_16x16x32_bf16 a[116:119], a[220:223], v[116:119], a[116:119]
ds_read_b128 a[136:139], v224 offset:43008
v_mfma_f32_16x16x32_bf16 a[120:123], a[220:223], v[120:123], a[120:123]
v_exp_f32_e32 v45, v45
v_mfma_f32_16x16x32_bf16 a[124:127], a[220:223], v[124:127], a[124:127]
v_cvt_pk_bf16_f32 v102, v44, v45
v_mfma_f32_16x16x32_bf16 v[144:147], v[248:251], v[112:115], v[144:147]
v_exp_f32_e32 v46, v46
v_mfma_f32_16x16x32_bf16 v[148:151], v[248:251], v[116:119], v[148:151]
ds_read_b128 a[140:143], v224 offset:47104
v_mfma_f32_16x16x32_bf16 v[152:155], v[248:251], v[120:123], v[152:155]
v_exp_f32_e32 v47, v47
v_mfma_f32_16x16x32_bf16 v[156:159], v[248:251], v[124:127], v[156:159]
v_cvt_pk_bf16_f32 v103, v46, v47
L_D2a_%=:
s_waitcnt lgkmcnt(2)
v_mfma_f32_16x16x32_bf16 v[64:67], a[128:131], v[160:163], v[128:131]
s_add_i32 s44, s44, 1
v_exp_f32_e32 v52, v52
v_mfma_f32_16x16x32_bf16 v[72:75], a[128:131], v[176:179], v[132:135]
ds_read_b128 a[144:147], v224 offset:35840
v_mfma_f32_16x16x32_bf16 v[80:83], a[128:131], v[192:195], v[136:139]
v_exp_f32_e32 v53, v53
v_mfma_f32_16x16x32_bf16 v[88:91], a[128:131], v[208:211], v[140:143]
v_cvt_pk_bf16_f32 v106, v52, v53
v_mfma_f32_16x16x32_bf16 v[64:67], a[132:135], v[164:167], v[64:67]
v_exp_f32_e32 v54, v54
v_mfma_f32_16x16x32_bf16 v[72:75], a[132:135], v[180:183], v[72:75]
ds_read_b128 a[148:151], v224 offset:39936
v_mfma_f32_16x16x32_bf16 v[80:83], a[132:135], v[196:199], v[80:83]
v_exp_f32_e32 v55, v55
v_mfma_f32_16x16x32_bf16 v[88:91], a[132:135], v[212:215], v[88:91]
v_cvt_pk_bf16_f32 v107, v54, v55
s_waitcnt lgkmcnt(2)
v_mfma_f32_16x16x32_bf16 v[64:67], a[136:139], v[168:171], v[64:67]
v_exp_f32_e32 v60, v60
v_mfma_f32_16x16x32_bf16 v[72:75], a[136:139], v[184:187], v[72:75]
ds_read_b128 a[152:155], v224 offset:44032
v_mfma_f32_16x16x32_bf16 v[80:83], a[136:139], v[200:203], v[80:83]
v_exp_f32_e32 v61, v61
v_mfma_f32_16x16x32_bf16 v[88:91], a[136:139], v[216:219], v[88:91]
v_cvt_pk_bf16_f32 v110, v60, v61
v_mfma_f32_16x16x32_bf16 v[64:67], a[140:143], v[172:175], v[64:67]
v_exp_f32_e32 v62, v62
v_mfma_f32_16x16x32_bf16 v[72:75], a[140:143], v[188:191], v[72:75]
ds_read_b128 a[156:159], v224 offset:48128
v_mfma_f32_16x16x32_bf16 v[80:83], a[140:143], v[204:207], v[80:83]
v_exp_f32_e32 v63, v63
v_mfma_f32_16x16x32_bf16 v[88:91], a[140:143], v[220:223], v[88:91]
v_cvt_pk_bf16_f32 v111, v62, v63
s_waitcnt lgkmcnt(2)
v_mfma_f32_16x16x32_bf16 v[68:71], a[144:147], v[160:163], v[128:131]
v_mfma_f32_16x16x32_bf16 v[76:79], a[144:147], v[176:179], v[132:135]
ds_read_b128 a[192:195], v228 offset:32768
v_mfma_f32_16x16x32_bf16 v[84:87], a[144:147], v[192:195], v[136:139]
v_mfma_f32_16x16x32_bf16 v[92:95], a[144:147], v[208:211], v[140:143]
s_cmp_ge_i32 s44, s43
s_cbranch_scc0 L_ms17_%=
s_cmp_eq_i32 s44, s43
s_cbranch_scc1 L_mt17_%=
v_mov_b32_e32 v80, v239
v_mov_b32_e32 v81, v239
v_mov_b32_e32 v82, v239
v_mov_b32_e32 v83, v239
v_mov_b32_e32 v88, v239
v_mov_b32_e32 v89, v239
v_mov_b32_e32 v90, v239
v_mov_b32_e32 v91, v239
s_branch L_ms17_%=
L_mt17_%=:
v_cndmask_b32_e64 v80, v80, v239, s[56:57]
v_cndmask_b32_e64 v81, v81, v239, s[58:59]
v_cndmask_b32_e64 v82, v82, v239, s[60:61]
v_cndmask_b32_e64 v83, v83, v239, s[62:63]
L_ms17_%=:
v_mfma_f32_16x16x32_bf16 v[68:71], a[148:151], v[164:167], v[68:71]
v_exp_f32_e32 v64, v64
v_mfma_f32_16x16x32_bf16 v[76:79], a[148:151], v[180:183], v[76:79]
ds_read_b128 a[196:199], v228 offset:33792
v_mfma_f32_16x16x32_bf16 v[84:87], a[148:151], v[196:199], v[84:87]
v_exp_f32_e32 v65, v65
v_mfma_f32_16x16x32_bf16 v[92:95], a[148:151], v[212:215], v[92:95]
v_cvt_pk_bf16_f32 v112, v64, v65
s_waitcnt lgkmcnt(2)
v_mfma_f32_16x16x32_bf16 v[68:71], a[152:155], v[168:171], v[68:71]
v_exp_f32_e32 v66, v66
v_mfma_f32_16x16x32_bf16 v[76:79], a[152:155], v[184:187], v[76:79]
ds_read_b128 a[200:203], v228 offset:34816
v_mfma_f32_16x16x32_bf16 v[84:87], a[152:155], v[200:203], v[84:87]
v_exp_f32_e32 v67, v67
v_mfma_f32_16x16x32_bf16 v[92:95], a[152:155], v[216:219], v[92:95]
v_cvt_pk_bf16_f32 v113, v66, v67
v_mfma_f32_16x16x32_bf16 v[68:71], a[156:159], v[172:175], v[68:71]
v_exp_f32_e32 v72, v72
v_mfma_f32_16x16x32_bf16 v[76:79], a[156:159], v[188:191], v[76:79]
ds_read_b128 a[204:207], v228 offset:35840
v_mfma_f32_16x16x32_bf16 v[84:87], a[156:159], v[204:207], v[84:87]
v_exp_f32_e32 v73, v73
v_mfma_f32_16x16x32_bf16 v[92:95], a[156:159], v[220:223], v[92:95]
v_cvt_pk_bf16_f32 v116, v72, v73
s_waitcnt lgkmcnt(2)
v_mfma_f32_16x16x32_bf16 a[0:3], a[192:195], v[96:99], a[0:3]
v_exp_f32_e32 v74, v74
v_mfma_f32_16x16x32_bf16 a[4:7], a[192:195], v[100:103], a[4:7]
ds_read_b128 a[208:211], v228 offset:36864
v_mfma_f32_16x16x32_bf16 a[8:11], a[192:195], v[104:107], a[8:11]
v_exp_f32_e32 v75, v75
v_mfma_f32_16x16x32_bf16 a[12:15], a[192:195], v[108:111], a[12:15]
v_cvt_pk_bf16_f32 v117, v74, v75
v_mfma_f32_16x16x32_bf16 a[16:19], a[196:199], v[96:99], a[16:19]
v_exp_f32_e32 v80, v80
v_mfma_f32_16x16x32_bf16 a[20:23], a[196:199], v[100:103], a[20:23]
ds_read_b128 a[212:215], v228 offset:37888
v_mfma_f32_16x16x32_bf16 a[24:27], a[196:199], v[104:107], a[24:27]
v_exp_f32_e32 v81, v81
v_mfma_f32_16x16x32_bf16 a[28:31], a[196:199], v[108:111], a[28:31]
v_cvt_pk_bf16_f32 v120, v80, v81
s_waitcnt lgkmcnt(2)
v_mfma_f32_16x16x32_bf16 a[32:35], a[200:203], v[96:99], a[32:35]
v_exp_f32_e32 v82, v82
v_mfma_f32_16x16x32_bf16 a[36:39], a[200:203], v[100:103], a[36:39]
ds_read_b128 a[216:219], v228 offset:38912
s_cmp_ge_i32 s44, s43
s_cbranch_scc0 L_ms18_%=
s_cmp_eq_i32 s44, s43
s_cbranch_scc1 L_mt18_%=
v_mov_b32_e32 v84, v239
v_mov_b32_e32 v85, v239
v_mov_b32_e32 v86, v239
v_mov_b32_e32 v87, v239
v_mov_b32_e32 v92, v239
v_mov_b32_e32 v93, v239
v_mov_b32_e32 v94, v239
v_mov_b32_e32 v95, v239
s_branch L_ms18_%=
L_mt18_%=:
v_mov_b32_e32 v84, v239
v_mov_b32_e32 v85, v239
v_mov_b32_e32 v86, v239
v_mov_b32_e32 v87, v239
v_cndmask_b32_e64 v92, v92, v239, s[56:57]
v_cndmask_b32_e64 v93, v93, v239, s[58:59]
v_cndmask_b32_e64 v94, v94, v239, s[60:61]
v_cndmask_b32_e64 v95, v95, v239, s[62:63]
L_ms18_%=:
v_mfma_f32_16x16x32_bf16 a[40:43], a[200:203], v[104:107], a[40:43]
v_exp_f32_e32 v83, v83
v_mfma_f32_16x16x32_bf16 a[44:47], a[200:203], v[108:111], a[44:47]
v_cvt_pk_bf16_f32 v121, v82, v83
v_mfma_f32_16x16x32_bf16 a[48:51], a[204:207], v[96:99], a[48:51]
v_exp_f32_e32 v88, v88
v_mfma_f32_16x16x32_bf16 a[52:55], a[204:207], v[100:103], a[52:55]
ds_read_b128 a[220:223], v228 offset:39936
v_mfma_f32_16x16x32_bf16 a[56:59], a[204:207], v[104:107], a[56:59]
v_exp_f32_e32 v89, v89
v_mfma_f32_16x16x32_bf16 a[60:63], a[204:207], v[108:111], a[60:63]
v_cvt_pk_bf16_f32 v124, v88, v89
s_waitcnt lgkmcnt(2)
v_mfma_f32_16x16x32_bf16 a[64:67], a[208:211], v[96:99], a[64:67]
s_waitcnt vmcnt(8)
s_barrier
v_exp_f32_e32 v90, v90
v_mfma_f32_16x16x32_bf16 a[68:71], a[208:211], v[100:103], a[68:71]
v_mfma_f32_16x16x32_bf16 a[72:75], a[208:211], v[104:107], a[72:75]
v_exp_f32_e32 v91, v91
v_mfma_f32_16x16x32_bf16 a[76:79], a[208:211], v[108:111], a[76:79]
v_cvt_pk_bf16_f32 v125, v90, v91
v_mfma_f32_16x16x32_bf16 a[80:83], a[212:215], v[96:99], a[80:83]
v_exp_f32_e32 v68, v68
v_mfma_f32_16x16x32_bf16 a[84:87], a[212:215], v[100:103], a[84:87]
ds_read_b128 a[128:131], v224 offset:49152
v_mfma_f32_16x16x32_bf16 a[88:91], a[212:215], v[104:107], a[88:91]
)ASM"
R"ASM(v_exp_f32_e32 v69, v69
v_mfma_f32_16x16x32_bf16 a[92:95], a[212:215], v[108:111], a[92:95]
v_cvt_pk_bf16_f32 v114, v68, v69
s_waitcnt lgkmcnt(1)
v_mfma_f32_16x16x32_bf16 a[96:99], a[216:219], v[96:99], a[96:99]
v_exp_f32_e32 v70, v70
v_mfma_f32_16x16x32_bf16 a[100:103], a[216:219], v[100:103], a[100:103]
ds_read_b128 a[132:135], v224 offset:53248
v_mfma_f32_16x16x32_bf16 a[104:107], a[216:219], v[104:107], a[104:107]
v_exp_f32_e32 v71, v71
v_mfma_f32_16x16x32_bf16 a[108:111], a[216:219], v[108:111], a[108:111]
v_cvt_pk_bf16_f32 v115, v70, v71
v_mfma_f32_16x16x32_bf16 a[112:115], a[220:223], v[96:99], a[112:115]
v_exp_f32_e32 v76, v76
v_mfma_f32_16x16x32_bf16 a[116:119], a[220:223], v[100:103], a[116:119]
ds_read_b128 a[136:139], v224 offset:57344
v_mfma_f32_16x16x32_bf16 a[120:123], a[220:223], v[104:107], a[120:123]
v_exp_f32_e32 v77, v77
v_mfma_f32_16x16x32_bf16 a[124:127], a[220:223], v[108:111], a[124:127]
v_cvt_pk_bf16_f32 v118, v76, v77
v_mfma_f32_16x16x32_bf16 v[144:147], v[248:251], v[96:99], v[144:147]
v_exp_f32_e32 v78, v78
v_mfma_f32_16x16x32_bf16 v[148:151], v[248:251], v[100:103], v[148:151]
ds_read_b128 a[140:143], v224 offset:61440
v_mfma_f32_16x16x32_bf16 v[152:155], v[248:251], v[104:107], v[152:155]
v_exp_f32_e32 v79, v79
v_mfma_f32_16x16x32_bf16 v[156:159], v[248:251], v[108:111], v[156:159]
v_cvt_pk_bf16_f32 v119, v78, v79
L_D2b_%=:
s_waitcnt lgkmcnt(2)
v_mfma_f32_16x16x32_bf16 v[32:35], a[128:131], v[160:163], v[128:131]
s_add_i32 s44, s44, 1
v_exp_f32_e32 v84, v84
v_mfma_f32_16x16x32_bf16 v[40:43], a[128:131], v[176:179], v[132:135]
s_add_u32 m0, s42, 32768
ds_read_b128 a[144:147], v224 offset:50176
v_mfma_f32_16x16x32_bf16 v[48:51], a[128:131], v[192:195], v[136:139]
buffer_load_dwordx4 v238, s[36:39], s41 offen sc1 lds
v_exp_f32_e32 v85, v85
v_mfma_f32_16x16x32_bf16 v[56:59], a[128:131], v[208:211], v[140:143]
v_cvt_pk_bf16_f32 v122, v84, v85
v_mfma_f32_16x16x32_bf16 v[32:35], a[132:135], v[164:167], v[32:35]
v_exp_f32_e32 v86, v86
v_mfma_f32_16x16x32_bf16 v[40:43], a[132:135], v[180:183], v[40:43]
ds_read_b128 a[148:151], v224 offset:54272
v_mfma_f32_16x16x32_bf16 v[48:51], a[132:135], v[196:199], v[48:51]
v_exp_f32_e32 v87, v87
v_mfma_f32_16x16x32_bf16 v[56:59], a[132:135], v[212:215], v[56:59]
v_cvt_pk_bf16_f32 v123, v86, v87
s_waitcnt lgkmcnt(2)
v_mfma_f32_16x16x32_bf16 v[32:35], a[136:139], v[168:171], v[32:35]
v_exp_f32_e32 v92, v92
v_mfma_f32_16x16x32_bf16 v[40:43], a[136:139], v[184:187], v[40:43]
ds_read_b128 a[152:155], v224 offset:58368
v_mfma_f32_16x16x32_bf16 v[48:51], a[136:139], v[200:203], v[48:51]
buffer_load_dwordx4 v238, s[36:39], s41 offen offset:1024 sc1 lds
v_exp_f32_e32 v93, v93
v_mfma_f32_16x16x32_bf16 v[56:59], a[136:139], v[216:219], v[56:59]
v_cvt_pk_bf16_f32 v126, v92, v93
v_mfma_f32_16x16x32_bf16 v[32:35], a[140:143], v[172:175], v[32:35]
v_exp_f32_e32 v94, v94
v_mfma_f32_16x16x32_bf16 v[40:43], a[140:143], v[188:191], v[40:43]
ds_read_b128 a[156:159], v224 offset:62464
v_mfma_f32_16x16x32_bf16 v[48:51], a[140:143], v[204:207], v[48:51]
v_exp_f32_e32 v95, v95
v_mfma_f32_16x16x32_bf16 v[56:59], a[140:143], v[220:223], v[56:59]
v_cvt_pk_bf16_f32 v127, v94, v95
s_waitcnt lgkmcnt(2)
v_mfma_f32_16x16x32_bf16 v[36:39], a[144:147], v[160:163], v[128:131]
v_mfma_f32_16x16x32_bf16 v[44:47], a[144:147], v[176:179], v[132:135]
ds_read_b128 a[192:195], v228 offset:40960
v_mfma_f32_16x16x32_bf16 v[52:55], a[144:147], v[192:195], v[136:139]
buffer_load_dwordx4 v238, s[36:39], s41 offen offset:2048 sc1 lds
v_mfma_f32_16x16x32_bf16 v[60:63], a[144:147], v[208:211], v[140:143]
s_cmp_ge_i32 s44, s43
s_cbranch_scc0 L_ms19_%=
s_cmp_eq_i32 s44, s43
s_cbranch_scc1 L_mt19_%=
v_mov_b32_e32 v48, v239
v_mov_b32_e32 v49, v239
v_mov_b32_e32 v50, v239
v_mov_b32_e32 v51, v239
v_mov_b32_e32 v56, v239
v_mov_b32_e32 v57, v239
v_mov_b32_e32 v58, v239
v_mov_b32_e32 v59, v239
s_branch L_ms19_%=
L_mt19_%=:
v_cndmask_b32_e64 v48, v48, v239, s[56:57]
v_cndmask_b32_e64 v49, v49, v239, s[58:59]
v_cndmask_b32_e64 v50, v50, v239, s[60:61]
v_cndmask_b32_e64 v51, v51, v239, s[62:63]
L_ms19_%=:
v_mfma_f32_16x16x32_bf16 v[36:39], a[148:151], v[164:167], v[36:39]
v_exp_f32_e32 v32, v32
v_mfma_f32_16x16x32_bf16 v[44:47], a[148:151], v[180:183], v[44:47]
ds_read_b128 a[196:199], v228 offset:41984
v_mfma_f32_16x16x32_bf16 v[52:55], a[148:151], v[196:199], v[52:55]
v_exp_f32_e32 v33, v33
v_mfma_f32_16x16x32_bf16 v[60:63], a[148:151], v[212:215], v[60:63]
v_cvt_pk_bf16_f32 v96, v32, v33
s_waitcnt lgkmcnt(2)
v_mfma_f32_16x16x32_bf16 v[36:39], a[152:155], v[168:171], v[36:39]
v_exp_f32_e32 v34, v34
v_mfma_f32_16x16x32_bf16 v[44:47], a[152:155], v[184:187], v[44:47]
ds_read_b128 a[200:203], v228 offset:43008
v_mfma_f32_16x16x32_bf16 v[52:55], a[152:155], v[200:203], v[52:55]
buffer_load_dwordx4 v238, s[36:39], s41 offen offset:3072 sc1 lds
v_exp_f32_e32 v35, v35
v_mfma_f32_16x16x32_bf16 v[60:63], a[152:155], v[216:219], v[60:63]
v_cvt_pk_bf16_f32 v97, v34, v35
v_mfma_f32_16x16x32_bf16 v[36:39], a[156:159], v[172:175], v[36:39]
v_exp_f32_e32 v40, v40
v_mfma_f32_16x16x32_bf16 v[44:47], a[156:159], v[188:191], v[44:47]
ds_read_b128 a[204:207], v228 offset:44032
v_mfma_f32_16x16x32_bf16 v[52:55], a[156:159], v[204:207], v[52:55]
v_exp_f32_e32 v41, v41
v_mfma_f32_16x16x32_bf16 v[60:63], a[156:159], v[220:223], v[60:63]
v_cvt_pk_bf16_f32 v100, v40, v41
s_waitcnt lgkmcnt(2)
v_mfma_f32_16x16x32_bf16 a[0:3], a[192:195], v[112:115], a[0:3]
v_exp_f32_e32 v42, v42
v_mfma_f32_16x16x32_bf16 a[4:7], a[192:195], v[116:119], a[4:7]
s_add_u32 m0, s42, 81920
ds_read_b128 a[208:211], v228 offset:45056
v_mfma_f32_16x16x32_bf16 a[8:11], a[192:195], v[120:123], a[8:11]
buffer_load_dwordx4 v238, s[36:39], s40 offen sc1 lds
v_exp_f32_e32 v43, v43
v_mfma_f32_16x16x32_bf16 a[12:15], a[192:195], v[124:127], a[12:15]
v_cvt_pk_bf16_f32 v101, v42, v43
v_mfma_f32_16x16x32_bf16 a[16:19], a[196:199], v[112:115], a[16:19]
v_exp_f32_e32 v48, v48
v_mfma_f32_16x16x32_bf16 a[20:23], a[196:199], v[116:119], a[20:23]
ds_read_b128 a[212:215], v228 offset:46080
v_mfma_f32_16x16x32_bf16 a[24:27], a[196:199], v[120:123], a[24:27]
v_exp_f32_e32 v49, v49
v_mfma_f32_16x16x32_bf16 a[28:31], a[196:199], v[124:127], a[28:31]
v_cvt_pk_bf16_f32 v104, v48, v49
s_waitcnt lgkmcnt(2)
v_mfma_f32_16x16x32_bf16 a[32:35], a[200:203], v[112:115], a[32:35]
v_exp_f32_e32 v50, v50
v_mfma_f32_16x16x32_bf16 a[36:39], a[200:203], v[116:119], a[36:39]
ds_read_b128 a[216:219], v228 offset:47104
s_cmp_ge_i32 s44, s43
s_cbranch_scc0 L_ms20_%=
s_cmp_eq_i32 s44, s43
s_cbranch_scc1 L_mt20_%=
v_mov_b32_e32 v52, v239
v_mov_b32_e32 v53, v239
v_mov_b32_e32 v54, v239
v_mov_b32_e32 v55, v239
v_mov_b32_e32 v60, v239
v_mov_b32_e32 v61, v239
v_mov_b32_e32 v62, v239
v_mov_b32_e32 v63, v239
s_branch L_ms20_%=
L_mt20_%=:
v_mov_b32_e32 v52, v239
v_mov_b32_e32 v53, v239
v_mov_b32_e32 v54, v239
v_mov_b32_e32 v55, v239
v_cndmask_b32_e64 v60, v60, v239, s[56:57]
v_cndmask_b32_e64 v61, v61, v239, s[58:59]
v_cndmask_b32_e64 v62, v62, v239, s[60:61]
v_cndmask_b32_e64 v63, v63, v239, s[62:63]
L_ms20_%=:
v_mfma_f32_16x16x32_bf16 a[40:43], a[200:203], v[120:123], a[40:43]
buffer_load_dwordx4 v238, s[36:39], s40 offen offset:1024 sc1 lds
v_exp_f32_e32 v51, v51
v_mfma_f32_16x16x32_bf16 a[44:47], a[200:203], v[124:127], a[44:47]
v_cvt_pk_bf16_f32 v105, v50, v51
v_mfma_f32_16x16x32_bf16 a[48:51], a[204:207], v[112:115], a[48:51]
v_exp_f32_e32 v56, v56
v_mfma_f32_16x16x32_bf16 a[52:55], a[204:207], v[116:119], a[52:55]
ds_read_b128 a[220:223], v228 offset:48128
v_mfma_f32_16x16x32_bf16 a[56:59], a[204:207], v[120:123], a[56:59]
v_exp_f32_e32 v57, v57
v_mfma_f32_16x16x32_bf16 a[60:63], a[204:207], v[124:127], a[60:63]
v_cvt_pk_bf16_f32 v108, v56, v57
s_waitcnt lgkmcnt(2)
v_mfma_f32_16x16x32_bf16 a[64:67], a[208:211], v[112:115], a[64:67]
v_exp_f32_e32 v58, v58
v_mfma_f32_16x16x32_bf16 a[68:71], a[208:211], v[116:119], a[68:71]
v_mfma_f32_16x16x32_bf16 a[72:75], a[208:211], v[120:123], a[72:75]
buffer_load_dwordx4 v238, s[36:39], s40 offen offset:2048 sc1 lds
v_exp_f32_e32 v59, v59
v_mfma_f32_16x16x32_bf16 a[76:79], a[208:211], v[124:127], a[76:79]
v_cvt_pk_bf16_f32 v109, v58, v59
v_mfma_f32_16x16x32_bf16 a[80:83], a[212:215], v[112:115], a[80:83]
v_exp_f32_e32 v36, v36
v_mfma_f32_16x16x32_bf16 a[84:87], a[212:215], v[116:119], a[84:87]
ds_read_b128 a[128:131], v224 offset:51200
v_mfma_f32_16x16x32_bf16 a[88:91], a[212:215], v[120:123], a[88:91]
v_exp_f32_e32 v37, v37
v_mfma_f32_16x16x32_bf16 a[92:95], a[212:215], v[124:127], a[92:95]
v_cvt_pk_bf16_f32 v98, v36, v37
s_waitcnt lgkmcnt(1)
v_mfma_f32_16x16x32_bf16 a[96:99], a[216:219], v[112:115], a[96:99]
v_exp_f32_e32 v38, v38
v_mfma_f32_16x16x32_bf16 a[100:103], a[216:219], v[116:119], a[100:103]
ds_read_b128 a[132:135], v224 offset:55296
v_mfma_f32_16x16x32_bf16 a[104:107], a[216:219], v[120:123], a[104:107]
buffer_load_dwordx4 v238, s[36:39], s40 offen offset:3072 sc1 lds
v_exp_f32_e32 v39, v39
v_mfma_f32_16x16x32_bf16 a[108:111], a[216:219], v[124:127], a[108:111]
s_add_u32 s41, s41, 0x8000
v_cvt_pk_bf16_f32 v99, v38, v39
v_mfma_f32_16x16x32_bf16 a[112:115], a[220:223], v[112:115], a[112:115]
s_add_u32 s40, s40, 0x8000
v_exp_f32_e32 v44, v44
v_mfma_f32_16x16x32_bf16 a[116:119], a[220:223], v[116:119], a[116:119]
ds_read_b128 a[136:139], v224 offset:59392
v_mfma_f32_16x16x32_bf16 a[120:123], a[220:223], v[120:123], a[120:123]
v_exp_f32_e32 v45, v45
v_mfma_f32_16x16x32_bf16 a[124:127], a[220:223], v[124:127], a[124:127]
v_cvt_pk_bf16_f32 v102, v44, v45
v_mfma_f32_16x16x32_bf16 v[144:147], v[248:251], v[112:115], v[144:147]
v_exp_f32_e32 v46, v46
v_mfma_f32_16x16x32_bf16 v[148:151], v[248:251], v[116:119], v[148:151]
ds_read_b128 a[140:143], v224 offset:63488
v_mfma_f32_16x16x32_bf16 v[152:155], v[248:251], v[120:123], v[152:155]
v_exp_f32_e32 v47, v47
v_mfma_f32_16x16x32_bf16 v[156:159], v[248:251], v[124:127], v[156:159]
v_cvt_pk_bf16_f32 v103, v46, v47
L_D3a_%=:
s_cmp_eq_u32 s48, s46
s_cbranch_scc1 L_PRE3_%=
s_add_u32 s48, s48, 2
s_waitcnt lgkmcnt(2)
v_mfma_f32_16x16x32_bf16 v[64:67], a[128:131], v[160:163], v[128:131]
s_add_i32 s44, s44, 1
v_exp_f32_e32 v52, v52
v_mfma_f32_16x16x32_bf16 v[72:75], a[128:131], v[176:179], v[132:135]
ds_read_b128 a[144:147], v224 offset:52224
v_mfma_f32_16x16x32_bf16 v[80:83], a[128:131], v[192:195], v[136:139]
v_exp_f32_e32 v53, v53
v_mfma_f32_16x16x32_bf16 v[88:91], a[128:131], v[208:211], v[140:143]
v_cvt_pk_bf16_f32 v106, v52, v53
v_mfma_f32_16x16x32_bf16 v[64:67], a[132:135], v[164:167], v[64:67]
v_exp_f32_e32 v54, v54
v_mfma_f32_16x16x32_bf16 v[72:75], a[132:135], v[180:183], v[72:75]
ds_read_b128 a[148:151], v224 offset:56320
v_mfma_f32_16x16x32_bf16 v[80:83], a[132:135], v[196:199], v[80:83]
v_exp_f32_e32 v55, v55
v_mfma_f32_16x16x32_bf16 v[88:91], a[132:135], v[212:215], v[88:91]
v_cvt_pk_bf16_f32 v107, v54, v55
s_waitcnt lgkmcnt(2)
v_mfma_f32_16x16x32_bf16 v[64:67], a[136:139], v[168:171], v[64:67]
v_exp_f32_e32 v60, v60
v_mfma_f32_16x16x32_bf16 v[72:75], a[136:139], v[184:187], v[72:75]
ds_read_b128 a[152:155], v224 offset:60416
v_mfma_f32_16x16x32_bf16 v[80:83], a[136:139], v[200:203], v[80:83]
v_exp_f32_e32 v61, v61
v_mfma_f32_16x16x32_bf16 v[88:91], a[136:139], v[216:219], v[88:91]
v_cvt_pk_bf16_f32 v110, v60, v61
v_mfma_f32_16x16x32_bf16 v[64:67], a[140:143], v[172:175], v[64:67]
v_exp_f32_e32 v62, v62
v_mfma_f32_16x16x32_bf16 v[72:75], a[140:143], v[188:191], v[72:75]
ds_read_b128 a[156:159], v224 offset:64512
v_mfma_f32_16x16x32_bf16 v[80:83], a[140:143], v[204:207], v[80:83]
v_exp_f32_e32 v63, v63
v_mfma_f32_16x16x32_bf16 v[88:91], a[140:143], v[220:223], v[88:91]
v_cvt_pk_bf16_f32 v111, v62, v63
s_waitcnt lgkmcnt(2)
v_mfma_f32_16x16x32_bf16 v[68:71], a[144:147], v[160:163], v[128:131]
v_mfma_f32_16x16x32_bf16 v[76:79], a[144:147], v[176:179], v[132:135]
ds_read_b128 a[192:195], v228 offset:49152
v_mfma_f32_16x16x32_bf16 v[84:87], a[144:147], v[192:195], v[136:139]
v_mfma_f32_16x16x32_bf16 v[92:95], a[144:147], v[208:211], v[140:143]
s_cmp_ge_i32 s44, s43
s_cbranch_scc0 L_ms23_%=
)ASM"
R"ASM(s_cmp_eq_i32 s44, s43
s_cbranch_scc1 L_mt23_%=
v_mov_b32_e32 v80, v239
v_mov_b32_e32 v81, v239
v_mov_b32_e32 v82, v239
v_mov_b32_e32 v83, v239
v_mov_b32_e32 v88, v239
v_mov_b32_e32 v89, v239
v_mov_b32_e32 v90, v239
v_mov_b32_e32 v91, v239
s_branch L_ms23_%=
L_mt23_%=:
v_cndmask_b32_e64 v80, v80, v239, s[56:57]
v_cndmask_b32_e64 v81, v81, v239, s[58:59]
v_cndmask_b32_e64 v82, v82, v239, s[60:61]
v_cndmask_b32_e64 v83, v83, v239, s[62:63]
L_ms23_%=:
v_mfma_f32_16x16x32_bf16 v[68:71], a[148:151], v[164:167], v[68:71]
v_exp_f32_e32 v64, v64
v_mfma_f32_16x16x32_bf16 v[76:79], a[148:151], v[180:183], v[76:79]
ds_read_b128 a[196:199], v228 offset:50176
v_mfma_f32_16x16x32_bf16 v[84:87], a[148:151], v[196:199], v[84:87]
v_exp_f32_e32 v65, v65
v_mfma_f32_16x16x32_bf16 v[92:95], a[148:151], v[212:215], v[92:95]
v_cvt_pk_bf16_f32 v112, v64, v65
s_waitcnt lgkmcnt(2)
v_mfma_f32_16x16x32_bf16 v[68:71], a[152:155], v[168:171], v[68:71]
v_exp_f32_e32 v66, v66
v_mfma_f32_16x16x32_bf16 v[76:79], a[152:155], v[184:187], v[76:79]
ds_read_b128 a[200:203], v228 offset:51200
v_mfma_f32_16x16x32_bf16 v[84:87], a[152:155], v[200:203], v[84:87]
v_exp_f32_e32 v67, v67
v_mfma_f32_16x16x32_bf16 v[92:95], a[152:155], v[216:219], v[92:95]
v_cvt_pk_bf16_f32 v113, v66, v67
v_mfma_f32_16x16x32_bf16 v[68:71], a[156:159], v[172:175], v[68:71]
v_exp_f32_e32 v72, v72
v_mfma_f32_16x16x32_bf16 v[76:79], a[156:159], v[188:191], v[76:79]
ds_read_b128 a[204:207], v228 offset:52224
v_mfma_f32_16x16x32_bf16 v[84:87], a[156:159], v[204:207], v[84:87]
v_exp_f32_e32 v73, v73
v_mfma_f32_16x16x32_bf16 v[92:95], a[156:159], v[220:223], v[92:95]
v_cvt_pk_bf16_f32 v116, v72, v73
s_waitcnt lgkmcnt(2)
v_mfma_f32_16x16x32_bf16 a[0:3], a[192:195], v[96:99], a[0:3]
v_exp_f32_e32 v74, v74
v_mfma_f32_16x16x32_bf16 a[4:7], a[192:195], v[100:103], a[4:7]
ds_read_b128 a[208:211], v228 offset:53248
v_mfma_f32_16x16x32_bf16 a[8:11], a[192:195], v[104:107], a[8:11]
v_exp_f32_e32 v75, v75
v_mfma_f32_16x16x32_bf16 a[12:15], a[192:195], v[108:111], a[12:15]
v_cvt_pk_bf16_f32 v117, v74, v75
v_mfma_f32_16x16x32_bf16 a[16:19], a[196:199], v[96:99], a[16:19]
v_exp_f32_e32 v80, v80
v_mfma_f32_16x16x32_bf16 a[20:23], a[196:199], v[100:103], a[20:23]
ds_read_b128 a[212:215], v228 offset:54272
v_mfma_f32_16x16x32_bf16 a[24:27], a[196:199], v[104:107], a[24:27]
v_exp_f32_e32 v81, v81
v_mfma_f32_16x16x32_bf16 a[28:31], a[196:199], v[108:111], a[28:31]
v_cvt_pk_bf16_f32 v120, v80, v81
s_waitcnt lgkmcnt(2)
v_mfma_f32_16x16x32_bf16 a[32:35], a[200:203], v[96:99], a[32:35]
v_exp_f32_e32 v82, v82
v_mfma_f32_16x16x32_bf16 a[36:39], a[200:203], v[100:103], a[36:39]
ds_read_b128 a[216:219], v228 offset:55296
s_cmp_ge_i32 s44, s43
s_cbranch_scc0 L_ms24_%=
s_cmp_eq_i32 s44, s43
s_cbranch_scc1 L_mt24_%=
v_mov_b32_e32 v84, v239
v_mov_b32_e32 v85, v239
v_mov_b32_e32 v86, v239
v_mov_b32_e32 v87, v239
v_mov_b32_e32 v92, v239
v_mov_b32_e32 v93, v239
v_mov_b32_e32 v94, v239
v_mov_b32_e32 v95, v239
s_branch L_ms24_%=
L_mt24_%=:
v_mov_b32_e32 v84, v239
v_mov_b32_e32 v85, v239
v_mov_b32_e32 v86, v239
v_mov_b32_e32 v87, v239
v_cndmask_b32_e64 v92, v92, v239, s[56:57]
v_cndmask_b32_e64 v93, v93, v239, s[58:59]
v_cndmask_b32_e64 v94, v94, v239, s[60:61]
v_cndmask_b32_e64 v95, v95, v239, s[62:63]
L_ms24_%=:
v_mfma_f32_16x16x32_bf16 a[40:43], a[200:203], v[104:107], a[40:43]
v_exp_f32_e32 v83, v83
v_mfma_f32_16x16x32_bf16 a[44:47], a[200:203], v[108:111], a[44:47]
v_cvt_pk_bf16_f32 v121, v82, v83
v_mfma_f32_16x16x32_bf16 a[48:51], a[204:207], v[96:99], a[48:51]
v_exp_f32_e32 v88, v88
v_mfma_f32_16x16x32_bf16 a[52:55], a[204:207], v[100:103], a[52:55]
ds_read_b128 a[220:223], v228 offset:56320
v_mfma_f32_16x16x32_bf16 a[56:59], a[204:207], v[104:107], a[56:59]
v_exp_f32_e32 v89, v89
v_mfma_f32_16x16x32_bf16 a[60:63], a[204:207], v[108:111], a[60:63]
v_cvt_pk_bf16_f32 v124, v88, v89
s_waitcnt lgkmcnt(2)
v_mfma_f32_16x16x32_bf16 a[64:67], a[208:211], v[96:99], a[64:67]
s_waitcnt vmcnt(8)
s_barrier
v_exp_f32_e32 v90, v90
v_mfma_f32_16x16x32_bf16 a[68:71], a[208:211], v[100:103], a[68:71]
v_mfma_f32_16x16x32_bf16 a[72:75], a[208:211], v[104:107], a[72:75]
v_exp_f32_e32 v91, v91
v_mfma_f32_16x16x32_bf16 a[76:79], a[208:211], v[108:111], a[76:79]
v_cvt_pk_bf16_f32 v125, v90, v91
v_mfma_f32_16x16x32_bf16 a[80:83], a[212:215], v[96:99], a[80:83]
v_exp_f32_e32 v68, v68
v_mfma_f32_16x16x32_bf16 a[84:87], a[212:215], v[100:103], a[84:87]
ds_read_b128 a[128:131], v224
v_mfma_f32_16x16x32_bf16 a[88:91], a[212:215], v[104:107], a[88:91]
v_exp_f32_e32 v69, v69
v_mfma_f32_16x16x32_bf16 a[92:95], a[212:215], v[108:111], a[92:95]
v_cvt_pk_bf16_f32 v114, v68, v69
s_waitcnt lgkmcnt(1)
v_mfma_f32_16x16x32_bf16 a[96:99], a[216:219], v[96:99], a[96:99]
v_exp_f32_e32 v70, v70
v_mfma_f32_16x16x32_bf16 a[100:103], a[216:219], v[100:103], a[100:103]
ds_read_b128 a[132:135], v224 offset:4096
v_mfma_f32_16x16x32_bf16 a[104:107], a[216:219], v[104:107], a[104:107]
v_exp_f32_e32 v71, v71
v_mfma_f32_16x16x32_bf16 a[108:111], a[216:219], v[108:111], a[108:111]
v_cvt_pk_bf16_f32 v115, v70, v71
v_mfma_f32_16x16x32_bf16 a[112:115], a[220:223], v[96:99], a[112:115]
v_exp_f32_e32 v76, v76
v_mfma_f32_16x16x32_bf16 a[116:119], a[220:223], v[100:103], a[116:119]
ds_read_b128 a[136:139], v224 offset:8192
v_mfma_f32_16x16x32_bf16 a[120:123], a[220:223], v[104:107], a[120:123]
v_exp_f32_e32 v77, v77
v_mfma_f32_16x16x32_bf16 a[124:127], a[220:223], v[108:111], a[124:127]
v_cvt_pk_bf16_f32 v118, v76, v77
v_mfma_f32_16x16x32_bf16 v[144:147], v[248:251], v[96:99], v[144:147]
v_exp_f32_e32 v78, v78
v_mfma_f32_16x16x32_bf16 v[148:151], v[248:251], v[100:103], v[148:151]
ds_read_b128 a[140:143], v224 offset:12288
v_mfma_f32_16x16x32_bf16 v[152:155], v[248:251], v[104:107], v[152:155]
v_exp_f32_e32 v79, v79
v_mfma_f32_16x16x32_bf16 v[156:159], v[248:251], v[108:111], v[156:159]
v_cvt_pk_bf16_f32 v119, v78, v79
L_D3b_%=:
s_waitcnt lgkmcnt(2)
v_mfma_f32_16x16x32_bf16 v[32:35], a[128:131], v[160:163], v[128:131]
s_add_i32 s44, s44, 1
v_exp_f32_e32 v84, v84
v_mfma_f32_16x16x32_bf16 v[40:43], a[128:131], v[176:179], v[132:135]
s_add_u32 m0, s42, 49152
ds_read_b128 a[144:147], v224 offset:1024
v_mfma_f32_16x16x32_bf16 v[48:51], a[128:131], v[192:195], v[136:139]
buffer_load_dwordx4 v238, s[36:39], s41 offen sc1 lds
v_exp_f32_e32 v85, v85
v_mfma_f32_16x16x32_bf16 v[56:59], a[128:131], v[208:211], v[140:143]
v_cvt_pk_bf16_f32 v122, v84, v85
v_mfma_f32_16x16x32_bf16 v[32:35], a[132:135], v[164:167], v[32:35]
v_exp_f32_e32 v86, v86
v_mfma_f32_16x16x32_bf16 v[40:43], a[132:135], v[180:183], v[40:43]
ds_read_b128 a[148:151], v224 offset:5120
v_mfma_f32_16x16x32_bf16 v[48:51], a[132:135], v[196:199], v[48:51]
v_exp_f32_e32 v87, v87
v_mfma_f32_16x16x32_bf16 v[56:59], a[132:135], v[212:215], v[56:59]
v_cvt_pk_bf16_f32 v123, v86, v87
s_waitcnt lgkmcnt(2)
v_mfma_f32_16x16x32_bf16 v[32:35], a[136:139], v[168:171], v[32:35]
v_exp_f32_e32 v92, v92
v_mfma_f32_16x16x32_bf16 v[40:43], a[136:139], v[184:187], v[40:43]
ds_read_b128 a[152:155], v224 offset:9216
v_mfma_f32_16x16x32_bf16 v[48:51], a[136:139], v[200:203], v[48:51]
buffer_load_dwordx4 v238, s[36:39], s41 offen offset:1024 sc1 lds
v_exp_f32_e32 v93, v93
v_mfma_f32_16x16x32_bf16 v[56:59], a[136:139], v[216:219], v[56:59]
v_cvt_pk_bf16_f32 v126, v92, v93
v_mfma_f32_16x16x32_bf16 v[32:35], a[140:143], v[172:175], v[32:35]
v_exp_f32_e32 v94, v94
v_mfma_f32_16x16x32_bf16 v[40:43], a[140:143], v[188:191], v[40:43]
ds_read_b128 a[156:159], v224 offset:13312
v_mfma_f32_16x16x32_bf16 v[48:51], a[140:143], v[204:207], v[48:51]
v_exp_f32_e32 v95, v95
v_mfma_f32_16x16x32_bf16 v[56:59], a[140:143], v[220:223], v[56:59]
v_cvt_pk_bf16_f32 v127, v94, v95
s_waitcnt lgkmcnt(2)
v_mfma_f32_16x16x32_bf16 v[36:39], a[144:147], v[160:163], v[128:131]
v_mfma_f32_16x16x32_bf16 v[44:47], a[144:147], v[176:179], v[132:135]
ds_read_b128 a[192:195], v228 offset:57344
v_mfma_f32_16x16x32_bf16 v[52:55], a[144:147], v[192:195], v[136:139]
buffer_load_dwordx4 v238, s[36:39], s41 offen offset:2048 sc1 lds
v_mfma_f32_16x16x32_bf16 v[60:63], a[144:147], v[208:211], v[140:143]
s_cmp_ge_i32 s44, s43
s_cbranch_scc0 L_ms25_%=
s_cmp_eq_i32 s44, s43
s_cbranch_scc1 L_mt25_%=
v_mov_b32_e32 v48, v239
v_mov_b32_e32 v49, v239
v_mov_b32_e32 v50, v239
v_mov_b32_e32 v51, v239
v_mov_b32_e32 v56, v239
v_mov_b32_e32 v57, v239
v_mov_b32_e32 v58, v239
v_mov_b32_e32 v59, v239
s_branch L_ms25_%=
L_mt25_%=:
v_cndmask_b32_e64 v48, v48, v239, s[56:57]
v_cndmask_b32_e64 v49, v49, v239, s[58:59]
v_cndmask_b32_e64 v50, v50, v239, s[60:61]
v_cndmask_b32_e64 v51, v51, v239, s[62:63]
L_ms25_%=:
v_mfma_f32_16x16x32_bf16 v[36:39], a[148:151], v[164:167], v[36:39]
v_exp_f32_e32 v32, v32
v_mfma_f32_16x16x32_bf16 v[44:47], a[148:151], v[180:183], v[44:47]
ds_read_b128 a[196:199], v228 offset:58368
v_mfma_f32_16x16x32_bf16 v[52:55], a[148:151], v[196:199], v[52:55]
v_exp_f32_e32 v33, v33
v_mfma_f32_16x16x32_bf16 v[60:63], a[148:151], v[212:215], v[60:63]
v_cvt_pk_bf16_f32 v96, v32, v33
s_waitcnt lgkmcnt(2)
v_mfma_f32_16x16x32_bf16 v[36:39], a[152:155], v[168:171], v[36:39]
v_exp_f32_e32 v34, v34
v_mfma_f32_16x16x32_bf16 v[44:47], a[152:155], v[184:187], v[44:47]
ds_read_b128 a[200:203], v228 offset:59392
v_mfma_f32_16x16x32_bf16 v[52:55], a[152:155], v[200:203], v[52:55]
buffer_load_dwordx4 v238, s[36:39], s41 offen offset:3072 sc1 lds
v_exp_f32_e32 v35, v35
v_mfma_f32_16x16x32_bf16 v[60:63], a[152:155], v[216:219], v[60:63]
v_cvt_pk_bf16_f32 v97, v34, v35
v_mfma_f32_16x16x32_bf16 v[36:39], a[156:159], v[172:175], v[36:39]
v_exp_f32_e32 v40, v40
v_mfma_f32_16x16x32_bf16 v[44:47], a[156:159], v[188:191], v[44:47]
ds_read_b128 a[204:207], v228 offset:60416
v_mfma_f32_16x16x32_bf16 v[52:55], a[156:159], v[204:207], v[52:55]
v_exp_f32_e32 v41, v41
v_mfma_f32_16x16x32_bf16 v[60:63], a[156:159], v[220:223], v[60:63]
v_cvt_pk_bf16_f32 v100, v40, v41
s_waitcnt lgkmcnt(2)
v_mfma_f32_16x16x32_bf16 a[0:3], a[192:195], v[112:115], a[0:3]
v_exp_f32_e32 v42, v42
v_mfma_f32_16x16x32_bf16 a[4:7], a[192:195], v[116:119], a[4:7]
s_add_u32 m0, s42, 98304
ds_read_b128 a[208:211], v228 offset:61440
v_mfma_f32_16x16x32_bf16 a[8:11], a[192:195], v[120:123], a[8:11]
buffer_load_dwordx4 v238, s[36:39], s40 offen sc1 lds
v_exp_f32_e32 v43, v43
v_mfma_f32_16x16x32_bf16 a[12:15], a[192:195], v[124:127], a[12:15]
v_cvt_pk_bf16_f32 v101, v42, v43
v_mfma_f32_16x16x32_bf16 a[16:19], a[196:199], v[112:115], a[16:19]
v_exp_f32_e32 v48, v48
v_mfma_f32_16x16x32_bf16 a[20:23], a[196:199], v[116:119], a[20:23]
ds_read_b128 a[212:215], v228 offset:62464
v_mfma_f32_16x16x32_bf16 a[24:27], a[196:199], v[120:123], a[24:27]
v_exp_f32_e32 v49, v49
v_mfma_f32_16x16x32_bf16 a[28:31], a[196:199], v[124:127], a[28:31]
v_cvt_pk_bf16_f32 v104, v48, v49
s_waitcnt lgkmcnt(2)
v_mfma_f32_16x16x32_bf16 a[32:35], a[200:203], v[112:115], a[32:35]
v_exp_f32_e32 v50, v50
v_mfma_f32_16x16x32_bf16 a[36:39], a[200:203], v[116:119], a[36:39]
ds_read_b128 a[216:219], v228 offset:63488
s_cmp_ge_i32 s44, s43
s_cbranch_scc0 L_ms26_%=
s_cmp_eq_i32 s44, s43
s_cbranch_scc1 L_mt26_%=
v_mov_b32_e32 v52, v239
v_mov_b32_e32 v53, v239
v_mov_b32_e32 v54, v239
v_mov_b32_e32 v55, v239
v_mov_b32_e32 v60, v239
v_mov_b32_e32 v61, v239
v_mov_b32_e32 v62, v239
v_mov_b32_e32 v63, v239
s_branch L_ms26_%=
L_mt26_%=:
v_mov_b32_e32 v52, v239
v_mov_b32_e32 v53, v239
v_mov_b32_e32 v54, v239
v_mov_b32_e32 v55, v239
v_cndmask_b32_e64 v60, v60, v239, s[56:57]
v_cndmask_b32_e64 v61, v61, v239, s[58:59]
v_cndmask_b32_e64 v62, v62, v239, s[60:61]
v_cndmask_b32_e64 v63, v63, v239, s[62:63]
L_ms26_%=:
v_mfma_f32_16x16x32_bf16 a[40:43], a[200:203], v[120:123], a[40:43]
buffer_load_dwordx4 v238, s[36:39], s40 offen offset:1024 sc1 lds
v_exp_f32_e32 v51, v51
v_mfma_f32_16x16x32_bf16 a[44:47], a[200:203], v[124:127], a[44:47]
v_cvt_pk_bf16_f32 v105, v50, v51
v_mfma_f32_16x16x32_bf16 a[48:51], a[204:207], v[112:115], a[48:51]
v_exp_f32_e32 v56, v56
v_mfma_f32_16x16x32_bf16 a[52:55], a[204:207], v[116:119], a[52:55]
ds_read_b128 a[220:223], v228 offset:64512
v_mfma_f32_16x16x32_bf16 a[56:59], a[204:207], v[120:123], a[56:59]
)ASM"
R"ASM(v_exp_f32_e32 v57, v57
v_mfma_f32_16x16x32_bf16 a[60:63], a[204:207], v[124:127], a[60:63]
v_cvt_pk_bf16_f32 v108, v56, v57
s_waitcnt lgkmcnt(2)
v_mfma_f32_16x16x32_bf16 a[64:67], a[208:211], v[112:115], a[64:67]
v_exp_f32_e32 v58, v58
v_mfma_f32_16x16x32_bf16 a[68:71], a[208:211], v[116:119], a[68:71]
v_mfma_f32_16x16x32_bf16 a[72:75], a[208:211], v[120:123], a[72:75]
buffer_load_dwordx4 v238, s[36:39], s40 offen offset:2048 sc1 lds
v_exp_f32_e32 v59, v59
v_mfma_f32_16x16x32_bf16 a[76:79], a[208:211], v[124:127], a[76:79]
v_cvt_pk_bf16_f32 v109, v58, v59
v_mfma_f32_16x16x32_bf16 a[80:83], a[212:215], v[112:115], a[80:83]
v_exp_f32_e32 v36, v36
v_mfma_f32_16x16x32_bf16 a[84:87], a[212:215], v[116:119], a[84:87]
ds_read_b128 a[128:131], v224 offset:2048
v_mfma_f32_16x16x32_bf16 a[88:91], a[212:215], v[120:123], a[88:91]
v_exp_f32_e32 v37, v37
v_mfma_f32_16x16x32_bf16 a[92:95], a[212:215], v[124:127], a[92:95]
v_cvt_pk_bf16_f32 v98, v36, v37
s_waitcnt lgkmcnt(1)
v_mfma_f32_16x16x32_bf16 a[96:99], a[216:219], v[112:115], a[96:99]
v_exp_f32_e32 v38, v38
v_mfma_f32_16x16x32_bf16 a[100:103], a[216:219], v[116:119], a[100:103]
ds_read_b128 a[132:135], v224 offset:6144
v_mfma_f32_16x16x32_bf16 a[104:107], a[216:219], v[120:123], a[104:107]
buffer_load_dwordx4 v238, s[36:39], s40 offen offset:3072 sc1 lds
v_exp_f32_e32 v39, v39
v_mfma_f32_16x16x32_bf16 a[108:111], a[216:219], v[124:127], a[108:111]
s_add_u32 s41, s41, 0x8000
v_cvt_pk_bf16_f32 v99, v38, v39
v_mfma_f32_16x16x32_bf16 a[112:115], a[220:223], v[112:115], a[112:115]
s_add_u32 s40, s40, 0x8000
v_exp_f32_e32 v44, v44
v_mfma_f32_16x16x32_bf16 a[116:119], a[220:223], v[116:119], a[116:119]
ds_read_b128 a[136:139], v224 offset:10240
v_mfma_f32_16x16x32_bf16 a[120:123], a[220:223], v[120:123], a[120:123]
v_exp_f32_e32 v45, v45
v_mfma_f32_16x16x32_bf16 a[124:127], a[220:223], v[124:127], a[124:127]
v_cvt_pk_bf16_f32 v102, v44, v45
v_mfma_f32_16x16x32_bf16 v[144:147], v[248:251], v[112:115], v[144:147]
v_exp_f32_e32 v46, v46
v_mfma_f32_16x16x32_bf16 v[148:151], v[248:251], v[116:119], v[148:151]
ds_read_b128 a[140:143], v224 offset:14336
v_mfma_f32_16x16x32_bf16 v[152:155], v[248:251], v[120:123], v[152:155]
v_exp_f32_e32 v47, v47
v_mfma_f32_16x16x32_bf16 v[156:159], v[248:251], v[124:127], v[156:159]
v_cvt_pk_bf16_f32 v103, v46, v47
s_branch L_D0a_%=
L_PRE1_%=:
s_add_u32 s48, s48, 2
s_waitcnt lgkmcnt(2)
v_mfma_f32_16x16x32_bf16 v[64:67], a[128:131], v[160:163], v[128:131]
s_add_i32 s44, s44, 1
v_exp_f32_e32 v52, v52
v_mfma_f32_16x16x32_bf16 v[72:75], a[128:131], v[176:179], v[132:135]
ds_read_b128 a[144:147], v224 offset:19456
v_mfma_f32_16x16x32_bf16 v[80:83], a[128:131], v[192:195], v[136:139]
v_exp_f32_e32 v53, v53
v_mfma_f32_16x16x32_bf16 v[88:91], a[128:131], v[208:211], v[140:143]
v_cvt_pk_bf16_f32 v106, v52, v53
v_mfma_f32_16x16x32_bf16 v[64:67], a[132:135], v[164:167], v[64:67]
v_exp_f32_e32 v54, v54
v_mfma_f32_16x16x32_bf16 v[72:75], a[132:135], v[180:183], v[72:75]
ds_read_b128 a[148:151], v224 offset:23552
v_mfma_f32_16x16x32_bf16 v[80:83], a[132:135], v[196:199], v[80:83]
v_exp_f32_e32 v55, v55
v_mfma_f32_16x16x32_bf16 v[88:91], a[132:135], v[212:215], v[88:91]
v_cvt_pk_bf16_f32 v107, v54, v55
s_waitcnt lgkmcnt(2)
v_mfma_f32_16x16x32_bf16 v[64:67], a[136:139], v[168:171], v[64:67]
v_exp_f32_e32 v60, v60
v_mfma_f32_16x16x32_bf16 v[72:75], a[136:139], v[184:187], v[72:75]
ds_read_b128 a[152:155], v224 offset:27648
v_mfma_f32_16x16x32_bf16 v[80:83], a[136:139], v[200:203], v[80:83]
v_exp_f32_e32 v61, v61
v_mfma_f32_16x16x32_bf16 v[88:91], a[136:139], v[216:219], v[88:91]
v_cvt_pk_bf16_f32 v110, v60, v61
v_mfma_f32_16x16x32_bf16 v[64:67], a[140:143], v[172:175], v[64:67]
v_exp_f32_e32 v62, v62
v_mfma_f32_16x16x32_bf16 v[72:75], a[140:143], v[188:191], v[72:75]
ds_read_b128 a[156:159], v224 offset:31744
v_mfma_f32_16x16x32_bf16 v[80:83], a[140:143], v[204:207], v[80:83]
v_exp_f32_e32 v63, v63
v_mfma_f32_16x16x32_bf16 v[88:91], a[140:143], v[220:223], v[88:91]
v_cvt_pk_bf16_f32 v111, v62, v63
s_waitcnt lgkmcnt(2)
v_mfma_f32_16x16x32_bf16 v[68:71], a[144:147], v[160:163], v[128:131]
v_mfma_f32_16x16x32_bf16 v[76:79], a[144:147], v[176:179], v[132:135]
ds_read_b128 a[192:195], v228 offset:16384
v_mfma_f32_16x16x32_bf16 v[84:87], a[144:147], v[192:195], v[136:139]
v_mfma_f32_16x16x32_bf16 v[92:95], a[144:147], v[208:211], v[140:143]
s_cmp_ge_i32 s44, s43
s_cbranch_scc0 L_ms11_%=
s_cmp_eq_i32 s44, s43
s_cbranch_scc1 L_mt11_%=
v_mov_b32_e32 v80, v239
v_mov_b32_e32 v81, v239
v_mov_b32_e32 v82, v239
v_mov_b32_e32 v83, v239
v_mov_b32_e32 v88, v239
v_mov_b32_e32 v89, v239
v_mov_b32_e32 v90, v239
v_mov_b32_e32 v91, v239
s_branch L_ms11_%=
L_mt11_%=:
v_cndmask_b32_e64 v80, v80, v239, s[56:57]
v_cndmask_b32_e64 v81, v81, v239, s[58:59]
v_cndmask_b32_e64 v82, v82, v239, s[60:61]
v_cndmask_b32_e64 v83, v83, v239, s[62:63]
L_ms11_%=:
v_mfma_f32_16x16x32_bf16 v[68:71], a[148:151], v[164:167], v[68:71]
v_exp_f32_e32 v64, v64
v_mfma_f32_16x16x32_bf16 v[76:79], a[148:151], v[180:183], v[76:79]
ds_read_b128 a[196:199], v228 offset:17408
v_mfma_f32_16x16x32_bf16 v[84:87], a[148:151], v[196:199], v[84:87]
v_exp_f32_e32 v65, v65
v_mfma_f32_16x16x32_bf16 v[92:95], a[148:151], v[212:215], v[92:95]
v_cvt_pk_bf16_f32 v112, v64, v65
s_waitcnt lgkmcnt(2)
v_mfma_f32_16x16x32_bf16 v[68:71], a[152:155], v[168:171], v[68:71]
v_exp_f32_e32 v66, v66
v_mfma_f32_16x16x32_bf16 v[76:79], a[152:155], v[184:187], v[76:79]
ds_read_b128 a[200:203], v228 offset:18432
v_mfma_f32_16x16x32_bf16 v[84:87], a[152:155], v[200:203], v[84:87]
v_exp_f32_e32 v67, v67
v_mfma_f32_16x16x32_bf16 v[92:95], a[152:155], v[216:219], v[92:95]
v_cvt_pk_bf16_f32 v113, v66, v67
v_mfma_f32_16x16x32_bf16 v[68:71], a[156:159], v[172:175], v[68:71]
v_exp_f32_e32 v72, v72
v_mfma_f32_16x16x32_bf16 v[76:79], a[156:159], v[188:191], v[76:79]
ds_read_b128 a[204:207], v228 offset:19456
v_mfma_f32_16x16x32_bf16 v[84:87], a[156:159], v[204:207], v[84:87]
v_exp_f32_e32 v73, v73
v_mfma_f32_16x16x32_bf16 v[92:95], a[156:159], v[220:223], v[92:95]
v_cvt_pk_bf16_f32 v116, v72, v73
s_waitcnt lgkmcnt(2)
v_mfma_f32_16x16x32_bf16 a[0:3], a[192:195], v[96:99], a[0:3]
v_exp_f32_e32 v74, v74
v_mfma_f32_16x16x32_bf16 a[4:7], a[192:195], v[100:103], a[4:7]
ds_read_b128 a[208:211], v228 offset:20480
v_mfma_f32_16x16x32_bf16 a[8:11], a[192:195], v[104:107], a[8:11]
v_exp_f32_e32 v75, v75
v_mfma_f32_16x16x32_bf16 a[12:15], a[192:195], v[108:111], a[12:15]
v_cvt_pk_bf16_f32 v117, v74, v75
v_mfma_f32_16x16x32_bf16 a[16:19], a[196:199], v[96:99], a[16:19]
v_exp_f32_e32 v80, v80
v_mfma_f32_16x16x32_bf16 a[20:23], a[196:199], v[100:103], a[20:23]
ds_read_b128 a[212:215], v228 offset:21504
v_mfma_f32_16x16x32_bf16 a[24:27], a[196:199], v[104:107], a[24:27]
v_exp_f32_e32 v81, v81
v_mfma_f32_16x16x32_bf16 a[28:31], a[196:199], v[108:111], a[28:31]
v_cvt_pk_bf16_f32 v120, v80, v81
s_waitcnt lgkmcnt(2)
v_mfma_f32_16x16x32_bf16 a[32:35], a[200:203], v[96:99], a[32:35]
v_exp_f32_e32 v82, v82
v_mfma_f32_16x16x32_bf16 a[36:39], a[200:203], v[100:103], a[36:39]
ds_read_b128 a[216:219], v228 offset:22528
s_cmp_ge_i32 s44, s43
s_cbranch_scc0 L_ms12_%=
s_cmp_eq_i32 s44, s43
s_cbranch_scc1 L_mt12_%=
v_mov_b32_e32 v84, v239
v_mov_b32_e32 v85, v239
v_mov_b32_e32 v86, v239
v_mov_b32_e32 v87, v239
v_mov_b32_e32 v92, v239
v_mov_b32_e32 v93, v239
v_mov_b32_e32 v94, v239
v_mov_b32_e32 v95, v239
s_branch L_ms12_%=
L_mt12_%=:
v_mov_b32_e32 v84, v239
v_mov_b32_e32 v85, v239
v_mov_b32_e32 v86, v239
v_mov_b32_e32 v87, v239
v_cndmask_b32_e64 v92, v92, v239, s[56:57]
v_cndmask_b32_e64 v93, v93, v239, s[58:59]
v_cndmask_b32_e64 v94, v94, v239, s[60:61]
v_cndmask_b32_e64 v95, v95, v239, s[62:63]
L_ms12_%=:
v_mfma_f32_16x16x32_bf16 a[40:43], a[200:203], v[104:107], a[40:43]
v_exp_f32_e32 v83, v83
v_mfma_f32_16x16x32_bf16 a[44:47], a[200:203], v[108:111], a[44:47]
v_cvt_pk_bf16_f32 v121, v82, v83
v_mfma_f32_16x16x32_bf16 a[48:51], a[204:207], v[96:99], a[48:51]
v_exp_f32_e32 v88, v88
v_mfma_f32_16x16x32_bf16 a[52:55], a[204:207], v[100:103], a[52:55]
ds_read_b128 a[220:223], v228 offset:23552
v_mfma_f32_16x16x32_bf16 a[56:59], a[204:207], v[104:107], a[56:59]
v_exp_f32_e32 v89, v89
v_mfma_f32_16x16x32_bf16 a[60:63], a[204:207], v[108:111], a[60:63]
v_cvt_pk_bf16_f32 v124, v88, v89
s_waitcnt lgkmcnt(2)
v_mfma_f32_16x16x32_bf16 a[64:67], a[208:211], v[96:99], a[64:67]
s_waitcnt vmcnt(8)
s_barrier
v_exp_f32_e32 v90, v90
v_mfma_f32_16x16x32_bf16 a[68:71], a[208:211], v[100:103], a[68:71]
v_mfma_f32_16x16x32_bf16 a[72:75], a[208:211], v[104:107], a[72:75]
v_exp_f32_e32 v91, v91
v_mfma_f32_16x16x32_bf16 a[76:79], a[208:211], v[108:111], a[76:79]
v_cvt_pk_bf16_f32 v125, v90, v91
v_mfma_f32_16x16x32_bf16 a[80:83], a[212:215], v[96:99], a[80:83]
v_exp_f32_e32 v68, v68
v_mfma_f32_16x16x32_bf16 a[84:87], a[212:215], v[100:103], a[84:87]
ds_read_b128 a[128:131], v224 offset:32768
v_mfma_f32_16x16x32_bf16 a[88:91], a[212:215], v[104:107], a[88:91]
v_exp_f32_e32 v69, v69
v_mfma_f32_16x16x32_bf16 a[92:95], a[212:215], v[108:111], a[92:95]
ds_read_b128 a[132:135], v224 offset:36864
v_cvt_pk_bf16_f32 v114, v68, v69
s_waitcnt lgkmcnt(2)
v_mfma_f32_16x16x32_bf16 a[96:99], a[216:219], v[96:99], a[96:99]
v_exp_f32_e32 v70, v70
v_mfma_f32_16x16x32_bf16 a[100:103], a[216:219], v[100:103], a[100:103]
ds_read_b128 a[136:139], v224 offset:40960
v_mfma_f32_16x16x32_bf16 a[104:107], a[216:219], v[104:107], a[104:107]
v_exp_f32_e32 v71, v71
v_mfma_f32_16x16x32_bf16 a[108:111], a[216:219], v[108:111], a[108:111]
ds_read_b128 a[140:143], v224 offset:45056
v_cvt_pk_bf16_f32 v115, v70, v71
v_mfma_f32_16x16x32_bf16 a[112:115], a[220:223], v[96:99], a[112:115]
v_exp_f32_e32 v76, v76
v_mfma_f32_16x16x32_bf16 a[116:119], a[220:223], v[100:103], a[116:119]
ds_read_b128 a[144:147], v224 offset:33792
v_mfma_f32_16x16x32_bf16 a[120:123], a[220:223], v[104:107], a[120:123]
v_exp_f32_e32 v77, v77
v_mfma_f32_16x16x32_bf16 a[124:127], a[220:223], v[108:111], a[124:127]
ds_read_b128 a[148:151], v224 offset:37888
v_cvt_pk_bf16_f32 v118, v76, v77
v_mfma_f32_16x16x32_bf16 v[144:147], v[248:251], v[96:99], v[144:147]
v_exp_f32_e32 v78, v78
v_mfma_f32_16x16x32_bf16 v[148:151], v[248:251], v[100:103], v[148:151]
ds_read_b128 a[152:155], v224 offset:41984
v_mfma_f32_16x16x32_bf16 v[152:155], v[248:251], v[104:107], v[152:155]
v_exp_f32_e32 v79, v79
v_mfma_f32_16x16x32_bf16 v[156:159], v[248:251], v[108:111], v[156:159]
ds_read_b128 a[156:159], v224 offset:46080
v_cvt_pk_bf16_f32 v119, v78, v79
L_TR1_%=:
s_waitcnt lgkmcnt(4)
v_mfma_f32_16x16x32_bf16 v[32:35], a[128:131], v[160:163], v[128:131]
v_exp_f32_e32 v84, v84
v_mfma_f32_16x16x32_bf16 v[40:43], a[128:131], v[176:179], v[132:135]
ds_read_b128 a[160:163], v224 offset:34816
v_mfma_f32_16x16x32_bf16 v[32:35], a[132:135], v[164:167], v[32:35]
v_exp_f32_e32 v85, v85
v_mfma_f32_16x16x32_bf16 v[40:43], a[132:135], v[180:183], v[40:43]
ds_read_b128 a[164:167], v224 offset:38912
v_cvt_pk_bf16_f32 v122, v84, v85
v_mfma_f32_16x16x32_bf16 v[32:35], a[136:139], v[168:171], v[32:35]
v_exp_f32_e32 v86, v86
v_mfma_f32_16x16x32_bf16 v[40:43], a[136:139], v[184:187], v[40:43]
ds_read_b128 a[168:171], v224 offset:43008
v_mfma_f32_16x16x32_bf16 v[32:35], a[140:143], v[172:175], v[32:35]
v_exp_f32_e32 v87, v87
v_mfma_f32_16x16x32_bf16 v[40:43], a[140:143], v[188:191], v[40:43]
ds_read_b128 a[172:175], v224 offset:47104
v_cvt_pk_bf16_f32 v123, v86, v87
s_waitcnt lgkmcnt(4)
v_mfma_f32_16x16x32_bf16 v[36:39], a[144:147], v[160:163], v[128:131]
v_exp_f32_e32 v92, v92
v_mfma_f32_16x16x32_bf16 v[44:47], a[144:147], v[176:179], v[132:135]
ds_read_b128 a[176:179], v224 offset:35840
v_mfma_f32_16x16x32_bf16 v[36:39], a[148:151], v[164:167], v[36:39]
v_exp_f32_e32 v93, v93
v_mfma_f32_16x16x32_bf16 v[44:47], a[148:151], v[180:183], v[44:47]
ds_read_b128 a[180:183], v224 offset:39936
v_cvt_pk_bf16_f32 v126, v92, v93
v_mfma_f32_16x16x32_bf16 v[36:39], a[152:155], v[168:171], v[36:39]
v_exp_f32_e32 v94, v94
v_mfma_f32_16x16x32_bf16 v[44:47], a[152:155], v[184:187], v[44:47]
ds_read_b128 a[184:187], v224 offset:44032
)ASM"
R"ASM(v_mfma_f32_16x16x32_bf16 v[36:39], a[156:159], v[172:175], v[36:39]
v_exp_f32_e32 v95, v95
v_mfma_f32_16x16x32_bf16 v[44:47], a[156:159], v[188:191], v[44:47]
ds_read_b128 a[188:191], v224 offset:48128
v_cvt_pk_bf16_f32 v127, v94, v95
s_waitcnt lgkmcnt(4)
v_mfma_f32_16x16x32_bf16 v[48:51], a[160:163], v[160:163], v[128:131]
v_mfma_f32_16x16x32_bf16 v[56:59], a[160:163], v[176:179], v[132:135]
ds_read_b128 a[192:195], v228 offset:24576
v_mfma_f32_16x16x32_bf16 v[48:51], a[164:167], v[164:167], v[48:51]
v_mfma_f32_16x16x32_bf16 v[56:59], a[164:167], v[180:183], v[56:59]
s_cmp_ge_i32 s45, s43
s_cbranch_scc0 L_ms13_%=
s_cmp_eq_i32 s45, s43
s_cbranch_scc1 L_mt13_%=
v_mov_b32_e32 v32, v239
v_mov_b32_e32 v33, v239
v_mov_b32_e32 v34, v239
v_mov_b32_e32 v35, v239
v_mov_b32_e32 v40, v239
v_mov_b32_e32 v41, v239
v_mov_b32_e32 v42, v239
v_mov_b32_e32 v43, v239
v_mov_b32_e32 v36, v239
v_mov_b32_e32 v37, v239
v_mov_b32_e32 v38, v239
v_mov_b32_e32 v39, v239
v_mov_b32_e32 v44, v239
v_mov_b32_e32 v45, v239
v_mov_b32_e32 v46, v239
v_mov_b32_e32 v47, v239
s_branch L_ms13_%=
L_mt13_%=:
v_cndmask_b32_e64 v32, v32, v239, s[56:57]
v_cndmask_b32_e64 v33, v33, v239, s[58:59]
v_cndmask_b32_e64 v34, v34, v239, s[60:61]
v_cndmask_b32_e64 v35, v35, v239, s[62:63]
v_mov_b32_e32 v36, v239
v_mov_b32_e32 v37, v239
v_mov_b32_e32 v38, v239
v_mov_b32_e32 v39, v239
v_cndmask_b32_e64 v44, v44, v239, s[56:57]
v_cndmask_b32_e64 v45, v45, v239, s[58:59]
v_cndmask_b32_e64 v46, v46, v239, s[60:61]
v_cndmask_b32_e64 v47, v47, v239, s[62:63]
L_ms13_%=:
v_mfma_f32_16x16x32_bf16 v[48:51], a[168:171], v[168:171], v[48:51]
v_exp_f32_e32 v32, v32
v_mfma_f32_16x16x32_bf16 v[56:59], a[168:171], v[184:187], v[56:59]
ds_read_b128 a[196:199], v228 offset:25600
v_mfma_f32_16x16x32_bf16 v[48:51], a[172:175], v[172:175], v[48:51]
v_exp_f32_e32 v33, v33
v_mfma_f32_16x16x32_bf16 v[56:59], a[172:175], v[188:191], v[56:59]
v_cvt_pk_bf16_f32 v96, v32, v33
s_waitcnt lgkmcnt(2)
v_mfma_f32_16x16x32_bf16 v[52:55], a[176:179], v[160:163], v[128:131]
v_exp_f32_e32 v34, v34
v_mfma_f32_16x16x32_bf16 v[60:63], a[176:179], v[176:179], v[132:135]
ds_read_b128 a[200:203], v228 offset:26624
v_mfma_f32_16x16x32_bf16 v[52:55], a[180:183], v[164:167], v[52:55]
v_exp_f32_e32 v35, v35
v_mfma_f32_16x16x32_bf16 v[60:63], a[180:183], v[180:183], v[60:63]
v_cvt_pk_bf16_f32 v97, v34, v35
v_mfma_f32_16x16x32_bf16 v[52:55], a[184:187], v[168:171], v[52:55]
v_exp_f32_e32 v40, v40
v_mfma_f32_16x16x32_bf16 v[60:63], a[184:187], v[184:187], v[60:63]
ds_read_b128 a[204:207], v228 offset:27648
v_mfma_f32_16x16x32_bf16 v[52:55], a[188:191], v[172:175], v[52:55]
v_exp_f32_e32 v41, v41
v_mfma_f32_16x16x32_bf16 v[60:63], a[188:191], v[188:191], v[60:63]
v_cvt_pk_bf16_f32 v100, v40, v41
s_waitcnt lgkmcnt(2)
v_mfma_f32_16x16x32_bf16 a[0:3], a[192:195], v[112:115], a[0:3]
v_exp_f32_e32 v42, v42
v_mfma_f32_16x16x32_bf16 a[4:7], a[192:195], v[116:119], a[4:7]
ds_read_b128 a[208:211], v228 offset:28672
v_mfma_f32_16x16x32_bf16 a[8:11], a[192:195], v[120:123], a[8:11]
v_exp_f32_e32 v43, v43
v_mfma_f32_16x16x32_bf16 a[12:15], a[192:195], v[124:127], a[12:15]
v_cvt_pk_bf16_f32 v101, v42, v43
v_mfma_f32_16x16x32_bf16 a[16:19], a[196:199], v[112:115], a[16:19]
v_exp_f32_e32 v36, v36
v_mfma_f32_16x16x32_bf16 a[20:23], a[196:199], v[116:119], a[20:23]
ds_read_b128 a[212:215], v228 offset:29696
v_mfma_f32_16x16x32_bf16 a[24:27], a[196:199], v[120:123], a[24:27]
v_exp_f32_e32 v37, v37
v_mfma_f32_16x16x32_bf16 a[28:31], a[196:199], v[124:127], a[28:31]
v_cvt_pk_bf16_f32 v98, v36, v37
s_waitcnt lgkmcnt(2)
v_mfma_f32_16x16x32_bf16 a[32:35], a[200:203], v[112:115], a[32:35]
v_exp_f32_e32 v38, v38
v_mfma_f32_16x16x32_bf16 a[36:39], a[200:203], v[116:119], a[36:39]
ds_read_b128 a[216:219], v228 offset:30720
s_cmp_ge_i32 s45, s49
s_cbranch_scc0 L_ms14_%=
s_cmp_eq_i32 s45, s49
s_cbranch_scc1 L_mt14_%=
v_mov_b32_e32 v48, v239
v_mov_b32_e32 v49, v239
v_mov_b32_e32 v50, v239
v_mov_b32_e32 v51, v239
v_mov_b32_e32 v56, v239
v_mov_b32_e32 v57, v239
v_mov_b32_e32 v58, v239
v_mov_b32_e32 v59, v239
v_mov_b32_e32 v52, v239
v_mov_b32_e32 v53, v239
v_mov_b32_e32 v54, v239
v_mov_b32_e32 v55, v239
v_mov_b32_e32 v60, v239
v_mov_b32_e32 v61, v239
v_mov_b32_e32 v62, v239
v_mov_b32_e32 v63, v239
s_branch L_ms14_%=
L_mt14_%=:
v_cndmask_b32_e64 v48, v48, v239, s[56:57]
v_cndmask_b32_e64 v49, v49, v239, s[58:59]
v_cndmask_b32_e64 v50, v50, v239, s[60:61]
v_cndmask_b32_e64 v51, v51, v239, s[62:63]
v_mov_b32_e32 v52, v239
v_mov_b32_e32 v53, v239
v_mov_b32_e32 v54, v239
v_mov_b32_e32 v55, v239
v_cndmask_b32_e64 v60, v60, v239, s[56:57]
v_cndmask_b32_e64 v61, v61, v239, s[58:59]
v_cndmask_b32_e64 v62, v62, v239, s[60:61]
v_cndmask_b32_e64 v63, v63, v239, s[62:63]
L_ms14_%=:
v_mfma_f32_16x16x32_bf16 a[40:43], a[200:203], v[120:123], a[40:43]
v_exp_f32_e32 v39, v39
v_mfma_f32_16x16x32_bf16 a[44:47], a[200:203], v[124:127], a[44:47]
v_cvt_pk_bf16_f32 v99, v38, v39
v_mfma_f32_16x16x32_bf16 a[48:51], a[204:207], v[112:115], a[48:51]
v_exp_f32_e32 v44, v44
v_mfma_f32_16x16x32_bf16 a[52:55], a[204:207], v[116:119], a[52:55]
ds_read_b128 a[220:223], v228 offset:31744
v_mfma_f32_16x16x32_bf16 a[56:59], a[204:207], v[120:123], a[56:59]
v_exp_f32_e32 v45, v45
v_mfma_f32_16x16x32_bf16 a[60:63], a[204:207], v[124:127], a[60:63]
v_cvt_pk_bf16_f32 v102, v44, v45
s_waitcnt lgkmcnt(2)
v_mfma_f32_16x16x32_bf16 a[64:67], a[208:211], v[112:115], a[64:67]
v_exp_f32_e32 v46, v46
v_mfma_f32_16x16x32_bf16 a[68:71], a[208:211], v[116:119], a[68:71]
v_mfma_f32_16x16x32_bf16 a[72:75], a[208:211], v[120:123], a[72:75]
v_exp_f32_e32 v47, v47
v_mfma_f32_16x16x32_bf16 a[76:79], a[208:211], v[124:127], a[76:79]
v_cvt_pk_bf16_f32 v103, v46, v47
v_mfma_f32_16x16x32_bf16 a[80:83], a[212:215], v[112:115], a[80:83]
v_exp_f32_e32 v48, v48
v_mfma_f32_16x16x32_bf16 a[84:87], a[212:215], v[116:119], a[84:87]
ds_read_b128 a[128:131], v224 offset:49152
v_mfma_f32_16x16x32_bf16 a[88:91], a[212:215], v[120:123], a[88:91]
v_exp_f32_e32 v49, v49
v_mfma_f32_16x16x32_bf16 a[92:95], a[212:215], v[124:127], a[92:95]
ds_read_b128 a[132:135], v224 offset:53248
v_cvt_pk_bf16_f32 v104, v48, v49
s_waitcnt lgkmcnt(2)
v_mfma_f32_16x16x32_bf16 a[96:99], a[216:219], v[112:115], a[96:99]
v_exp_f32_e32 v50, v50
v_mfma_f32_16x16x32_bf16 a[100:103], a[216:219], v[116:119], a[100:103]
ds_read_b128 a[136:139], v224 offset:57344
v_mfma_f32_16x16x32_bf16 a[104:107], a[216:219], v[120:123], a[104:107]
v_exp_f32_e32 v51, v51
v_mfma_f32_16x16x32_bf16 a[108:111], a[216:219], v[124:127], a[108:111]
ds_read_b128 a[140:143], v224 offset:61440
v_cvt_pk_bf16_f32 v105, v50, v51
v_mfma_f32_16x16x32_bf16 a[112:115], a[220:223], v[112:115], a[112:115]
v_exp_f32_e32 v56, v56
v_mfma_f32_16x16x32_bf16 a[116:119], a[220:223], v[116:119], a[116:119]
ds_read_b128 a[144:147], v224 offset:50176
v_mfma_f32_16x16x32_bf16 a[120:123], a[220:223], v[120:123], a[120:123]
v_exp_f32_e32 v57, v57
v_mfma_f32_16x16x32_bf16 a[124:127], a[220:223], v[124:127], a[124:127]
ds_read_b128 a[148:151], v224 offset:54272
v_cvt_pk_bf16_f32 v108, v56, v57
v_mfma_f32_16x16x32_bf16 v[144:147], v[248:251], v[112:115], v[144:147]
v_exp_f32_e32 v58, v58
v_mfma_f32_16x16x32_bf16 v[148:151], v[248:251], v[116:119], v[148:151]
ds_read_b128 a[152:155], v224 offset:58368
v_mfma_f32_16x16x32_bf16 v[152:155], v[248:251], v[120:123], v[152:155]
v_exp_f32_e32 v59, v59
v_mfma_f32_16x16x32_bf16 v[156:159], v[248:251], v[124:127], v[156:159]
ds_read_b128 a[156:159], v224 offset:62464
v_cvt_pk_bf16_f32 v109, v58, v59
s_branch L_SS2_%=
L_PRE3_%=:
s_add_u32 s48, s48, 2
s_waitcnt lgkmcnt(2)
v_mfma_f32_16x16x32_bf16 v[64:67], a[128:131], v[160:163], v[128:131]
s_add_i32 s44, s44, 1
v_exp_f32_e32 v52, v52
v_mfma_f32_16x16x32_bf16 v[72:75], a[128:131], v[176:179], v[132:135]
ds_read_b128 a[144:147], v224 offset:52224
v_mfma_f32_16x16x32_bf16 v[80:83], a[128:131], v[192:195], v[136:139]
v_exp_f32_e32 v53, v53
v_mfma_f32_16x16x32_bf16 v[88:91], a[128:131], v[208:211], v[140:143]
v_cvt_pk_bf16_f32 v106, v52, v53
v_mfma_f32_16x16x32_bf16 v[64:67], a[132:135], v[164:167], v[64:67]
v_exp_f32_e32 v54, v54
v_mfma_f32_16x16x32_bf16 v[72:75], a[132:135], v[180:183], v[72:75]
ds_read_b128 a[148:151], v224 offset:56320
v_mfma_f32_16x16x32_bf16 v[80:83], a[132:135], v[196:199], v[80:83]
v_exp_f32_e32 v55, v55
v_mfma_f32_16x16x32_bf16 v[88:91], a[132:135], v[212:215], v[88:91]
v_cvt_pk_bf16_f32 v107, v54, v55
s_waitcnt lgkmcnt(2)
v_mfma_f32_16x16x32_bf16 v[64:67], a[136:139], v[168:171], v[64:67]
v_exp_f32_e32 v60, v60
v_mfma_f32_16x16x32_bf16 v[72:75], a[136:139], v[184:187], v[72:75]
ds_read_b128 a[152:155], v224 offset:60416
v_mfma_f32_16x16x32_bf16 v[80:83], a[136:139], v[200:203], v[80:83]
v_exp_f32_e32 v61, v61
v_mfma_f32_16x16x32_bf16 v[88:91], a[136:139], v[216:219], v[88:91]
v_cvt_pk_bf16_f32 v110, v60, v61
v_mfma_f32_16x16x32_bf16 v[64:67], a[140:143], v[172:175], v[64:67]
v_exp_f32_e32 v62, v62
v_mfma_f32_16x16x32_bf16 v[72:75], a[140:143], v[188:191], v[72:75]
ds_read_b128 a[156:159], v224 offset:64512
v_mfma_f32_16x16x32_bf16 v[80:83], a[140:143], v[204:207], v[80:83]
v_exp_f32_e32 v63, v63
v_mfma_f32_16x16x32_bf16 v[88:91], a[140:143], v[220:223], v[88:91]
v_cvt_pk_bf16_f32 v111, v62, v63
s_waitcnt lgkmcnt(2)
v_mfma_f32_16x16x32_bf16 v[68:71], a[144:147], v[160:163], v[128:131]
v_mfma_f32_16x16x32_bf16 v[76:79], a[144:147], v[176:179], v[132:135]
ds_read_b128 a[192:195], v228 offset:49152
v_mfma_f32_16x16x32_bf16 v[84:87], a[144:147], v[192:195], v[136:139]
v_mfma_f32_16x16x32_bf16 v[92:95], a[144:147], v[208:211], v[140:143]
s_cmp_ge_i32 s44, s43
s_cbranch_scc0 L_ms27_%=
s_cmp_eq_i32 s44, s43
s_cbranch_scc1 L_mt27_%=
v_mov_b32_e32 v80, v239
v_mov_b32_e32 v81, v239
v_mov_b32_e32 v82, v239
v_mov_b32_e32 v83, v239
v_mov_b32_e32 v88, v239
v_mov_b32_e32 v89, v239
v_mov_b32_e32 v90, v239
v_mov_b32_e32 v91, v239
s_branch L_ms27_%=
L_mt27_%=:
v_cndmask_b32_e64 v80, v80, v239, s[56:57]
v_cndmask_b32_e64 v81, v81, v239, s[58:59]
v_cndmask_b32_e64 v82, v82, v239, s[60:61]
v_cndmask_b32_e64 v83, v83, v239, s[62:63]
L_ms27_%=:
v_mfma_f32_16x16x32_bf16 v[68:71], a[148:151], v[164:167], v[68:71]
v_exp_f32_e32 v64, v64
v_mfma_f32_16x16x32_bf16 v[76:79], a[148:151], v[180:183], v[76:79]
ds_read_b128 a[196:199], v228 offset:50176
v_mfma_f32_16x16x32_bf16 v[84:87], a[148:151], v[196:199], v[84:87]
v_exp_f32_e32 v65, v65
v_mfma_f32_16x16x32_bf16 v[92:95], a[148:151], v[212:215], v[92:95]
v_cvt_pk_bf16_f32 v112, v64, v65
s_waitcnt lgkmcnt(2)
v_mfma_f32_16x16x32_bf16 v[68:71], a[152:155], v[168:171], v[68:71]
v_exp_f32_e32 v66, v66
v_mfma_f32_16x16x32_bf16 v[76:79], a[152:155], v[184:187], v[76:79]
ds_read_b128 a[200:203], v228 offset:51200
v_mfma_f32_16x16x32_bf16 v[84:87], a[152:155], v[200:203], v[84:87]
v_exp_f32_e32 v67, v67
v_mfma_f32_16x16x32_bf16 v[92:95], a[152:155], v[216:219], v[92:95]
v_cvt_pk_bf16_f32 v113, v66, v67
v_mfma_f32_16x16x32_bf16 v[68:71], a[156:159], v[172:175], v[68:71]
v_exp_f32_e32 v72, v72
v_mfma_f32_16x16x32_bf16 v[76:79], a[156:159], v[188:191], v[76:79]
ds_read_b128 a[204:207], v228 offset:52224
v_mfma_f32_16x16x32_bf16 v[84:87], a[156:159], v[204:207], v[84:87]
v_exp_f32_e32 v73, v73
v_mfma_f32_16x16x32_bf16 v[92:95], a[156:159], v[220:223], v[92:95]
v_cvt_pk_bf16_f32 v116, v72, v73
s_waitcnt lgkmcnt(2)
v_mfma_f32_16x16x32_bf16 a[0:3], a[192:195], v[96:99], a[0:3]
v_exp_f32_e32 v74, v74
v_mfma_f32_16x16x32_bf16 a[4:7], a[192:195], v[100:103], a[4:7]
ds_read_b128 a[208:211], v228 offset:53248
v_mfma_f32_16x16x32_bf16 a[8:11], a[192:195], v[104:107], a[8:11]
v_exp_f32_e32 v75, v75
v_mfma_f32_16x16x32_bf16 a[12:15], a[192:195], v[108:111], a[12:15]
v_cvt_pk_bf16_f32 v117, v74, v75
v_mfma_f32_16x16x32_bf16 a[16:19], a[196:199], v[96:99], a[16:19]
v_exp_f32_e32 v80, v80
v_mfma_f32_16x16x32_bf16 a[20:23], a[196:199], v[100:103], a[20:23]
ds_read_b128 a[212:215], v228 offset:54272
v_mfma_f32_16x16x32_bf16 a[24:27], a[196:199], v[104:107], a[24:27]
v_exp_f32_e32 v81, v81
v_mfma_f32_16x16x32_bf16 a[28:31], a[196:199], v[108:111], a[28:31]
v_cvt_pk_bf16_f32 v120, v80, v81
)ASM"
R"ASM(s_waitcnt lgkmcnt(2)
v_mfma_f32_16x16x32_bf16 a[32:35], a[200:203], v[96:99], a[32:35]
v_exp_f32_e32 v82, v82
v_mfma_f32_16x16x32_bf16 a[36:39], a[200:203], v[100:103], a[36:39]
ds_read_b128 a[216:219], v228 offset:55296
s_cmp_ge_i32 s44, s43
s_cbranch_scc0 L_ms28_%=
s_cmp_eq_i32 s44, s43
s_cbranch_scc1 L_mt28_%=
v_mov_b32_e32 v84, v239
v_mov_b32_e32 v85, v239
v_mov_b32_e32 v86, v239
v_mov_b32_e32 v87, v239
v_mov_b32_e32 v92, v239
v_mov_b32_e32 v93, v239
v_mov_b32_e32 v94, v239
v_mov_b32_e32 v95, v239
s_branch L_ms28_%=
L_mt28_%=:
v_mov_b32_e32 v84, v239
v_mov_b32_e32 v85, v239
v_mov_b32_e32 v86, v239
v_mov_b32_e32 v87, v239
v_cndmask_b32_e64 v92, v92, v239, s[56:57]
v_cndmask_b32_e64 v93, v93, v239, s[58:59]
v_cndmask_b32_e64 v94, v94, v239, s[60:61]
v_cndmask_b32_e64 v95, v95, v239, s[62:63]
L_ms28_%=:
v_mfma_f32_16x16x32_bf16 a[40:43], a[200:203], v[104:107], a[40:43]
v_exp_f32_e32 v83, v83
v_mfma_f32_16x16x32_bf16 a[44:47], a[200:203], v[108:111], a[44:47]
v_cvt_pk_bf16_f32 v121, v82, v83
v_mfma_f32_16x16x32_bf16 a[48:51], a[204:207], v[96:99], a[48:51]
v_exp_f32_e32 v88, v88
v_mfma_f32_16x16x32_bf16 a[52:55], a[204:207], v[100:103], a[52:55]
ds_read_b128 a[220:223], v228 offset:56320
v_mfma_f32_16x16x32_bf16 a[56:59], a[204:207], v[104:107], a[56:59]
v_exp_f32_e32 v89, v89
v_mfma_f32_16x16x32_bf16 a[60:63], a[204:207], v[108:111], a[60:63]
v_cvt_pk_bf16_f32 v124, v88, v89
s_waitcnt lgkmcnt(2)
v_mfma_f32_16x16x32_bf16 a[64:67], a[208:211], v[96:99], a[64:67]
s_waitcnt vmcnt(8)
s_barrier
v_exp_f32_e32 v90, v90
v_mfma_f32_16x16x32_bf16 a[68:71], a[208:211], v[100:103], a[68:71]
v_mfma_f32_16x16x32_bf16 a[72:75], a[208:211], v[104:107], a[72:75]
v_exp_f32_e32 v91, v91
v_mfma_f32_16x16x32_bf16 a[76:79], a[208:211], v[108:111], a[76:79]
v_cvt_pk_bf16_f32 v125, v90, v91
v_mfma_f32_16x16x32_bf16 a[80:83], a[212:215], v[96:99], a[80:83]
v_exp_f32_e32 v68, v68
v_mfma_f32_16x16x32_bf16 a[84:87], a[212:215], v[100:103], a[84:87]
ds_read_b128 a[128:131], v224
v_mfma_f32_16x16x32_bf16 a[88:91], a[212:215], v[104:107], a[88:91]
v_exp_f32_e32 v69, v69
v_mfma_f32_16x16x32_bf16 a[92:95], a[212:215], v[108:111], a[92:95]
ds_read_b128 a[132:135], v224 offset:4096
v_cvt_pk_bf16_f32 v114, v68, v69
s_waitcnt lgkmcnt(2)
v_mfma_f32_16x16x32_bf16 a[96:99], a[216:219], v[96:99], a[96:99]
v_exp_f32_e32 v70, v70
v_mfma_f32_16x16x32_bf16 a[100:103], a[216:219], v[100:103], a[100:103]
ds_read_b128 a[136:139], v224 offset:8192
v_mfma_f32_16x16x32_bf16 a[104:107], a[216:219], v[104:107], a[104:107]
v_exp_f32_e32 v71, v71
v_mfma_f32_16x16x32_bf16 a[108:111], a[216:219], v[108:111], a[108:111]
ds_read_b128 a[140:143], v224 offset:12288
v_cvt_pk_bf16_f32 v115, v70, v71
v_mfma_f32_16x16x32_bf16 a[112:115], a[220:223], v[96:99], a[112:115]
v_exp_f32_e32 v76, v76
v_mfma_f32_16x16x32_bf16 a[116:119], a[220:223], v[100:103], a[116:119]
ds_read_b128 a[144:147], v224 offset:1024
v_mfma_f32_16x16x32_bf16 a[120:123], a[220:223], v[104:107], a[120:123]
v_exp_f32_e32 v77, v77
v_mfma_f32_16x16x32_bf16 a[124:127], a[220:223], v[108:111], a[124:127]
ds_read_b128 a[148:151], v224 offset:5120
v_cvt_pk_bf16_f32 v118, v76, v77
v_mfma_f32_16x16x32_bf16 v[144:147], v[248:251], v[96:99], v[144:147]
v_exp_f32_e32 v78, v78
v_mfma_f32_16x16x32_bf16 v[148:151], v[248:251], v[100:103], v[148:151]
ds_read_b128 a[152:155], v224 offset:9216
v_mfma_f32_16x16x32_bf16 v[152:155], v[248:251], v[104:107], v[152:155]
v_exp_f32_e32 v79, v79
v_mfma_f32_16x16x32_bf16 v[156:159], v[248:251], v[108:111], v[156:159]
ds_read_b128 a[156:159], v224 offset:13312
v_cvt_pk_bf16_f32 v119, v78, v79
L_TR3_%=:
s_waitcnt lgkmcnt(4)
v_mfma_f32_16x16x32_bf16 v[32:35], a[128:131], v[160:163], v[128:131]
v_exp_f32_e32 v84, v84
v_mfma_f32_16x16x32_bf16 v[40:43], a[128:131], v[176:179], v[132:135]
ds_read_b128 a[160:163], v224 offset:2048
v_mfma_f32_16x16x32_bf16 v[32:35], a[132:135], v[164:167], v[32:35]
v_exp_f32_e32 v85, v85
v_mfma_f32_16x16x32_bf16 v[40:43], a[132:135], v[180:183], v[40:43]
ds_read_b128 a[164:167], v224 offset:6144
v_cvt_pk_bf16_f32 v122, v84, v85
v_mfma_f32_16x16x32_bf16 v[32:35], a[136:139], v[168:171], v[32:35]
v_exp_f32_e32 v86, v86
v_mfma_f32_16x16x32_bf16 v[40:43], a[136:139], v[184:187], v[40:43]
ds_read_b128 a[168:171], v224 offset:10240
v_mfma_f32_16x16x32_bf16 v[32:35], a[140:143], v[172:175], v[32:35]
v_exp_f32_e32 v87, v87
v_mfma_f32_16x16x32_bf16 v[40:43], a[140:143], v[188:191], v[40:43]
ds_read_b128 a[172:175], v224 offset:14336
v_cvt_pk_bf16_f32 v123, v86, v87
s_waitcnt lgkmcnt(4)
v_mfma_f32_16x16x32_bf16 v[36:39], a[144:147], v[160:163], v[128:131]
v_exp_f32_e32 v92, v92
v_mfma_f32_16x16x32_bf16 v[44:47], a[144:147], v[176:179], v[132:135]
ds_read_b128 a[176:179], v224 offset:3072
v_mfma_f32_16x16x32_bf16 v[36:39], a[148:151], v[164:167], v[36:39]
v_exp_f32_e32 v93, v93
v_mfma_f32_16x16x32_bf16 v[44:47], a[148:151], v[180:183], v[44:47]
ds_read_b128 a[180:183], v224 offset:7168
v_cvt_pk_bf16_f32 v126, v92, v93
v_mfma_f32_16x16x32_bf16 v[36:39], a[152:155], v[168:171], v[36:39]
v_exp_f32_e32 v94, v94
v_mfma_f32_16x16x32_bf16 v[44:47], a[152:155], v[184:187], v[44:47]
ds_read_b128 a[184:187], v224 offset:11264
v_mfma_f32_16x16x32_bf16 v[36:39], a[156:159], v[172:175], v[36:39]
v_exp_f32_e32 v95, v95
v_mfma_f32_16x16x32_bf16 v[44:47], a[156:159], v[188:191], v[44:47]
ds_read_b128 a[188:191], v224 offset:15360
v_cvt_pk_bf16_f32 v127, v94, v95
s_waitcnt lgkmcnt(4)
v_mfma_f32_16x16x32_bf16 v[48:51], a[160:163], v[160:163], v[128:131]
v_mfma_f32_16x16x32_bf16 v[56:59], a[160:163], v[176:179], v[132:135]
ds_read_b128 a[192:195], v228 offset:57344
v_mfma_f32_16x16x32_bf16 v[48:51], a[164:167], v[164:167], v[48:51]
v_mfma_f32_16x16x32_bf16 v[56:59], a[164:167], v[180:183], v[56:59]
s_cmp_ge_i32 s45, s43
s_cbranch_scc0 L_ms29_%=
s_cmp_eq_i32 s45, s43
s_cbranch_scc1 L_mt29_%=
v_mov_b32_e32 v32, v239
v_mov_b32_e32 v33, v239
v_mov_b32_e32 v34, v239
v_mov_b32_e32 v35, v239
v_mov_b32_e32 v40, v239
v_mov_b32_e32 v41, v239
v_mov_b32_e32 v42, v239
v_mov_b32_e32 v43, v239
v_mov_b32_e32 v36, v239
v_mov_b32_e32 v37, v239
v_mov_b32_e32 v38, v239
v_mov_b32_e32 v39, v239
v_mov_b32_e32 v44, v239
v_mov_b32_e32 v45, v239
v_mov_b32_e32 v46, v239
v_mov_b32_e32 v47, v239
s_branch L_ms29_%=
L_mt29_%=:
v_cndmask_b32_e64 v32, v32, v239, s[56:57]
v_cndmask_b32_e64 v33, v33, v239, s[58:59]
v_cndmask_b32_e64 v34, v34, v239, s[60:61]
v_cndmask_b32_e64 v35, v35, v239, s[62:63]
v_mov_b32_e32 v36, v239
v_mov_b32_e32 v37, v239
v_mov_b32_e32 v38, v239
v_mov_b32_e32 v39, v239
v_cndmask_b32_e64 v44, v44, v239, s[56:57]
v_cndmask_b32_e64 v45, v45, v239, s[58:59]
v_cndmask_b32_e64 v46, v46, v239, s[60:61]
v_cndmask_b32_e64 v47, v47, v239, s[62:63]
L_ms29_%=:
v_mfma_f32_16x16x32_bf16 v[48:51], a[168:171], v[168:171], v[48:51]
v_exp_f32_e32 v32, v32
v_mfma_f32_16x16x32_bf16 v[56:59], a[168:171], v[184:187], v[56:59]
ds_read_b128 a[196:199], v228 offset:58368
v_mfma_f32_16x16x32_bf16 v[48:51], a[172:175], v[172:175], v[48:51]
v_exp_f32_e32 v33, v33
v_mfma_f32_16x16x32_bf16 v[56:59], a[172:175], v[188:191], v[56:59]
v_cvt_pk_bf16_f32 v96, v32, v33
s_waitcnt lgkmcnt(2)
v_mfma_f32_16x16x32_bf16 v[52:55], a[176:179], v[160:163], v[128:131]
v_exp_f32_e32 v34, v34
v_mfma_f32_16x16x32_bf16 v[60:63], a[176:179], v[176:179], v[132:135]
ds_read_b128 a[200:203], v228 offset:59392
v_mfma_f32_16x16x32_bf16 v[52:55], a[180:183], v[164:167], v[52:55]
v_exp_f32_e32 v35, v35
v_mfma_f32_16x16x32_bf16 v[60:63], a[180:183], v[180:183], v[60:63]
v_cvt_pk_bf16_f32 v97, v34, v35
v_mfma_f32_16x16x32_bf16 v[52:55], a[184:187], v[168:171], v[52:55]
v_exp_f32_e32 v40, v40
v_mfma_f32_16x16x32_bf16 v[60:63], a[184:187], v[184:187], v[60:63]
ds_read_b128 a[204:207], v228 offset:60416
v_mfma_f32_16x16x32_bf16 v[52:55], a[188:191], v[172:175], v[52:55]
v_exp_f32_e32 v41, v41
v_mfma_f32_16x16x32_bf16 v[60:63], a[188:191], v[188:191], v[60:63]
v_cvt_pk_bf16_f32 v100, v40, v41
s_waitcnt lgkmcnt(2)
v_mfma_f32_16x16x32_bf16 a[0:3], a[192:195], v[112:115], a[0:3]
v_exp_f32_e32 v42, v42
v_mfma_f32_16x16x32_bf16 a[4:7], a[192:195], v[116:119], a[4:7]
ds_read_b128 a[208:211], v228 offset:61440
v_mfma_f32_16x16x32_bf16 a[8:11], a[192:195], v[120:123], a[8:11]
v_exp_f32_e32 v43, v43
v_mfma_f32_16x16x32_bf16 a[12:15], a[192:195], v[124:127], a[12:15]
v_cvt_pk_bf16_f32 v101, v42, v43
v_mfma_f32_16x16x32_bf16 a[16:19], a[196:199], v[112:115], a[16:19]
v_exp_f32_e32 v36, v36
v_mfma_f32_16x16x32_bf16 a[20:23], a[196:199], v[116:119], a[20:23]
ds_read_b128 a[212:215], v228 offset:62464
v_mfma_f32_16x16x32_bf16 a[24:27], a[196:199], v[120:123], a[24:27]
v_exp_f32_e32 v37, v37
v_mfma_f32_16x16x32_bf16 a[28:31], a[196:199], v[124:127], a[28:31]
v_cvt_pk_bf16_f32 v98, v36, v37
s_waitcnt lgkmcnt(2)
v_mfma_f32_16x16x32_bf16 a[32:35], a[200:203], v[112:115], a[32:35]
v_exp_f32_e32 v38, v38
v_mfma_f32_16x16x32_bf16 a[36:39], a[200:203], v[116:119], a[36:39]
ds_read_b128 a[216:219], v228 offset:63488
s_cmp_ge_i32 s45, s49
s_cbranch_scc0 L_ms30_%=
s_cmp_eq_i32 s45, s49
s_cbranch_scc1 L_mt30_%=
v_mov_b32_e32 v48, v239
v_mov_b32_e32 v49, v239
v_mov_b32_e32 v50, v239
v_mov_b32_e32 v51, v239
v_mov_b32_e32 v56, v239
v_mov_b32_e32 v57, v239
v_mov_b32_e32 v58, v239
v_mov_b32_e32 v59, v239
v_mov_b32_e32 v52, v239
v_mov_b32_e32 v53, v239
v_mov_b32_e32 v54, v239
v_mov_b32_e32 v55, v239
v_mov_b32_e32 v60, v239
v_mov_b32_e32 v61, v239
v_mov_b32_e32 v62, v239
v_mov_b32_e32 v63, v239
s_branch L_ms30_%=
L_mt30_%=:
v_cndmask_b32_e64 v48, v48, v239, s[56:57]
v_cndmask_b32_e64 v49, v49, v239, s[58:59]
v_cndmask_b32_e64 v50, v50, v239, s[60:61]
v_cndmask_b32_e64 v51, v51, v239, s[62:63]
v_mov_b32_e32 v52, v239
v_mov_b32_e32 v53, v239
v_mov_b32_e32 v54, v239
v_mov_b32_e32 v55, v239
v_cndmask_b32_e64 v60, v60, v239, s[56:57]
v_cndmask_b32_e64 v61, v61, v239, s[58:59]
v_cndmask_b32_e64 v62, v62, v239, s[60:61]
v_cndmask_b32_e64 v63, v63, v239, s[62:63]
L_ms30_%=:
v_mfma_f32_16x16x32_bf16 a[40:43], a[200:203], v[120:123], a[40:43]
v_exp_f32_e32 v39, v39
v_mfma_f32_16x16x32_bf16 a[44:47], a[200:203], v[124:127], a[44:47]
v_cvt_pk_bf16_f32 v99, v38, v39
v_mfma_f32_16x16x32_bf16 a[48:51], a[204:207], v[112:115], a[48:51]
v_exp_f32_e32 v44, v44
v_mfma_f32_16x16x32_bf16 a[52:55], a[204:207], v[116:119], a[52:55]
ds_read_b128 a[220:223], v228 offset:64512
v_mfma_f32_16x16x32_bf16 a[56:59], a[204:207], v[120:123], a[56:59]
v_exp_f32_e32 v45, v45
v_mfma_f32_16x16x32_bf16 a[60:63], a[204:207], v[124:127], a[60:63]
v_cvt_pk_bf16_f32 v102, v44, v45
s_waitcnt lgkmcnt(2)
v_mfma_f32_16x16x32_bf16 a[64:67], a[208:211], v[112:115], a[64:67]
v_exp_f32_e32 v46, v46
v_mfma_f32_16x16x32_bf16 a[68:71], a[208:211], v[116:119], a[68:71]
v_mfma_f32_16x16x32_bf16 a[72:75], a[208:211], v[120:123], a[72:75]
v_exp_f32_e32 v47, v47
v_mfma_f32_16x16x32_bf16 a[76:79], a[208:211], v[124:127], a[76:79]
v_cvt_pk_bf16_f32 v103, v46, v47
v_mfma_f32_16x16x32_bf16 a[80:83], a[212:215], v[112:115], a[80:83]
v_exp_f32_e32 v48, v48
v_mfma_f32_16x16x32_bf16 a[84:87], a[212:215], v[116:119], a[84:87]
ds_read_b128 a[128:131], v224 offset:16384
v_mfma_f32_16x16x32_bf16 a[88:91], a[212:215], v[120:123], a[88:91]
v_exp_f32_e32 v49, v49
v_mfma_f32_16x16x32_bf16 a[92:95], a[212:215], v[124:127], a[92:95]
ds_read_b128 a[132:135], v224 offset:20480
v_cvt_pk_bf16_f32 v104, v48, v49
s_waitcnt lgkmcnt(2)
v_mfma_f32_16x16x32_bf16 a[96:99], a[216:219], v[112:115], a[96:99]
v_exp_f32_e32 v50, v50
v_mfma_f32_16x16x32_bf16 a[100:103], a[216:219], v[116:119], a[100:103]
ds_read_b128 a[136:139], v224 offset:24576
v_mfma_f32_16x16x32_bf16 a[104:107], a[216:219], v[120:123], a[104:107]
v_exp_f32_e32 v51, v51
v_mfma_f32_16x16x32_bf16 a[108:111], a[216:219], v[124:127], a[108:111]
ds_read_b128 a[140:143], v224 offset:28672
v_cvt_pk_bf16_f32 v105, v50, v51
v_mfma_f32_16x16x32_bf16 a[112:115], a[220:223], v[112:115], a[112:115]
v_exp_f32_e32 v56, v56
v_mfma_f32_16x16x32_bf16 a[116:119], a[220:223], v[116:119], a[116:119]
ds_read_b128 a[144:147], v224 offset:17408
v_mfma_f32_16x16x32_bf16 a[120:123], a[220:223], v[120:123], a[120:123]
)ASM"
R"ASM(v_exp_f32_e32 v57, v57
v_mfma_f32_16x16x32_bf16 a[124:127], a[220:223], v[124:127], a[124:127]
ds_read_b128 a[148:151], v224 offset:21504
v_cvt_pk_bf16_f32 v108, v56, v57
v_mfma_f32_16x16x32_bf16 v[144:147], v[248:251], v[112:115], v[144:147]
v_exp_f32_e32 v58, v58
v_mfma_f32_16x16x32_bf16 v[148:151], v[248:251], v[116:119], v[148:151]
ds_read_b128 a[152:155], v224 offset:25600
v_mfma_f32_16x16x32_bf16 v[152:155], v[248:251], v[120:123], v[152:155]
v_exp_f32_e32 v59, v59
v_mfma_f32_16x16x32_bf16 v[156:159], v[248:251], v[124:127], v[156:159]
ds_read_b128 a[156:159], v224 offset:29696
v_cvt_pk_bf16_f32 v109, v58, v59
s_branch L_SS0_%=
L_SS0_%=:
s_waitcnt lgkmcnt(4)
v_mfma_f32_16x16x32_bf16 v[64:67], a[128:131], v[160:163], v[128:131]
s_add_i32 s45, s45, 2
v_exp_f32_e32 v52, v52
v_mfma_f32_16x16x32_bf16 v[72:75], a[128:131], v[176:179], v[132:135]
s_add_u32 m0, s42, 49152
ds_read_b128 a[160:163], v224 offset:18432
v_mfma_f32_16x16x32_bf16 v[64:67], a[132:135], v[164:167], v[64:67]
buffer_load_dwordx4 v238, s[36:39], s41 offen sc1 lds
v_exp_f32_e32 v53, v53
v_mfma_f32_16x16x32_bf16 v[72:75], a[132:135], v[180:183], v[72:75]
ds_read_b128 a[164:167], v224 offset:22528
v_cvt_pk_bf16_f32 v106, v52, v53
v_mfma_f32_16x16x32_bf16 v[64:67], a[136:139], v[168:171], v[64:67]
v_exp_f32_e32 v54, v54
v_mfma_f32_16x16x32_bf16 v[72:75], a[136:139], v[184:187], v[72:75]
ds_read_b128 a[168:171], v224 offset:26624
v_mfma_f32_16x16x32_bf16 v[64:67], a[140:143], v[172:175], v[64:67]
v_exp_f32_e32 v55, v55
v_mfma_f32_16x16x32_bf16 v[72:75], a[140:143], v[188:191], v[72:75]
ds_read_b128 a[172:175], v224 offset:30720
v_cvt_pk_bf16_f32 v107, v54, v55
s_waitcnt lgkmcnt(4)
v_mfma_f32_16x16x32_bf16 v[68:71], a[144:147], v[160:163], v[128:131]
buffer_load_dwordx4 v238, s[36:39], s41 offen offset:1024 sc1 lds
v_exp_f32_e32 v60, v60
v_mfma_f32_16x16x32_bf16 v[76:79], a[144:147], v[176:179], v[132:135]
ds_read_b128 a[176:179], v224 offset:19456
v_mfma_f32_16x16x32_bf16 v[68:71], a[148:151], v[164:167], v[68:71]
v_exp_f32_e32 v61, v61
v_mfma_f32_16x16x32_bf16 v[76:79], a[148:151], v[180:183], v[76:79]
ds_read_b128 a[180:183], v224 offset:23552
v_cvt_pk_bf16_f32 v110, v60, v61
v_mfma_f32_16x16x32_bf16 v[68:71], a[152:155], v[168:171], v[68:71]
v_exp_f32_e32 v62, v62
v_mfma_f32_16x16x32_bf16 v[76:79], a[152:155], v[184:187], v[76:79]
ds_read_b128 a[184:187], v224 offset:27648
v_mfma_f32_16x16x32_bf16 v[68:71], a[156:159], v[172:175], v[68:71]
buffer_load_dwordx4 v238, s[36:39], s41 offen offset:2048 sc1 lds
v_exp_f32_e32 v63, v63
v_mfma_f32_16x16x32_bf16 v[76:79], a[156:159], v[188:191], v[76:79]
ds_read_b128 a[188:191], v224 offset:31744
v_cvt_pk_bf16_f32 v111, v62, v63
s_waitcnt lgkmcnt(4)
v_mfma_f32_16x16x32_bf16 v[80:83], a[160:163], v[160:163], v[128:131]
v_mfma_f32_16x16x32_bf16 v[88:91], a[160:163], v[176:179], v[132:135]
ds_read_b128 a[192:195], v228
v_mfma_f32_16x16x32_bf16 v[80:83], a[164:167], v[164:167], v[80:83]
v_mfma_f32_16x16x32_bf16 v[88:91], a[164:167], v[180:183], v[88:91]
ds_read_b128 a[196:199], v228 offset:1024
s_cmp_ge_i32 s45, s43
s_cbranch_scc0 L_ms5_%=
s_cmp_eq_i32 s45, s43
s_cbranch_scc1 L_mt5_%=
v_mov_b32_e32 v64, v239
v_mov_b32_e32 v65, v239
v_mov_b32_e32 v66, v239
v_mov_b32_e32 v67, v239
v_mov_b32_e32 v72, v239
v_mov_b32_e32 v73, v239
v_mov_b32_e32 v74, v239
v_mov_b32_e32 v75, v239
v_mov_b32_e32 v68, v239
v_mov_b32_e32 v69, v239
v_mov_b32_e32 v70, v239
v_mov_b32_e32 v71, v239
v_mov_b32_e32 v76, v239
v_mov_b32_e32 v77, v239
v_mov_b32_e32 v78, v239
v_mov_b32_e32 v79, v239
s_branch L_ms5_%=
L_mt5_%=:
v_cndmask_b32_e64 v64, v64, v239, s[56:57]
v_cndmask_b32_e64 v65, v65, v239, s[58:59]
v_cndmask_b32_e64 v66, v66, v239, s[60:61]
v_cndmask_b32_e64 v67, v67, v239, s[62:63]
v_mov_b32_e32 v68, v239
v_mov_b32_e32 v69, v239
v_mov_b32_e32 v70, v239
v_mov_b32_e32 v71, v239
v_cndmask_b32_e64 v76, v76, v239, s[56:57]
v_cndmask_b32_e64 v77, v77, v239, s[58:59]
v_cndmask_b32_e64 v78, v78, v239, s[60:61]
v_cndmask_b32_e64 v79, v79, v239, s[62:63]
L_ms5_%=:
v_mfma_f32_16x16x32_bf16 v[80:83], a[168:171], v[168:171], v[80:83]
buffer_load_dwordx4 v238, s[36:39], s41 offen offset:3072 sc1 lds
v_exp_f32_e32 v64, v64
v_mfma_f32_16x16x32_bf16 v[88:91], a[168:171], v[184:187], v[88:91]
ds_read_b128 a[200:203], v228 offset:2048
v_mfma_f32_16x16x32_bf16 v[80:83], a[172:175], v[172:175], v[80:83]
v_exp_f32_e32 v65, v65
v_mfma_f32_16x16x32_bf16 v[88:91], a[172:175], v[188:191], v[88:91]
ds_read_b128 a[204:207], v228 offset:3072
v_cvt_pk_bf16_f32 v112, v64, v65
s_waitcnt lgkmcnt(4)
v_mfma_f32_16x16x32_bf16 v[84:87], a[176:179], v[160:163], v[128:131]
v_exp_f32_e32 v66, v66
v_mfma_f32_16x16x32_bf16 v[92:95], a[176:179], v[176:179], v[132:135]
s_add_u32 m0, s42, 98304
ds_read_b128 a[208:211], v228 offset:4096
v_mfma_f32_16x16x32_bf16 v[84:87], a[180:183], v[164:167], v[84:87]
buffer_load_dwordx4 v238, s[36:39], s40 offen sc1 lds
v_exp_f32_e32 v67, v67
v_mfma_f32_16x16x32_bf16 v[92:95], a[180:183], v[180:183], v[92:95]
ds_read_b128 a[212:215], v228 offset:5120
v_cvt_pk_bf16_f32 v113, v66, v67
v_mfma_f32_16x16x32_bf16 v[84:87], a[184:187], v[168:171], v[84:87]
v_exp_f32_e32 v72, v72
v_mfma_f32_16x16x32_bf16 v[92:95], a[184:187], v[184:187], v[92:95]
ds_read_b128 a[216:219], v228 offset:6144
v_mfma_f32_16x16x32_bf16 v[84:87], a[188:191], v[172:175], v[84:87]
v_exp_f32_e32 v73, v73
v_mfma_f32_16x16x32_bf16 v[92:95], a[188:191], v[188:191], v[92:95]
ds_read_b128 a[220:223], v228 offset:7168
v_cvt_pk_bf16_f32 v116, v72, v73
s_waitcnt lgkmcnt(4)
v_mfma_f32_16x16x32_bf16 a[0:3], a[192:195], v[96:99], a[0:3]
buffer_load_dwordx4 v238, s[36:39], s40 offen offset:1024 sc1 lds
v_exp_f32_e32 v74, v74
v_mfma_f32_16x16x32_bf16 a[4:7], a[192:195], v[100:103], a[4:7]
ds_read_b128 a[224:227], v228 offset:8192
v_mfma_f32_16x16x32_bf16 a[16:19], a[196:199], v[96:99], a[16:19]
v_exp_f32_e32 v75, v75
v_mfma_f32_16x16x32_bf16 a[20:23], a[196:199], v[100:103], a[20:23]
ds_read_b128 a[228:231], v228 offset:9216
v_cvt_pk_bf16_f32 v117, v74, v75
v_mfma_f32_16x16x32_bf16 a[32:35], a[200:203], v[96:99], a[32:35]
v_exp_f32_e32 v68, v68
v_mfma_f32_16x16x32_bf16 a[36:39], a[200:203], v[100:103], a[36:39]
ds_read_b128 a[232:235], v228 offset:10240
v_mfma_f32_16x16x32_bf16 a[48:51], a[204:207], v[96:99], a[48:51]
buffer_load_dwordx4 v238, s[36:39], s40 offen offset:2048 sc1 lds
v_exp_f32_e32 v69, v69
v_mfma_f32_16x16x32_bf16 a[52:55], a[204:207], v[100:103], a[52:55]
ds_read_b128 a[236:239], v228 offset:11264
v_cvt_pk_bf16_f32 v114, v68, v69
s_waitcnt lgkmcnt(4)
v_mfma_f32_16x16x32_bf16 a[64:67], a[208:211], v[96:99], a[64:67]
v_exp_f32_e32 v70, v70
v_mfma_f32_16x16x32_bf16 a[68:71], a[208:211], v[100:103], a[68:71]
ds_read_b128 a[240:243], v228 offset:12288
s_cmp_ge_i32 s45, s49
s_cbranch_scc0 L_ms6_%=
s_cmp_eq_i32 s45, s49
s_cbranch_scc1 L_mt6_%=
v_mov_b32_e32 v80, v239
v_mov_b32_e32 v81, v239
v_mov_b32_e32 v82, v239
v_mov_b32_e32 v83, v239
v_mov_b32_e32 v88, v239
v_mov_b32_e32 v89, v239
v_mov_b32_e32 v90, v239
v_mov_b32_e32 v91, v239
v_mov_b32_e32 v84, v239
v_mov_b32_e32 v85, v239
v_mov_b32_e32 v86, v239
v_mov_b32_e32 v87, v239
v_mov_b32_e32 v92, v239
v_mov_b32_e32 v93, v239
v_mov_b32_e32 v94, v239
v_mov_b32_e32 v95, v239
s_branch L_ms6_%=
L_mt6_%=:
v_cndmask_b32_e64 v80, v80, v239, s[56:57]
v_cndmask_b32_e64 v81, v81, v239, s[58:59]
v_cndmask_b32_e64 v82, v82, v239, s[60:61]
v_cndmask_b32_e64 v83, v83, v239, s[62:63]
v_mov_b32_e32 v84, v239
v_mov_b32_e32 v85, v239
v_mov_b32_e32 v86, v239
v_mov_b32_e32 v87, v239
v_cndmask_b32_e64 v92, v92, v239, s[56:57]
v_cndmask_b32_e64 v93, v93, v239, s[58:59]
v_cndmask_b32_e64 v94, v94, v239, s[60:61]
v_cndmask_b32_e64 v95, v95, v239, s[62:63]
L_ms6_%=:
v_mfma_f32_16x16x32_bf16 a[80:83], a[212:215], v[96:99], a[80:83]
v_exp_f32_e32 v71, v71
v_mfma_f32_16x16x32_bf16 a[84:87], a[212:215], v[100:103], a[84:87]
ds_read_b128 a[244:247], v228 offset:13312
v_cvt_pk_bf16_f32 v115, v70, v71
v_mfma_f32_16x16x32_bf16 a[96:99], a[216:219], v[96:99], a[96:99]
buffer_load_dwordx4 v238, s[36:39], s40 offen offset:3072 sc1 lds
v_exp_f32_e32 v76, v76
v_mfma_f32_16x16x32_bf16 a[100:103], a[216:219], v[100:103], a[100:103]
s_add_u32 s41, s41, 0x8000
ds_read_b128 a[248:251], v228 offset:14336
v_mfma_f32_16x16x32_bf16 a[112:115], a[220:223], v[96:99], a[112:115]
s_add_u32 s40, s40, 0x8000
v_exp_f32_e32 v77, v77
v_mfma_f32_16x16x32_bf16 a[116:119], a[220:223], v[100:103], a[116:119]
ds_read_b128 a[252:255], v228 offset:15360
v_cvt_pk_bf16_f32 v118, v76, v77
s_waitcnt lgkmcnt(4)
v_mfma_f32_16x16x32_bf16 a[0:3], a[224:227], v[104:107], a[0:3]
s_waitcnt vmcnt(8)
s_barrier
v_exp_f32_e32 v78, v78
v_mfma_f32_16x16x32_bf16 a[4:7], a[224:227], v[108:111], a[4:7]
v_mfma_f32_16x16x32_bf16 a[16:19], a[228:231], v[104:107], a[16:19]
v_exp_f32_e32 v79, v79
v_mfma_f32_16x16x32_bf16 a[20:23], a[228:231], v[108:111], a[20:23]
v_cvt_pk_bf16_f32 v119, v78, v79
v_mfma_f32_16x16x32_bf16 a[32:35], a[232:235], v[104:107], a[32:35]
v_exp_f32_e32 v80, v80
v_mfma_f32_16x16x32_bf16 a[36:39], a[232:235], v[108:111], a[36:39]
ds_read_b128 a[128:131], v224 offset:32768
v_mfma_f32_16x16x32_bf16 a[48:51], a[236:239], v[104:107], a[48:51]
v_exp_f32_e32 v81, v81
v_mfma_f32_16x16x32_bf16 a[52:55], a[236:239], v[108:111], a[52:55]
ds_read_b128 a[132:135], v224 offset:36864
v_cvt_pk_bf16_f32 v120, v80, v81
s_waitcnt lgkmcnt(2)
v_mfma_f32_16x16x32_bf16 a[64:67], a[240:243], v[104:107], a[64:67]
v_exp_f32_e32 v82, v82
v_mfma_f32_16x16x32_bf16 a[68:71], a[240:243], v[108:111], a[68:71]
ds_read_b128 a[136:139], v224 offset:40960
v_mfma_f32_16x16x32_bf16 a[80:83], a[244:247], v[104:107], a[80:83]
v_exp_f32_e32 v83, v83
v_mfma_f32_16x16x32_bf16 a[84:87], a[244:247], v[108:111], a[84:87]
ds_read_b128 a[140:143], v224 offset:45056
v_cvt_pk_bf16_f32 v121, v82, v83
v_mfma_f32_16x16x32_bf16 a[96:99], a[248:251], v[104:107], a[96:99]
v_exp_f32_e32 v88, v88
v_mfma_f32_16x16x32_bf16 a[100:103], a[248:251], v[108:111], a[100:103]
ds_read_b128 a[144:147], v224 offset:33792
v_mfma_f32_16x16x32_bf16 a[112:115], a[252:255], v[104:107], a[112:115]
v_exp_f32_e32 v89, v89
v_mfma_f32_16x16x32_bf16 a[116:119], a[252:255], v[108:111], a[116:119]
ds_read_b128 a[148:151], v224 offset:37888
v_cvt_pk_bf16_f32 v124, v88, v89
v_mfma_f32_16x16x32_bf16 v[144:147], v[248:251], v[96:99], v[144:147]
v_exp_f32_e32 v90, v90
v_mfma_f32_16x16x32_bf16 v[148:151], v[248:251], v[100:103], v[148:151]
ds_read_b128 a[152:155], v224 offset:41984
v_mfma_f32_16x16x32_bf16 v[144:147], v[248:251], v[104:107], v[144:147]
v_exp_f32_e32 v91, v91
v_mfma_f32_16x16x32_bf16 v[148:151], v[248:251], v[108:111], v[148:151]
ds_read_b128 a[156:159], v224 offset:46080
v_cvt_pk_bf16_f32 v125, v90, v91
L_SS1_%=:
s_cmp_eq_u32 s48, s47
s_cbranch_scc1 L_DR1_%=
s_add_u32 s48, s48, 2
s_waitcnt lgkmcnt(4)
v_mfma_f32_16x16x32_bf16 v[32:35], a[128:131], v[160:163], v[128:131]
s_add_i32 s45, s45, 2
v_exp_f32_e32 v84, v84
v_mfma_f32_16x16x32_bf16 v[40:43], a[128:131], v[176:179], v[132:135]
s_add_u32 m0, s42, 0
ds_read_b128 a[160:163], v224 offset:34816
v_mfma_f32_16x16x32_bf16 v[32:35], a[132:135], v[164:167], v[32:35]
buffer_load_dwordx4 v238, s[36:39], s41 offen sc1 lds
v_exp_f32_e32 v85, v85
v_mfma_f32_16x16x32_bf16 v[40:43], a[132:135], v[180:183], v[40:43]
ds_read_b128 a[164:167], v224 offset:38912
v_cvt_pk_bf16_f32 v122, v84, v85
v_mfma_f32_16x16x32_bf16 v[32:35], a[136:139], v[168:171], v[32:35]
v_exp_f32_e32 v86, v86
v_mfma_f32_16x16x32_bf16 v[40:43], a[136:139], v[184:187], v[40:43]
ds_read_b128 a[168:171], v224 offset:43008
v_mfma_f32_16x16x32_bf16 v[32:35], a[140:143], v[172:175], v[32:35]
v_exp_f32_e32 v87, v87
v_mfma_f32_16x16x32_bf16 v[40:43], a[140:143], v[188:191], v[40:43]
ds_read_b128 a[172:175], v224 offset:47104
v_cvt_pk_bf16_f32 v123, v86, v87
s_waitcnt lgkmcnt(4)
v_mfma_f32_16x16x32_bf16 v[36:39], a[144:147], v[160:163], v[128:131]
buffer_load_dwordx4 v238, s[36:39], s41 offen offset:1024 sc1 lds
v_exp_f32_e32 v92, v92
v_mfma_f32_16x16x32_bf16 v[44:47], a[144:147], v[176:179], v[132:135]
ds_read_b128 a[176:179], v224 offset:35840
)ASM"
R"ASM(v_mfma_f32_16x16x32_bf16 v[36:39], a[148:151], v[164:167], v[36:39]
v_exp_f32_e32 v93, v93
v_mfma_f32_16x16x32_bf16 v[44:47], a[148:151], v[180:183], v[44:47]
ds_read_b128 a[180:183], v224 offset:39936
v_cvt_pk_bf16_f32 v126, v92, v93
v_mfma_f32_16x16x32_bf16 v[36:39], a[152:155], v[168:171], v[36:39]
v_exp_f32_e32 v94, v94
v_mfma_f32_16x16x32_bf16 v[44:47], a[152:155], v[184:187], v[44:47]
ds_read_b128 a[184:187], v224 offset:44032
v_mfma_f32_16x16x32_bf16 v[36:39], a[156:159], v[172:175], v[36:39]
buffer_load_dwordx4 v238, s[36:39], s41 offen offset:2048 sc1 lds
v_exp_f32_e32 v95, v95
v_mfma_f32_16x16x32_bf16 v[44:47], a[156:159], v[188:191], v[44:47]
ds_read_b128 a[188:191], v224 offset:48128
v_cvt_pk_bf16_f32 v127, v94, v95
s_waitcnt lgkmcnt(4)
v_mfma_f32_16x16x32_bf16 v[48:51], a[160:163], v[160:163], v[128:131]
v_mfma_f32_16x16x32_bf16 v[56:59], a[160:163], v[176:179], v[132:135]
ds_read_b128 a[192:195], v228 offset:16384
v_mfma_f32_16x16x32_bf16 v[48:51], a[164:167], v[164:167], v[48:51]
v_mfma_f32_16x16x32_bf16 v[56:59], a[164:167], v[180:183], v[56:59]
ds_read_b128 a[196:199], v228 offset:17408
s_cmp_ge_i32 s45, s43
s_cbranch_scc0 L_ms15_%=
s_cmp_eq_i32 s45, s43
s_cbranch_scc1 L_mt15_%=
v_mov_b32_e32 v32, v239
v_mov_b32_e32 v33, v239
v_mov_b32_e32 v34, v239
v_mov_b32_e32 v35, v239
v_mov_b32_e32 v40, v239
v_mov_b32_e32 v41, v239
v_mov_b32_e32 v42, v239
v_mov_b32_e32 v43, v239
v_mov_b32_e32 v36, v239
v_mov_b32_e32 v37, v239
v_mov_b32_e32 v38, v239
v_mov_b32_e32 v39, v239
v_mov_b32_e32 v44, v239
v_mov_b32_e32 v45, v239
v_mov_b32_e32 v46, v239
v_mov_b32_e32 v47, v239
s_branch L_ms15_%=
L_mt15_%=:
v_cndmask_b32_e64 v32, v32, v239, s[56:57]
v_cndmask_b32_e64 v33, v33, v239, s[58:59]
v_cndmask_b32_e64 v34, v34, v239, s[60:61]
v_cndmask_b32_e64 v35, v35, v239, s[62:63]
v_mov_b32_e32 v36, v239
v_mov_b32_e32 v37, v239
v_mov_b32_e32 v38, v239
v_mov_b32_e32 v39, v239
v_cndmask_b32_e64 v44, v44, v239, s[56:57]
v_cndmask_b32_e64 v45, v45, v239, s[58:59]
v_cndmask_b32_e64 v46, v46, v239, s[60:61]
v_cndmask_b32_e64 v47, v47, v239, s[62:63]
L_ms15_%=:
v_mfma_f32_16x16x32_bf16 v[48:51], a[168:171], v[168:171], v[48:51]
buffer_load_dwordx4 v238, s[36:39], s41 offen offset:3072 sc1 lds
v_exp_f32_e32 v32, v32
v_mfma_f32_16x16x32_bf16 v[56:59], a[168:171], v[184:187], v[56:59]
ds_read_b128 a[200:203], v228 offset:18432
v_mfma_f32_16x16x32_bf16 v[48:51], a[172:175], v[172:175], v[48:51]
v_exp_f32_e32 v33, v33
v_mfma_f32_16x16x32_bf16 v[56:59], a[172:175], v[188:191], v[56:59]
ds_read_b128 a[204:207], v228 offset:19456
v_cvt_pk_bf16_f32 v96, v32, v33
s_waitcnt lgkmcnt(4)
v_mfma_f32_16x16x32_bf16 v[52:55], a[176:179], v[160:163], v[128:131]
v_exp_f32_e32 v34, v34
v_mfma_f32_16x16x32_bf16 v[60:63], a[176:179], v[176:179], v[132:135]
s_add_u32 m0, s42, 114688
ds_read_b128 a[208:211], v228 offset:20480
v_mfma_f32_16x16x32_bf16 v[52:55], a[180:183], v[164:167], v[52:55]
buffer_load_dwordx4 v238, s[36:39], s40 offen sc1 lds
v_exp_f32_e32 v35, v35
v_mfma_f32_16x16x32_bf16 v[60:63], a[180:183], v[180:183], v[60:63]
ds_read_b128 a[212:215], v228 offset:21504
v_cvt_pk_bf16_f32 v97, v34, v35
v_mfma_f32_16x16x32_bf16 v[52:55], a[184:187], v[168:171], v[52:55]
v_exp_f32_e32 v40, v40
v_mfma_f32_16x16x32_bf16 v[60:63], a[184:187], v[184:187], v[60:63]
ds_read_b128 a[216:219], v228 offset:22528
v_mfma_f32_16x16x32_bf16 v[52:55], a[188:191], v[172:175], v[52:55]
v_exp_f32_e32 v41, v41
v_mfma_f32_16x16x32_bf16 v[60:63], a[188:191], v[188:191], v[60:63]
ds_read_b128 a[220:223], v228 offset:23552
v_cvt_pk_bf16_f32 v100, v40, v41
s_waitcnt lgkmcnt(4)
v_mfma_f32_16x16x32_bf16 a[0:3], a[192:195], v[112:115], a[0:3]
buffer_load_dwordx4 v238, s[36:39], s40 offen offset:1024 sc1 lds
v_exp_f32_e32 v42, v42
v_mfma_f32_16x16x32_bf16 a[4:7], a[192:195], v[116:119], a[4:7]
ds_read_b128 a[224:227], v228 offset:24576
v_mfma_f32_16x16x32_bf16 a[16:19], a[196:199], v[112:115], a[16:19]
v_exp_f32_e32 v43, v43
v_mfma_f32_16x16x32_bf16 a[20:23], a[196:199], v[116:119], a[20:23]
ds_read_b128 a[228:231], v228 offset:25600
v_cvt_pk_bf16_f32 v101, v42, v43
v_mfma_f32_16x16x32_bf16 a[32:35], a[200:203], v[112:115], a[32:35]
v_exp_f32_e32 v36, v36
v_mfma_f32_16x16x32_bf16 a[36:39], a[200:203], v[116:119], a[36:39]
ds_read_b128 a[232:235], v228 offset:26624
v_mfma_f32_16x16x32_bf16 a[48:51], a[204:207], v[112:115], a[48:51]
buffer_load_dwordx4 v238, s[36:39], s40 offen offset:2048 sc1 lds
v_exp_f32_e32 v37, v37
v_mfma_f32_16x16x32_bf16 a[52:55], a[204:207], v[116:119], a[52:55]
ds_read_b128 a[236:239], v228 offset:27648
v_cvt_pk_bf16_f32 v98, v36, v37
s_waitcnt lgkmcnt(4)
v_mfma_f32_16x16x32_bf16 a[64:67], a[208:211], v[112:115], a[64:67]
v_exp_f32_e32 v38, v38
v_mfma_f32_16x16x32_bf16 a[68:71], a[208:211], v[116:119], a[68:71]
ds_read_b128 a[240:243], v228 offset:28672
s_cmp_ge_i32 s45, s49
s_cbranch_scc0 L_ms16_%=
s_cmp_eq_i32 s45, s49
s_cbranch_scc1 L_mt16_%=
v_mov_b32_e32 v48, v239
v_mov_b32_e32 v49, v239
v_mov_b32_e32 v50, v239
v_mov_b32_e32 v51, v239
v_mov_b32_e32 v56, v239
v_mov_b32_e32 v57, v239
v_mov_b32_e32 v58, v239
v_mov_b32_e32 v59, v239
v_mov_b32_e32 v52, v239
v_mov_b32_e32 v53, v239
v_mov_b32_e32 v54, v239
v_mov_b32_e32 v55, v239
v_mov_b32_e32 v60, v239
v_mov_b32_e32 v61, v239
v_mov_b32_e32 v62, v239
v_mov_b32_e32 v63, v239
s_branch L_ms16_%=
L_mt16_%=:
v_cndmask_b32_e64 v48, v48, v239, s[56:57]
v_cndmask_b32_e64 v49, v49, v239, s[58:59]
v_cndmask_b32_e64 v50, v50, v239, s[60:61]
v_cndmask_b32_e64 v51, v51, v239, s[62:63]
v_mov_b32_e32 v52, v239
v_mov_b32_e32 v53, v239
v_mov_b32_e32 v54, v239
v_mov_b32_e32 v55, v239
v_cndmask_b32_e64 v60, v60, v239, s[56:57]
v_cndmask_b32_e64 v61, v61, v239, s[58:59]
v_cndmask_b32_e64 v62, v62, v239, s[60:61]
v_cndmask_b32_e64 v63, v63, v239, s[62:63]
L_ms16_%=:
v_mfma_f32_16x16x32_bf16 a[80:83], a[212:215], v[112:115], a[80:83]
v_exp_f32_e32 v39, v39
v_mfma_f32_16x16x32_bf16 a[84:87], a[212:215], v[116:119], a[84:87]
ds_read_b128 a[244:247], v228 offset:29696
v_cvt_pk_bf16_f32 v99, v38, v39
v_mfma_f32_16x16x32_bf16 a[96:99], a[216:219], v[112:115], a[96:99]
buffer_load_dwordx4 v238, s[36:39], s40 offen offset:3072 sc1 lds
v_exp_f32_e32 v44, v44
v_mfma_f32_16x16x32_bf16 a[100:103], a[216:219], v[116:119], a[100:103]
s_add_u32 s41, s41, 0x8000
ds_read_b128 a[248:251], v228 offset:30720
v_mfma_f32_16x16x32_bf16 a[112:115], a[220:223], v[112:115], a[112:115]
s_add_u32 s40, s40, 0x8000
v_exp_f32_e32 v45, v45
v_mfma_f32_16x16x32_bf16 a[116:119], a[220:223], v[116:119], a[116:119]
ds_read_b128 a[252:255], v228 offset:31744
v_cvt_pk_bf16_f32 v102, v44, v45
s_waitcnt lgkmcnt(4)
v_mfma_f32_16x16x32_bf16 a[0:3], a[224:227], v[120:123], a[0:3]
s_waitcnt vmcnt(8)
s_barrier
v_exp_f32_e32 v46, v46
v_mfma_f32_16x16x32_bf16 a[4:7], a[224:227], v[124:127], a[4:7]
v_mfma_f32_16x16x32_bf16 a[16:19], a[228:231], v[120:123], a[16:19]
v_exp_f32_e32 v47, v47
v_mfma_f32_16x16x32_bf16 a[20:23], a[228:231], v[124:127], a[20:23]
v_cvt_pk_bf16_f32 v103, v46, v47
v_mfma_f32_16x16x32_bf16 a[32:35], a[232:235], v[120:123], a[32:35]
v_exp_f32_e32 v48, v48
v_mfma_f32_16x16x32_bf16 a[36:39], a[232:235], v[124:127], a[36:39]
ds_read_b128 a[128:131], v224 offset:49152
v_mfma_f32_16x16x32_bf16 a[48:51], a[236:239], v[120:123], a[48:51]
v_exp_f32_e32 v49, v49
v_mfma_f32_16x16x32_bf16 a[52:55], a[236:239], v[124:127], a[52:55]
ds_read_b128 a[132:135], v224 offset:53248
v_cvt_pk_bf16_f32 v104, v48, v49
s_waitcnt lgkmcnt(2)
v_mfma_f32_16x16x32_bf16 a[64:67], a[240:243], v[120:123], a[64:67]
v_exp_f32_e32 v50, v50
v_mfma_f32_16x16x32_bf16 a[68:71], a[240:243], v[124:127], a[68:71]
ds_read_b128 a[136:139], v224 offset:57344
v_mfma_f32_16x16x32_bf16 a[80:83], a[244:247], v[120:123], a[80:83]
v_exp_f32_e32 v51, v51
v_mfma_f32_16x16x32_bf16 a[84:87], a[244:247], v[124:127], a[84:87]
ds_read_b128 a[140:143], v224 offset:61440
v_cvt_pk_bf16_f32 v105, v50, v51
v_mfma_f32_16x16x32_bf16 a[96:99], a[248:251], v[120:123], a[96:99]
v_exp_f32_e32 v56, v56
v_mfma_f32_16x16x32_bf16 a[100:103], a[248:251], v[124:127], a[100:103]
ds_read_b128 a[144:147], v224 offset:50176
v_mfma_f32_16x16x32_bf16 a[112:115], a[252:255], v[120:123], a[112:115]
v_exp_f32_e32 v57, v57
v_mfma_f32_16x16x32_bf16 a[116:119], a[252:255], v[124:127], a[116:119]
ds_read_b128 a[148:151], v224 offset:54272
v_cvt_pk_bf16_f32 v108, v56, v57
v_mfma_f32_16x16x32_bf16 v[144:147], v[248:251], v[112:115], v[144:147]
v_exp_f32_e32 v58, v58
v_mfma_f32_16x16x32_bf16 v[148:151], v[248:251], v[116:119], v[148:151]
ds_read_b128 a[152:155], v224 offset:58368
v_mfma_f32_16x16x32_bf16 v[144:147], v[248:251], v[120:123], v[144:147]
v_exp_f32_e32 v59, v59
v_mfma_f32_16x16x32_bf16 v[148:151], v[248:251], v[124:127], v[148:151]
ds_read_b128 a[156:159], v224 offset:62464
v_cvt_pk_bf16_f32 v109, v58, v59
L_SS2_%=:
s_waitcnt lgkmcnt(4)
v_mfma_f32_16x16x32_bf16 v[64:67], a[128:131], v[160:163], v[128:131]
s_add_i32 s45, s45, 2
v_exp_f32_e32 v52, v52
v_mfma_f32_16x16x32_bf16 v[72:75], a[128:131], v[176:179], v[132:135]
s_add_u32 m0, s42, 16384
ds_read_b128 a[160:163], v224 offset:51200
v_mfma_f32_16x16x32_bf16 v[64:67], a[132:135], v[164:167], v[64:67]
buffer_load_dwordx4 v238, s[36:39], s41 offen sc1 lds
v_exp_f32_e32 v53, v53
v_mfma_f32_16x16x32_bf16 v[72:75], a[132:135], v[180:183], v[72:75]
ds_read_b128 a[164:167], v224 offset:55296
v_cvt_pk_bf16_f32 v106, v52, v53
v_mfma_f32_16x16x32_bf16 v[64:67], a[136:139], v[168:171], v[64:67]
v_exp_f32_e32 v54, v54
v_mfma_f32_16x16x32_bf16 v[72:75], a[136:139], v[184:187], v[72:75]
ds_read_b128 a[168:171], v224 offset:59392
v_mfma_f32_16x16x32_bf16 v[64:67], a[140:143], v[172:175], v[64:67]
v_exp_f32_e32 v55, v55
v_mfma_f32_16x16x32_bf16 v[72:75], a[140:143], v[188:191], v[72:75]
ds_read_b128 a[172:175], v224 offset:63488
v_cvt_pk_bf16_f32 v107, v54, v55
s_waitcnt lgkmcnt(4)
v_mfma_f32_16x16x32_bf16 v[68:71], a[144:147], v[160:163], v[128:131]
buffer_load_dwordx4 v238, s[36:39], s41 offen offset:1024 sc1 lds
v_exp_f32_e32 v60, v60
v_mfma_f32_16x16x32_bf16 v[76:79], a[144:147], v[176:179], v[132:135]
ds_read_b128 a[176:179], v224 offset:52224
v_mfma_f32_16x16x32_bf16 v[68:71], a[148:151], v[164:167], v[68:71]
v_exp_f32_e32 v61, v61
v_mfma_f32_16x16x32_bf16 v[76:79], a[148:151], v[180:183], v[76:79]
ds_read_b128 a[180:183], v224 offset:56320
v_cvt_pk_bf16_f32 v110, v60, v61
v_mfma_f32_16x16x32_bf16 v[68:71], a[152:155], v[168:171], v[68:71]
v_exp_f32_e32 v62, v62
v_mfma_f32_16x16x32_bf16 v[76:79], a[152:155], v[184:187], v[76:79]
ds_read_b128 a[184:187], v224 offset:60416
v_mfma_f32_16x16x32_bf16 v[68:71], a[156:159], v[172:175], v[68:71]
buffer_load_dwordx4 v238, s[36:39], s41 offen offset:2048 sc1 lds
v_exp_f32_e32 v63, v63
v_mfma_f32_16x16x32_bf16 v[76:79], a[156:159], v[188:191], v[76:79]
ds_read_b128 a[188:191], v224 offset:64512
v_cvt_pk_bf16_f32 v111, v62, v63
s_waitcnt lgkmcnt(4)
v_mfma_f32_16x16x32_bf16 v[80:83], a[160:163], v[160:163], v[128:131]
v_mfma_f32_16x16x32_bf16 v[88:91], a[160:163], v[176:179], v[132:135]
ds_read_b128 a[192:195], v228 offset:32768
v_mfma_f32_16x16x32_bf16 v[80:83], a[164:167], v[164:167], v[80:83]
v_mfma_f32_16x16x32_bf16 v[88:91], a[164:167], v[180:183], v[88:91]
ds_read_b128 a[196:199], v228 offset:33792
s_cmp_ge_i32 s45, s43
s_cbranch_scc0 L_ms21_%=
s_cmp_eq_i32 s45, s43
s_cbranch_scc1 L_mt21_%=
v_mov_b32_e32 v64, v239
v_mov_b32_e32 v65, v239
v_mov_b32_e32 v66, v239
v_mov_b32_e32 v67, v239
v_mov_b32_e32 v72, v239
v_mov_b32_e32 v73, v239
v_mov_b32_e32 v74, v239
v_mov_b32_e32 v75, v239
v_mov_b32_e32 v68, v239
v_mov_b32_e32 v69, v239
v_mov_b32_e32 v70, v239
v_mov_b32_e32 v71, v239
v_mov_b32_e32 v76, v239
v_mov_b32_e32 v77, v239
v_mov_b32_e32 v78, v239
v_mov_b32_e32 v79, v239
s_branch L_ms21_%=
L_mt21_%=:
v_cndmask_b32_e64 v64, v64, v239, s[56:57]
v_cndmask_b32_e64 v65, v65, v239, s[58:59]
v_cndmask_b32_e64 v66, v66, v239, s[60:61]
v_cndmask_b32_e64 v67, v67, v239, s[62:63]
v_mov_b32_e32 v68, v239
v_mov_b32_e32 v69, v239
v_mov_b32_e32 v70, v239
v_mov_b32_e32 v71, v239
v_cndmask_b32_e64 v76, v76, v239, s[56:57]
v_cndmask_b32_e64 v77, v77, v239, s[58:59]
)ASM"
R"ASM(v_cndmask_b32_e64 v78, v78, v239, s[60:61]
v_cndmask_b32_e64 v79, v79, v239, s[62:63]
L_ms21_%=:
v_mfma_f32_16x16x32_bf16 v[80:83], a[168:171], v[168:171], v[80:83]
buffer_load_dwordx4 v238, s[36:39], s41 offen offset:3072 sc1 lds
v_exp_f32_e32 v64, v64
v_mfma_f32_16x16x32_bf16 v[88:91], a[168:171], v[184:187], v[88:91]
ds_read_b128 a[200:203], v228 offset:34816
v_mfma_f32_16x16x32_bf16 v[80:83], a[172:175], v[172:175], v[80:83]
v_exp_f32_e32 v65, v65
v_mfma_f32_16x16x32_bf16 v[88:91], a[172:175], v[188:191], v[88:91]
ds_read_b128 a[204:207], v228 offset:35840
v_cvt_pk_bf16_f32 v112, v64, v65
s_waitcnt lgkmcnt(4)
v_mfma_f32_16x16x32_bf16 v[84:87], a[176:179], v[160:163], v[128:131]
v_exp_f32_e32 v66, v66
v_mfma_f32_16x16x32_bf16 v[92:95], a[176:179], v[176:179], v[132:135]
s_add_u32 m0, s42, 65536
ds_read_b128 a[208:211], v228 offset:36864
v_mfma_f32_16x16x32_bf16 v[84:87], a[180:183], v[164:167], v[84:87]
buffer_load_dwordx4 v238, s[36:39], s40 offen sc1 lds
v_exp_f32_e32 v67, v67
v_mfma_f32_16x16x32_bf16 v[92:95], a[180:183], v[180:183], v[92:95]
ds_read_b128 a[212:215], v228 offset:37888
v_cvt_pk_bf16_f32 v113, v66, v67
v_mfma_f32_16x16x32_bf16 v[84:87], a[184:187], v[168:171], v[84:87]
v_exp_f32_e32 v72, v72
v_mfma_f32_16x16x32_bf16 v[92:95], a[184:187], v[184:187], v[92:95]
ds_read_b128 a[216:219], v228 offset:38912
v_mfma_f32_16x16x32_bf16 v[84:87], a[188:191], v[172:175], v[84:87]
v_exp_f32_e32 v73, v73
v_mfma_f32_16x16x32_bf16 v[92:95], a[188:191], v[188:191], v[92:95]
ds_read_b128 a[220:223], v228 offset:39936
v_cvt_pk_bf16_f32 v116, v72, v73
s_waitcnt lgkmcnt(4)
v_mfma_f32_16x16x32_bf16 a[0:3], a[192:195], v[96:99], a[0:3]
buffer_load_dwordx4 v238, s[36:39], s40 offen offset:1024 sc1 lds
v_exp_f32_e32 v74, v74
v_mfma_f32_16x16x32_bf16 a[4:7], a[192:195], v[100:103], a[4:7]
ds_read_b128 a[224:227], v228 offset:40960
v_mfma_f32_16x16x32_bf16 a[16:19], a[196:199], v[96:99], a[16:19]
v_exp_f32_e32 v75, v75
v_mfma_f32_16x16x32_bf16 a[20:23], a[196:199], v[100:103], a[20:23]
ds_read_b128 a[228:231], v228 offset:41984
v_cvt_pk_bf16_f32 v117, v74, v75
v_mfma_f32_16x16x32_bf16 a[32:35], a[200:203], v[96:99], a[32:35]
v_exp_f32_e32 v68, v68
v_mfma_f32_16x16x32_bf16 a[36:39], a[200:203], v[100:103], a[36:39]
ds_read_b128 a[232:235], v228 offset:43008
v_mfma_f32_16x16x32_bf16 a[48:51], a[204:207], v[96:99], a[48:51]
buffer_load_dwordx4 v238, s[36:39], s40 offen offset:2048 sc1 lds
v_exp_f32_e32 v69, v69
v_mfma_f32_16x16x32_bf16 a[52:55], a[204:207], v[100:103], a[52:55]
ds_read_b128 a[236:239], v228 offset:44032
v_cvt_pk_bf16_f32 v114, v68, v69
s_waitcnt lgkmcnt(4)
v_mfma_f32_16x16x32_bf16 a[64:67], a[208:211], v[96:99], a[64:67]
v_exp_f32_e32 v70, v70
v_mfma_f32_16x16x32_bf16 a[68:71], a[208:211], v[100:103], a[68:71]
ds_read_b128 a[240:243], v228 offset:45056
s_cmp_ge_i32 s45, s49
s_cbranch_scc0 L_ms22_%=
s_cmp_eq_i32 s45, s49
s_cbranch_scc1 L_mt22_%=
v_mov_b32_e32 v80, v239
v_mov_b32_e32 v81, v239
v_mov_b32_e32 v82, v239
v_mov_b32_e32 v83, v239
v_mov_b32_e32 v88, v239
v_mov_b32_e32 v89, v239
v_mov_b32_e32 v90, v239
v_mov_b32_e32 v91, v239
v_mov_b32_e32 v84, v239
v_mov_b32_e32 v85, v239
v_mov_b32_e32 v86, v239
v_mov_b32_e32 v87, v239
v_mov_b32_e32 v92, v239
v_mov_b32_e32 v93, v239
v_mov_b32_e32 v94, v239
v_mov_b32_e32 v95, v239
s_branch L_ms22_%=
L_mt22_%=:
v_cndmask_b32_e64 v80, v80, v239, s[56:57]
v_cndmask_b32_e64 v81, v81, v239, s[58:59]
v_cndmask_b32_e64 v82, v82, v239, s[60:61]
v_cndmask_b32_e64 v83, v83, v239, s[62:63]
v_mov_b32_e32 v84, v239
v_mov_b32_e32 v85, v239
v_mov_b32_e32 v86, v239
v_mov_b32_e32 v87, v239
v_cndmask_b32_e64 v92, v92, v239, s[56:57]
v_cndmask_b32_e64 v93, v93, v239, s[58:59]
v_cndmask_b32_e64 v94, v94, v239, s[60:61]
v_cndmask_b32_e64 v95, v95, v239, s[62:63]
L_ms22_%=:
v_mfma_f32_16x16x32_bf16 a[80:83], a[212:215], v[96:99], a[80:83]
v_exp_f32_e32 v71, v71
v_mfma_f32_16x16x32_bf16 a[84:87], a[212:215], v[100:103], a[84:87]
ds_read_b128 a[244:247], v228 offset:46080
v_cvt_pk_bf16_f32 v115, v70, v71
v_mfma_f32_16x16x32_bf16 a[96:99], a[216:219], v[96:99], a[96:99]
buffer_load_dwordx4 v238, s[36:39], s40 offen offset:3072 sc1 lds
v_exp_f32_e32 v76, v76
v_mfma_f32_16x16x32_bf16 a[100:103], a[216:219], v[100:103], a[100:103]
s_add_u32 s41, s41, 0x8000
ds_read_b128 a[248:251], v228 offset:47104
v_mfma_f32_16x16x32_bf16 a[112:115], a[220:223], v[96:99], a[112:115]
s_add_u32 s40, s40, 0x8000
v_exp_f32_e32 v77, v77
v_mfma_f32_16x16x32_bf16 a[116:119], a[220:223], v[100:103], a[116:119]
ds_read_b128 a[252:255], v228 offset:48128
v_cvt_pk_bf16_f32 v118, v76, v77
s_waitcnt lgkmcnt(4)
v_mfma_f32_16x16x32_bf16 a[0:3], a[224:227], v[104:107], a[0:3]
s_waitcnt vmcnt(8)
s_barrier
v_exp_f32_e32 v78, v78
v_mfma_f32_16x16x32_bf16 a[4:7], a[224:227], v[108:111], a[4:7]
v_mfma_f32_16x16x32_bf16 a[16:19], a[228:231], v[104:107], a[16:19]
v_exp_f32_e32 v79, v79
v_mfma_f32_16x16x32_bf16 a[20:23], a[228:231], v[108:111], a[20:23]
v_cvt_pk_bf16_f32 v119, v78, v79
v_mfma_f32_16x16x32_bf16 a[32:35], a[232:235], v[104:107], a[32:35]
v_exp_f32_e32 v80, v80
v_mfma_f32_16x16x32_bf16 a[36:39], a[232:235], v[108:111], a[36:39]
ds_read_b128 a[128:131], v224
v_mfma_f32_16x16x32_bf16 a[48:51], a[236:239], v[104:107], a[48:51]
v_exp_f32_e32 v81, v81
v_mfma_f32_16x16x32_bf16 a[52:55], a[236:239], v[108:111], a[52:55]
ds_read_b128 a[132:135], v224 offset:4096
v_cvt_pk_bf16_f32 v120, v80, v81
s_waitcnt lgkmcnt(2)
v_mfma_f32_16x16x32_bf16 a[64:67], a[240:243], v[104:107], a[64:67]
v_exp_f32_e32 v82, v82
v_mfma_f32_16x16x32_bf16 a[68:71], a[240:243], v[108:111], a[68:71]
ds_read_b128 a[136:139], v224 offset:8192
v_mfma_f32_16x16x32_bf16 a[80:83], a[244:247], v[104:107], a[80:83]
v_exp_f32_e32 v83, v83
v_mfma_f32_16x16x32_bf16 a[84:87], a[244:247], v[108:111], a[84:87]
ds_read_b128 a[140:143], v224 offset:12288
v_cvt_pk_bf16_f32 v121, v82, v83
v_mfma_f32_16x16x32_bf16 a[96:99], a[248:251], v[104:107], a[96:99]
v_exp_f32_e32 v88, v88
v_mfma_f32_16x16x32_bf16 a[100:103], a[248:251], v[108:111], a[100:103]
ds_read_b128 a[144:147], v224 offset:1024
v_mfma_f32_16x16x32_bf16 a[112:115], a[252:255], v[104:107], a[112:115]
v_exp_f32_e32 v89, v89
v_mfma_f32_16x16x32_bf16 a[116:119], a[252:255], v[108:111], a[116:119]
ds_read_b128 a[148:151], v224 offset:5120
v_cvt_pk_bf16_f32 v124, v88, v89
v_mfma_f32_16x16x32_bf16 v[144:147], v[248:251], v[96:99], v[144:147]
v_exp_f32_e32 v90, v90
v_mfma_f32_16x16x32_bf16 v[148:151], v[248:251], v[100:103], v[148:151]
ds_read_b128 a[152:155], v224 offset:9216
v_mfma_f32_16x16x32_bf16 v[144:147], v[248:251], v[104:107], v[144:147]
v_exp_f32_e32 v91, v91
v_mfma_f32_16x16x32_bf16 v[148:151], v[248:251], v[108:111], v[148:151]
ds_read_b128 a[156:159], v224 offset:13312
v_cvt_pk_bf16_f32 v125, v90, v91
L_SS3_%=:
s_cmp_eq_u32 s48, s47
s_cbranch_scc1 L_DR3_%=
s_add_u32 s48, s48, 2
s_waitcnt lgkmcnt(4)
v_mfma_f32_16x16x32_bf16 v[32:35], a[128:131], v[160:163], v[128:131]
s_add_i32 s45, s45, 2
v_exp_f32_e32 v84, v84
v_mfma_f32_16x16x32_bf16 v[40:43], a[128:131], v[176:179], v[132:135]
s_add_u32 m0, s42, 32768
ds_read_b128 a[160:163], v224 offset:2048
v_mfma_f32_16x16x32_bf16 v[32:35], a[132:135], v[164:167], v[32:35]
buffer_load_dwordx4 v238, s[36:39], s41 offen sc1 lds
v_exp_f32_e32 v85, v85
v_mfma_f32_16x16x32_bf16 v[40:43], a[132:135], v[180:183], v[40:43]
ds_read_b128 a[164:167], v224 offset:6144
v_cvt_pk_bf16_f32 v122, v84, v85
v_mfma_f32_16x16x32_bf16 v[32:35], a[136:139], v[168:171], v[32:35]
v_exp_f32_e32 v86, v86
v_mfma_f32_16x16x32_bf16 v[40:43], a[136:139], v[184:187], v[40:43]
ds_read_b128 a[168:171], v224 offset:10240
v_mfma_f32_16x16x32_bf16 v[32:35], a[140:143], v[172:175], v[32:35]
v_exp_f32_e32 v87, v87
v_mfma_f32_16x16x32_bf16 v[40:43], a[140:143], v[188:191], v[40:43]
ds_read_b128 a[172:175], v224 offset:14336
v_cvt_pk_bf16_f32 v123, v86, v87
s_waitcnt lgkmcnt(4)
v_mfma_f32_16x16x32_bf16 v[36:39], a[144:147], v[160:163], v[128:131]
buffer_load_dwordx4 v238, s[36:39], s41 offen offset:1024 sc1 lds
v_exp_f32_e32 v92, v92
v_mfma_f32_16x16x32_bf16 v[44:47], a[144:147], v[176:179], v[132:135]
ds_read_b128 a[176:179], v224 offset:3072
v_mfma_f32_16x16x32_bf16 v[36:39], a[148:151], v[164:167], v[36:39]
v_exp_f32_e32 v93, v93
v_mfma_f32_16x16x32_bf16 v[44:47], a[148:151], v[180:183], v[44:47]
ds_read_b128 a[180:183], v224 offset:7168
v_cvt_pk_bf16_f32 v126, v92, v93
v_mfma_f32_16x16x32_bf16 v[36:39], a[152:155], v[168:171], v[36:39]
v_exp_f32_e32 v94, v94
v_mfma_f32_16x16x32_bf16 v[44:47], a[152:155], v[184:187], v[44:47]
ds_read_b128 a[184:187], v224 offset:11264
v_mfma_f32_16x16x32_bf16 v[36:39], a[156:159], v[172:175], v[36:39]
buffer_load_dwordx4 v238, s[36:39], s41 offen offset:2048 sc1 lds
v_exp_f32_e32 v95, v95
v_mfma_f32_16x16x32_bf16 v[44:47], a[156:159], v[188:191], v[44:47]
ds_read_b128 a[188:191], v224 offset:15360
v_cvt_pk_bf16_f32 v127, v94, v95
s_waitcnt lgkmcnt(4)
v_mfma_f32_16x16x32_bf16 v[48:51], a[160:163], v[160:163], v[128:131]
v_mfma_f32_16x16x32_bf16 v[56:59], a[160:163], v[176:179], v[132:135]
ds_read_b128 a[192:195], v228 offset:49152
v_mfma_f32_16x16x32_bf16 v[48:51], a[164:167], v[164:167], v[48:51]
v_mfma_f32_16x16x32_bf16 v[56:59], a[164:167], v[180:183], v[56:59]
ds_read_b128 a[196:199], v228 offset:50176
s_cmp_ge_i32 s45, s43
s_cbranch_scc0 L_ms31_%=
s_cmp_eq_i32 s45, s43
s_cbranch_scc1 L_mt31_%=
v_mov_b32_e32 v32, v239
v_mov_b32_e32 v33, v239
v_mov_b32_e32 v34, v239
v_mov_b32_e32 v35, v239
v_mov_b32_e32 v40, v239
v_mov_b32_e32 v41, v239
v_mov_b32_e32 v42, v239
v_mov_b32_e32 v43, v239
v_mov_b32_e32 v36, v239
v_mov_b32_e32 v37, v239
v_mov_b32_e32 v38, v239
v_mov_b32_e32 v39, v239
v_mov_b32_e32 v44, v239
v_mov_b32_e32 v45, v239
v_mov_b32_e32 v46, v239
v_mov_b32_e32 v47, v239
s_branch L_ms31_%=
L_mt31_%=:
v_cndmask_b32_e64 v32, v32, v239, s[56:57]
v_cndmask_b32_e64 v33, v33, v239, s[58:59]
v_cndmask_b32_e64 v34, v34, v239, s[60:61]
v_cndmask_b32_e64 v35, v35, v239, s[62:63]
v_mov_b32_e32 v36, v239
v_mov_b32_e32 v37, v239
v_mov_b32_e32 v38, v239
v_mov_b32_e32 v39, v239
v_cndmask_b32_e64 v44, v44, v239, s[56:57]
v_cndmask_b32_e64 v45, v45, v239, s[58:59]
v_cndmask_b32_e64 v46, v46, v239, s[60:61]
v_cndmask_b32_e64 v47, v47, v239, s[62:63]
L_ms31_%=:
v_mfma_f32_16x16x32_bf16 v[48:51], a[168:171], v[168:171], v[48:51]
buffer_load_dwordx4 v238, s[36:39], s41 offen offset:3072 sc1 lds
v_exp_f32_e32 v32, v32
v_mfma_f32_16x16x32_bf16 v[56:59], a[168:171], v[184:187], v[56:59]
ds_read_b128 a[200:203], v228 offset:51200
v_mfma_f32_16x16x32_bf16 v[48:51], a[172:175], v[172:175], v[48:51]
v_exp_f32_e32 v33, v33
v_mfma_f32_16x16x32_bf16 v[56:59], a[172:175], v[188:191], v[56:59]
ds_read_b128 a[204:207], v228 offset:52224
v_cvt_pk_bf16_f32 v96, v32, v33
s_waitcnt lgkmcnt(4)
v_mfma_f32_16x16x32_bf16 v[52:55], a[176:179], v[160:163], v[128:131]
v_exp_f32_e32 v34, v34
v_mfma_f32_16x16x32_bf16 v[60:63], a[176:179], v[176:179], v[132:135]
s_add_u32 m0, s42, 81920
ds_read_b128 a[208:211], v228 offset:53248
v_mfma_f32_16x16x32_bf16 v[52:55], a[180:183], v[164:167], v[52:55]
buffer_load_dwordx4 v238, s[36:39], s40 offen sc1 lds
v_exp_f32_e32 v35, v35
v_mfma_f32_16x16x32_bf16 v[60:63], a[180:183], v[180:183], v[60:63]
ds_read_b128 a[212:215], v228 offset:54272
v_cvt_pk_bf16_f32 v97, v34, v35
v_mfma_f32_16x16x32_bf16 v[52:55], a[184:187], v[168:171], v[52:55]
v_exp_f32_e32 v40, v40
v_mfma_f32_16x16x32_bf16 v[60:63], a[184:187], v[184:187], v[60:63]
ds_read_b128 a[216:219], v228 offset:55296
v_mfma_f32_16x16x32_bf16 v[52:55], a[188:191], v[172:175], v[52:55]
v_exp_f32_e32 v41, v41
v_mfma_f32_16x16x32_bf16 v[60:63], a[188:191], v[188:191], v[60:63]
ds_read_b128 a[220:223], v228 offset:56320
v_cvt_pk_bf16_f32 v100, v40, v41
s_waitcnt lgkmcnt(4)
v_mfma_f32_16x16x32_bf16 a[0:3], a[192:195], v[112:115], a[0:3]
buffer_load_dwordx4 v238, s[36:39], s40 offen offset:1024 sc1 lds
v_exp_f32_e32 v42, v42
v_mfma_f32_16x16x32_bf16 a[4:7], a[192:195], v[116:119], a[4:7]
ds_read_b128 a[224:227], v228 offset:57344
v_mfma_f32_16x16x32_bf16 a[16:19], a[196:199], v[112:115], a[16:19]
)ASM"
R"ASM(v_exp_f32_e32 v43, v43
v_mfma_f32_16x16x32_bf16 a[20:23], a[196:199], v[116:119], a[20:23]
ds_read_b128 a[228:231], v228 offset:58368
v_cvt_pk_bf16_f32 v101, v42, v43
v_mfma_f32_16x16x32_bf16 a[32:35], a[200:203], v[112:115], a[32:35]
v_exp_f32_e32 v36, v36
v_mfma_f32_16x16x32_bf16 a[36:39], a[200:203], v[116:119], a[36:39]
ds_read_b128 a[232:235], v228 offset:59392
v_mfma_f32_16x16x32_bf16 a[48:51], a[204:207], v[112:115], a[48:51]
buffer_load_dwordx4 v238, s[36:39], s40 offen offset:2048 sc1 lds
v_exp_f32_e32 v37, v37
v_mfma_f32_16x16x32_bf16 a[52:55], a[204:207], v[116:119], a[52:55]
ds_read_b128 a[236:239], v228 offset:60416
v_cvt_pk_bf16_f32 v98, v36, v37
s_waitcnt lgkmcnt(4)
v_mfma_f32_16x16x32_bf16 a[64:67], a[208:211], v[112:115], a[64:67]
v_exp_f32_e32 v38, v38
v_mfma_f32_16x16x32_bf16 a[68:71], a[208:211], v[116:119], a[68:71]
ds_read_b128 a[240:243], v228 offset:61440
s_cmp_ge_i32 s45, s49
s_cbranch_scc0 L_ms32_%=
s_cmp_eq_i32 s45, s49
s_cbranch_scc1 L_mt32_%=
v_mov_b32_e32 v48, v239
v_mov_b32_e32 v49, v239
v_mov_b32_e32 v50, v239
v_mov_b32_e32 v51, v239
v_mov_b32_e32 v56, v239
v_mov_b32_e32 v57, v239
v_mov_b32_e32 v58, v239
v_mov_b32_e32 v59, v239
v_mov_b32_e32 v52, v239
v_mov_b32_e32 v53, v239
v_mov_b32_e32 v54, v239
v_mov_b32_e32 v55, v239
v_mov_b32_e32 v60, v239
v_mov_b32_e32 v61, v239
v_mov_b32_e32 v62, v239
v_mov_b32_e32 v63, v239
s_branch L_ms32_%=
L_mt32_%=:
v_cndmask_b32_e64 v48, v48, v239, s[56:57]
v_cndmask_b32_e64 v49, v49, v239, s[58:59]
v_cndmask_b32_e64 v50, v50, v239, s[60:61]
v_cndmask_b32_e64 v51, v51, v239, s[62:63]
v_mov_b32_e32 v52, v239
v_mov_b32_e32 v53, v239
v_mov_b32_e32 v54, v239
v_mov_b32_e32 v55, v239
v_cndmask_b32_e64 v60, v60, v239, s[56:57]
v_cndmask_b32_e64 v61, v61, v239, s[58:59]
v_cndmask_b32_e64 v62, v62, v239, s[60:61]
v_cndmask_b32_e64 v63, v63, v239, s[62:63]
L_ms32_%=:
v_mfma_f32_16x16x32_bf16 a[80:83], a[212:215], v[112:115], a[80:83]
v_exp_f32_e32 v39, v39
v_mfma_f32_16x16x32_bf16 a[84:87], a[212:215], v[116:119], a[84:87]
ds_read_b128 a[244:247], v228 offset:62464
v_cvt_pk_bf16_f32 v99, v38, v39
v_mfma_f32_16x16x32_bf16 a[96:99], a[216:219], v[112:115], a[96:99]
buffer_load_dwordx4 v238, s[36:39], s40 offen offset:3072 sc1 lds
v_exp_f32_e32 v44, v44
v_mfma_f32_16x16x32_bf16 a[100:103], a[216:219], v[116:119], a[100:103]
s_add_u32 s41, s41, 0x8000
ds_read_b128 a[248:251], v228 offset:63488
v_mfma_f32_16x16x32_bf16 a[112:115], a[220:223], v[112:115], a[112:115]
s_add_u32 s40, s40, 0x8000
v_exp_f32_e32 v45, v45
v_mfma_f32_16x16x32_bf16 a[116:119], a[220:223], v[116:119], a[116:119]
ds_read_b128 a[252:255], v228 offset:64512
v_cvt_pk_bf16_f32 v102, v44, v45
s_waitcnt lgkmcnt(4)
v_mfma_f32_16x16x32_bf16 a[0:3], a[224:227], v[120:123], a[0:3]
s_waitcnt vmcnt(8)
s_barrier
v_exp_f32_e32 v46, v46
v_mfma_f32_16x16x32_bf16 a[4:7], a[224:227], v[124:127], a[4:7]
v_mfma_f32_16x16x32_bf16 a[16:19], a[228:231], v[120:123], a[16:19]
v_exp_f32_e32 v47, v47
v_mfma_f32_16x16x32_bf16 a[20:23], a[228:231], v[124:127], a[20:23]
v_cvt_pk_bf16_f32 v103, v46, v47
v_mfma_f32_16x16x32_bf16 a[32:35], a[232:235], v[120:123], a[32:35]
v_exp_f32_e32 v48, v48
v_mfma_f32_16x16x32_bf16 a[36:39], a[232:235], v[124:127], a[36:39]
ds_read_b128 a[128:131], v224 offset:16384
v_mfma_f32_16x16x32_bf16 a[48:51], a[236:239], v[120:123], a[48:51]
v_exp_f32_e32 v49, v49
v_mfma_f32_16x16x32_bf16 a[52:55], a[236:239], v[124:127], a[52:55]
ds_read_b128 a[132:135], v224 offset:20480
v_cvt_pk_bf16_f32 v104, v48, v49
s_waitcnt lgkmcnt(2)
v_mfma_f32_16x16x32_bf16 a[64:67], a[240:243], v[120:123], a[64:67]
v_exp_f32_e32 v50, v50
v_mfma_f32_16x16x32_bf16 a[68:71], a[240:243], v[124:127], a[68:71]
ds_read_b128 a[136:139], v224 offset:24576
v_mfma_f32_16x16x32_bf16 a[80:83], a[244:247], v[120:123], a[80:83]
v_exp_f32_e32 v51, v51
v_mfma_f32_16x16x32_bf16 a[84:87], a[244:247], v[124:127], a[84:87]
ds_read_b128 a[140:143], v224 offset:28672
v_cvt_pk_bf16_f32 v105, v50, v51
v_mfma_f32_16x16x32_bf16 a[96:99], a[248:251], v[120:123], a[96:99]
v_exp_f32_e32 v56, v56
v_mfma_f32_16x16x32_bf16 a[100:103], a[248:251], v[124:127], a[100:103]
ds_read_b128 a[144:147], v224 offset:17408
v_mfma_f32_16x16x32_bf16 a[112:115], a[252:255], v[120:123], a[112:115]
v_exp_f32_e32 v57, v57
v_mfma_f32_16x16x32_bf16 a[116:119], a[252:255], v[124:127], a[116:119]
ds_read_b128 a[148:151], v224 offset:21504
v_cvt_pk_bf16_f32 v108, v56, v57
v_mfma_f32_16x16x32_bf16 v[144:147], v[248:251], v[112:115], v[144:147]
v_exp_f32_e32 v58, v58
v_mfma_f32_16x16x32_bf16 v[148:151], v[248:251], v[116:119], v[148:151]
ds_read_b128 a[152:155], v224 offset:25600
v_mfma_f32_16x16x32_bf16 v[144:147], v[248:251], v[120:123], v[144:147]
v_exp_f32_e32 v59, v59
v_mfma_f32_16x16x32_bf16 v[148:151], v[248:251], v[124:127], v[148:151]
ds_read_b128 a[156:159], v224 offset:29696
v_cvt_pk_bf16_f32 v109, v58, v59
s_branch L_SS0_%=
L_DR1_%=:
s_nop 0
v_exp_f32_e32 v84, v84
s_nop 0
s_nop 0
v_exp_f32_e32 v85, v85
s_nop 0
v_cvt_pk_bf16_f32 v122, v84, v85
s_nop 0
v_exp_f32_e32 v86, v86
s_nop 0
s_nop 0
v_exp_f32_e32 v87, v87
s_nop 0
v_cvt_pk_bf16_f32 v123, v86, v87
s_nop 0
v_exp_f32_e32 v92, v92
s_nop 0
s_nop 0
v_exp_f32_e32 v93, v93
s_nop 0
v_cvt_pk_bf16_f32 v126, v92, v93
s_nop 0
v_exp_f32_e32 v94, v94
s_nop 0
s_nop 0
v_exp_f32_e32 v95, v95
s_nop 0
v_cvt_pk_bf16_f32 v127, v94, v95
s_nop 0
s_nop 0
ds_read_b128 a[192:195], v228 offset:16384
s_nop 0
s_nop 0
ds_read_b128 a[196:199], v228 offset:17408
s_nop 0
s_nop 0
ds_read_b128 a[200:203], v228 offset:18432
s_nop 0
s_nop 0
ds_read_b128 a[204:207], v228 offset:19456
s_nop 0
s_nop 0
ds_read_b128 a[208:211], v228 offset:20480
s_nop 0
s_nop 0
ds_read_b128 a[212:215], v228 offset:21504
s_nop 0
s_nop 0
ds_read_b128 a[216:219], v228 offset:22528
s_nop 0
s_nop 0
ds_read_b128 a[220:223], v228 offset:23552
s_waitcnt lgkmcnt(4)
v_mfma_f32_16x16x32_bf16 a[0:3], a[192:195], v[112:115], a[0:3]
v_mfma_f32_16x16x32_bf16 a[4:7], a[192:195], v[116:119], a[4:7]
ds_read_b128 a[224:227], v228 offset:24576
v_mfma_f32_16x16x32_bf16 a[16:19], a[196:199], v[112:115], a[16:19]
v_mfma_f32_16x16x32_bf16 a[20:23], a[196:199], v[116:119], a[20:23]
ds_read_b128 a[228:231], v228 offset:25600
v_mfma_f32_16x16x32_bf16 a[32:35], a[200:203], v[112:115], a[32:35]
v_mfma_f32_16x16x32_bf16 a[36:39], a[200:203], v[116:119], a[36:39]
ds_read_b128 a[232:235], v228 offset:26624
v_mfma_f32_16x16x32_bf16 a[48:51], a[204:207], v[112:115], a[48:51]
v_mfma_f32_16x16x32_bf16 a[52:55], a[204:207], v[116:119], a[52:55]
ds_read_b128 a[236:239], v228 offset:27648
s_waitcnt lgkmcnt(4)
v_mfma_f32_16x16x32_bf16 a[64:67], a[208:211], v[112:115], a[64:67]
v_mfma_f32_16x16x32_bf16 a[68:71], a[208:211], v[116:119], a[68:71]
ds_read_b128 a[240:243], v228 offset:28672
v_mfma_f32_16x16x32_bf16 a[80:83], a[212:215], v[112:115], a[80:83]
v_mfma_f32_16x16x32_bf16 a[84:87], a[212:215], v[116:119], a[84:87]
ds_read_b128 a[244:247], v228 offset:29696
v_mfma_f32_16x16x32_bf16 a[96:99], a[216:219], v[112:115], a[96:99]
v_mfma_f32_16x16x32_bf16 a[100:103], a[216:219], v[116:119], a[100:103]
ds_read_b128 a[248:251], v228 offset:30720
v_mfma_f32_16x16x32_bf16 a[112:115], a[220:223], v[112:115], a[112:115]
v_mfma_f32_16x16x32_bf16 a[116:119], a[220:223], v[116:119], a[116:119]
ds_read_b128 a[252:255], v228 offset:31744
s_waitcnt lgkmcnt(4)
v_mfma_f32_16x16x32_bf16 a[0:3], a[224:227], v[120:123], a[0:3]
v_mfma_f32_16x16x32_bf16 a[4:7], a[224:227], v[124:127], a[4:7]
v_mfma_f32_16x16x32_bf16 a[16:19], a[228:231], v[120:123], a[16:19]
v_mfma_f32_16x16x32_bf16 a[20:23], a[228:231], v[124:127], a[20:23]
v_mfma_f32_16x16x32_bf16 a[32:35], a[232:235], v[120:123], a[32:35]
v_mfma_f32_16x16x32_bf16 a[36:39], a[232:235], v[124:127], a[36:39]
v_mfma_f32_16x16x32_bf16 a[48:51], a[236:239], v[120:123], a[48:51]
v_mfma_f32_16x16x32_bf16 a[52:55], a[236:239], v[124:127], a[52:55]
s_waitcnt lgkmcnt(0)
v_mfma_f32_16x16x32_bf16 a[64:67], a[240:243], v[120:123], a[64:67]
v_mfma_f32_16x16x32_bf16 a[68:71], a[240:243], v[124:127], a[68:71]
v_mfma_f32_16x16x32_bf16 a[80:83], a[244:247], v[120:123], a[80:83]
v_mfma_f32_16x16x32_bf16 a[84:87], a[244:247], v[124:127], a[84:87]
v_mfma_f32_16x16x32_bf16 a[96:99], a[248:251], v[120:123], a[96:99]
v_mfma_f32_16x16x32_bf16 a[100:103], a[248:251], v[124:127], a[100:103]
v_mfma_f32_16x16x32_bf16 a[112:115], a[252:255], v[120:123], a[112:115]
v_mfma_f32_16x16x32_bf16 a[116:119], a[252:255], v[124:127], a[116:119]
v_mfma_f32_16x16x32_bf16 v[144:147], v[248:251], v[112:115], v[144:147]
v_mfma_f32_16x16x32_bf16 v[148:151], v[248:251], v[116:119], v[148:151]
v_mfma_f32_16x16x32_bf16 v[144:147], v[248:251], v[120:123], v[144:147]
v_mfma_f32_16x16x32_bf16 v[148:151], v[248:251], v[124:127], v[148:151]
s_branch L_END_%=
L_DR3_%=:
s_nop 0
v_exp_f32_e32 v84, v84
s_nop 0
s_nop 0
v_exp_f32_e32 v85, v85
s_nop 0
v_cvt_pk_bf16_f32 v122, v84, v85
s_nop 0
v_exp_f32_e32 v86, v86
s_nop 0
s_nop 0
v_exp_f32_e32 v87, v87
s_nop 0
v_cvt_pk_bf16_f32 v123, v86, v87
s_nop 0
v_exp_f32_e32 v92, v92
s_nop 0
s_nop 0
v_exp_f32_e32 v93, v93
s_nop 0
v_cvt_pk_bf16_f32 v126, v92, v93
s_nop 0
v_exp_f32_e32 v94, v94
s_nop 0
s_nop 0
v_exp_f32_e32 v95, v95
s_nop 0
v_cvt_pk_bf16_f32 v127, v94, v95
s_nop 0
s_nop 0
ds_read_b128 a[192:195], v228 offset:49152
s_nop 0
s_nop 0
ds_read_b128 a[196:199], v228 offset:50176
s_nop 0
s_nop 0
ds_read_b128 a[200:203], v228 offset:51200
s_nop 0
s_nop 0
ds_read_b128 a[204:207], v228 offset:52224
s_nop 0
s_nop 0
ds_read_b128 a[208:211], v228 offset:53248
s_nop 0
s_nop 0
ds_read_b128 a[212:215], v228 offset:54272
s_nop 0
s_nop 0
ds_read_b128 a[216:219], v228 offset:55296
s_nop 0
s_nop 0
ds_read_b128 a[220:223], v228 offset:56320
s_waitcnt lgkmcnt(4)
v_mfma_f32_16x16x32_bf16 a[0:3], a[192:195], v[112:115], a[0:3]
v_mfma_f32_16x16x32_bf16 a[4:7], a[192:195], v[116:119], a[4:7]
ds_read_b128 a[224:227], v228 offset:57344
v_mfma_f32_16x16x32_bf16 a[16:19], a[196:199], v[112:115], a[16:19]
v_mfma_f32_16x16x32_bf16 a[20:23], a[196:199], v[116:119], a[20:23]
ds_read_b128 a[228:231], v228 offset:58368
v_mfma_f32_16x16x32_bf16 a[32:35], a[200:203], v[112:115], a[32:35]
v_mfma_f32_16x16x32_bf16 a[36:39], a[200:203], v[116:119], a[36:39]
ds_read_b128 a[232:235], v228 offset:59392
v_mfma_f32_16x16x32_bf16 a[48:51], a[204:207], v[112:115], a[48:51]
v_mfma_f32_16x16x32_bf16 a[52:55], a[204:207], v[116:119], a[52:55]
ds_read_b128 a[236:239], v228 offset:60416
s_waitcnt lgkmcnt(4)
v_mfma_f32_16x16x32_bf16 a[64:67], a[208:211], v[112:115], a[64:67]
v_mfma_f32_16x16x32_bf16 a[68:71], a[208:211], v[116:119], a[68:71]
ds_read_b128 a[240:243], v228 offset:61440
v_mfma_f32_16x16x32_bf16 a[80:83], a[212:215], v[112:115], a[80:83]
v_mfma_f32_16x16x32_bf16 a[84:87], a[212:215], v[116:119], a[84:87]
ds_read_b128 a[244:247], v228 offset:62464
v_mfma_f32_16x16x32_bf16 a[96:99], a[216:219], v[112:115], a[96:99]
v_mfma_f32_16x16x32_bf16 a[100:103], a[216:219], v[116:119], a[100:103]
ds_read_b128 a[248:251], v228 offset:63488
v_mfma_f32_16x16x32_bf16 a[112:115], a[220:223], v[112:115], a[112:115]
v_mfma_f32_16x16x32_bf16 a[116:119], a[220:223], v[116:119], a[116:119]
ds_read_b128 a[252:255], v228 offset:64512
s_waitcnt lgkmcnt(4)
v_mfma_f32_16x16x32_bf16 a[0:3], a[224:227], v[120:123], a[0:3]
v_mfma_f32_16x16x32_bf16 a[4:7], a[224:227], v[124:127], a[4:7]
v_mfma_f32_16x16x32_bf16 a[16:19], a[228:231], v[120:123], a[16:19]
v_mfma_f32_16x16x32_bf16 a[20:23], a[228:231], v[124:127], a[20:23]
v_mfma_f32_16x16x32_bf16 a[32:35], a[232:235], v[120:123], a[32:35]
v_mfma_f32_16x16x32_bf16 a[36:39], a[232:235], v[124:127], a[36:39]
v_mfma_f32_16x16x32_bf16 a[48:51], a[236:239], v[120:123], a[48:51]
v_mfma_f32_16x16x32_bf16 a[52:55], a[236:239], v[124:127], a[52:55]
s_waitcnt lgkmcnt(0)
v_mfma_f32_16x16x32_bf16 a[64:67], a[240:243], v[120:123], a[64:67]
v_mfma_f32_16x16x32_bf16 a[68:71], a[240:243], v[124:127], a[68:71]
v_mfma_f32_16x16x32_bf16 a[80:83], a[244:247], v[120:123], a[80:83]
v_mfma_f32_16x16x32_bf16 a[84:87], a[244:247], v[124:127], a[84:87]
v_mfma_f32_16x16x32_bf16 a[96:99], a[248:251], v[120:123], a[96:99]
)ASM"
R"ASM(v_mfma_f32_16x16x32_bf16 a[100:103], a[248:251], v[124:127], a[100:103]
v_mfma_f32_16x16x32_bf16 a[112:115], a[252:255], v[120:123], a[112:115]
v_mfma_f32_16x16x32_bf16 a[116:119], a[252:255], v[124:127], a[116:119]
v_mfma_f32_16x16x32_bf16 v[144:147], v[248:251], v[112:115], v[144:147]
v_mfma_f32_16x16x32_bf16 v[148:151], v[248:251], v[116:119], v[148:151]
v_mfma_f32_16x16x32_bf16 v[144:147], v[248:251], v[120:123], v[144:147]
v_mfma_f32_16x16x32_bf16 v[148:151], v[248:251], v[124:127], v[148:151]
L_END_%=:
s_nop 7
v_mov_b32_e32 v232, v144
v_mov_b32_e32 v233, v148
v_mov_b32_e32 v234, v152
v_mov_b32_e32 v235, v156
v_add_f32_e32 v241, v232, v233
v_add_f32_e32 v240, v234, v235
v_add_f32_e32 v241, v241, v240
v_mov_b32_e32 v242, 0x5f800000
v_cmp_nle_f32_e32 vcc, v241, v242
v_rcp_f32_e32 v232, v232
v_rcp_f32_e32 v233, v233
v_rcp_f32_e32 v234, v234
v_rcp_f32_e32 v235, v235
v_mbcnt_lo_u32_b32 v242, -1, 0
v_mbcnt_hi_u32_b32 v242, -1, v242
v_and_b32_e32 v243, 15, v242
v_lshrrev_b32_e32 v244, 4, v242
v_lshrrev_b32_e32 v245, 2, v243
v_sub_u32_e32 v245, 4, v245
v_and_b32_e32 v245, 3, v245
v_xor_b32_e32 v245, v245, v244
v_lshlrev_b32_e32 v245, 4, v245
v_lshl_add_u32 v245, v243, 6, v245
s_cmp_lg_u64 vcc, 0
s_cselect_b32 %[flag], 1, 0
s_mov_b32 s36, %[o0]
s_mov_b32 s37, %[o1]
s_mov_b32 s38, %[on]
s_cbranch_scc0 L_NOFB_%=
s_mov_b32 s48, %[qpark]
buffer_store_dwordx4 v[160:163], v238, s[36:39], s48 offen
s_add_u32 s48, s48, 1024
buffer_store_dwordx4 v[164:167], v238, s[36:39], s48 offen
s_add_u32 s48, s48, 1024
buffer_store_dwordx4 v[168:171], v238, s[36:39], s48 offen
s_add_u32 s48, s48, 1024
buffer_store_dwordx4 v[172:175], v238, s[36:39], s48 offen
s_add_u32 s48, s48, 1024
buffer_store_dwordx4 v[176:179], v238, s[36:39], s48 offen
s_add_u32 s48, s48, 1024
buffer_store_dwordx4 v[180:183], v238, s[36:39], s48 offen
s_add_u32 s48, s48, 1024
buffer_store_dwordx4 v[184:187], v238, s[36:39], s48 offen
s_add_u32 s48, s48, 1024
buffer_store_dwordx4 v[188:191], v238, s[36:39], s48 offen
s_add_u32 s48, s48, 1024
buffer_store_dwordx4 v[192:195], v238, s[36:39], s48 offen
s_add_u32 s48, s48, 1024
buffer_store_dwordx4 v[196:199], v238, s[36:39], s48 offen
s_add_u32 s48, s48, 1024
buffer_store_dwordx4 v[200:203], v238, s[36:39], s48 offen
s_add_u32 s48, s48, 1024
buffer_store_dwordx4 v[204:207], v238, s[36:39], s48 offen
s_add_u32 s48, s48, 1024
buffer_store_dwordx4 v[208:211], v238, s[36:39], s48 offen
s_add_u32 s48, s48, 1024
buffer_store_dwordx4 v[212:215], v238, s[36:39], s48 offen
s_add_u32 s48, s48, 1024
buffer_store_dwordx4 v[216:219], v238, s[36:39], s48 offen
s_add_u32 s48, s48, 1024
buffer_store_dwordx4 v[220:223], v238, s[36:39], s48 offen
s_add_u32 s48, s48, 1024
s_waitcnt vmcnt(0)
s_branch L_DONE_%=
L_NOFB_%=:
s_add_u32 s48, %[oq0], 0
v_accvgpr_read_b32 v32, a0
v_accvgpr_read_b32 v33, a1
v_accvgpr_read_b32 v34, a2
v_accvgpr_read_b32 v35, a3
v_accvgpr_read_b32 v36, a16
v_accvgpr_read_b32 v37, a17
v_accvgpr_read_b32 v38, a18
v_accvgpr_read_b32 v39, a19
v_mul_f32_e32 v32, v32, v232
v_mul_f32_e32 v33, v33, v232
v_mul_f32_e32 v34, v34, v232
v_mul_f32_e32 v35, v35, v232
v_mul_f32_e32 v36, v36, v232
v_mul_f32_e32 v37, v37, v232
v_mul_f32_e32 v38, v38, v232
v_mul_f32_e32 v39, v39, v232
v_cvt_pk_bf16_f32 v40, v32, v33
v_cvt_pk_bf16_f32 v41, v34, v35
v_cvt_pk_bf16_f32 v42, v36, v37
v_cvt_pk_bf16_f32 v43, v38, v39
buffer_store_dwordx4 v[40:43], v245, s[36:39], s48 offen sc1
s_add_u32 s48, %[oq0], 1024
v_accvgpr_read_b32 v44, a4
v_accvgpr_read_b32 v45, a5
v_accvgpr_read_b32 v46, a6
v_accvgpr_read_b32 v47, a7
v_accvgpr_read_b32 v48, a20
v_accvgpr_read_b32 v49, a21
v_accvgpr_read_b32 v50, a22
v_accvgpr_read_b32 v51, a23
v_mul_f32_e32 v44, v44, v233
v_mul_f32_e32 v45, v45, v233
v_mul_f32_e32 v46, v46, v233
v_mul_f32_e32 v47, v47, v233
v_mul_f32_e32 v48, v48, v233
v_mul_f32_e32 v49, v49, v233
v_mul_f32_e32 v50, v50, v233
v_mul_f32_e32 v51, v51, v233
v_cvt_pk_bf16_f32 v52, v44, v45
v_cvt_pk_bf16_f32 v53, v46, v47
v_cvt_pk_bf16_f32 v54, v48, v49
v_cvt_pk_bf16_f32 v55, v50, v51
buffer_store_dwordx4 v[52:55], v245, s[36:39], s48 offen sc1
s_add_u32 s48, %[oq0], 131072
v_accvgpr_read_b32 v56, a32
v_accvgpr_read_b32 v57, a33
v_accvgpr_read_b32 v58, a34
v_accvgpr_read_b32 v59, a35
v_accvgpr_read_b32 v60, a48
v_accvgpr_read_b32 v61, a49
v_accvgpr_read_b32 v62, a50
v_accvgpr_read_b32 v63, a51
v_mul_f32_e32 v56, v56, v232
v_mul_f32_e32 v57, v57, v232
v_mul_f32_e32 v58, v58, v232
v_mul_f32_e32 v59, v59, v232
v_mul_f32_e32 v60, v60, v232
v_mul_f32_e32 v61, v61, v232
v_mul_f32_e32 v62, v62, v232
v_mul_f32_e32 v63, v63, v232
v_cvt_pk_bf16_f32 v64, v56, v57
v_cvt_pk_bf16_f32 v65, v58, v59
v_cvt_pk_bf16_f32 v66, v60, v61
v_cvt_pk_bf16_f32 v67, v62, v63
buffer_store_dwordx4 v[64:67], v245, s[36:39], s48 offen sc1
s_add_u32 s48, %[oq0], 132096
v_accvgpr_read_b32 v68, a36
v_accvgpr_read_b32 v69, a37
v_accvgpr_read_b32 v70, a38
v_accvgpr_read_b32 v71, a39
v_accvgpr_read_b32 v72, a52
v_accvgpr_read_b32 v73, a53
v_accvgpr_read_b32 v74, a54
v_accvgpr_read_b32 v75, a55
v_mul_f32_e32 v68, v68, v233
v_mul_f32_e32 v69, v69, v233
v_mul_f32_e32 v70, v70, v233
v_mul_f32_e32 v71, v71, v233
v_mul_f32_e32 v72, v72, v233
v_mul_f32_e32 v73, v73, v233
v_mul_f32_e32 v74, v74, v233
v_mul_f32_e32 v75, v75, v233
v_cvt_pk_bf16_f32 v76, v68, v69
v_cvt_pk_bf16_f32 v77, v70, v71
v_cvt_pk_bf16_f32 v78, v72, v73
v_cvt_pk_bf16_f32 v79, v74, v75
buffer_store_dwordx4 v[76:79], v245, s[36:39], s48 offen sc1
s_add_u32 s48, %[oq0], 262144
v_accvgpr_read_b32 v80, a64
v_accvgpr_read_b32 v81, a65
v_accvgpr_read_b32 v82, a66
v_accvgpr_read_b32 v83, a67
v_accvgpr_read_b32 v84, a80
v_accvgpr_read_b32 v85, a81
v_accvgpr_read_b32 v86, a82
v_accvgpr_read_b32 v87, a83
v_mul_f32_e32 v80, v80, v232
v_mul_f32_e32 v81, v81, v232
v_mul_f32_e32 v82, v82, v232
v_mul_f32_e32 v83, v83, v232
v_mul_f32_e32 v84, v84, v232
v_mul_f32_e32 v85, v85, v232
v_mul_f32_e32 v86, v86, v232
v_mul_f32_e32 v87, v87, v232
v_cvt_pk_bf16_f32 v88, v80, v81
v_cvt_pk_bf16_f32 v89, v82, v83
v_cvt_pk_bf16_f32 v90, v84, v85
v_cvt_pk_bf16_f32 v91, v86, v87
buffer_store_dwordx4 v[88:91], v245, s[36:39], s48 offen sc1
s_add_u32 s48, %[oq0], 263168
v_accvgpr_read_b32 v92, a68
v_accvgpr_read_b32 v93, a69
v_accvgpr_read_b32 v94, a70
v_accvgpr_read_b32 v95, a71
v_accvgpr_read_b32 v96, a84
v_accvgpr_read_b32 v97, a85
v_accvgpr_read_b32 v98, a86
v_accvgpr_read_b32 v99, a87
v_mul_f32_e32 v92, v92, v233
v_mul_f32_e32 v93, v93, v233
v_mul_f32_e32 v94, v94, v233
v_mul_f32_e32 v95, v95, v233
v_mul_f32_e32 v96, v96, v233
v_mul_f32_e32 v97, v97, v233
v_mul_f32_e32 v98, v98, v233
v_mul_f32_e32 v99, v99, v233
v_cvt_pk_bf16_f32 v100, v92, v93
v_cvt_pk_bf16_f32 v101, v94, v95
v_cvt_pk_bf16_f32 v102, v96, v97
v_cvt_pk_bf16_f32 v103, v98, v99
buffer_store_dwordx4 v[100:103], v245, s[36:39], s48 offen sc1
s_add_u32 s48, %[oq0], 393216
v_accvgpr_read_b32 v104, a96
v_accvgpr_read_b32 v105, a97
v_accvgpr_read_b32 v106, a98
v_accvgpr_read_b32 v107, a99
v_accvgpr_read_b32 v108, a112
v_accvgpr_read_b32 v109, a113
v_accvgpr_read_b32 v110, a114
v_accvgpr_read_b32 v111, a115
v_mul_f32_e32 v104, v104, v232
v_mul_f32_e32 v105, v105, v232
v_mul_f32_e32 v106, v106, v232
v_mul_f32_e32 v107, v107, v232
v_mul_f32_e32 v108, v108, v232
v_mul_f32_e32 v109, v109, v232
v_mul_f32_e32 v110, v110, v232
v_mul_f32_e32 v111, v111, v232
v_cvt_pk_bf16_f32 v112, v104, v105
v_cvt_pk_bf16_f32 v113, v106, v107
v_cvt_pk_bf16_f32 v114, v108, v109
v_cvt_pk_bf16_f32 v115, v110, v111
buffer_store_dwordx4 v[112:115], v245, s[36:39], s48 offen sc1
s_add_u32 s48, %[oq0], 394240
v_accvgpr_read_b32 v116, a100
v_accvgpr_read_b32 v117, a101
v_accvgpr_read_b32 v118, a102
v_accvgpr_read_b32 v119, a103
v_accvgpr_read_b32 v120, a116
v_accvgpr_read_b32 v121, a117
v_accvgpr_read_b32 v122, a118
v_accvgpr_read_b32 v123, a119
v_mul_f32_e32 v116, v116, v233
v_mul_f32_e32 v117, v117, v233
v_mul_f32_e32 v118, v118, v233
v_mul_f32_e32 v119, v119, v233
v_mul_f32_e32 v120, v120, v233
v_mul_f32_e32 v121, v121, v233
v_mul_f32_e32 v122, v122, v233
v_mul_f32_e32 v123, v123, v233
v_cvt_pk_bf16_f32 v124, v116, v117
v_cvt_pk_bf16_f32 v125, v118, v119
v_cvt_pk_bf16_f32 v126, v120, v121
v_cvt_pk_bf16_f32 v127, v122, v123
buffer_store_dwordx4 v[124:127], v245, s[36:39], s48 offen sc1
s_add_u32 s48, %[oq1], 0
v_accvgpr_read_b32 v32, a8
v_accvgpr_read_b32 v33, a9
v_accvgpr_read_b32 v34, a10
v_accvgpr_read_b32 v35, a11
v_accvgpr_read_b32 v36, a24
v_accvgpr_read_b32 v37, a25
v_accvgpr_read_b32 v38, a26
v_accvgpr_read_b32 v39, a27
v_mul_f32_e32 v32, v32, v234
v_mul_f32_e32 v33, v33, v234
v_mul_f32_e32 v34, v34, v234
v_mul_f32_e32 v35, v35, v234
v_mul_f32_e32 v36, v36, v234
v_mul_f32_e32 v37, v37, v234
v_mul_f32_e32 v38, v38, v234
v_mul_f32_e32 v39, v39, v234
v_cvt_pk_bf16_f32 v40, v32, v33
v_cvt_pk_bf16_f32 v41, v34, v35
v_cvt_pk_bf16_f32 v42, v36, v37
v_cvt_pk_bf16_f32 v43, v38, v39
buffer_store_dwordx4 v[40:43], v245, s[36:39], s48 offen sc1
s_add_u32 s48, %[oq1], 1024
v_accvgpr_read_b32 v44, a12
v_accvgpr_read_b32 v45, a13
v_accvgpr_read_b32 v46, a14
v_accvgpr_read_b32 v47, a15
v_accvgpr_read_b32 v48, a28
v_accvgpr_read_b32 v49, a29
v_accvgpr_read_b32 v50, a30
v_accvgpr_read_b32 v51, a31
v_mul_f32_e32 v44, v44, v235
v_mul_f32_e32 v45, v45, v235
v_mul_f32_e32 v46, v46, v235
v_mul_f32_e32 v47, v47, v235
v_mul_f32_e32 v48, v48, v235
v_mul_f32_e32 v49, v49, v235
v_mul_f32_e32 v50, v50, v235
v_mul_f32_e32 v51, v51, v235
v_cvt_pk_bf16_f32 v52, v44, v45
v_cvt_pk_bf16_f32 v53, v46, v47
v_cvt_pk_bf16_f32 v54, v48, v49
v_cvt_pk_bf16_f32 v55, v50, v51
buffer_store_dwordx4 v[52:55], v245, s[36:39], s48 offen sc1
s_add_u32 s48, %[oq1], 131072
v_accvgpr_read_b32 v56, a40
v_accvgpr_read_b32 v57, a41
v_accvgpr_read_b32 v58, a42
v_accvgpr_read_b32 v59, a43
v_accvgpr_read_b32 v60, a56
v_accvgpr_read_b32 v61, a57
v_accvgpr_read_b32 v62, a58
v_accvgpr_read_b32 v63, a59
v_mul_f32_e32 v56, v56, v234
v_mul_f32_e32 v57, v57, v234
v_mul_f32_e32 v58, v58, v234
v_mul_f32_e32 v59, v59, v234
v_mul_f32_e32 v60, v60, v234
v_mul_f32_e32 v61, v61, v234
v_mul_f32_e32 v62, v62, v234
v_mul_f32_e32 v63, v63, v234
v_cvt_pk_bf16_f32 v64, v56, v57
v_cvt_pk_bf16_f32 v65, v58, v59
v_cvt_pk_bf16_f32 v66, v60, v61
v_cvt_pk_bf16_f32 v67, v62, v63
buffer_store_dwordx4 v[64:67], v245, s[36:39], s48 offen sc1
s_add_u32 s48, %[oq1], 132096
v_accvgpr_read_b32 v68, a44
v_accvgpr_read_b32 v69, a45
v_accvgpr_read_b32 v70, a46
v_accvgpr_read_b32 v71, a47
v_accvgpr_read_b32 v72, a60
v_accvgpr_read_b32 v73, a61
v_accvgpr_read_b32 v74, a62
v_accvgpr_read_b32 v75, a63
v_mul_f32_e32 v68, v68, v235
v_mul_f32_e32 v69, v69, v235
v_mul_f32_e32 v70, v70, v235
v_mul_f32_e32 v71, v71, v235
v_mul_f32_e32 v72, v72, v235
v_mul_f32_e32 v73, v73, v235
v_mul_f32_e32 v74, v74, v235
v_mul_f32_e32 v75, v75, v235
v_cvt_pk_bf16_f32 v76, v68, v69
v_cvt_pk_bf16_f32 v77, v70, v71
v_cvt_pk_bf16_f32 v78, v72, v73
v_cvt_pk_bf16_f32 v79, v74, v75
buffer_store_dwordx4 v[76:79], v245, s[36:39], s48 offen sc1
s_add_u32 s48, %[oq1], 262144
v_accvgpr_read_b32 v80, a72
v_accvgpr_read_b32 v81, a73
v_accvgpr_read_b32 v82, a74
v_accvgpr_read_b32 v83, a75
v_accvgpr_read_b32 v84, a88
v_accvgpr_read_b32 v85, a89
v_accvgpr_read_b32 v86, a90
v_accvgpr_read_b32 v87, a91
v_mul_f32_e32 v80, v80, v234
v_mul_f32_e32 v81, v81, v234
v_mul_f32_e32 v82, v82, v234
v_mul_f32_e32 v83, v83, v234
v_mul_f32_e32 v84, v84, v234
v_mul_f32_e32 v85, v85, v234
v_mul_f32_e32 v86, v86, v234
v_mul_f32_e32 v87, v87, v234
v_cvt_pk_bf16_f32 v88, v80, v81
v_cvt_pk_bf16_f32 v89, v82, v83
v_cvt_pk_bf16_f32 v90, v84, v85
v_cvt_pk_bf16_f32 v91, v86, v87
buffer_store_dwordx4 v[88:91], v245, s[36:39], s48 offen sc1
s_add_u32 s48, %[oq1], 263168
v_accvgpr_read_b32 v92, a76
v_accvgpr_read_b32 v93, a77
v_accvgpr_read_b32 v94, a78
v_accvgpr_read_b32 v95, a79
v_accvgpr_read_b32 v96, a92
v_accvgpr_read_b32 v97, a93
v_accvgpr_read_b32 v98, a94
v_accvgpr_read_b32 v99, a95
v_mul_f32_e32 v92, v92, v235
v_mul_f32_e32 v93, v93, v235
v_mul_f32_e32 v94, v94, v235
v_mul_f32_e32 v95, v95, v235
v_mul_f32_e32 v96, v96, v235
v_mul_f32_e32 v97, v97, v235
)ASM"
R"ASM(v_mul_f32_e32 v98, v98, v235
v_mul_f32_e32 v99, v99, v235
v_cvt_pk_bf16_f32 v100, v92, v93
v_cvt_pk_bf16_f32 v101, v94, v95
v_cvt_pk_bf16_f32 v102, v96, v97
v_cvt_pk_bf16_f32 v103, v98, v99
buffer_store_dwordx4 v[100:103], v245, s[36:39], s48 offen sc1
s_add_u32 s48, %[oq1], 393216
v_accvgpr_read_b32 v104, a104
v_accvgpr_read_b32 v105, a105
v_accvgpr_read_b32 v106, a106
v_accvgpr_read_b32 v107, a107
v_accvgpr_read_b32 v108, a120
v_accvgpr_read_b32 v109, a121
v_accvgpr_read_b32 v110, a122
v_accvgpr_read_b32 v111, a123
v_mul_f32_e32 v104, v104, v234
v_mul_f32_e32 v105, v105, v234
v_mul_f32_e32 v106, v106, v234
v_mul_f32_e32 v107, v107, v234
v_mul_f32_e32 v108, v108, v234
v_mul_f32_e32 v109, v109, v234
v_mul_f32_e32 v110, v110, v234
v_mul_f32_e32 v111, v111, v234
v_cvt_pk_bf16_f32 v112, v104, v105
v_cvt_pk_bf16_f32 v113, v106, v107
v_cvt_pk_bf16_f32 v114, v108, v109
v_cvt_pk_bf16_f32 v115, v110, v111
buffer_store_dwordx4 v[112:115], v245, s[36:39], s48 offen sc1
s_add_u32 s48, %[oq1], 394240
v_accvgpr_read_b32 v116, a108
v_accvgpr_read_b32 v117, a109
v_accvgpr_read_b32 v118, a110
v_accvgpr_read_b32 v119, a111
v_accvgpr_read_b32 v120, a124
v_accvgpr_read_b32 v121, a125
v_accvgpr_read_b32 v122, a126
v_accvgpr_read_b32 v123, a127
v_mul_f32_e32 v116, v116, v235
v_mul_f32_e32 v117, v117, v235
v_mul_f32_e32 v118, v118, v235
v_mul_f32_e32 v119, v119, v235
v_mul_f32_e32 v120, v120, v235
v_mul_f32_e32 v121, v121, v235
v_mul_f32_e32 v122, v122, v235
v_mul_f32_e32 v123, v123, v235
v_cvt_pk_bf16_f32 v124, v116, v117
v_cvt_pk_bf16_f32 v125, v118, v119
v_cvt_pk_bf16_f32 v126, v120, v121
v_cvt_pk_bf16_f32 v127, v122, v123
buffer_store_dwordx4 v[124:127], v245, s[36:39], s48 offen sc1
L_DONE_%=:
s_waitcnt vmcnt(16)
)ASM"
            : [flag] "=&s"(flag)
            : [kv0] "s"(LO32(kvb)), [kv1] "s"(HI16(kvb)), [kvn] "s"(T * 32768), [w] "s"(w), [lds] "s"((unsigned)(size_t)lds),
              [ud0] "s"(-4 * bs), [ul0] "s"(2 * Ts - 4 * bl), [ts1] "s"(Ts - 1), [t1] "s"(T - 1),
              [xi0] "s"(LO32(xb)), [xi1] "s"(HI16(xb)), [xin] "i"(20 * S * 64), [wq0] "s"(LO32(wb)), [wq1] "s"(HI16(wb)), [wqn] "i"(M * HD * 2 + 896),
              [xo0] "s"((128 * bl + 32 * w) * 64), [xo1] "s"((128 * bs + 32 * w) * 64), [wqo] "s"(h * 256),
              [o0] "s"(LO32(ws)), [o1] "s"(HI16(ws)), [on] "i"((unsigned)WS_USED), [oq0] "s"((unsigned)OFF_CU + (4 * h * S + 128 * bl + 32 * w) * 64), [oq1] "s"((unsigned)OFF_CU + (4 * h * S + 128 * bs + 32 * w) * 64),
              [qpark] "s"((unsigned)OFF_QPARK + (bid * 4 + w) * 16384),
              [tag0] "s"((unsigned)tag), [tag1] "s"((unsigned)(tag >> 32)), [fkv] "s"(8 * (F_KV + bid)), [flags] "s"(fb), [pk] "v"(pk),
              [fd1] "s"(8 * (zrole ? F_Z1 + rb * 7 + ct : F_KV + bid)), [fd2] "s"(8 * (zrole ? F_Z2N + rb * 7 + ct : F_KV + bid)), [dbg] "s"(ws + OFF_DBG + 256 + (bid * 4 + w) * 64)
            :
"v32", "v33", "v34", "v35", "v36", "v37", "v38", "v39", "v40", "v41", "v42", "v43", "v44", "v45", "v46", "v47", "v48", "v49", "v50", "v51", "v52", "v53", "v54", "v55", "v56", "v57", "v58", "v59", "v60", "v61", "v62", "v63", "v64", "v65", "v66", "v67", "v68", "v69", "v70", "v71", "v72", "v73", "v74", "v75", "v76", "v77", "v78", "v79", "v80", "v81", "v82", "v83", "v84", "v85", "v86", "v87", "v88", "v89", "v90", "v91", "v92", "v93", "v94", "v95", "v96", "v97", "v98", "v99", "v100", "v101", "v102", "v103", "v104", "v105", "v106", "v107", "v108", "v109", "v110", "v111", "v112", "v113", "v114", "v115", "v116", "v117", "v118", "v119", "v120", "v121", "v122", "v123", "v124", "v125", "v126", "v127", "v128", "v129", "v130", "v131", "v132", "v133", "v134", "v135", "v136", "v137", "v138", "v139", "v140", "v141", "v142", "v143", "v144", "v145", "v146", "v147", "v148", "v149", "v150", "v151", "v152", "v153", "v154", "v155", "v156", "v157", "v158", "v159", "v160", "v161", "v162", "v163", "v164", "v165", "v166", "v167", "v168", "v169", "v170", "v171", "v172", "v173", "v174", "v175", "v176", "v177", "v178", "v179", "v180", "v181", "v182", "v183", "v184", "v185", "v186", "v187", "v188", "v189", "v190", "v191", "v192", "v193", "v194", "v195", "v196", "v197", "v198", "v199", "v200", "v201", "v202", "v203", "v204", "v205", "v206", "v207", "v208", "v209", "v210", "v211", "v212", "v213", "v214", "v215", "v216", "v217", "v218", "v219", "v220", "v221", "v222", "v223", "v224", "v225", "v226", "v227", "v228", "v229", "v230", "v231", "v232", "v233", "v234", "v235", "v236", "v237", "v238", "v239", "v240", "v241", "v242", "v243", "v244", "v245", "v246", "v247", "v248", "v249", "v250", "v251", "v252", "v253", "v254", "v255", "a0", "a1", "a2", "a3", "a4", "a5", "a6", "a7", "a8", "a9", "a10", "a11", "a12", "a13", "a14", "a15", "a16", "a17", "a18", "a19", "a20", "a21", "a22", "a23", "a24", "a25", "a26", "a27", "a28", "a29", "a30", "a31", "a32", "a33", "a34", "a35", "a36", "a37", "a38", "a39", "a40", "a41", "a42", "a43", "a44", "a45", "a46", "a47", "a48", "a49", "a50", "a51", "a52", "a53", "a54", "a55", "a56", "a57", "a58", "a59", "a60", "a61", "a62", "a63", "a64", "a65", "a66", "a67", "a68", "a69", "a70", "a71", "a72", "a73", "a74", "a75", "a76", "a77", "a78", "a79", "a80", "a81", "a82", "a83", "a84", "a85", "a86", "a87", "a88", "a89", "a90", "a91", "a92", "a93", "a94", "a95", "a96", "a97", "a98", "a99", "a100", "a101", "a102", "a103", "a104", "a105", "a106", "a107", "a108", "a109", "a110", "a111", "a112", "a113", "a114", "a115", "a116", "a117", "a118", "a119", "a120", "a121", "a122", "a123", "a124", "a125", "a126", "a127", "a128", "a129", "a130", "a131", "a132", "a133", "a134", "a135", "a136", "a137", "a138", "a139", "a140", "a141", "a142", "a143", "a144", "a145", "a146", "a147", "a148", "a149", "a150", "a151", "a152", "a153", "a154", "a155", "a156", "a157", "a158", "a159", "a160", "a161", "a162", "a163", "a164", "a165", "a166", "a167", "a168", "a169", "a170", "a171", "a172", "a173", "a174", "a175", "a176", "a177", "a178", "a179", "a180", "a181", "a182", "a183", "a184", "a185", "a186", "a187", "a188", "a189", "a190", "a191", "a192", "a193", "a194", "a195", "a196", "a197", "a198", "a199", "a200", "a201", "a202", "a203", "a204", "a205", "a206", "a207", "a208", "a209", "a210", "a211", "a212", "a213", "a214", "a215", "a216", "a217", "a218", "a219", "a220", "a221", "a222", "a223", "a224", "a225", "a226", "a227", "a228", "a229", "a230", "a231", "a232", "a233", "a234", "a235", "a236", "a237", "a238", "a239", "a240", "a241", "a242", "a243", "a244", "a245", "a246", "a247", "a248", "a249", "a250", "a251", "a252", "a253", "a254", "a255", "s36", "s37", "s38", "s39", "s40", "s41", "s42", "s43", "s44", "s45", "s46", "s47", "s48", "s49", "s50", "s51", "s52", "s53", "s54", "s55", "s56", "s57", "s58", "s59", "s60", "s61", "s62", "s63", "s64", "s65", "s66", "s67", "s68", "s69", "s70", "s71", "s72", "s73", "s74", "s75", "s76", "s77", "s78", "s79", "s80", "s81", "s82", "s83", "s84", "s85", "s86", "s87", "s88", "s89", "s90", "s91", "s92", "s93", "s94", "s95", "s96", "s97", "s98", "s99", "vcc", "scc", "memory"
        );
        if (flag) { PHASE_VARS(); attn_slow(ws, wsr, lane, h, 128 * bl + 32 * w, 128 * bs + 32 * w, bid * 4 + w); }
    }
    {
        PHASE_VARS();
        if (!srole && idx >= 30) {
            asm volatile("s_waitcnt vmcnt(0)" ::: "memory"); __builtin_amdgcn_s_barrier(); asm volatile("" ::: "memory");
            if (tid < 2) __hip_atomic_store(flagp(ws, F_CTX + (tid ? bs : bl) * 32 + h), tag, __ATOMIC_RELAXED, __HIP_MEMORY_SCOPE_AGENT);
        } else { SYNC(); }
    }
    {
        PHASE_VARS();
        if (srole) {
            AWAIT(lane < 16 ? F_Z2N + lane * 7 + cts : lane == 16 ? F_Z1 + rbs * 7 + cts : (lane == 17 && cts > 0) ? F_Z1 + rbs * 7 + cts - 1 : -1);
            const int m0 = rbs * 128;
            const char* wb = ws + OFF_Z2I; const u16* wsp = ap->wsp;
            f32x4 c[16];
            asm volatile(
R"ASM(s_mov_b32 s36, %[x0]
s_mov_b32 s37, %[x1]
s_mov_b32 s38, %[xn]
s_mov_b32 s39, 0x20000
s_mov_b32 s40, %[w0]
s_mov_b32 s41, %[w1]
s_mov_b32 s42, %[wn]
s_mov_b32 s43, 0x20000
s_mov_b32 s47, %[w]
s_mov_b32 s52, %[tag0]
s_mov_b32 s53, %[tag1]
s_lshl_b32 s48, s47, 11
s_add_u32 s44, %[lds], s48
s_mov_b32 s45, %[xo]
s_mov_b32 s46, %[wo]
v_mbcnt_lo_u32_b32 v57, -1, 0
v_mbcnt_hi_u32_b32 v57, -1, v57
v_lshlrev_b32_e32 v32, 4, v57
s_lshl_b32 s48, s47, 12
s_add_u32 s50, %[lds], s48
v_lshrrev_b32_e32 v63, 3, v57
v_mul_u32_u24_e32 v63, %[xs], v63
v_lshrrev_b32_e32 v62, 4, v57
v_add_u32_e32 v62, 0, v62
v_and_b32_e32 v64, 7, v57
v_xor_b32_e32 v62, v62, v64
v_lshl_add_u32 v33, v62, 4, v63
v_lshrrev_b32_e32 v62, 4, v57
v_add_u32_e32 v62, 4, v62
v_and_b32_e32 v64, 7, v57
v_xor_b32_e32 v62, v62, v64
v_lshl_add_u32 v34, v62, 4, v63
v_add_u32_e32 v34, %[xp1], v34
v_add_u32_e32 v35, %[xp2], v33
v_add_u32_e32 v36, %[xp2], v34
s_add_u32 m0, s50, 0
s_nop 0
buffer_load_dwordx4 v33, s[36:39], s45 offen lds
buffer_load_dwordx4 v34, s[36:39], s45 offen offset:1024 lds
buffer_load_dwordx4 v35, s[36:39], s45 offen offset:2048 lds
buffer_load_dwordx4 v36, s[36:39], s45 offen offset:3072 lds
s_add_u32 m0, s44, 16384
s_nop 0
buffer_load_dwordx4 v32, s[40:43], s46 offen sc1 lds
buffer_load_dwordx4 v32, s[40:43], s46 offen offset:1024 sc1 lds
s_add_u32 s46, s46, %[wadv]
s_add_u32 m0, s44, 24576
s_nop 0
buffer_load_dwordx4 v32, s[40:43], s46 offen sc1 lds
buffer_load_dwordx4 v32, s[40:43], s46 offen offset:1024 sc1 lds
s_add_u32 s46, s46, %[wadv]
s_add_u32 s45, s45, %[xadv]
s_add_u32 m0, s50, 32768
s_nop 0
buffer_load_dwordx4 v33, s[36:39], s45 offen lds
buffer_load_dwordx4 v34, s[36:39], s45 offen offset:1024 lds
buffer_load_dwordx4 v35, s[36:39], s45 offen offset:2048 lds
buffer_load_dwordx4 v36, s[36:39], s45 offen offset:3072 lds
s_add_u32 m0, s44, 49152
s_nop 0
buffer_load_dwordx4 v32, s[40:43], s46 offen sc1 lds
buffer_load_dwordx4 v32, s[40:43], s46 offen offset:1024 sc1 lds
s_add_u32 s46, s46, %[wadv]
s_add_u32 m0, s44, 57344
s_nop 0
buffer_load_dwordx4 v32, s[40:43], s46 offen sc1 lds
buffer_load_dwordx4 v32, s[40:43], s46 offen offset:1024 sc1 lds
s_add_u32 s46, s46, %[wadv]
s_add_u32 s45, s45, %[xadv]
v_and_b32_e32 v58, 15, v57
v_lshrrev_b32_e32 v59, 4, v57
s_lshr_b32 s48, s47, 1
s_lshl_b32 s48, s48, 13
s_add_u32 s48, s48, %[lds]
v_lshrrev_b32_e32 v62, 1, v58
v_or_b32_e32 v63, 0, v59
v_xor_b32_e32 v62, v62, v63
v_lshlrev_b32_e32 v62, 4, v62
v_lshl_add_u32 v62, v58, 7, v62
v_add_u32_e32 v62, s48, v62
v_add_u32_e32 v39, 0, v62
v_add_u32_e32 v40, 65536, v62
v_add_u32_e32 v41, 131072, v62
v_lshrrev_b32_e32 v62, 1, v58
v_or_b32_e32 v63, 4, v59
v_xor_b32_e32 v62, v62, v63
v_lshlrev_b32_e32 v62, 4, v62
v_lshl_add_u32 v62, v58, 7, v62
v_add_u32_e32 v62, s48, v62
v_add_u32_e32 v42, 0, v62
v_add_u32_e32 v43, 65536, v62
v_add_u32_e32 v44, 131072, v62
v_lshrrev_b32_e32 v60, 2, v58
v_and_b32_e32 v61, 3, v57
s_and_b32 s48, s47, 1
s_lshl_b32 s48, s48, 10
s_add_u32 s48, s48, %[lds]
s_add_u32 s48, s48, 16384
v_lshlrev_b32_e32 v62, 1, v59
v_add_u32_e32 v62, 0, v62
v_and_b32_e32 v62, 3, v62
v_lshrrev_b32_e32 v63, 1, v61
v_or_b32_e32 v63, 0, v63
v_xor_b32_e32 v62, v62, v63
v_lshlrev_b32_e32 v62, 4, v62
v_and_b32_e32 v63, 1, v61
v_lshl_add_u32 v62, v63, 3, v62
v_lshl_add_u32 v62, v59, 11, v62
v_add_u32_e32 v63, 0, v60
v_lshl_add_u32 v62, v63, 6, v62
v_add_u32_e32 v62, s48, v62
v_add_u32_e32 v45, 0, v62
v_add_u32_e32 v49, 65536, v62
v_add_u32_e32 v53, 131072, v62
v_lshlrev_b32_e32 v62, 1, v59
v_add_u32_e32 v62, 0, v62
v_and_b32_e32 v62, 3, v62
v_lshrrev_b32_e32 v63, 1, v61
v_or_b32_e32 v63, 2, v63
v_xor_b32_e32 v62, v62, v63
v_lshlrev_b32_e32 v62, 4, v62
v_and_b32_e32 v63, 1, v61
v_lshl_add_u32 v62, v63, 3, v62
v_lshl_add_u32 v62, v59, 11, v62
v_add_u32_e32 v63, 0, v60
v_lshl_add_u32 v62, v63, 6, v62
v_add_u32_e32 v62, s48, v62
v_add_u32_e32 v46, 0, v62
v_add_u32_e32 v50, 65536, v62
v_add_u32_e32 v54, 131072, v62
v_lshlrev_b32_e32 v62, 1, v59
v_add_u32_e32 v62, 1, v62
v_and_b32_e32 v62, 3, v62
v_lshrrev_b32_e32 v63, 1, v61
v_or_b32_e32 v63, 0, v63
v_xor_b32_e32 v62, v62, v63
v_lshlrev_b32_e32 v62, 4, v62
v_and_b32_e32 v63, 1, v61
v_lshl_add_u32 v62, v63, 3, v62
v_lshl_add_u32 v62, v59, 11, v62
v_add_u32_e32 v63, 4, v60
v_lshl_add_u32 v62, v63, 6, v62
v_add_u32_e32 v62, s48, v62
v_add_u32_e32 v47, 0, v62
v_add_u32_e32 v51, 65536, v62
v_add_u32_e32 v55, 131072, v62
v_lshlrev_b32_e32 v62, 1, v59
v_add_u32_e32 v62, 1, v62
v_and_b32_e32 v62, 3, v62
v_lshrrev_b32_e32 v63, 1, v61
v_or_b32_e32 v63, 2, v63
v_xor_b32_e32 v62, v62, v63
v_lshlrev_b32_e32 v62, 4, v62
v_and_b32_e32 v63, 1, v61
v_lshl_add_u32 v62, v63, 3, v62
v_lshl_add_u32 v62, v59, 11, v62
v_add_u32_e32 v63, 4, v60
v_lshl_add_u32 v62, v63, 6, v62
v_add_u32_e32 v62, s48, v62
v_add_u32_e32 v48, 0, v62
v_add_u32_e32 v52, 65536, v62
v_add_u32_e32 v56, 131072, v62
s_waitcnt vmcnt(8)
s_barrier
ds_read_b64_tr_b16 a[0:1], v45
ds_read_b64_tr_b16 a[2:3], v47
ds_read_b64_tr_b16 a[4:5], v46
ds_read_b64_tr_b16 a[6:7], v48
ds_read_b128 a[32:35], v39
ds_read_b64_tr_b16 a[8:9], v45 offset:512
ds_read_b64_tr_b16 a[10:11], v47 offset:512
ds_read_b128 a[36:39], v39 offset:2048
ds_read_b64_tr_b16 a[12:13], v46 offset:512
ds_read_b64_tr_b16 a[14:15], v48 offset:512
ds_read_b128 a[40:43], v39 offset:4096
s_waitcnt lgkmcnt(1)
v_mfma_f32_16x16x32_bf16 %[c0], a[0:3], a[32:35], 0
v_mfma_f32_16x16x32_bf16 %[c1], a[4:7], a[32:35], 0
v_mfma_f32_16x16x32_bf16 %[c2], a[8:11], a[32:35], 0
ds_read_b128 a[44:47], v39 offset:6144
v_mfma_f32_16x16x32_bf16 %[c3], a[12:15], a[32:35], 0
v_mfma_f32_16x16x32_bf16 %[c4], a[0:3], a[36:39], 0
ds_read_b64_tr_b16 a[16:17], v45 offset:8192
ds_read_b64_tr_b16 a[18:19], v47 offset:8192
v_mfma_f32_16x16x32_bf16 %[c5], a[4:7], a[36:39], 0
ds_read_b64_tr_b16 a[20:21], v46 offset:8192
ds_read_b64_tr_b16 a[22:23], v48 offset:8192
v_mfma_f32_16x16x32_bf16 %[c6], a[8:11], a[36:39], 0
s_waitcnt vmcnt(0)
s_barrier
ds_read_b128 a[48:51], v42
v_mfma_f32_16x16x32_bf16 %[c7], a[12:15], a[36:39], 0
s_add_u32 m0, s50, 65536
s_waitcnt lgkmcnt(5)
v_mfma_f32_16x16x32_bf16 %[c8], a[0:3], a[40:43], 0
ds_read_b64_tr_b16 a[24:25], v45 offset:8704
ds_read_b64_tr_b16 a[26:27], v47 offset:8704
buffer_load_dwordx4 v33, s[36:39], s45 offen lds
v_mfma_f32_16x16x32_bf16 %[c9], a[4:7], a[40:43], 0
buffer_load_dwordx4 v34, s[36:39], s45 offen offset:1024 lds
v_mfma_f32_16x16x32_bf16 %[c10], a[8:11], a[40:43], 0
ds_read_b128 a[52:55], v42 offset:2048
buffer_load_dwordx4 v35, s[36:39], s45 offen offset:2048 lds
v_mfma_f32_16x16x32_bf16 %[c11], a[12:15], a[40:43], 0
ds_read_b64_tr_b16 a[28:29], v46 offset:8704
ds_read_b64_tr_b16 a[30:31], v48 offset:8704
buffer_load_dwordx4 v36, s[36:39], s45 offen offset:3072 lds
v_mfma_f32_16x16x32_bf16 %[c12], a[0:3], a[44:47], 0
s_add_u32 m0, s44, 81920
v_mfma_f32_16x16x32_bf16 %[c13], a[4:7], a[44:47], 0
buffer_load_dwordx4 v32, s[40:43], s46 offen sc1 lds
v_mfma_f32_16x16x32_bf16 %[c14], a[8:11], a[44:47], 0
ds_read_b128 a[56:59], v42 offset:4096
buffer_load_dwordx4 v32, s[40:43], s46 offen offset:1024 sc1 lds
v_mfma_f32_16x16x32_bf16 %[c15], a[12:15], a[44:47], 0
s_add_u32 s46, s46, %[wadv]
s_waitcnt lgkmcnt(1)
v_mfma_f32_16x16x32_bf16 %[c0], a[16:19], a[48:51], %[c0]
s_add_u32 m0, s44, 90112
v_mfma_f32_16x16x32_bf16 %[c1], a[20:23], a[48:51], %[c1]
v_mfma_f32_16x16x32_bf16 %[c2], a[24:27], a[48:51], %[c2]
ds_read_b128 a[60:63], v42 offset:6144
buffer_load_dwordx4 v32, s[40:43], s46 offen sc1 lds
v_mfma_f32_16x16x32_bf16 %[c3], a[28:31], a[48:51], %[c3]
buffer_load_dwordx4 v32, s[40:43], s46 offen offset:1024 sc1 lds
v_mfma_f32_16x16x32_bf16 %[c4], a[16:19], a[52:55], %[c4]
s_add_u32 s46, s46, %[wadv]
ds_read_b64_tr_b16 a[0:1], v45 offset:32768
ds_read_b64_tr_b16 a[2:3], v47 offset:32768
v_mfma_f32_16x16x32_bf16 %[c5], a[20:23], a[52:55], %[c5]
s_add_u32 s45, s45, %[xadv]
ds_read_b64_tr_b16 a[4:5], v46 offset:32768
ds_read_b64_tr_b16 a[6:7], v48 offset:32768
v_mfma_f32_16x16x32_bf16 %[c6], a[24:27], a[52:55], %[c6]
s_add_u32 m0, s50, 98304
ds_read_b128 a[32:35], v39 offset:32768
v_mfma_f32_16x16x32_bf16 %[c7], a[28:31], a[52:55], %[c7]
buffer_load_dwordx4 v33, s[36:39], s45 offen lds
s_waitcnt lgkmcnt(5)
v_mfma_f32_16x16x32_bf16 %[c8], a[16:19], a[56:59], %[c8]
ds_read_b64_tr_b16 a[8:9], v45 offset:33280
ds_read_b64_tr_b16 a[10:11], v47 offset:33280
buffer_load_dwordx4 v34, s[36:39], s45 offen offset:1024 lds
v_mfma_f32_16x16x32_bf16 %[c9], a[20:23], a[56:59], %[c9]
buffer_load_dwordx4 v35, s[36:39], s45 offen offset:2048 lds
v_mfma_f32_16x16x32_bf16 %[c10], a[24:27], a[56:59], %[c10]
ds_read_b128 a[36:39], v39 offset:34816
buffer_load_dwordx4 v36, s[36:39], s45 offen offset:3072 lds
v_mfma_f32_16x16x32_bf16 %[c11], a[28:31], a[56:59], %[c11]
ds_read_b64_tr_b16 a[12:13], v46 offset:33280
ds_read_b64_tr_b16 a[14:15], v48 offset:33280
v_mfma_f32_16x16x32_bf16 %[c12], a[16:19], a[60:63], %[c12]
s_add_u32 m0, s44, 114688
v_mfma_f32_16x16x32_bf16 %[c13], a[20:23], a[60:63], %[c13]
buffer_load_dwordx4 v32, s[40:43], s46 offen sc1 lds
v_mfma_f32_16x16x32_bf16 %[c14], a[24:27], a[60:63], %[c14]
ds_read_b128 a[40:43], v39 offset:36864
buffer_load_dwordx4 v32, s[40:43], s46 offen offset:1024 sc1 lds
v_mfma_f32_16x16x32_bf16 %[c15], a[28:31], a[60:63], %[c15]
s_add_u32 s46, s46, %[wadv]
s_waitcnt lgkmcnt(1)
v_mfma_f32_16x16x32_bf16 %[c0], a[0:3], a[32:35], %[c0]
s_add_u32 m0, s44, 122880
v_mfma_f32_16x16x32_bf16 %[c1], a[4:7], a[32:35], %[c1]
buffer_load_dwordx4 v32, s[40:43], s46 offen sc1 lds
v_mfma_f32_16x16x32_bf16 %[c2], a[8:11], a[32:35], %[c2]
ds_read_b128 a[44:47], v39 offset:38912
buffer_load_dwordx4 v32, s[40:43], s46 offen offset:1024 sc1 lds
v_mfma_f32_16x16x32_bf16 %[c3], a[12:15], a[32:35], %[c3]
s_add_u32 s46, s46, %[wadv]
v_mfma_f32_16x16x32_bf16 %[c4], a[0:3], a[36:39], %[c4]
s_add_u32 s45, s45, %[xadv]
ds_read_b64_tr_b16 a[16:17], v45 offset:40960
ds_read_b64_tr_b16 a[18:19], v47 offset:40960
v_mfma_f32_16x16x32_bf16 %[c5], a[4:7], a[36:39], %[c5]
ds_read_b64_tr_b16 a[20:21], v46 offset:40960
ds_read_b64_tr_b16 a[22:23], v48 offset:40960
v_mfma_f32_16x16x32_bf16 %[c6], a[8:11], a[36:39], %[c6]
s_waitcnt vmcnt(8)
s_barrier
s_cmp_lg_u32 s47, 0
s_cbranch_scc1 L_fs2_%=
v_mov_b32 v60, s52
v_mov_b32 v61, s53
s_mov_b64 exec, 1
v_mov_b32 v62, %[fdef]
global_store_dwordx2 v62, v[60:61], %[flags] sc1
v_mov_b32 v63, %[fdef2]
global_store_dwordx2 v63, v[60:61], %[flags] sc1
s_mov_b64 exec, -1
L_fs2_%=:
ds_read_b128 a[48:51], v42 offset:32768
v_mfma_f32_16x16x32_bf16 %[c7], a[12:15], a[36:39], %[c7]
s_add_u32 m0, s50, 131072
s_waitcnt lgkmcnt(5)
v_mfma_f32_16x16x32_bf16 %[c8], a[0:3], a[40:43], %[c8]
ds_read_b64_tr_b16 a[24:25], v45 offset:41472
ds_read_b64_tr_b16 a[26:27], v47 offset:41472
buffer_load_dwordx4 v33, s[36:39], s45 offen lds
v_mfma_f32_16x16x32_bf16 %[c9], a[4:7], a[40:43], %[c9]
buffer_load_dwordx4 v34, s[36:39], s45 offen offset:1024 lds
v_mfma_f32_16x16x32_bf16 %[c10], a[8:11], a[40:43], %[c10]
ds_read_b128 a[52:55], v42 offset:34816
buffer_load_dwordx4 v35, s[36:39], s45 offen offset:2048 lds
v_mfma_f32_16x16x32_bf16 %[c11], a[12:15], a[40:43], %[c11]
ds_read_b64_tr_b16 a[28:29], v46 offset:41472
ds_read_b64_tr_b16 a[30:31], v48 offset:41472
buffer_load_dwordx4 v36, s[36:39], s45 offen offset:3072 lds
v_mfma_f32_16x16x32_bf16 %[c12], a[0:3], a[44:47], %[c12]
s_add_u32 m0, s44, 147456
v_mfma_f32_16x16x32_bf16 %[c13], a[4:7], a[44:47], %[c13]
buffer_load_dwordx4 v32, s[40:43], s46 offen sc1 lds
v_mfma_f32_16x16x32_bf16 %[c14], a[8:11], a[44:47], %[c14]
ds_read_b128 a[56:59], v42 offset:36864
buffer_load_dwordx4 v32, s[40:43], s46 offen offset:1024 sc1 lds
v_mfma_f32_16x16x32_bf16 %[c15], a[12:15], a[44:47], %[c15]
s_add_u32 s46, s46, %[wadv]
s_waitcnt lgkmcnt(1)
v_mfma_f32_16x16x32_bf16 %[c0], a[16:19], a[48:51], %[c0]
s_add_u32 m0, s44, 155648
v_mfma_f32_16x16x32_bf16 %[c1], a[20:23], a[48:51], %[c1]
v_mfma_f32_16x16x32_bf16 %[c2], a[24:27], a[48:51], %[c2]
ds_read_b128 a[60:63], v42 offset:38912
buffer_load_dwordx4 v32, s[40:43], s46 offen sc1 lds
)ASM"
R"ASM(v_mfma_f32_16x16x32_bf16 %[c3], a[28:31], a[48:51], %[c3]
buffer_load_dwordx4 v32, s[40:43], s46 offen offset:1024 sc1 lds
v_mfma_f32_16x16x32_bf16 %[c4], a[16:19], a[52:55], %[c4]
s_add_u32 s46, s46, %[wadv]
ds_read_b64_tr_b16 a[0:1], v49
ds_read_b64_tr_b16 a[2:3], v51
v_mfma_f32_16x16x32_bf16 %[c5], a[20:23], a[52:55], %[c5]
s_add_u32 s45, s45, %[xadv]
ds_read_b64_tr_b16 a[4:5], v50
ds_read_b64_tr_b16 a[6:7], v52
v_mfma_f32_16x16x32_bf16 %[c6], a[24:27], a[52:55], %[c6]
s_add_u32 m0, s50, 0
ds_read_b128 a[32:35], v40
v_mfma_f32_16x16x32_bf16 %[c7], a[28:31], a[52:55], %[c7]
buffer_load_dwordx4 v33, s[36:39], s45 offen lds
s_waitcnt lgkmcnt(5)
v_mfma_f32_16x16x32_bf16 %[c8], a[16:19], a[56:59], %[c8]
ds_read_b64_tr_b16 a[8:9], v49 offset:512
ds_read_b64_tr_b16 a[10:11], v51 offset:512
buffer_load_dwordx4 v34, s[36:39], s45 offen offset:1024 lds
v_mfma_f32_16x16x32_bf16 %[c9], a[20:23], a[56:59], %[c9]
buffer_load_dwordx4 v35, s[36:39], s45 offen offset:2048 lds
v_mfma_f32_16x16x32_bf16 %[c10], a[24:27], a[56:59], %[c10]
ds_read_b128 a[36:39], v40 offset:2048
buffer_load_dwordx4 v36, s[36:39], s45 offen offset:3072 lds
v_mfma_f32_16x16x32_bf16 %[c11], a[28:31], a[56:59], %[c11]
ds_read_b64_tr_b16 a[12:13], v50 offset:512
ds_read_b64_tr_b16 a[14:15], v52 offset:512
v_mfma_f32_16x16x32_bf16 %[c12], a[16:19], a[60:63], %[c12]
s_add_u32 m0, s44, 16384
v_mfma_f32_16x16x32_bf16 %[c13], a[20:23], a[60:63], %[c13]
buffer_load_dwordx4 v32, s[40:43], s46 offen sc1 lds
v_mfma_f32_16x16x32_bf16 %[c14], a[24:27], a[60:63], %[c14]
ds_read_b128 a[40:43], v40 offset:4096
buffer_load_dwordx4 v32, s[40:43], s46 offen offset:1024 sc1 lds
v_mfma_f32_16x16x32_bf16 %[c15], a[28:31], a[60:63], %[c15]
s_add_u32 s46, s46, %[wadv]
s_waitcnt lgkmcnt(1)
v_mfma_f32_16x16x32_bf16 %[c0], a[0:3], a[32:35], %[c0]
s_add_u32 m0, s44, 24576
v_mfma_f32_16x16x32_bf16 %[c1], a[4:7], a[32:35], %[c1]
buffer_load_dwordx4 v32, s[40:43], s46 offen sc1 lds
v_mfma_f32_16x16x32_bf16 %[c2], a[8:11], a[32:35], %[c2]
ds_read_b128 a[44:47], v40 offset:6144
buffer_load_dwordx4 v32, s[40:43], s46 offen offset:1024 sc1 lds
v_mfma_f32_16x16x32_bf16 %[c3], a[12:15], a[32:35], %[c3]
s_add_u32 s46, s46, %[wadv]
v_mfma_f32_16x16x32_bf16 %[c4], a[0:3], a[36:39], %[c4]
s_add_u32 s45, s45, %[xadv]
ds_read_b64_tr_b16 a[16:17], v49 offset:8192
ds_read_b64_tr_b16 a[18:19], v51 offset:8192
v_mfma_f32_16x16x32_bf16 %[c5], a[4:7], a[36:39], %[c5]
ds_read_b64_tr_b16 a[20:21], v50 offset:8192
ds_read_b64_tr_b16 a[22:23], v52 offset:8192
v_mfma_f32_16x16x32_bf16 %[c6], a[8:11], a[36:39], %[c6]
s_waitcnt vmcnt(16)
s_barrier
ds_read_b128 a[48:51], v43
v_mfma_f32_16x16x32_bf16 %[c7], a[12:15], a[36:39], %[c7]
s_add_u32 m0, s50, 32768
s_waitcnt lgkmcnt(5)
v_mfma_f32_16x16x32_bf16 %[c8], a[0:3], a[40:43], %[c8]
ds_read_b64_tr_b16 a[24:25], v49 offset:8704
ds_read_b64_tr_b16 a[26:27], v51 offset:8704
v_mfma_f32_16x16x32_bf16 %[c9], a[4:7], a[40:43], %[c9]
buffer_load_dwordx4 v33, s[36:39], s45 offen lds
v_mfma_f32_16x16x32_bf16 %[c10], a[8:11], a[40:43], %[c10]
ds_read_b128 a[52:55], v43 offset:2048
v_mfma_f32_16x16x32_bf16 %[c11], a[12:15], a[40:43], %[c11]
ds_read_b64_tr_b16 a[28:29], v50 offset:8704
ds_read_b64_tr_b16 a[30:31], v52 offset:8704
buffer_load_dwordx4 v34, s[36:39], s45 offen offset:1024 lds
v_mfma_f32_16x16x32_bf16 %[c12], a[0:3], a[44:47], %[c12]
v_mfma_f32_16x16x32_bf16 %[c13], a[4:7], a[44:47], %[c13]
buffer_load_dwordx4 v35, s[36:39], s45 offen offset:2048 lds
v_mfma_f32_16x16x32_bf16 %[c14], a[8:11], a[44:47], %[c14]
ds_read_b128 a[56:59], v43 offset:4096
v_mfma_f32_16x16x32_bf16 %[c15], a[12:15], a[44:47], %[c15]
buffer_load_dwordx4 v36, s[36:39], s45 offen offset:3072 lds
s_waitcnt lgkmcnt(1)
v_mfma_f32_16x16x32_bf16 %[c0], a[16:19], a[48:51], %[c0]
v_mfma_f32_16x16x32_bf16 %[c1], a[20:23], a[48:51], %[c1]
v_mfma_f32_16x16x32_bf16 %[c2], a[24:27], a[48:51], %[c2]
s_add_u32 m0, s44, 49152
ds_read_b128 a[60:63], v43 offset:6144
v_mfma_f32_16x16x32_bf16 %[c3], a[28:31], a[48:51], %[c3]
v_mfma_f32_16x16x32_bf16 %[c4], a[16:19], a[52:55], %[c4]
ds_read_b64_tr_b16 a[0:1], v49 offset:32768
ds_read_b64_tr_b16 a[2:3], v51 offset:32768
buffer_load_dwordx4 v32, s[40:43], s46 offen sc1 lds
v_mfma_f32_16x16x32_bf16 %[c5], a[20:23], a[52:55], %[c5]
ds_read_b64_tr_b16 a[4:5], v50 offset:32768
ds_read_b64_tr_b16 a[6:7], v52 offset:32768
v_mfma_f32_16x16x32_bf16 %[c6], a[24:27], a[52:55], %[c6]
ds_read_b128 a[32:35], v40 offset:32768
buffer_load_dwordx4 v32, s[40:43], s46 offen offset:1024 sc1 lds
v_mfma_f32_16x16x32_bf16 %[c7], a[28:31], a[52:55], %[c7]
s_waitcnt lgkmcnt(5)
v_mfma_f32_16x16x32_bf16 %[c8], a[16:19], a[56:59], %[c8]
s_add_u32 s46, s46, %[wadv]
ds_read_b64_tr_b16 a[8:9], v49 offset:33280
ds_read_b64_tr_b16 a[10:11], v51 offset:33280
v_mfma_f32_16x16x32_bf16 %[c9], a[20:23], a[56:59], %[c9]
v_mfma_f32_16x16x32_bf16 %[c10], a[24:27], a[56:59], %[c10]
s_add_u32 m0, s44, 57344
ds_read_b128 a[36:39], v40 offset:34816
v_mfma_f32_16x16x32_bf16 %[c11], a[28:31], a[56:59], %[c11]
ds_read_b64_tr_b16 a[12:13], v50 offset:33280
ds_read_b64_tr_b16 a[14:15], v52 offset:33280
v_mfma_f32_16x16x32_bf16 %[c12], a[16:19], a[60:63], %[c12]
v_mfma_f32_16x16x32_bf16 %[c13], a[20:23], a[60:63], %[c13]
buffer_load_dwordx4 v32, s[40:43], s46 offen sc1 lds
v_mfma_f32_16x16x32_bf16 %[c14], a[24:27], a[60:63], %[c14]
ds_read_b128 a[40:43], v40 offset:36864
v_mfma_f32_16x16x32_bf16 %[c15], a[28:31], a[60:63], %[c15]
buffer_load_dwordx4 v32, s[40:43], s46 offen offset:1024 sc1 lds
s_waitcnt lgkmcnt(1)
v_mfma_f32_16x16x32_bf16 %[c0], a[0:3], a[32:35], %[c0]
v_mfma_f32_16x16x32_bf16 %[c1], a[4:7], a[32:35], %[c1]
s_add_u32 s46, s46, %[wadv]
v_mfma_f32_16x16x32_bf16 %[c2], a[8:11], a[32:35], %[c2]
ds_read_b128 a[44:47], v40 offset:38912
v_mfma_f32_16x16x32_bf16 %[c3], a[12:15], a[32:35], %[c3]
s_add_u32 s45, s45, %[xadv]
v_mfma_f32_16x16x32_bf16 %[c4], a[0:3], a[36:39], %[c4]
ds_read_b64_tr_b16 a[16:17], v49 offset:40960
ds_read_b64_tr_b16 a[18:19], v51 offset:40960
v_mfma_f32_16x16x32_bf16 %[c5], a[4:7], a[36:39], %[c5]
ds_read_b64_tr_b16 a[20:21], v50 offset:40960
ds_read_b64_tr_b16 a[22:23], v52 offset:40960
v_mfma_f32_16x16x32_bf16 %[c6], a[8:11], a[36:39], %[c6]
s_waitcnt vmcnt(16)
s_barrier
ds_read_b128 a[48:51], v43 offset:32768
v_mfma_f32_16x16x32_bf16 %[c7], a[12:15], a[36:39], %[c7]
s_add_u32 m0, s50, 65536
s_waitcnt lgkmcnt(5)
v_mfma_f32_16x16x32_bf16 %[c8], a[0:3], a[40:43], %[c8]
ds_read_b64_tr_b16 a[24:25], v49 offset:41472
ds_read_b64_tr_b16 a[26:27], v51 offset:41472
v_mfma_f32_16x16x32_bf16 %[c9], a[4:7], a[40:43], %[c9]
buffer_load_dwordx4 v33, s[36:39], s45 offen lds
v_mfma_f32_16x16x32_bf16 %[c10], a[8:11], a[40:43], %[c10]
ds_read_b128 a[52:55], v43 offset:34816
v_mfma_f32_16x16x32_bf16 %[c11], a[12:15], a[40:43], %[c11]
ds_read_b64_tr_b16 a[28:29], v50 offset:41472
ds_read_b64_tr_b16 a[30:31], v52 offset:41472
buffer_load_dwordx4 v34, s[36:39], s45 offen offset:1024 lds
v_mfma_f32_16x16x32_bf16 %[c12], a[0:3], a[44:47], %[c12]
v_mfma_f32_16x16x32_bf16 %[c13], a[4:7], a[44:47], %[c13]
buffer_load_dwordx4 v35, s[36:39], s45 offen offset:2048 lds
v_mfma_f32_16x16x32_bf16 %[c14], a[8:11], a[44:47], %[c14]
ds_read_b128 a[56:59], v43 offset:36864
v_mfma_f32_16x16x32_bf16 %[c15], a[12:15], a[44:47], %[c15]
buffer_load_dwordx4 v36, s[36:39], s45 offen offset:3072 lds
s_waitcnt lgkmcnt(1)
v_mfma_f32_16x16x32_bf16 %[c0], a[16:19], a[48:51], %[c0]
v_mfma_f32_16x16x32_bf16 %[c1], a[20:23], a[48:51], %[c1]
v_mfma_f32_16x16x32_bf16 %[c2], a[24:27], a[48:51], %[c2]
s_add_u32 m0, s44, 81920
ds_read_b128 a[60:63], v43 offset:38912
v_mfma_f32_16x16x32_bf16 %[c3], a[28:31], a[48:51], %[c3]
v_mfma_f32_16x16x32_bf16 %[c4], a[16:19], a[52:55], %[c4]
ds_read_b64_tr_b16 a[0:1], v53
ds_read_b64_tr_b16 a[2:3], v55
buffer_load_dwordx4 v32, s[40:43], s46 offen sc1 lds
v_mfma_f32_16x16x32_bf16 %[c5], a[20:23], a[52:55], %[c5]
ds_read_b64_tr_b16 a[4:5], v54
ds_read_b64_tr_b16 a[6:7], v56
v_mfma_f32_16x16x32_bf16 %[c6], a[24:27], a[52:55], %[c6]
ds_read_b128 a[32:35], v41
buffer_load_dwordx4 v32, s[40:43], s46 offen offset:1024 sc1 lds
v_mfma_f32_16x16x32_bf16 %[c7], a[28:31], a[52:55], %[c7]
s_waitcnt lgkmcnt(5)
v_mfma_f32_16x16x32_bf16 %[c8], a[16:19], a[56:59], %[c8]
s_add_u32 s46, s46, %[wadv]
ds_read_b64_tr_b16 a[8:9], v53 offset:512
ds_read_b64_tr_b16 a[10:11], v55 offset:512
v_mfma_f32_16x16x32_bf16 %[c9], a[20:23], a[56:59], %[c9]
v_mfma_f32_16x16x32_bf16 %[c10], a[24:27], a[56:59], %[c10]
s_add_u32 m0, s44, 90112
ds_read_b128 a[36:39], v41 offset:2048
v_mfma_f32_16x16x32_bf16 %[c11], a[28:31], a[56:59], %[c11]
ds_read_b64_tr_b16 a[12:13], v54 offset:512
ds_read_b64_tr_b16 a[14:15], v56 offset:512
v_mfma_f32_16x16x32_bf16 %[c12], a[16:19], a[60:63], %[c12]
v_mfma_f32_16x16x32_bf16 %[c13], a[20:23], a[60:63], %[c13]
buffer_load_dwordx4 v32, s[40:43], s46 offen sc1 lds
v_mfma_f32_16x16x32_bf16 %[c14], a[24:27], a[60:63], %[c14]
ds_read_b128 a[40:43], v41 offset:4096
v_mfma_f32_16x16x32_bf16 %[c15], a[28:31], a[60:63], %[c15]
buffer_load_dwordx4 v32, s[40:43], s46 offen offset:1024 sc1 lds
s_waitcnt lgkmcnt(1)
v_mfma_f32_16x16x32_bf16 %[c0], a[0:3], a[32:35], %[c0]
v_mfma_f32_16x16x32_bf16 %[c1], a[4:7], a[32:35], %[c1]
s_add_u32 s46, s46, %[wadv]
v_mfma_f32_16x16x32_bf16 %[c2], a[8:11], a[32:35], %[c2]
ds_read_b128 a[44:47], v41 offset:6144
v_mfma_f32_16x16x32_bf16 %[c3], a[12:15], a[32:35], %[c3]
s_add_u32 s45, s45, %[xadv]
v_mfma_f32_16x16x32_bf16 %[c4], a[0:3], a[36:39], %[c4]
ds_read_b64_tr_b16 a[16:17], v53 offset:8192
ds_read_b64_tr_b16 a[18:19], v55 offset:8192
v_mfma_f32_16x16x32_bf16 %[c5], a[4:7], a[36:39], %[c5]
ds_read_b64_tr_b16 a[20:21], v54 offset:8192
ds_read_b64_tr_b16 a[22:23], v56 offset:8192
v_mfma_f32_16x16x32_bf16 %[c6], a[8:11], a[36:39], %[c6]
s_waitcnt vmcnt(16)
s_barrier
ds_read_b128 a[48:51], v44
v_mfma_f32_16x16x32_bf16 %[c7], a[12:15], a[36:39], %[c7]
s_add_u32 m0, s50, 98304
s_waitcnt lgkmcnt(5)
v_mfma_f32_16x16x32_bf16 %[c8], a[0:3], a[40:43], %[c8]
ds_read_b64_tr_b16 a[24:25], v53 offset:8704
ds_read_b64_tr_b16 a[26:27], v55 offset:8704
v_mfma_f32_16x16x32_bf16 %[c9], a[4:7], a[40:43], %[c9]
buffer_load_dwordx4 v33, s[36:39], s45 offen lds
v_mfma_f32_16x16x32_bf16 %[c10], a[8:11], a[40:43], %[c10]
ds_read_b128 a[52:55], v44 offset:2048
v_mfma_f32_16x16x32_bf16 %[c11], a[12:15], a[40:43], %[c11]
ds_read_b64_tr_b16 a[28:29], v54 offset:8704
ds_read_b64_tr_b16 a[30:31], v56 offset:8704
buffer_load_dwordx4 v34, s[36:39], s45 offen offset:1024 lds
v_mfma_f32_16x16x32_bf16 %[c12], a[0:3], a[44:47], %[c12]
v_mfma_f32_16x16x32_bf16 %[c13], a[4:7], a[44:47], %[c13]
buffer_load_dwordx4 v35, s[36:39], s45 offen offset:2048 lds
v_mfma_f32_16x16x32_bf16 %[c14], a[8:11], a[44:47], %[c14]
ds_read_b128 a[56:59], v44 offset:4096
v_mfma_f32_16x16x32_bf16 %[c15], a[12:15], a[44:47], %[c15]
buffer_load_dwordx4 v36, s[36:39], s45 offen offset:3072 lds
s_waitcnt lgkmcnt(1)
v_mfma_f32_16x16x32_bf16 %[c0], a[16:19], a[48:51], %[c0]
v_mfma_f32_16x16x32_bf16 %[c1], a[20:23], a[48:51], %[c1]
v_mfma_f32_16x16x32_bf16 %[c2], a[24:27], a[48:51], %[c2]
s_add_u32 m0, s44, 114688
ds_read_b128 a[60:63], v44 offset:6144
v_mfma_f32_16x16x32_bf16 %[c3], a[28:31], a[48:51], %[c3]
v_mfma_f32_16x16x32_bf16 %[c4], a[16:19], a[52:55], %[c4]
ds_read_b64_tr_b16 a[0:1], v45
ds_read_b64_tr_b16 a[2:3], v47
buffer_load_dwordx4 v32, s[40:43], s46 offen sc1 lds
v_mfma_f32_16x16x32_bf16 %[c5], a[20:23], a[52:55], %[c5]
ds_read_b64_tr_b16 a[4:5], v46
ds_read_b64_tr_b16 a[6:7], v48
v_mfma_f32_16x16x32_bf16 %[c6], a[24:27], a[52:55], %[c6]
ds_read_b128 a[32:35], v39
buffer_load_dwordx4 v32, s[40:43], s46 offen offset:1024 sc1 lds
v_mfma_f32_16x16x32_bf16 %[c7], a[28:31], a[52:55], %[c7]
s_waitcnt lgkmcnt(5)
v_mfma_f32_16x16x32_bf16 %[c8], a[16:19], a[56:59], %[c8]
s_add_u32 s46, s46, %[wadv]
ds_read_b64_tr_b16 a[8:9], v45 offset:512
ds_read_b64_tr_b16 a[10:11], v47 offset:512
v_mfma_f32_16x16x32_bf16 %[c9], a[20:23], a[56:59], %[c9]
v_mfma_f32_16x16x32_bf16 %[c10], a[24:27], a[56:59], %[c10]
)ASM"
R"ASM(s_add_u32 m0, s44, 122880
ds_read_b128 a[36:39], v39 offset:2048
v_mfma_f32_16x16x32_bf16 %[c11], a[28:31], a[56:59], %[c11]
ds_read_b64_tr_b16 a[12:13], v46 offset:512
ds_read_b64_tr_b16 a[14:15], v48 offset:512
v_mfma_f32_16x16x32_bf16 %[c12], a[16:19], a[60:63], %[c12]
v_mfma_f32_16x16x32_bf16 %[c13], a[20:23], a[60:63], %[c13]
buffer_load_dwordx4 v32, s[40:43], s46 offen sc1 lds
v_mfma_f32_16x16x32_bf16 %[c14], a[24:27], a[60:63], %[c14]
ds_read_b128 a[40:43], v39 offset:4096
v_mfma_f32_16x16x32_bf16 %[c15], a[28:31], a[60:63], %[c15]
buffer_load_dwordx4 v32, s[40:43], s46 offen offset:1024 sc1 lds
s_waitcnt lgkmcnt(1)
v_mfma_f32_16x16x32_bf16 %[c0], a[0:3], a[32:35], %[c0]
v_mfma_f32_16x16x32_bf16 %[c1], a[4:7], a[32:35], %[c1]
s_add_u32 s46, s46, %[wadv]
v_mfma_f32_16x16x32_bf16 %[c2], a[8:11], a[32:35], %[c2]
ds_read_b128 a[44:47], v39 offset:6144
v_mfma_f32_16x16x32_bf16 %[c3], a[12:15], a[32:35], %[c3]
s_add_u32 s45, s45, %[xadv]
v_mfma_f32_16x16x32_bf16 %[c4], a[0:3], a[36:39], %[c4]
ds_read_b64_tr_b16 a[16:17], v45 offset:8192
ds_read_b64_tr_b16 a[18:19], v47 offset:8192
v_mfma_f32_16x16x32_bf16 %[c5], a[4:7], a[36:39], %[c5]
ds_read_b64_tr_b16 a[20:21], v46 offset:8192
ds_read_b64_tr_b16 a[22:23], v48 offset:8192
v_mfma_f32_16x16x32_bf16 %[c6], a[8:11], a[36:39], %[c6]
s_waitcnt vmcnt(16)
s_barrier
ds_read_b128 a[48:51], v42
v_mfma_f32_16x16x32_bf16 %[c7], a[12:15], a[36:39], %[c7]
s_add_u32 m0, s50, 131072
s_waitcnt lgkmcnt(5)
v_mfma_f32_16x16x32_bf16 %[c8], a[0:3], a[40:43], %[c8]
ds_read_b64_tr_b16 a[24:25], v45 offset:8704
ds_read_b64_tr_b16 a[26:27], v47 offset:8704
v_mfma_f32_16x16x32_bf16 %[c9], a[4:7], a[40:43], %[c9]
buffer_load_dwordx4 v33, s[36:39], s45 offen lds
v_mfma_f32_16x16x32_bf16 %[c10], a[8:11], a[40:43], %[c10]
ds_read_b128 a[52:55], v42 offset:2048
v_mfma_f32_16x16x32_bf16 %[c11], a[12:15], a[40:43], %[c11]
ds_read_b64_tr_b16 a[28:29], v46 offset:8704
ds_read_b64_tr_b16 a[30:31], v48 offset:8704
buffer_load_dwordx4 v34, s[36:39], s45 offen offset:1024 lds
v_mfma_f32_16x16x32_bf16 %[c12], a[0:3], a[44:47], %[c12]
v_mfma_f32_16x16x32_bf16 %[c13], a[4:7], a[44:47], %[c13]
buffer_load_dwordx4 v35, s[36:39], s45 offen offset:2048 lds
v_mfma_f32_16x16x32_bf16 %[c14], a[8:11], a[44:47], %[c14]
ds_read_b128 a[56:59], v42 offset:4096
v_mfma_f32_16x16x32_bf16 %[c15], a[12:15], a[44:47], %[c15]
buffer_load_dwordx4 v36, s[36:39], s45 offen offset:3072 lds
s_waitcnt lgkmcnt(1)
v_mfma_f32_16x16x32_bf16 %[c0], a[16:19], a[48:51], %[c0]
v_mfma_f32_16x16x32_bf16 %[c1], a[20:23], a[48:51], %[c1]
v_mfma_f32_16x16x32_bf16 %[c2], a[24:27], a[48:51], %[c2]
s_add_u32 m0, s44, 147456
ds_read_b128 a[60:63], v42 offset:6144
v_mfma_f32_16x16x32_bf16 %[c3], a[28:31], a[48:51], %[c3]
v_mfma_f32_16x16x32_bf16 %[c4], a[16:19], a[52:55], %[c4]
ds_read_b64_tr_b16 a[0:1], v45 offset:32768
ds_read_b64_tr_b16 a[2:3], v47 offset:32768
buffer_load_dwordx4 v32, s[40:43], s46 offen sc1 lds
v_mfma_f32_16x16x32_bf16 %[c5], a[20:23], a[52:55], %[c5]
ds_read_b64_tr_b16 a[4:5], v46 offset:32768
ds_read_b64_tr_b16 a[6:7], v48 offset:32768
v_mfma_f32_16x16x32_bf16 %[c6], a[24:27], a[52:55], %[c6]
ds_read_b128 a[32:35], v39 offset:32768
buffer_load_dwordx4 v32, s[40:43], s46 offen offset:1024 sc1 lds
v_mfma_f32_16x16x32_bf16 %[c7], a[28:31], a[52:55], %[c7]
s_waitcnt lgkmcnt(5)
v_mfma_f32_16x16x32_bf16 %[c8], a[16:19], a[56:59], %[c8]
s_add_u32 s46, s46, %[wadv]
ds_read_b64_tr_b16 a[8:9], v45 offset:33280
ds_read_b64_tr_b16 a[10:11], v47 offset:33280
v_mfma_f32_16x16x32_bf16 %[c9], a[20:23], a[56:59], %[c9]
v_mfma_f32_16x16x32_bf16 %[c10], a[24:27], a[56:59], %[c10]
s_add_u32 m0, s44, 155648
ds_read_b128 a[36:39], v39 offset:34816
v_mfma_f32_16x16x32_bf16 %[c11], a[28:31], a[56:59], %[c11]
ds_read_b64_tr_b16 a[12:13], v46 offset:33280
ds_read_b64_tr_b16 a[14:15], v48 offset:33280
v_mfma_f32_16x16x32_bf16 %[c12], a[16:19], a[60:63], %[c12]
v_mfma_f32_16x16x32_bf16 %[c13], a[20:23], a[60:63], %[c13]
buffer_load_dwordx4 v32, s[40:43], s46 offen sc1 lds
v_mfma_f32_16x16x32_bf16 %[c14], a[24:27], a[60:63], %[c14]
ds_read_b128 a[40:43], v39 offset:36864
v_mfma_f32_16x16x32_bf16 %[c15], a[28:31], a[60:63], %[c15]
buffer_load_dwordx4 v32, s[40:43], s46 offen offset:1024 sc1 lds
s_waitcnt lgkmcnt(1)
v_mfma_f32_16x16x32_bf16 %[c0], a[0:3], a[32:35], %[c0]
v_mfma_f32_16x16x32_bf16 %[c1], a[4:7], a[32:35], %[c1]
s_add_u32 s46, s46, %[wadv]
v_mfma_f32_16x16x32_bf16 %[c2], a[8:11], a[32:35], %[c2]
ds_read_b128 a[44:47], v39 offset:38912
v_mfma_f32_16x16x32_bf16 %[c3], a[12:15], a[32:35], %[c3]
s_add_u32 s45, s45, %[xadv]
v_mfma_f32_16x16x32_bf16 %[c4], a[0:3], a[36:39], %[c4]
ds_read_b64_tr_b16 a[16:17], v45 offset:40960
ds_read_b64_tr_b16 a[18:19], v47 offset:40960
v_mfma_f32_16x16x32_bf16 %[c5], a[4:7], a[36:39], %[c5]
ds_read_b64_tr_b16 a[20:21], v46 offset:40960
ds_read_b64_tr_b16 a[22:23], v48 offset:40960
v_mfma_f32_16x16x32_bf16 %[c6], a[8:11], a[36:39], %[c6]
s_waitcnt vmcnt(16)
s_barrier
ds_read_b128 a[48:51], v42 offset:32768
v_mfma_f32_16x16x32_bf16 %[c7], a[12:15], a[36:39], %[c7]
s_add_u32 m0, s50, 0
s_waitcnt lgkmcnt(5)
v_mfma_f32_16x16x32_bf16 %[c8], a[0:3], a[40:43], %[c8]
ds_read_b64_tr_b16 a[24:25], v45 offset:41472
ds_read_b64_tr_b16 a[26:27], v47 offset:41472
v_mfma_f32_16x16x32_bf16 %[c9], a[4:7], a[40:43], %[c9]
buffer_load_dwordx4 v33, s[36:39], s45 offen lds
v_mfma_f32_16x16x32_bf16 %[c10], a[8:11], a[40:43], %[c10]
ds_read_b128 a[52:55], v42 offset:34816
v_mfma_f32_16x16x32_bf16 %[c11], a[12:15], a[40:43], %[c11]
ds_read_b64_tr_b16 a[28:29], v46 offset:41472
ds_read_b64_tr_b16 a[30:31], v48 offset:41472
buffer_load_dwordx4 v34, s[36:39], s45 offen offset:1024 lds
v_mfma_f32_16x16x32_bf16 %[c12], a[0:3], a[44:47], %[c12]
v_mfma_f32_16x16x32_bf16 %[c13], a[4:7], a[44:47], %[c13]
buffer_load_dwordx4 v35, s[36:39], s45 offen offset:2048 lds
v_mfma_f32_16x16x32_bf16 %[c14], a[8:11], a[44:47], %[c14]
ds_read_b128 a[56:59], v42 offset:36864
v_mfma_f32_16x16x32_bf16 %[c15], a[12:15], a[44:47], %[c15]
buffer_load_dwordx4 v36, s[36:39], s45 offen offset:3072 lds
s_waitcnt lgkmcnt(1)
v_mfma_f32_16x16x32_bf16 %[c0], a[16:19], a[48:51], %[c0]
v_mfma_f32_16x16x32_bf16 %[c1], a[20:23], a[48:51], %[c1]
v_mfma_f32_16x16x32_bf16 %[c2], a[24:27], a[48:51], %[c2]
s_add_u32 m0, s44, 16384
ds_read_b128 a[60:63], v42 offset:38912
v_mfma_f32_16x16x32_bf16 %[c3], a[28:31], a[48:51], %[c3]
v_mfma_f32_16x16x32_bf16 %[c4], a[16:19], a[52:55], %[c4]
ds_read_b64_tr_b16 a[0:1], v49
ds_read_b64_tr_b16 a[2:3], v51
buffer_load_dwordx4 v32, s[40:43], s46 offen sc1 lds
v_mfma_f32_16x16x32_bf16 %[c5], a[20:23], a[52:55], %[c5]
ds_read_b64_tr_b16 a[4:5], v50
ds_read_b64_tr_b16 a[6:7], v52
v_mfma_f32_16x16x32_bf16 %[c6], a[24:27], a[52:55], %[c6]
ds_read_b128 a[32:35], v40
buffer_load_dwordx4 v32, s[40:43], s46 offen offset:1024 sc1 lds
v_mfma_f32_16x16x32_bf16 %[c7], a[28:31], a[52:55], %[c7]
s_waitcnt lgkmcnt(5)
v_mfma_f32_16x16x32_bf16 %[c8], a[16:19], a[56:59], %[c8]
s_add_u32 s46, s46, %[wadv]
ds_read_b64_tr_b16 a[8:9], v49 offset:512
ds_read_b64_tr_b16 a[10:11], v51 offset:512
v_mfma_f32_16x16x32_bf16 %[c9], a[20:23], a[56:59], %[c9]
v_mfma_f32_16x16x32_bf16 %[c10], a[24:27], a[56:59], %[c10]
s_add_u32 m0, s44, 24576
ds_read_b128 a[36:39], v40 offset:2048
v_mfma_f32_16x16x32_bf16 %[c11], a[28:31], a[56:59], %[c11]
ds_read_b64_tr_b16 a[12:13], v50 offset:512
ds_read_b64_tr_b16 a[14:15], v52 offset:512
v_mfma_f32_16x16x32_bf16 %[c12], a[16:19], a[60:63], %[c12]
v_mfma_f32_16x16x32_bf16 %[c13], a[20:23], a[60:63], %[c13]
buffer_load_dwordx4 v32, s[40:43], s46 offen sc1 lds
v_mfma_f32_16x16x32_bf16 %[c14], a[24:27], a[60:63], %[c14]
ds_read_b128 a[40:43], v40 offset:4096
v_mfma_f32_16x16x32_bf16 %[c15], a[28:31], a[60:63], %[c15]
buffer_load_dwordx4 v32, s[40:43], s46 offen offset:1024 sc1 lds
s_waitcnt lgkmcnt(1)
v_mfma_f32_16x16x32_bf16 %[c0], a[0:3], a[32:35], %[c0]
v_mfma_f32_16x16x32_bf16 %[c1], a[4:7], a[32:35], %[c1]
s_add_u32 s46, s46, %[wadv]
v_mfma_f32_16x16x32_bf16 %[c2], a[8:11], a[32:35], %[c2]
ds_read_b128 a[44:47], v40 offset:6144
v_mfma_f32_16x16x32_bf16 %[c3], a[12:15], a[32:35], %[c3]
s_add_u32 s45, s45, %[xadv]
v_mfma_f32_16x16x32_bf16 %[c4], a[0:3], a[36:39], %[c4]
ds_read_b64_tr_b16 a[16:17], v49 offset:8192
ds_read_b64_tr_b16 a[18:19], v51 offset:8192
v_mfma_f32_16x16x32_bf16 %[c5], a[4:7], a[36:39], %[c5]
ds_read_b64_tr_b16 a[20:21], v50 offset:8192
ds_read_b64_tr_b16 a[22:23], v52 offset:8192
v_mfma_f32_16x16x32_bf16 %[c6], a[8:11], a[36:39], %[c6]
s_waitcnt vmcnt(16)
s_barrier
ds_read_b128 a[48:51], v43
v_mfma_f32_16x16x32_bf16 %[c7], a[12:15], a[36:39], %[c7]
s_add_u32 m0, s50, 32768
s_waitcnt lgkmcnt(5)
v_mfma_f32_16x16x32_bf16 %[c8], a[0:3], a[40:43], %[c8]
ds_read_b64_tr_b16 a[24:25], v49 offset:8704
ds_read_b64_tr_b16 a[26:27], v51 offset:8704
v_mfma_f32_16x16x32_bf16 %[c9], a[4:7], a[40:43], %[c9]
buffer_load_dwordx4 v33, s[36:39], s45 offen lds
v_mfma_f32_16x16x32_bf16 %[c10], a[8:11], a[40:43], %[c10]
ds_read_b128 a[52:55], v43 offset:2048
v_mfma_f32_16x16x32_bf16 %[c11], a[12:15], a[40:43], %[c11]
ds_read_b64_tr_b16 a[28:29], v50 offset:8704
ds_read_b64_tr_b16 a[30:31], v52 offset:8704
buffer_load_dwordx4 v34, s[36:39], s45 offen offset:1024 lds
v_mfma_f32_16x16x32_bf16 %[c12], a[0:3], a[44:47], %[c12]
v_mfma_f32_16x16x32_bf16 %[c13], a[4:7], a[44:47], %[c13]
buffer_load_dwordx4 v35, s[36:39], s45 offen offset:2048 lds
v_mfma_f32_16x16x32_bf16 %[c14], a[8:11], a[44:47], %[c14]
ds_read_b128 a[56:59], v43 offset:4096
v_mfma_f32_16x16x32_bf16 %[c15], a[12:15], a[44:47], %[c15]
buffer_load_dwordx4 v36, s[36:39], s45 offen offset:3072 lds
s_waitcnt lgkmcnt(1)
v_mfma_f32_16x16x32_bf16 %[c0], a[16:19], a[48:51], %[c0]
v_mfma_f32_16x16x32_bf16 %[c1], a[20:23], a[48:51], %[c1]
v_mfma_f32_16x16x32_bf16 %[c2], a[24:27], a[48:51], %[c2]
s_add_u32 m0, s44, 49152
ds_read_b128 a[60:63], v43 offset:6144
v_mfma_f32_16x16x32_bf16 %[c3], a[28:31], a[48:51], %[c3]
v_mfma_f32_16x16x32_bf16 %[c4], a[16:19], a[52:55], %[c4]
ds_read_b64_tr_b16 a[0:1], v49 offset:32768
ds_read_b64_tr_b16 a[2:3], v51 offset:32768
buffer_load_dwordx4 v32, s[40:43], s46 offen sc1 lds
v_mfma_f32_16x16x32_bf16 %[c5], a[20:23], a[52:55], %[c5]
ds_read_b64_tr_b16 a[4:5], v50 offset:32768
ds_read_b64_tr_b16 a[6:7], v52 offset:32768
v_mfma_f32_16x16x32_bf16 %[c6], a[24:27], a[52:55], %[c6]
ds_read_b128 a[32:35], v40 offset:32768
buffer_load_dwordx4 v32, s[40:43], s46 offen offset:1024 sc1 lds
v_mfma_f32_16x16x32_bf16 %[c7], a[28:31], a[52:55], %[c7]
s_waitcnt lgkmcnt(5)
v_mfma_f32_16x16x32_bf16 %[c8], a[16:19], a[56:59], %[c8]
s_add_u32 s46, s46, %[wadv]
ds_read_b64_tr_b16 a[8:9], v49 offset:33280
ds_read_b64_tr_b16 a[10:11], v51 offset:33280
v_mfma_f32_16x16x32_bf16 %[c9], a[20:23], a[56:59], %[c9]
v_mfma_f32_16x16x32_bf16 %[c10], a[24:27], a[56:59], %[c10]
s_add_u32 m0, s44, 57344
ds_read_b128 a[36:39], v40 offset:34816
v_mfma_f32_16x16x32_bf16 %[c11], a[28:31], a[56:59], %[c11]
ds_read_b64_tr_b16 a[12:13], v50 offset:33280
ds_read_b64_tr_b16 a[14:15], v52 offset:33280
v_mfma_f32_16x16x32_bf16 %[c12], a[16:19], a[60:63], %[c12]
v_mfma_f32_16x16x32_bf16 %[c13], a[20:23], a[60:63], %[c13]
buffer_load_dwordx4 v32, s[40:43], s46 offen sc1 lds
v_mfma_f32_16x16x32_bf16 %[c14], a[24:27], a[60:63], %[c14]
ds_read_b128 a[40:43], v40 offset:36864
v_mfma_f32_16x16x32_bf16 %[c15], a[28:31], a[60:63], %[c15]
buffer_load_dwordx4 v32, s[40:43], s46 offen offset:1024 sc1 lds
s_waitcnt lgkmcnt(1)
v_mfma_f32_16x16x32_bf16 %[c0], a[0:3], a[32:35], %[c0]
v_mfma_f32_16x16x32_bf16 %[c1], a[4:7], a[32:35], %[c1]
s_add_u32 s46, s46, %[wadv]
v_mfma_f32_16x16x32_bf16 %[c2], a[8:11], a[32:35], %[c2]
ds_read_b128 a[44:47], v40 offset:38912
v_mfma_f32_16x16x32_bf16 %[c3], a[12:15], a[32:35], %[c3]
s_add_u32 s45, s45, %[xadv]
v_mfma_f32_16x16x32_bf16 %[c4], a[0:3], a[36:39], %[c4]
ds_read_b64_tr_b16 a[16:17], v49 offset:40960
ds_read_b64_tr_b16 a[18:19], v51 offset:40960
v_mfma_f32_16x16x32_bf16 %[c5], a[4:7], a[36:39], %[c5]
ds_read_b64_tr_b16 a[20:21], v50 offset:40960
)ASM"
R"ASM(ds_read_b64_tr_b16 a[22:23], v52 offset:40960
v_mfma_f32_16x16x32_bf16 %[c6], a[8:11], a[36:39], %[c6]
s_waitcnt vmcnt(16)
s_barrier
ds_read_b128 a[48:51], v43 offset:32768
v_mfma_f32_16x16x32_bf16 %[c7], a[12:15], a[36:39], %[c7]
s_add_u32 m0, s50, 65536
s_waitcnt lgkmcnt(5)
v_mfma_f32_16x16x32_bf16 %[c8], a[0:3], a[40:43], %[c8]
ds_read_b64_tr_b16 a[24:25], v49 offset:41472
ds_read_b64_tr_b16 a[26:27], v51 offset:41472
v_mfma_f32_16x16x32_bf16 %[c9], a[4:7], a[40:43], %[c9]
buffer_load_dwordx4 v33, s[36:39], s45 offen lds
v_mfma_f32_16x16x32_bf16 %[c10], a[8:11], a[40:43], %[c10]
ds_read_b128 a[52:55], v43 offset:34816
v_mfma_f32_16x16x32_bf16 %[c11], a[12:15], a[40:43], %[c11]
ds_read_b64_tr_b16 a[28:29], v50 offset:41472
ds_read_b64_tr_b16 a[30:31], v52 offset:41472
buffer_load_dwordx4 v34, s[36:39], s45 offen offset:1024 lds
v_mfma_f32_16x16x32_bf16 %[c12], a[0:3], a[44:47], %[c12]
v_mfma_f32_16x16x32_bf16 %[c13], a[4:7], a[44:47], %[c13]
buffer_load_dwordx4 v35, s[36:39], s45 offen offset:2048 lds
v_mfma_f32_16x16x32_bf16 %[c14], a[8:11], a[44:47], %[c14]
ds_read_b128 a[56:59], v43 offset:36864
v_mfma_f32_16x16x32_bf16 %[c15], a[12:15], a[44:47], %[c15]
buffer_load_dwordx4 v36, s[36:39], s45 offen offset:3072 lds
s_waitcnt lgkmcnt(1)
v_mfma_f32_16x16x32_bf16 %[c0], a[16:19], a[48:51], %[c0]
v_mfma_f32_16x16x32_bf16 %[c1], a[20:23], a[48:51], %[c1]
v_mfma_f32_16x16x32_bf16 %[c2], a[24:27], a[48:51], %[c2]
s_add_u32 m0, s44, 81920
ds_read_b128 a[60:63], v43 offset:38912
v_mfma_f32_16x16x32_bf16 %[c3], a[28:31], a[48:51], %[c3]
v_mfma_f32_16x16x32_bf16 %[c4], a[16:19], a[52:55], %[c4]
ds_read_b64_tr_b16 a[0:1], v53
ds_read_b64_tr_b16 a[2:3], v55
buffer_load_dwordx4 v32, s[40:43], s46 offen sc1 lds
v_mfma_f32_16x16x32_bf16 %[c5], a[20:23], a[52:55], %[c5]
ds_read_b64_tr_b16 a[4:5], v54
ds_read_b64_tr_b16 a[6:7], v56
v_mfma_f32_16x16x32_bf16 %[c6], a[24:27], a[52:55], %[c6]
ds_read_b128 a[32:35], v41
buffer_load_dwordx4 v32, s[40:43], s46 offen offset:1024 sc1 lds
v_mfma_f32_16x16x32_bf16 %[c7], a[28:31], a[52:55], %[c7]
s_waitcnt lgkmcnt(5)
v_mfma_f32_16x16x32_bf16 %[c8], a[16:19], a[56:59], %[c8]
s_add_u32 s46, s46, %[wadv]
ds_read_b64_tr_b16 a[8:9], v53 offset:512
ds_read_b64_tr_b16 a[10:11], v55 offset:512
v_mfma_f32_16x16x32_bf16 %[c9], a[20:23], a[56:59], %[c9]
v_mfma_f32_16x16x32_bf16 %[c10], a[24:27], a[56:59], %[c10]
s_add_u32 m0, s44, 90112
ds_read_b128 a[36:39], v41 offset:2048
v_mfma_f32_16x16x32_bf16 %[c11], a[28:31], a[56:59], %[c11]
ds_read_b64_tr_b16 a[12:13], v54 offset:512
ds_read_b64_tr_b16 a[14:15], v56 offset:512
v_mfma_f32_16x16x32_bf16 %[c12], a[16:19], a[60:63], %[c12]
v_mfma_f32_16x16x32_bf16 %[c13], a[20:23], a[60:63], %[c13]
buffer_load_dwordx4 v32, s[40:43], s46 offen sc1 lds
v_mfma_f32_16x16x32_bf16 %[c14], a[24:27], a[60:63], %[c14]
ds_read_b128 a[40:43], v41 offset:4096
v_mfma_f32_16x16x32_bf16 %[c15], a[28:31], a[60:63], %[c15]
buffer_load_dwordx4 v32, s[40:43], s46 offen offset:1024 sc1 lds
s_waitcnt lgkmcnt(1)
v_mfma_f32_16x16x32_bf16 %[c0], a[0:3], a[32:35], %[c0]
v_mfma_f32_16x16x32_bf16 %[c1], a[4:7], a[32:35], %[c1]
s_add_u32 s46, s46, %[wadv]
v_mfma_f32_16x16x32_bf16 %[c2], a[8:11], a[32:35], %[c2]
ds_read_b128 a[44:47], v41 offset:6144
v_mfma_f32_16x16x32_bf16 %[c3], a[12:15], a[32:35], %[c3]
s_add_u32 s45, s45, %[xadv]
v_mfma_f32_16x16x32_bf16 %[c4], a[0:3], a[36:39], %[c4]
ds_read_b64_tr_b16 a[16:17], v53 offset:8192
ds_read_b64_tr_b16 a[18:19], v55 offset:8192
v_mfma_f32_16x16x32_bf16 %[c5], a[4:7], a[36:39], %[c5]
ds_read_b64_tr_b16 a[20:21], v54 offset:8192
ds_read_b64_tr_b16 a[22:23], v56 offset:8192
v_mfma_f32_16x16x32_bf16 %[c6], a[8:11], a[36:39], %[c6]
s_waitcnt vmcnt(16)
s_barrier
ds_read_b128 a[48:51], v44
v_mfma_f32_16x16x32_bf16 %[c7], a[12:15], a[36:39], %[c7]
s_add_u32 m0, s50, 98304
s_waitcnt lgkmcnt(5)
v_mfma_f32_16x16x32_bf16 %[c8], a[0:3], a[40:43], %[c8]
ds_read_b64_tr_b16 a[24:25], v53 offset:8704
ds_read_b64_tr_b16 a[26:27], v55 offset:8704
v_mfma_f32_16x16x32_bf16 %[c9], a[4:7], a[40:43], %[c9]
buffer_load_dwordx4 v33, s[36:39], s45 offen lds
v_mfma_f32_16x16x32_bf16 %[c10], a[8:11], a[40:43], %[c10]
ds_read_b128 a[52:55], v44 offset:2048
v_mfma_f32_16x16x32_bf16 %[c11], a[12:15], a[40:43], %[c11]
ds_read_b64_tr_b16 a[28:29], v54 offset:8704
ds_read_b64_tr_b16 a[30:31], v56 offset:8704
buffer_load_dwordx4 v34, s[36:39], s45 offen offset:1024 lds
v_mfma_f32_16x16x32_bf16 %[c12], a[0:3], a[44:47], %[c12]
v_mfma_f32_16x16x32_bf16 %[c13], a[4:7], a[44:47], %[c13]
buffer_load_dwordx4 v35, s[36:39], s45 offen offset:2048 lds
v_mfma_f32_16x16x32_bf16 %[c14], a[8:11], a[44:47], %[c14]
ds_read_b128 a[56:59], v44 offset:4096
v_mfma_f32_16x16x32_bf16 %[c15], a[12:15], a[44:47], %[c15]
buffer_load_dwordx4 v36, s[36:39], s45 offen offset:3072 lds
s_waitcnt lgkmcnt(1)
v_mfma_f32_16x16x32_bf16 %[c0], a[16:19], a[48:51], %[c0]
v_mfma_f32_16x16x32_bf16 %[c1], a[20:23], a[48:51], %[c1]
v_mfma_f32_16x16x32_bf16 %[c2], a[24:27], a[48:51], %[c2]
s_add_u32 m0, s44, 114688
ds_read_b128 a[60:63], v44 offset:6144
v_mfma_f32_16x16x32_bf16 %[c3], a[28:31], a[48:51], %[c3]
v_mfma_f32_16x16x32_bf16 %[c4], a[16:19], a[52:55], %[c4]
ds_read_b64_tr_b16 a[0:1], v45
ds_read_b64_tr_b16 a[2:3], v47
buffer_load_dwordx4 v32, s[40:43], s46 offen sc1 lds
v_mfma_f32_16x16x32_bf16 %[c5], a[20:23], a[52:55], %[c5]
ds_read_b64_tr_b16 a[4:5], v46
ds_read_b64_tr_b16 a[6:7], v48
v_mfma_f32_16x16x32_bf16 %[c6], a[24:27], a[52:55], %[c6]
ds_read_b128 a[32:35], v39
buffer_load_dwordx4 v32, s[40:43], s46 offen offset:1024 sc1 lds
v_mfma_f32_16x16x32_bf16 %[c7], a[28:31], a[52:55], %[c7]
s_waitcnt lgkmcnt(5)
v_mfma_f32_16x16x32_bf16 %[c8], a[16:19], a[56:59], %[c8]
s_add_u32 s46, s46, %[wadv]
ds_read_b64_tr_b16 a[8:9], v45 offset:512
ds_read_b64_tr_b16 a[10:11], v47 offset:512
v_mfma_f32_16x16x32_bf16 %[c9], a[20:23], a[56:59], %[c9]
v_mfma_f32_16x16x32_bf16 %[c10], a[24:27], a[56:59], %[c10]
s_add_u32 m0, s44, 122880
ds_read_b128 a[36:39], v39 offset:2048
v_mfma_f32_16x16x32_bf16 %[c11], a[28:31], a[56:59], %[c11]
ds_read_b64_tr_b16 a[12:13], v46 offset:512
ds_read_b64_tr_b16 a[14:15], v48 offset:512
v_mfma_f32_16x16x32_bf16 %[c12], a[16:19], a[60:63], %[c12]
v_mfma_f32_16x16x32_bf16 %[c13], a[20:23], a[60:63], %[c13]
buffer_load_dwordx4 v32, s[40:43], s46 offen sc1 lds
v_mfma_f32_16x16x32_bf16 %[c14], a[24:27], a[60:63], %[c14]
ds_read_b128 a[40:43], v39 offset:4096
v_mfma_f32_16x16x32_bf16 %[c15], a[28:31], a[60:63], %[c15]
buffer_load_dwordx4 v32, s[40:43], s46 offen offset:1024 sc1 lds
s_waitcnt lgkmcnt(1)
v_mfma_f32_16x16x32_bf16 %[c0], a[0:3], a[32:35], %[c0]
v_mfma_f32_16x16x32_bf16 %[c1], a[4:7], a[32:35], %[c1]
s_add_u32 s46, s46, %[wadv]
v_mfma_f32_16x16x32_bf16 %[c2], a[8:11], a[32:35], %[c2]
ds_read_b128 a[44:47], v39 offset:6144
v_mfma_f32_16x16x32_bf16 %[c3], a[12:15], a[32:35], %[c3]
s_add_u32 s45, s45, %[xadv]
v_mfma_f32_16x16x32_bf16 %[c4], a[0:3], a[36:39], %[c4]
ds_read_b64_tr_b16 a[16:17], v45 offset:8192
ds_read_b64_tr_b16 a[18:19], v47 offset:8192
v_mfma_f32_16x16x32_bf16 %[c5], a[4:7], a[36:39], %[c5]
ds_read_b64_tr_b16 a[20:21], v46 offset:8192
ds_read_b64_tr_b16 a[22:23], v48 offset:8192
v_mfma_f32_16x16x32_bf16 %[c6], a[8:11], a[36:39], %[c6]
s_waitcnt vmcnt(16)
s_barrier
ds_read_b128 a[48:51], v42
v_mfma_f32_16x16x32_bf16 %[c7], a[12:15], a[36:39], %[c7]
s_add_u32 m0, s50, 131072
s_waitcnt lgkmcnt(5)
v_mfma_f32_16x16x32_bf16 %[c8], a[0:3], a[40:43], %[c8]
ds_read_b64_tr_b16 a[24:25], v45 offset:8704
ds_read_b64_tr_b16 a[26:27], v47 offset:8704
v_mfma_f32_16x16x32_bf16 %[c9], a[4:7], a[40:43], %[c9]
buffer_load_dwordx4 v33, s[36:39], s45 offen lds
v_mfma_f32_16x16x32_bf16 %[c10], a[8:11], a[40:43], %[c10]
ds_read_b128 a[52:55], v42 offset:2048
v_mfma_f32_16x16x32_bf16 %[c11], a[12:15], a[40:43], %[c11]
ds_read_b64_tr_b16 a[28:29], v46 offset:8704
ds_read_b64_tr_b16 a[30:31], v48 offset:8704
buffer_load_dwordx4 v34, s[36:39], s45 offen offset:1024 lds
v_mfma_f32_16x16x32_bf16 %[c12], a[0:3], a[44:47], %[c12]
v_mfma_f32_16x16x32_bf16 %[c13], a[4:7], a[44:47], %[c13]
buffer_load_dwordx4 v35, s[36:39], s45 offen offset:2048 lds
v_mfma_f32_16x16x32_bf16 %[c14], a[8:11], a[44:47], %[c14]
ds_read_b128 a[56:59], v42 offset:4096
v_mfma_f32_16x16x32_bf16 %[c15], a[12:15], a[44:47], %[c15]
buffer_load_dwordx4 v36, s[36:39], s45 offen offset:3072 lds
s_waitcnt lgkmcnt(1)
v_mfma_f32_16x16x32_bf16 %[c0], a[16:19], a[48:51], %[c0]
v_mfma_f32_16x16x32_bf16 %[c1], a[20:23], a[48:51], %[c1]
v_mfma_f32_16x16x32_bf16 %[c2], a[24:27], a[48:51], %[c2]
s_add_u32 m0, s44, 147456
ds_read_b128 a[60:63], v42 offset:6144
v_mfma_f32_16x16x32_bf16 %[c3], a[28:31], a[48:51], %[c3]
v_mfma_f32_16x16x32_bf16 %[c4], a[16:19], a[52:55], %[c4]
ds_read_b64_tr_b16 a[0:1], v45 offset:32768
ds_read_b64_tr_b16 a[2:3], v47 offset:32768
buffer_load_dwordx4 v32, s[40:43], s46 offen sc1 lds
v_mfma_f32_16x16x32_bf16 %[c5], a[20:23], a[52:55], %[c5]
ds_read_b64_tr_b16 a[4:5], v46 offset:32768
ds_read_b64_tr_b16 a[6:7], v48 offset:32768
v_mfma_f32_16x16x32_bf16 %[c6], a[24:27], a[52:55], %[c6]
ds_read_b128 a[32:35], v39 offset:32768
buffer_load_dwordx4 v32, s[40:43], s46 offen offset:1024 sc1 lds
v_mfma_f32_16x16x32_bf16 %[c7], a[28:31], a[52:55], %[c7]
s_waitcnt lgkmcnt(5)
v_mfma_f32_16x16x32_bf16 %[c8], a[16:19], a[56:59], %[c8]
s_add_u32 s46, s46, %[wadv]
ds_read_b64_tr_b16 a[8:9], v45 offset:33280
ds_read_b64_tr_b16 a[10:11], v47 offset:33280
v_mfma_f32_16x16x32_bf16 %[c9], a[20:23], a[56:59], %[c9]
v_mfma_f32_16x16x32_bf16 %[c10], a[24:27], a[56:59], %[c10]
s_add_u32 m0, s44, 155648
ds_read_b128 a[36:39], v39 offset:34816
v_mfma_f32_16x16x32_bf16 %[c11], a[28:31], a[56:59], %[c11]
ds_read_b64_tr_b16 a[12:13], v46 offset:33280
ds_read_b64_tr_b16 a[14:15], v48 offset:33280
v_mfma_f32_16x16x32_bf16 %[c12], a[16:19], a[60:63], %[c12]
v_mfma_f32_16x16x32_bf16 %[c13], a[20:23], a[60:63], %[c13]
buffer_load_dwordx4 v32, s[40:43], s46 offen sc1 lds
v_mfma_f32_16x16x32_bf16 %[c14], a[24:27], a[60:63], %[c14]
ds_read_b128 a[40:43], v39 offset:36864
v_mfma_f32_16x16x32_bf16 %[c15], a[28:31], a[60:63], %[c15]
buffer_load_dwordx4 v32, s[40:43], s46 offen offset:1024 sc1 lds
s_waitcnt lgkmcnt(1)
v_mfma_f32_16x16x32_bf16 %[c0], a[0:3], a[32:35], %[c0]
v_mfma_f32_16x16x32_bf16 %[c1], a[4:7], a[32:35], %[c1]
s_add_u32 s46, s46, %[wadv]
v_mfma_f32_16x16x32_bf16 %[c2], a[8:11], a[32:35], %[c2]
ds_read_b128 a[44:47], v39 offset:38912
v_mfma_f32_16x16x32_bf16 %[c3], a[12:15], a[32:35], %[c3]
s_add_u32 s45, s45, %[xadv]
v_mfma_f32_16x16x32_bf16 %[c4], a[0:3], a[36:39], %[c4]
ds_read_b64_tr_b16 a[16:17], v45 offset:40960
ds_read_b64_tr_b16 a[18:19], v47 offset:40960
v_mfma_f32_16x16x32_bf16 %[c5], a[4:7], a[36:39], %[c5]
ds_read_b64_tr_b16 a[20:21], v46 offset:40960
ds_read_b64_tr_b16 a[22:23], v48 offset:40960
v_mfma_f32_16x16x32_bf16 %[c6], a[8:11], a[36:39], %[c6]
s_waitcnt vmcnt(16)
s_barrier
ds_read_b128 a[48:51], v42 offset:32768
v_mfma_f32_16x16x32_bf16 %[c7], a[12:15], a[36:39], %[c7]
s_add_u32 m0, s50, 0
s_waitcnt lgkmcnt(5)
v_mfma_f32_16x16x32_bf16 %[c8], a[0:3], a[40:43], %[c8]
ds_read_b64_tr_b16 a[24:25], v45 offset:41472
ds_read_b64_tr_b16 a[26:27], v47 offset:41472
v_mfma_f32_16x16x32_bf16 %[c9], a[4:7], a[40:43], %[c9]
buffer_load_dwordx4 v33, s[36:39], s45 offen lds
v_mfma_f32_16x16x32_bf16 %[c10], a[8:11], a[40:43], %[c10]
ds_read_b128 a[52:55], v42 offset:34816
v_mfma_f32_16x16x32_bf16 %[c11], a[12:15], a[40:43], %[c11]
ds_read_b64_tr_b16 a[28:29], v46 offset:41472
ds_read_b64_tr_b16 a[30:31], v48 offset:41472
buffer_load_dwordx4 v34, s[36:39], s45 offen offset:1024 lds
v_mfma_f32_16x16x32_bf16 %[c12], a[0:3], a[44:47], %[c12]
v_mfma_f32_16x16x32_bf16 %[c13], a[4:7], a[44:47], %[c13]
buffer_load_dwordx4 v35, s[36:39], s45 offen offset:2048 lds
v_mfma_f32_16x16x32_bf16 %[c14], a[8:11], a[44:47], %[c14]
ds_read_b128 a[56:59], v42 offset:36864
v_mfma_f32_16x16x32_bf16 %[c15], a[12:15], a[44:47], %[c15]
buffer_load_dwordx4 v36, s[36:39], s45 offen offset:3072 lds
s_waitcnt lgkmcnt(1)
)ASM"
R"ASM(v_mfma_f32_16x16x32_bf16 %[c0], a[16:19], a[48:51], %[c0]
v_mfma_f32_16x16x32_bf16 %[c1], a[20:23], a[48:51], %[c1]
v_mfma_f32_16x16x32_bf16 %[c2], a[24:27], a[48:51], %[c2]
s_add_u32 m0, s44, 16384
ds_read_b128 a[60:63], v42 offset:38912
v_mfma_f32_16x16x32_bf16 %[c3], a[28:31], a[48:51], %[c3]
v_mfma_f32_16x16x32_bf16 %[c4], a[16:19], a[52:55], %[c4]
ds_read_b64_tr_b16 a[0:1], v49
ds_read_b64_tr_b16 a[2:3], v51
buffer_load_dwordx4 v32, s[40:43], s46 offen sc1 lds
v_mfma_f32_16x16x32_bf16 %[c5], a[20:23], a[52:55], %[c5]
ds_read_b64_tr_b16 a[4:5], v50
ds_read_b64_tr_b16 a[6:7], v52
v_mfma_f32_16x16x32_bf16 %[c6], a[24:27], a[52:55], %[c6]
ds_read_b128 a[32:35], v40
buffer_load_dwordx4 v32, s[40:43], s46 offen offset:1024 sc1 lds
v_mfma_f32_16x16x32_bf16 %[c7], a[28:31], a[52:55], %[c7]
s_waitcnt lgkmcnt(5)
v_mfma_f32_16x16x32_bf16 %[c8], a[16:19], a[56:59], %[c8]
s_add_u32 s46, s46, %[wadv]
ds_read_b64_tr_b16 a[8:9], v49 offset:512
ds_read_b64_tr_b16 a[10:11], v51 offset:512
v_mfma_f32_16x16x32_bf16 %[c9], a[20:23], a[56:59], %[c9]
v_mfma_f32_16x16x32_bf16 %[c10], a[24:27], a[56:59], %[c10]
s_add_u32 m0, s44, 24576
ds_read_b128 a[36:39], v40 offset:2048
v_mfma_f32_16x16x32_bf16 %[c11], a[28:31], a[56:59], %[c11]
ds_read_b64_tr_b16 a[12:13], v50 offset:512
ds_read_b64_tr_b16 a[14:15], v52 offset:512
v_mfma_f32_16x16x32_bf16 %[c12], a[16:19], a[60:63], %[c12]
v_mfma_f32_16x16x32_bf16 %[c13], a[20:23], a[60:63], %[c13]
buffer_load_dwordx4 v32, s[40:43], s46 offen sc1 lds
v_mfma_f32_16x16x32_bf16 %[c14], a[24:27], a[60:63], %[c14]
ds_read_b128 a[40:43], v40 offset:4096
v_mfma_f32_16x16x32_bf16 %[c15], a[28:31], a[60:63], %[c15]
buffer_load_dwordx4 v32, s[40:43], s46 offen offset:1024 sc1 lds
s_waitcnt lgkmcnt(1)
v_mfma_f32_16x16x32_bf16 %[c0], a[0:3], a[32:35], %[c0]
v_mfma_f32_16x16x32_bf16 %[c1], a[4:7], a[32:35], %[c1]
s_add_u32 s46, s46, %[wadv]
v_mfma_f32_16x16x32_bf16 %[c2], a[8:11], a[32:35], %[c2]
ds_read_b128 a[44:47], v40 offset:6144
v_mfma_f32_16x16x32_bf16 %[c3], a[12:15], a[32:35], %[c3]
s_add_u32 s45, s45, %[xadv]
v_mfma_f32_16x16x32_bf16 %[c4], a[0:3], a[36:39], %[c4]
ds_read_b64_tr_b16 a[16:17], v49 offset:8192
ds_read_b64_tr_b16 a[18:19], v51 offset:8192
v_mfma_f32_16x16x32_bf16 %[c5], a[4:7], a[36:39], %[c5]
ds_read_b64_tr_b16 a[20:21], v50 offset:8192
ds_read_b64_tr_b16 a[22:23], v52 offset:8192
v_mfma_f32_16x16x32_bf16 %[c6], a[8:11], a[36:39], %[c6]
s_waitcnt vmcnt(16)
s_barrier
ds_read_b128 a[48:51], v43
v_mfma_f32_16x16x32_bf16 %[c7], a[12:15], a[36:39], %[c7]
s_add_u32 m0, s50, 32768
s_waitcnt lgkmcnt(5)
v_mfma_f32_16x16x32_bf16 %[c8], a[0:3], a[40:43], %[c8]
ds_read_b64_tr_b16 a[24:25], v49 offset:8704
ds_read_b64_tr_b16 a[26:27], v51 offset:8704
v_mfma_f32_16x16x32_bf16 %[c9], a[4:7], a[40:43], %[c9]
buffer_load_dwordx4 v33, s[36:39], s45 offen lds
v_mfma_f32_16x16x32_bf16 %[c10], a[8:11], a[40:43], %[c10]
ds_read_b128 a[52:55], v43 offset:2048
v_mfma_f32_16x16x32_bf16 %[c11], a[12:15], a[40:43], %[c11]
ds_read_b64_tr_b16 a[28:29], v50 offset:8704
ds_read_b64_tr_b16 a[30:31], v52 offset:8704
buffer_load_dwordx4 v34, s[36:39], s45 offen offset:1024 lds
v_mfma_f32_16x16x32_bf16 %[c12], a[0:3], a[44:47], %[c12]
v_mfma_f32_16x16x32_bf16 %[c13], a[4:7], a[44:47], %[c13]
buffer_load_dwordx4 v35, s[36:39], s45 offen offset:2048 lds
v_mfma_f32_16x16x32_bf16 %[c14], a[8:11], a[44:47], %[c14]
ds_read_b128 a[56:59], v43 offset:4096
v_mfma_f32_16x16x32_bf16 %[c15], a[12:15], a[44:47], %[c15]
buffer_load_dwordx4 v36, s[36:39], s45 offen offset:3072 lds
s_waitcnt lgkmcnt(1)
v_mfma_f32_16x16x32_bf16 %[c0], a[16:19], a[48:51], %[c0]
v_mfma_f32_16x16x32_bf16 %[c1], a[20:23], a[48:51], %[c1]
v_mfma_f32_16x16x32_bf16 %[c2], a[24:27], a[48:51], %[c2]
s_add_u32 m0, s44, 49152
ds_read_b128 a[60:63], v43 offset:6144
v_mfma_f32_16x16x32_bf16 %[c3], a[28:31], a[48:51], %[c3]
v_mfma_f32_16x16x32_bf16 %[c4], a[16:19], a[52:55], %[c4]
ds_read_b64_tr_b16 a[0:1], v49 offset:32768
ds_read_b64_tr_b16 a[2:3], v51 offset:32768
buffer_load_dwordx4 v32, s[40:43], s46 offen sc1 lds
v_mfma_f32_16x16x32_bf16 %[c5], a[20:23], a[52:55], %[c5]
ds_read_b64_tr_b16 a[4:5], v50 offset:32768
ds_read_b64_tr_b16 a[6:7], v52 offset:32768
v_mfma_f32_16x16x32_bf16 %[c6], a[24:27], a[52:55], %[c6]
ds_read_b128 a[32:35], v40 offset:32768
buffer_load_dwordx4 v32, s[40:43], s46 offen offset:1024 sc1 lds
v_mfma_f32_16x16x32_bf16 %[c7], a[28:31], a[52:55], %[c7]
s_waitcnt lgkmcnt(5)
v_mfma_f32_16x16x32_bf16 %[c8], a[16:19], a[56:59], %[c8]
s_add_u32 s46, s46, %[wadv]
ds_read_b64_tr_b16 a[8:9], v49 offset:33280
ds_read_b64_tr_b16 a[10:11], v51 offset:33280
v_mfma_f32_16x16x32_bf16 %[c9], a[20:23], a[56:59], %[c9]
v_mfma_f32_16x16x32_bf16 %[c10], a[24:27], a[56:59], %[c10]
s_add_u32 m0, s44, 57344
ds_read_b128 a[36:39], v40 offset:34816
v_mfma_f32_16x16x32_bf16 %[c11], a[28:31], a[56:59], %[c11]
ds_read_b64_tr_b16 a[12:13], v50 offset:33280
ds_read_b64_tr_b16 a[14:15], v52 offset:33280
v_mfma_f32_16x16x32_bf16 %[c12], a[16:19], a[60:63], %[c12]
v_mfma_f32_16x16x32_bf16 %[c13], a[20:23], a[60:63], %[c13]
buffer_load_dwordx4 v32, s[40:43], s46 offen sc1 lds
v_mfma_f32_16x16x32_bf16 %[c14], a[24:27], a[60:63], %[c14]
ds_read_b128 a[40:43], v40 offset:36864
v_mfma_f32_16x16x32_bf16 %[c15], a[28:31], a[60:63], %[c15]
buffer_load_dwordx4 v32, s[40:43], s46 offen offset:1024 sc1 lds
s_waitcnt lgkmcnt(1)
v_mfma_f32_16x16x32_bf16 %[c0], a[0:3], a[32:35], %[c0]
v_mfma_f32_16x16x32_bf16 %[c1], a[4:7], a[32:35], %[c1]
s_add_u32 s46, s46, %[wadv]
v_mfma_f32_16x16x32_bf16 %[c2], a[8:11], a[32:35], %[c2]
ds_read_b128 a[44:47], v40 offset:38912
v_mfma_f32_16x16x32_bf16 %[c3], a[12:15], a[32:35], %[c3]
s_add_u32 s45, s45, %[xadv]
v_mfma_f32_16x16x32_bf16 %[c4], a[0:3], a[36:39], %[c4]
ds_read_b64_tr_b16 a[16:17], v49 offset:40960
ds_read_b64_tr_b16 a[18:19], v51 offset:40960
v_mfma_f32_16x16x32_bf16 %[c5], a[4:7], a[36:39], %[c5]
ds_read_b64_tr_b16 a[20:21], v50 offset:40960
ds_read_b64_tr_b16 a[22:23], v52 offset:40960
v_mfma_f32_16x16x32_bf16 %[c6], a[8:11], a[36:39], %[c6]
s_waitcnt vmcnt(16)
s_barrier
ds_read_b128 a[48:51], v43 offset:32768
v_mfma_f32_16x16x32_bf16 %[c7], a[12:15], a[36:39], %[c7]
s_add_u32 m0, s50, 65536
s_waitcnt lgkmcnt(5)
v_mfma_f32_16x16x32_bf16 %[c8], a[0:3], a[40:43], %[c8]
ds_read_b64_tr_b16 a[24:25], v49 offset:41472
ds_read_b64_tr_b16 a[26:27], v51 offset:41472
v_mfma_f32_16x16x32_bf16 %[c9], a[4:7], a[40:43], %[c9]
buffer_load_dwordx4 v33, s[36:39], s45 offen lds
v_mfma_f32_16x16x32_bf16 %[c10], a[8:11], a[40:43], %[c10]
ds_read_b128 a[52:55], v43 offset:34816
v_mfma_f32_16x16x32_bf16 %[c11], a[12:15], a[40:43], %[c11]
ds_read_b64_tr_b16 a[28:29], v50 offset:41472
ds_read_b64_tr_b16 a[30:31], v52 offset:41472
buffer_load_dwordx4 v34, s[36:39], s45 offen offset:1024 lds
v_mfma_f32_16x16x32_bf16 %[c12], a[0:3], a[44:47], %[c12]
v_mfma_f32_16x16x32_bf16 %[c13], a[4:7], a[44:47], %[c13]
buffer_load_dwordx4 v35, s[36:39], s45 offen offset:2048 lds
v_mfma_f32_16x16x32_bf16 %[c14], a[8:11], a[44:47], %[c14]
ds_read_b128 a[56:59], v43 offset:36864
v_mfma_f32_16x16x32_bf16 %[c15], a[12:15], a[44:47], %[c15]
buffer_load_dwordx4 v36, s[36:39], s45 offen offset:3072 lds
s_waitcnt lgkmcnt(1)
v_mfma_f32_16x16x32_bf16 %[c0], a[16:19], a[48:51], %[c0]
v_mfma_f32_16x16x32_bf16 %[c1], a[20:23], a[48:51], %[c1]
v_mfma_f32_16x16x32_bf16 %[c2], a[24:27], a[48:51], %[c2]
s_add_u32 m0, s44, 81920
ds_read_b128 a[60:63], v43 offset:38912
v_mfma_f32_16x16x32_bf16 %[c3], a[28:31], a[48:51], %[c3]
v_mfma_f32_16x16x32_bf16 %[c4], a[16:19], a[52:55], %[c4]
ds_read_b64_tr_b16 a[0:1], v53
ds_read_b64_tr_b16 a[2:3], v55
buffer_load_dwordx4 v32, s[40:43], s46 offen sc1 lds
v_mfma_f32_16x16x32_bf16 %[c5], a[20:23], a[52:55], %[c5]
ds_read_b64_tr_b16 a[4:5], v54
ds_read_b64_tr_b16 a[6:7], v56
v_mfma_f32_16x16x32_bf16 %[c6], a[24:27], a[52:55], %[c6]
ds_read_b128 a[32:35], v41
buffer_load_dwordx4 v32, s[40:43], s46 offen offset:1024 sc1 lds
v_mfma_f32_16x16x32_bf16 %[c7], a[28:31], a[52:55], %[c7]
s_waitcnt lgkmcnt(5)
v_mfma_f32_16x16x32_bf16 %[c8], a[16:19], a[56:59], %[c8]
s_add_u32 s46, s46, %[wadv]
ds_read_b64_tr_b16 a[8:9], v53 offset:512
ds_read_b64_tr_b16 a[10:11], v55 offset:512
v_mfma_f32_16x16x32_bf16 %[c9], a[20:23], a[56:59], %[c9]
v_mfma_f32_16x16x32_bf16 %[c10], a[24:27], a[56:59], %[c10]
s_add_u32 m0, s44, 90112
ds_read_b128 a[36:39], v41 offset:2048
v_mfma_f32_16x16x32_bf16 %[c11], a[28:31], a[56:59], %[c11]
ds_read_b64_tr_b16 a[12:13], v54 offset:512
ds_read_b64_tr_b16 a[14:15], v56 offset:512
v_mfma_f32_16x16x32_bf16 %[c12], a[16:19], a[60:63], %[c12]
v_mfma_f32_16x16x32_bf16 %[c13], a[20:23], a[60:63], %[c13]
buffer_load_dwordx4 v32, s[40:43], s46 offen sc1 lds
v_mfma_f32_16x16x32_bf16 %[c14], a[24:27], a[60:63], %[c14]
ds_read_b128 a[40:43], v41 offset:4096
v_mfma_f32_16x16x32_bf16 %[c15], a[28:31], a[60:63], %[c15]
buffer_load_dwordx4 v32, s[40:43], s46 offen offset:1024 sc1 lds
s_waitcnt lgkmcnt(1)
v_mfma_f32_16x16x32_bf16 %[c0], a[0:3], a[32:35], %[c0]
v_mfma_f32_16x16x32_bf16 %[c1], a[4:7], a[32:35], %[c1]
s_add_u32 s46, s46, %[wadv]
v_mfma_f32_16x16x32_bf16 %[c2], a[8:11], a[32:35], %[c2]
ds_read_b128 a[44:47], v41 offset:6144
v_mfma_f32_16x16x32_bf16 %[c3], a[12:15], a[32:35], %[c3]
s_add_u32 s45, s45, %[xadv]
v_mfma_f32_16x16x32_bf16 %[c4], a[0:3], a[36:39], %[c4]
ds_read_b64_tr_b16 a[16:17], v53 offset:8192
ds_read_b64_tr_b16 a[18:19], v55 offset:8192
v_mfma_f32_16x16x32_bf16 %[c5], a[4:7], a[36:39], %[c5]
ds_read_b64_tr_b16 a[20:21], v54 offset:8192
ds_read_b64_tr_b16 a[22:23], v56 offset:8192
v_mfma_f32_16x16x32_bf16 %[c6], a[8:11], a[36:39], %[c6]
s_waitcnt vmcnt(16)
s_barrier
ds_read_b128 a[48:51], v44
v_mfma_f32_16x16x32_bf16 %[c7], a[12:15], a[36:39], %[c7]
s_add_u32 m0, s50, 98304
s_waitcnt lgkmcnt(5)
v_mfma_f32_16x16x32_bf16 %[c8], a[0:3], a[40:43], %[c8]
ds_read_b64_tr_b16 a[24:25], v53 offset:8704
ds_read_b64_tr_b16 a[26:27], v55 offset:8704
v_mfma_f32_16x16x32_bf16 %[c9], a[4:7], a[40:43], %[c9]
buffer_load_dwordx4 v33, s[36:39], s45 offen lds
v_mfma_f32_16x16x32_bf16 %[c10], a[8:11], a[40:43], %[c10]
ds_read_b128 a[52:55], v44 offset:2048
v_mfma_f32_16x16x32_bf16 %[c11], a[12:15], a[40:43], %[c11]
ds_read_b64_tr_b16 a[28:29], v54 offset:8704
ds_read_b64_tr_b16 a[30:31], v56 offset:8704
buffer_load_dwordx4 v34, s[36:39], s45 offen offset:1024 lds
v_mfma_f32_16x16x32_bf16 %[c12], a[0:3], a[44:47], %[c12]
v_mfma_f32_16x16x32_bf16 %[c13], a[4:7], a[44:47], %[c13]
buffer_load_dwordx4 v35, s[36:39], s45 offen offset:2048 lds
v_mfma_f32_16x16x32_bf16 %[c14], a[8:11], a[44:47], %[c14]
ds_read_b128 a[56:59], v44 offset:4096
v_mfma_f32_16x16x32_bf16 %[c15], a[12:15], a[44:47], %[c15]
buffer_load_dwordx4 v36, s[36:39], s45 offen offset:3072 lds
s_waitcnt lgkmcnt(1)
v_mfma_f32_16x16x32_bf16 %[c0], a[16:19], a[48:51], %[c0]
v_mfma_f32_16x16x32_bf16 %[c1], a[20:23], a[48:51], %[c1]
v_mfma_f32_16x16x32_bf16 %[c2], a[24:27], a[48:51], %[c2]
s_add_u32 m0, s44, 114688
ds_read_b128 a[60:63], v44 offset:6144
v_mfma_f32_16x16x32_bf16 %[c3], a[28:31], a[48:51], %[c3]
v_mfma_f32_16x16x32_bf16 %[c4], a[16:19], a[52:55], %[c4]
ds_read_b64_tr_b16 a[0:1], v45
ds_read_b64_tr_b16 a[2:3], v47
buffer_load_dwordx4 v32, s[40:43], s46 offen sc1 lds
v_mfma_f32_16x16x32_bf16 %[c5], a[20:23], a[52:55], %[c5]
ds_read_b64_tr_b16 a[4:5], v46
ds_read_b64_tr_b16 a[6:7], v48
v_mfma_f32_16x16x32_bf16 %[c6], a[24:27], a[52:55], %[c6]
ds_read_b128 a[32:35], v39
buffer_load_dwordx4 v32, s[40:43], s46 offen offset:1024 sc1 lds
v_mfma_f32_16x16x32_bf16 %[c7], a[28:31], a[52:55], %[c7]
s_waitcnt lgkmcnt(5)
v_mfma_f32_16x16x32_bf16 %[c8], a[16:19], a[56:59], %[c8]
s_add_u32 s46, s46, %[wadv]
ds_read_b64_tr_b16 a[8:9], v45 offset:512
ds_read_b64_tr_b16 a[10:11], v47 offset:512
v_mfma_f32_16x16x32_bf16 %[c9], a[20:23], a[56:59], %[c9]
v_mfma_f32_16x16x32_bf16 %[c10], a[24:27], a[56:59], %[c10]
s_add_u32 m0, s44, 122880
ds_read_b128 a[36:39], v39 offset:2048
v_mfma_f32_16x16x32_bf16 %[c11], a[28:31], a[56:59], %[c11]
ds_read_b64_tr_b16 a[12:13], v46 offset:512
)ASM"
R"ASM(ds_read_b64_tr_b16 a[14:15], v48 offset:512
v_mfma_f32_16x16x32_bf16 %[c12], a[16:19], a[60:63], %[c12]
v_mfma_f32_16x16x32_bf16 %[c13], a[20:23], a[60:63], %[c13]
buffer_load_dwordx4 v32, s[40:43], s46 offen sc1 lds
v_mfma_f32_16x16x32_bf16 %[c14], a[24:27], a[60:63], %[c14]
ds_read_b128 a[40:43], v39 offset:4096
v_mfma_f32_16x16x32_bf16 %[c15], a[28:31], a[60:63], %[c15]
buffer_load_dwordx4 v32, s[40:43], s46 offen offset:1024 sc1 lds
s_waitcnt lgkmcnt(1)
v_mfma_f32_16x16x32_bf16 %[c0], a[0:3], a[32:35], %[c0]
v_mfma_f32_16x16x32_bf16 %[c1], a[4:7], a[32:35], %[c1]
s_add_u32 s46, s46, %[wadv]
v_mfma_f32_16x16x32_bf16 %[c2], a[8:11], a[32:35], %[c2]
ds_read_b128 a[44:47], v39 offset:6144
v_mfma_f32_16x16x32_bf16 %[c3], a[12:15], a[32:35], %[c3]
s_add_u32 s45, s45, %[xadv]
v_mfma_f32_16x16x32_bf16 %[c4], a[0:3], a[36:39], %[c4]
ds_read_b64_tr_b16 a[16:17], v45 offset:8192
ds_read_b64_tr_b16 a[18:19], v47 offset:8192
v_mfma_f32_16x16x32_bf16 %[c5], a[4:7], a[36:39], %[c5]
ds_read_b64_tr_b16 a[20:21], v46 offset:8192
ds_read_b64_tr_b16 a[22:23], v48 offset:8192
v_mfma_f32_16x16x32_bf16 %[c6], a[8:11], a[36:39], %[c6]
s_waitcnt vmcnt(16)
s_barrier
ds_read_b128 a[48:51], v42
v_mfma_f32_16x16x32_bf16 %[c7], a[12:15], a[36:39], %[c7]
s_add_u32 m0, s50, 131072
s_waitcnt lgkmcnt(5)
v_mfma_f32_16x16x32_bf16 %[c8], a[0:3], a[40:43], %[c8]
ds_read_b64_tr_b16 a[24:25], v45 offset:8704
ds_read_b64_tr_b16 a[26:27], v47 offset:8704
v_mfma_f32_16x16x32_bf16 %[c9], a[4:7], a[40:43], %[c9]
buffer_load_dwordx4 v33, s[36:39], s45 offen lds
v_mfma_f32_16x16x32_bf16 %[c10], a[8:11], a[40:43], %[c10]
ds_read_b128 a[52:55], v42 offset:2048
v_mfma_f32_16x16x32_bf16 %[c11], a[12:15], a[40:43], %[c11]
ds_read_b64_tr_b16 a[28:29], v46 offset:8704
ds_read_b64_tr_b16 a[30:31], v48 offset:8704
buffer_load_dwordx4 v34, s[36:39], s45 offen offset:1024 lds
v_mfma_f32_16x16x32_bf16 %[c12], a[0:3], a[44:47], %[c12]
v_mfma_f32_16x16x32_bf16 %[c13], a[4:7], a[44:47], %[c13]
buffer_load_dwordx4 v35, s[36:39], s45 offen offset:2048 lds
v_mfma_f32_16x16x32_bf16 %[c14], a[8:11], a[44:47], %[c14]
ds_read_b128 a[56:59], v42 offset:4096
v_mfma_f32_16x16x32_bf16 %[c15], a[12:15], a[44:47], %[c15]
buffer_load_dwordx4 v36, s[36:39], s45 offen offset:3072 lds
s_waitcnt lgkmcnt(1)
v_mfma_f32_16x16x32_bf16 %[c0], a[16:19], a[48:51], %[c0]
v_mfma_f32_16x16x32_bf16 %[c1], a[20:23], a[48:51], %[c1]
v_mfma_f32_16x16x32_bf16 %[c2], a[24:27], a[48:51], %[c2]
s_add_u32 m0, s44, 147456
ds_read_b128 a[60:63], v42 offset:6144
v_mfma_f32_16x16x32_bf16 %[c3], a[28:31], a[48:51], %[c3]
v_mfma_f32_16x16x32_bf16 %[c4], a[16:19], a[52:55], %[c4]
ds_read_b64_tr_b16 a[0:1], v45 offset:32768
ds_read_b64_tr_b16 a[2:3], v47 offset:32768
buffer_load_dwordx4 v32, s[40:43], s46 offen sc1 lds
v_mfma_f32_16x16x32_bf16 %[c5], a[20:23], a[52:55], %[c5]
ds_read_b64_tr_b16 a[4:5], v46 offset:32768
ds_read_b64_tr_b16 a[6:7], v48 offset:32768
v_mfma_f32_16x16x32_bf16 %[c6], a[24:27], a[52:55], %[c6]
ds_read_b128 a[32:35], v39 offset:32768
buffer_load_dwordx4 v32, s[40:43], s46 offen offset:1024 sc1 lds
v_mfma_f32_16x16x32_bf16 %[c7], a[28:31], a[52:55], %[c7]
s_waitcnt lgkmcnt(5)
v_mfma_f32_16x16x32_bf16 %[c8], a[16:19], a[56:59], %[c8]
s_add_u32 s46, s46, %[wadv]
ds_read_b64_tr_b16 a[8:9], v45 offset:33280
ds_read_b64_tr_b16 a[10:11], v47 offset:33280
v_mfma_f32_16x16x32_bf16 %[c9], a[20:23], a[56:59], %[c9]
v_mfma_f32_16x16x32_bf16 %[c10], a[24:27], a[56:59], %[c10]
s_add_u32 m0, s44, 155648
ds_read_b128 a[36:39], v39 offset:34816
v_mfma_f32_16x16x32_bf16 %[c11], a[28:31], a[56:59], %[c11]
ds_read_b64_tr_b16 a[12:13], v46 offset:33280
ds_read_b64_tr_b16 a[14:15], v48 offset:33280
v_mfma_f32_16x16x32_bf16 %[c12], a[16:19], a[60:63], %[c12]
v_mfma_f32_16x16x32_bf16 %[c13], a[20:23], a[60:63], %[c13]
buffer_load_dwordx4 v32, s[40:43], s46 offen sc1 lds
v_mfma_f32_16x16x32_bf16 %[c14], a[24:27], a[60:63], %[c14]
ds_read_b128 a[40:43], v39 offset:36864
v_mfma_f32_16x16x32_bf16 %[c15], a[28:31], a[60:63], %[c15]
buffer_load_dwordx4 v32, s[40:43], s46 offen offset:1024 sc1 lds
s_waitcnt lgkmcnt(1)
v_mfma_f32_16x16x32_bf16 %[c0], a[0:3], a[32:35], %[c0]
v_mfma_f32_16x16x32_bf16 %[c1], a[4:7], a[32:35], %[c1]
s_add_u32 s46, s46, %[wadv]
v_mfma_f32_16x16x32_bf16 %[c2], a[8:11], a[32:35], %[c2]
ds_read_b128 a[44:47], v39 offset:38912
v_mfma_f32_16x16x32_bf16 %[c3], a[12:15], a[32:35], %[c3]
s_add_u32 s45, s45, %[xadv]
v_mfma_f32_16x16x32_bf16 %[c4], a[0:3], a[36:39], %[c4]
ds_read_b64_tr_b16 a[16:17], v45 offset:40960
ds_read_b64_tr_b16 a[18:19], v47 offset:40960
v_mfma_f32_16x16x32_bf16 %[c5], a[4:7], a[36:39], %[c5]
ds_read_b64_tr_b16 a[20:21], v46 offset:40960
ds_read_b64_tr_b16 a[22:23], v48 offset:40960
v_mfma_f32_16x16x32_bf16 %[c6], a[8:11], a[36:39], %[c6]
s_waitcnt vmcnt(16)
s_barrier
ds_read_b128 a[48:51], v42 offset:32768
v_mfma_f32_16x16x32_bf16 %[c7], a[12:15], a[36:39], %[c7]
s_add_u32 m0, s50, 0
s_waitcnt lgkmcnt(5)
v_mfma_f32_16x16x32_bf16 %[c8], a[0:3], a[40:43], %[c8]
ds_read_b64_tr_b16 a[24:25], v45 offset:41472
ds_read_b64_tr_b16 a[26:27], v47 offset:41472
v_mfma_f32_16x16x32_bf16 %[c9], a[4:7], a[40:43], %[c9]
buffer_load_dwordx4 v33, s[36:39], s45 offen lds
v_mfma_f32_16x16x32_bf16 %[c10], a[8:11], a[40:43], %[c10]
ds_read_b128 a[52:55], v42 offset:34816
v_mfma_f32_16x16x32_bf16 %[c11], a[12:15], a[40:43], %[c11]
ds_read_b64_tr_b16 a[28:29], v46 offset:41472
ds_read_b64_tr_b16 a[30:31], v48 offset:41472
buffer_load_dwordx4 v34, s[36:39], s45 offen offset:1024 lds
v_mfma_f32_16x16x32_bf16 %[c12], a[0:3], a[44:47], %[c12]
v_mfma_f32_16x16x32_bf16 %[c13], a[4:7], a[44:47], %[c13]
buffer_load_dwordx4 v35, s[36:39], s45 offen offset:2048 lds
v_mfma_f32_16x16x32_bf16 %[c14], a[8:11], a[44:47], %[c14]
ds_read_b128 a[56:59], v42 offset:36864
v_mfma_f32_16x16x32_bf16 %[c15], a[12:15], a[44:47], %[c15]
buffer_load_dwordx4 v36, s[36:39], s45 offen offset:3072 lds
s_waitcnt lgkmcnt(1)
v_mfma_f32_16x16x32_bf16 %[c0], a[16:19], a[48:51], %[c0]
v_mfma_f32_16x16x32_bf16 %[c1], a[20:23], a[48:51], %[c1]
v_mfma_f32_16x16x32_bf16 %[c2], a[24:27], a[48:51], %[c2]
s_add_u32 m0, s44, 16384
ds_read_b128 a[60:63], v42 offset:38912
v_mfma_f32_16x16x32_bf16 %[c3], a[28:31], a[48:51], %[c3]
v_mfma_f32_16x16x32_bf16 %[c4], a[16:19], a[52:55], %[c4]
ds_read_b64_tr_b16 a[0:1], v49
ds_read_b64_tr_b16 a[2:3], v51
buffer_load_dwordx4 v32, s[40:43], s46 offen sc1 lds
v_mfma_f32_16x16x32_bf16 %[c5], a[20:23], a[52:55], %[c5]
ds_read_b64_tr_b16 a[4:5], v50
ds_read_b64_tr_b16 a[6:7], v52
v_mfma_f32_16x16x32_bf16 %[c6], a[24:27], a[52:55], %[c6]
ds_read_b128 a[32:35], v40
buffer_load_dwordx4 v32, s[40:43], s46 offen offset:1024 sc1 lds
v_mfma_f32_16x16x32_bf16 %[c7], a[28:31], a[52:55], %[c7]
s_waitcnt lgkmcnt(5)
v_mfma_f32_16x16x32_bf16 %[c8], a[16:19], a[56:59], %[c8]
s_add_u32 s46, s46, %[wadv]
ds_read_b64_tr_b16 a[8:9], v49 offset:512
ds_read_b64_tr_b16 a[10:11], v51 offset:512
v_mfma_f32_16x16x32_bf16 %[c9], a[20:23], a[56:59], %[c9]
v_mfma_f32_16x16x32_bf16 %[c10], a[24:27], a[56:59], %[c10]
s_add_u32 m0, s44, 24576
ds_read_b128 a[36:39], v40 offset:2048
v_mfma_f32_16x16x32_bf16 %[c11], a[28:31], a[56:59], %[c11]
ds_read_b64_tr_b16 a[12:13], v50 offset:512
ds_read_b64_tr_b16 a[14:15], v52 offset:512
v_mfma_f32_16x16x32_bf16 %[c12], a[16:19], a[60:63], %[c12]
v_mfma_f32_16x16x32_bf16 %[c13], a[20:23], a[60:63], %[c13]
buffer_load_dwordx4 v32, s[40:43], s46 offen sc1 lds
v_mfma_f32_16x16x32_bf16 %[c14], a[24:27], a[60:63], %[c14]
ds_read_b128 a[40:43], v40 offset:4096
v_mfma_f32_16x16x32_bf16 %[c15], a[28:31], a[60:63], %[c15]
buffer_load_dwordx4 v32, s[40:43], s46 offen offset:1024 sc1 lds
s_waitcnt lgkmcnt(1)
v_mfma_f32_16x16x32_bf16 %[c0], a[0:3], a[32:35], %[c0]
v_mfma_f32_16x16x32_bf16 %[c1], a[4:7], a[32:35], %[c1]
s_add_u32 s46, s46, %[wadv]
v_mfma_f32_16x16x32_bf16 %[c2], a[8:11], a[32:35], %[c2]
ds_read_b128 a[44:47], v40 offset:6144
v_mfma_f32_16x16x32_bf16 %[c3], a[12:15], a[32:35], %[c3]
s_add_u32 s45, s45, %[xadv]
v_mfma_f32_16x16x32_bf16 %[c4], a[0:3], a[36:39], %[c4]
ds_read_b64_tr_b16 a[16:17], v49 offset:8192
ds_read_b64_tr_b16 a[18:19], v51 offset:8192
v_mfma_f32_16x16x32_bf16 %[c5], a[4:7], a[36:39], %[c5]
ds_read_b64_tr_b16 a[20:21], v50 offset:8192
ds_read_b64_tr_b16 a[22:23], v52 offset:8192
v_mfma_f32_16x16x32_bf16 %[c6], a[8:11], a[36:39], %[c6]
s_waitcnt vmcnt(16)
s_barrier
ds_read_b128 a[48:51], v43
v_mfma_f32_16x16x32_bf16 %[c7], a[12:15], a[36:39], %[c7]
s_add_u32 m0, s50, 32768
s_waitcnt lgkmcnt(5)
v_mfma_f32_16x16x32_bf16 %[c8], a[0:3], a[40:43], %[c8]
ds_read_b64_tr_b16 a[24:25], v49 offset:8704
ds_read_b64_tr_b16 a[26:27], v51 offset:8704
v_mfma_f32_16x16x32_bf16 %[c9], a[4:7], a[40:43], %[c9]
buffer_load_dwordx4 v33, s[36:39], s45 offen lds
v_mfma_f32_16x16x32_bf16 %[c10], a[8:11], a[40:43], %[c10]
ds_read_b128 a[52:55], v43 offset:2048
v_mfma_f32_16x16x32_bf16 %[c11], a[12:15], a[40:43], %[c11]
ds_read_b64_tr_b16 a[28:29], v50 offset:8704
ds_read_b64_tr_b16 a[30:31], v52 offset:8704
buffer_load_dwordx4 v34, s[36:39], s45 offen offset:1024 lds
v_mfma_f32_16x16x32_bf16 %[c12], a[0:3], a[44:47], %[c12]
v_mfma_f32_16x16x32_bf16 %[c13], a[4:7], a[44:47], %[c13]
buffer_load_dwordx4 v35, s[36:39], s45 offen offset:2048 lds
v_mfma_f32_16x16x32_bf16 %[c14], a[8:11], a[44:47], %[c14]
ds_read_b128 a[56:59], v43 offset:4096
v_mfma_f32_16x16x32_bf16 %[c15], a[12:15], a[44:47], %[c15]
buffer_load_dwordx4 v36, s[36:39], s45 offen offset:3072 lds
s_waitcnt lgkmcnt(1)
v_mfma_f32_16x16x32_bf16 %[c0], a[16:19], a[48:51], %[c0]
v_mfma_f32_16x16x32_bf16 %[c1], a[20:23], a[48:51], %[c1]
v_mfma_f32_16x16x32_bf16 %[c2], a[24:27], a[48:51], %[c2]
s_add_u32 m0, s44, 49152
ds_read_b128 a[60:63], v43 offset:6144
v_mfma_f32_16x16x32_bf16 %[c3], a[28:31], a[48:51], %[c3]
v_mfma_f32_16x16x32_bf16 %[c4], a[16:19], a[52:55], %[c4]
ds_read_b64_tr_b16 a[0:1], v49 offset:32768
ds_read_b64_tr_b16 a[2:3], v51 offset:32768
buffer_load_dwordx4 v32, s[40:43], s46 offen sc1 lds
v_mfma_f32_16x16x32_bf16 %[c5], a[20:23], a[52:55], %[c5]
ds_read_b64_tr_b16 a[4:5], v50 offset:32768
ds_read_b64_tr_b16 a[6:7], v52 offset:32768
v_mfma_f32_16x16x32_bf16 %[c6], a[24:27], a[52:55], %[c6]
ds_read_b128 a[32:35], v40 offset:32768
buffer_load_dwordx4 v32, s[40:43], s46 offen offset:1024 sc1 lds
v_mfma_f32_16x16x32_bf16 %[c7], a[28:31], a[52:55], %[c7]
s_waitcnt lgkmcnt(5)
v_mfma_f32_16x16x32_bf16 %[c8], a[16:19], a[56:59], %[c8]
s_add_u32 s46, s46, %[wadv]
ds_read_b64_tr_b16 a[8:9], v49 offset:33280
ds_read_b64_tr_b16 a[10:11], v51 offset:33280
v_mfma_f32_16x16x32_bf16 %[c9], a[20:23], a[56:59], %[c9]
v_mfma_f32_16x16x32_bf16 %[c10], a[24:27], a[56:59], %[c10]
s_add_u32 m0, s44, 57344
ds_read_b128 a[36:39], v40 offset:34816
v_mfma_f32_16x16x32_bf16 %[c11], a[28:31], a[56:59], %[c11]
ds_read_b64_tr_b16 a[12:13], v50 offset:33280
ds_read_b64_tr_b16 a[14:15], v52 offset:33280
v_mfma_f32_16x16x32_bf16 %[c12], a[16:19], a[60:63], %[c12]
v_mfma_f32_16x16x32_bf16 %[c13], a[20:23], a[60:63], %[c13]
buffer_load_dwordx4 v32, s[40:43], s46 offen sc1 lds
v_mfma_f32_16x16x32_bf16 %[c14], a[24:27], a[60:63], %[c14]
ds_read_b128 a[40:43], v40 offset:36864
v_mfma_f32_16x16x32_bf16 %[c15], a[28:31], a[60:63], %[c15]
buffer_load_dwordx4 v32, s[40:43], s46 offen offset:1024 sc1 lds
s_waitcnt lgkmcnt(1)
v_mfma_f32_16x16x32_bf16 %[c0], a[0:3], a[32:35], %[c0]
v_mfma_f32_16x16x32_bf16 %[c1], a[4:7], a[32:35], %[c1]
s_add_u32 s46, s46, %[wadv]
v_mfma_f32_16x16x32_bf16 %[c2], a[8:11], a[32:35], %[c2]
ds_read_b128 a[44:47], v40 offset:38912
v_mfma_f32_16x16x32_bf16 %[c3], a[12:15], a[32:35], %[c3]
s_add_u32 s45, s45, %[xadv]
v_mfma_f32_16x16x32_bf16 %[c4], a[0:3], a[36:39], %[c4]
ds_read_b64_tr_b16 a[16:17], v49 offset:40960
ds_read_b64_tr_b16 a[18:19], v51 offset:40960
v_mfma_f32_16x16x32_bf16 %[c5], a[4:7], a[36:39], %[c5]
ds_read_b64_tr_b16 a[20:21], v50 offset:40960
ds_read_b64_tr_b16 a[22:23], v52 offset:40960
v_mfma_f32_16x16x32_bf16 %[c6], a[8:11], a[36:39], %[c6]
s_waitcnt vmcnt(16)
s_barrier
ds_read_b128 a[48:51], v43 offset:32768
)ASM"
R"ASM(v_mfma_f32_16x16x32_bf16 %[c7], a[12:15], a[36:39], %[c7]
s_add_u32 m0, s50, 65536
s_waitcnt lgkmcnt(5)
v_mfma_f32_16x16x32_bf16 %[c8], a[0:3], a[40:43], %[c8]
ds_read_b64_tr_b16 a[24:25], v49 offset:41472
ds_read_b64_tr_b16 a[26:27], v51 offset:41472
v_mfma_f32_16x16x32_bf16 %[c9], a[4:7], a[40:43], %[c9]
buffer_load_dwordx4 v33, s[36:39], s45 offen lds
v_mfma_f32_16x16x32_bf16 %[c10], a[8:11], a[40:43], %[c10]
ds_read_b128 a[52:55], v43 offset:34816
v_mfma_f32_16x16x32_bf16 %[c11], a[12:15], a[40:43], %[c11]
ds_read_b64_tr_b16 a[28:29], v50 offset:41472
ds_read_b64_tr_b16 a[30:31], v52 offset:41472
buffer_load_dwordx4 v34, s[36:39], s45 offen offset:1024 lds
v_mfma_f32_16x16x32_bf16 %[c12], a[0:3], a[44:47], %[c12]
v_mfma_f32_16x16x32_bf16 %[c13], a[4:7], a[44:47], %[c13]
buffer_load_dwordx4 v35, s[36:39], s45 offen offset:2048 lds
v_mfma_f32_16x16x32_bf16 %[c14], a[8:11], a[44:47], %[c14]
ds_read_b128 a[56:59], v43 offset:36864
v_mfma_f32_16x16x32_bf16 %[c15], a[12:15], a[44:47], %[c15]
buffer_load_dwordx4 v36, s[36:39], s45 offen offset:3072 lds
s_waitcnt lgkmcnt(1)
v_mfma_f32_16x16x32_bf16 %[c0], a[16:19], a[48:51], %[c0]
v_mfma_f32_16x16x32_bf16 %[c1], a[20:23], a[48:51], %[c1]
v_mfma_f32_16x16x32_bf16 %[c2], a[24:27], a[48:51], %[c2]
s_add_u32 m0, s44, 81920
ds_read_b128 a[60:63], v43 offset:38912
v_mfma_f32_16x16x32_bf16 %[c3], a[28:31], a[48:51], %[c3]
v_mfma_f32_16x16x32_bf16 %[c4], a[16:19], a[52:55], %[c4]
ds_read_b64_tr_b16 a[0:1], v53
ds_read_b64_tr_b16 a[2:3], v55
buffer_load_dwordx4 v32, s[40:43], s46 offen sc1 lds
v_mfma_f32_16x16x32_bf16 %[c5], a[20:23], a[52:55], %[c5]
ds_read_b64_tr_b16 a[4:5], v54
ds_read_b64_tr_b16 a[6:7], v56
v_mfma_f32_16x16x32_bf16 %[c6], a[24:27], a[52:55], %[c6]
ds_read_b128 a[32:35], v41
buffer_load_dwordx4 v32, s[40:43], s46 offen offset:1024 sc1 lds
v_mfma_f32_16x16x32_bf16 %[c7], a[28:31], a[52:55], %[c7]
s_waitcnt lgkmcnt(5)
v_mfma_f32_16x16x32_bf16 %[c8], a[16:19], a[56:59], %[c8]
s_add_u32 s46, s46, %[wadv]
ds_read_b64_tr_b16 a[8:9], v53 offset:512
ds_read_b64_tr_b16 a[10:11], v55 offset:512
v_mfma_f32_16x16x32_bf16 %[c9], a[20:23], a[56:59], %[c9]
v_mfma_f32_16x16x32_bf16 %[c10], a[24:27], a[56:59], %[c10]
s_add_u32 m0, s44, 90112
ds_read_b128 a[36:39], v41 offset:2048
v_mfma_f32_16x16x32_bf16 %[c11], a[28:31], a[56:59], %[c11]
ds_read_b64_tr_b16 a[12:13], v54 offset:512
ds_read_b64_tr_b16 a[14:15], v56 offset:512
v_mfma_f32_16x16x32_bf16 %[c12], a[16:19], a[60:63], %[c12]
v_mfma_f32_16x16x32_bf16 %[c13], a[20:23], a[60:63], %[c13]
buffer_load_dwordx4 v32, s[40:43], s46 offen sc1 lds
v_mfma_f32_16x16x32_bf16 %[c14], a[24:27], a[60:63], %[c14]
ds_read_b128 a[40:43], v41 offset:4096
v_mfma_f32_16x16x32_bf16 %[c15], a[28:31], a[60:63], %[c15]
buffer_load_dwordx4 v32, s[40:43], s46 offen offset:1024 sc1 lds
s_waitcnt lgkmcnt(1)
v_mfma_f32_16x16x32_bf16 %[c0], a[0:3], a[32:35], %[c0]
v_mfma_f32_16x16x32_bf16 %[c1], a[4:7], a[32:35], %[c1]
s_add_u32 s46, s46, %[wadv]
v_mfma_f32_16x16x32_bf16 %[c2], a[8:11], a[32:35], %[c2]
ds_read_b128 a[44:47], v41 offset:6144
v_mfma_f32_16x16x32_bf16 %[c3], a[12:15], a[32:35], %[c3]
s_add_u32 s45, s45, %[xadv]
v_mfma_f32_16x16x32_bf16 %[c4], a[0:3], a[36:39], %[c4]
ds_read_b64_tr_b16 a[16:17], v53 offset:8192
ds_read_b64_tr_b16 a[18:19], v55 offset:8192
v_mfma_f32_16x16x32_bf16 %[c5], a[4:7], a[36:39], %[c5]
ds_read_b64_tr_b16 a[20:21], v54 offset:8192
ds_read_b64_tr_b16 a[22:23], v56 offset:8192
v_mfma_f32_16x16x32_bf16 %[c6], a[8:11], a[36:39], %[c6]
s_waitcnt vmcnt(16)
s_barrier
ds_read_b128 a[48:51], v44
v_mfma_f32_16x16x32_bf16 %[c7], a[12:15], a[36:39], %[c7]
s_add_u32 m0, s50, 98304
s_waitcnt lgkmcnt(5)
v_mfma_f32_16x16x32_bf16 %[c8], a[0:3], a[40:43], %[c8]
ds_read_b64_tr_b16 a[24:25], v53 offset:8704
ds_read_b64_tr_b16 a[26:27], v55 offset:8704
v_mfma_f32_16x16x32_bf16 %[c9], a[4:7], a[40:43], %[c9]
buffer_load_dwordx4 v33, s[36:39], s45 offen lds
v_mfma_f32_16x16x32_bf16 %[c10], a[8:11], a[40:43], %[c10]
ds_read_b128 a[52:55], v44 offset:2048
v_mfma_f32_16x16x32_bf16 %[c11], a[12:15], a[40:43], %[c11]
ds_read_b64_tr_b16 a[28:29], v54 offset:8704
ds_read_b64_tr_b16 a[30:31], v56 offset:8704
buffer_load_dwordx4 v34, s[36:39], s45 offen offset:1024 lds
v_mfma_f32_16x16x32_bf16 %[c12], a[0:3], a[44:47], %[c12]
v_mfma_f32_16x16x32_bf16 %[c13], a[4:7], a[44:47], %[c13]
buffer_load_dwordx4 v35, s[36:39], s45 offen offset:2048 lds
v_mfma_f32_16x16x32_bf16 %[c14], a[8:11], a[44:47], %[c14]
ds_read_b128 a[56:59], v44 offset:4096
v_mfma_f32_16x16x32_bf16 %[c15], a[12:15], a[44:47], %[c15]
buffer_load_dwordx4 v36, s[36:39], s45 offen offset:3072 lds
s_waitcnt lgkmcnt(1)
v_mfma_f32_16x16x32_bf16 %[c0], a[16:19], a[48:51], %[c0]
v_mfma_f32_16x16x32_bf16 %[c1], a[20:23], a[48:51], %[c1]
v_mfma_f32_16x16x32_bf16 %[c2], a[24:27], a[48:51], %[c2]
s_add_u32 m0, s44, 114688
ds_read_b128 a[60:63], v44 offset:6144
v_mfma_f32_16x16x32_bf16 %[c3], a[28:31], a[48:51], %[c3]
v_mfma_f32_16x16x32_bf16 %[c4], a[16:19], a[52:55], %[c4]
ds_read_b64_tr_b16 a[0:1], v45
ds_read_b64_tr_b16 a[2:3], v47
buffer_load_dwordx4 v32, s[40:43], s46 offen sc1 lds
v_mfma_f32_16x16x32_bf16 %[c5], a[20:23], a[52:55], %[c5]
ds_read_b64_tr_b16 a[4:5], v46
ds_read_b64_tr_b16 a[6:7], v48
v_mfma_f32_16x16x32_bf16 %[c6], a[24:27], a[52:55], %[c6]
ds_read_b128 a[32:35], v39
buffer_load_dwordx4 v32, s[40:43], s46 offen offset:1024 sc1 lds
v_mfma_f32_16x16x32_bf16 %[c7], a[28:31], a[52:55], %[c7]
s_waitcnt lgkmcnt(5)
v_mfma_f32_16x16x32_bf16 %[c8], a[16:19], a[56:59], %[c8]
s_add_u32 s46, s46, %[wadv]
ds_read_b64_tr_b16 a[8:9], v45 offset:512
ds_read_b64_tr_b16 a[10:11], v47 offset:512
v_mfma_f32_16x16x32_bf16 %[c9], a[20:23], a[56:59], %[c9]
v_mfma_f32_16x16x32_bf16 %[c10], a[24:27], a[56:59], %[c10]
s_add_u32 m0, s44, 122880
ds_read_b128 a[36:39], v39 offset:2048
v_mfma_f32_16x16x32_bf16 %[c11], a[28:31], a[56:59], %[c11]
ds_read_b64_tr_b16 a[12:13], v46 offset:512
ds_read_b64_tr_b16 a[14:15], v48 offset:512
v_mfma_f32_16x16x32_bf16 %[c12], a[16:19], a[60:63], %[c12]
v_mfma_f32_16x16x32_bf16 %[c13], a[20:23], a[60:63], %[c13]
buffer_load_dwordx4 v32, s[40:43], s46 offen sc1 lds
v_mfma_f32_16x16x32_bf16 %[c14], a[24:27], a[60:63], %[c14]
ds_read_b128 a[40:43], v39 offset:4096
v_mfma_f32_16x16x32_bf16 %[c15], a[28:31], a[60:63], %[c15]
buffer_load_dwordx4 v32, s[40:43], s46 offen offset:1024 sc1 lds
s_waitcnt lgkmcnt(1)
v_mfma_f32_16x16x32_bf16 %[c0], a[0:3], a[32:35], %[c0]
v_mfma_f32_16x16x32_bf16 %[c1], a[4:7], a[32:35], %[c1]
s_add_u32 s46, s46, %[wadv]
v_mfma_f32_16x16x32_bf16 %[c2], a[8:11], a[32:35], %[c2]
ds_read_b128 a[44:47], v39 offset:6144
v_mfma_f32_16x16x32_bf16 %[c3], a[12:15], a[32:35], %[c3]
s_add_u32 s45, s45, %[xadv]
v_mfma_f32_16x16x32_bf16 %[c4], a[0:3], a[36:39], %[c4]
ds_read_b64_tr_b16 a[16:17], v45 offset:8192
ds_read_b64_tr_b16 a[18:19], v47 offset:8192
v_mfma_f32_16x16x32_bf16 %[c5], a[4:7], a[36:39], %[c5]
ds_read_b64_tr_b16 a[20:21], v46 offset:8192
ds_read_b64_tr_b16 a[22:23], v48 offset:8192
v_mfma_f32_16x16x32_bf16 %[c6], a[8:11], a[36:39], %[c6]
s_waitcnt vmcnt(16)
s_barrier
ds_read_b128 a[48:51], v42
v_mfma_f32_16x16x32_bf16 %[c7], a[12:15], a[36:39], %[c7]
s_add_u32 m0, s50, 131072
s_waitcnt lgkmcnt(5)
v_mfma_f32_16x16x32_bf16 %[c8], a[0:3], a[40:43], %[c8]
ds_read_b64_tr_b16 a[24:25], v45 offset:8704
ds_read_b64_tr_b16 a[26:27], v47 offset:8704
v_mfma_f32_16x16x32_bf16 %[c9], a[4:7], a[40:43], %[c9]
buffer_load_dwordx4 v33, s[36:39], s45 offen lds
v_mfma_f32_16x16x32_bf16 %[c10], a[8:11], a[40:43], %[c10]
ds_read_b128 a[52:55], v42 offset:2048
v_mfma_f32_16x16x32_bf16 %[c11], a[12:15], a[40:43], %[c11]
ds_read_b64_tr_b16 a[28:29], v46 offset:8704
ds_read_b64_tr_b16 a[30:31], v48 offset:8704
buffer_load_dwordx4 v34, s[36:39], s45 offen offset:1024 lds
v_mfma_f32_16x16x32_bf16 %[c12], a[0:3], a[44:47], %[c12]
v_mfma_f32_16x16x32_bf16 %[c13], a[4:7], a[44:47], %[c13]
buffer_load_dwordx4 v35, s[36:39], s45 offen offset:2048 lds
v_mfma_f32_16x16x32_bf16 %[c14], a[8:11], a[44:47], %[c14]
ds_read_b128 a[56:59], v42 offset:4096
v_mfma_f32_16x16x32_bf16 %[c15], a[12:15], a[44:47], %[c15]
buffer_load_dwordx4 v36, s[36:39], s45 offen offset:3072 lds
s_waitcnt lgkmcnt(1)
v_mfma_f32_16x16x32_bf16 %[c0], a[16:19], a[48:51], %[c0]
v_mfma_f32_16x16x32_bf16 %[c1], a[20:23], a[48:51], %[c1]
v_mfma_f32_16x16x32_bf16 %[c2], a[24:27], a[48:51], %[c2]
s_add_u32 m0, s44, 147456
ds_read_b128 a[60:63], v42 offset:6144
v_mfma_f32_16x16x32_bf16 %[c3], a[28:31], a[48:51], %[c3]
v_mfma_f32_16x16x32_bf16 %[c4], a[16:19], a[52:55], %[c4]
ds_read_b64_tr_b16 a[0:1], v45 offset:32768
ds_read_b64_tr_b16 a[2:3], v47 offset:32768
buffer_load_dwordx4 v32, s[40:43], s46 offen sc1 lds
v_mfma_f32_16x16x32_bf16 %[c5], a[20:23], a[52:55], %[c5]
ds_read_b64_tr_b16 a[4:5], v46 offset:32768
ds_read_b64_tr_b16 a[6:7], v48 offset:32768
v_mfma_f32_16x16x32_bf16 %[c6], a[24:27], a[52:55], %[c6]
ds_read_b128 a[32:35], v39 offset:32768
buffer_load_dwordx4 v32, s[40:43], s46 offen offset:1024 sc1 lds
v_mfma_f32_16x16x32_bf16 %[c7], a[28:31], a[52:55], %[c7]
s_waitcnt lgkmcnt(5)
v_mfma_f32_16x16x32_bf16 %[c8], a[16:19], a[56:59], %[c8]
s_add_u32 s46, s46, %[wadv]
ds_read_b64_tr_b16 a[8:9], v45 offset:33280
ds_read_b64_tr_b16 a[10:11], v47 offset:33280
v_mfma_f32_16x16x32_bf16 %[c9], a[20:23], a[56:59], %[c9]
v_mfma_f32_16x16x32_bf16 %[c10], a[24:27], a[56:59], %[c10]
s_add_u32 m0, s44, 155648
ds_read_b128 a[36:39], v39 offset:34816
v_mfma_f32_16x16x32_bf16 %[c11], a[28:31], a[56:59], %[c11]
ds_read_b64_tr_b16 a[12:13], v46 offset:33280
ds_read_b64_tr_b16 a[14:15], v48 offset:33280
v_mfma_f32_16x16x32_bf16 %[c12], a[16:19], a[60:63], %[c12]
v_mfma_f32_16x16x32_bf16 %[c13], a[20:23], a[60:63], %[c13]
buffer_load_dwordx4 v32, s[40:43], s46 offen sc1 lds
v_mfma_f32_16x16x32_bf16 %[c14], a[24:27], a[60:63], %[c14]
ds_read_b128 a[40:43], v39 offset:36864
v_mfma_f32_16x16x32_bf16 %[c15], a[28:31], a[60:63], %[c15]
buffer_load_dwordx4 v32, s[40:43], s46 offen offset:1024 sc1 lds
s_waitcnt lgkmcnt(1)
v_mfma_f32_16x16x32_bf16 %[c0], a[0:3], a[32:35], %[c0]
v_mfma_f32_16x16x32_bf16 %[c1], a[4:7], a[32:35], %[c1]
s_add_u32 s46, s46, %[wadv]
v_mfma_f32_16x16x32_bf16 %[c2], a[8:11], a[32:35], %[c2]
ds_read_b128 a[44:47], v39 offset:38912
v_mfma_f32_16x16x32_bf16 %[c3], a[12:15], a[32:35], %[c3]
s_add_u32 s45, s45, %[xadv]
v_mfma_f32_16x16x32_bf16 %[c4], a[0:3], a[36:39], %[c4]
ds_read_b64_tr_b16 a[16:17], v45 offset:40960
ds_read_b64_tr_b16 a[18:19], v47 offset:40960
v_mfma_f32_16x16x32_bf16 %[c5], a[4:7], a[36:39], %[c5]
ds_read_b64_tr_b16 a[20:21], v46 offset:40960
ds_read_b64_tr_b16 a[22:23], v48 offset:40960
v_mfma_f32_16x16x32_bf16 %[c6], a[8:11], a[36:39], %[c6]
s_waitcnt vmcnt(16)
s_barrier
ds_read_b128 a[48:51], v42 offset:32768
v_mfma_f32_16x16x32_bf16 %[c7], a[12:15], a[36:39], %[c7]
s_add_u32 m0, s50, 0
s_waitcnt lgkmcnt(5)
v_mfma_f32_16x16x32_bf16 %[c8], a[0:3], a[40:43], %[c8]
ds_read_b64_tr_b16 a[24:25], v45 offset:41472
ds_read_b64_tr_b16 a[26:27], v47 offset:41472
v_mfma_f32_16x16x32_bf16 %[c9], a[4:7], a[40:43], %[c9]
buffer_load_dwordx4 v33, s[36:39], s45 offen lds
v_mfma_f32_16x16x32_bf16 %[c10], a[8:11], a[40:43], %[c10]
ds_read_b128 a[52:55], v42 offset:34816
v_mfma_f32_16x16x32_bf16 %[c11], a[12:15], a[40:43], %[c11]
ds_read_b64_tr_b16 a[28:29], v46 offset:41472
ds_read_b64_tr_b16 a[30:31], v48 offset:41472
buffer_load_dwordx4 v34, s[36:39], s45 offen offset:1024 lds
v_mfma_f32_16x16x32_bf16 %[c12], a[0:3], a[44:47], %[c12]
v_mfma_f32_16x16x32_bf16 %[c13], a[4:7], a[44:47], %[c13]
buffer_load_dwordx4 v35, s[36:39], s45 offen offset:2048 lds
v_mfma_f32_16x16x32_bf16 %[c14], a[8:11], a[44:47], %[c14]
ds_read_b128 a[56:59], v42 offset:36864
v_mfma_f32_16x16x32_bf16 %[c15], a[12:15], a[44:47], %[c15]
buffer_load_dwordx4 v36, s[36:39], s45 offen offset:3072 lds
s_waitcnt lgkmcnt(1)
v_mfma_f32_16x16x32_bf16 %[c0], a[16:19], a[48:51], %[c0]
v_mfma_f32_16x16x32_bf16 %[c1], a[20:23], a[48:51], %[c1]
v_mfma_f32_16x16x32_bf16 %[c2], a[24:27], a[48:51], %[c2]
)ASM"
R"ASM(s_add_u32 m0, s44, 16384
ds_read_b128 a[60:63], v42 offset:38912
v_mfma_f32_16x16x32_bf16 %[c3], a[28:31], a[48:51], %[c3]
v_mfma_f32_16x16x32_bf16 %[c4], a[16:19], a[52:55], %[c4]
ds_read_b64_tr_b16 a[0:1], v49
ds_read_b64_tr_b16 a[2:3], v51
buffer_load_dwordx4 v32, s[40:43], s46 offen sc1 lds
v_mfma_f32_16x16x32_bf16 %[c5], a[20:23], a[52:55], %[c5]
ds_read_b64_tr_b16 a[4:5], v50
ds_read_b64_tr_b16 a[6:7], v52
v_mfma_f32_16x16x32_bf16 %[c6], a[24:27], a[52:55], %[c6]
ds_read_b128 a[32:35], v40
buffer_load_dwordx4 v32, s[40:43], s46 offen offset:1024 sc1 lds
v_mfma_f32_16x16x32_bf16 %[c7], a[28:31], a[52:55], %[c7]
s_waitcnt lgkmcnt(5)
v_mfma_f32_16x16x32_bf16 %[c8], a[16:19], a[56:59], %[c8]
s_add_u32 s46, s46, %[wadv]
ds_read_b64_tr_b16 a[8:9], v49 offset:512
ds_read_b64_tr_b16 a[10:11], v51 offset:512
v_mfma_f32_16x16x32_bf16 %[c9], a[20:23], a[56:59], %[c9]
v_mfma_f32_16x16x32_bf16 %[c10], a[24:27], a[56:59], %[c10]
s_add_u32 m0, s44, 24576
ds_read_b128 a[36:39], v40 offset:2048
v_mfma_f32_16x16x32_bf16 %[c11], a[28:31], a[56:59], %[c11]
ds_read_b64_tr_b16 a[12:13], v50 offset:512
ds_read_b64_tr_b16 a[14:15], v52 offset:512
v_mfma_f32_16x16x32_bf16 %[c12], a[16:19], a[60:63], %[c12]
v_mfma_f32_16x16x32_bf16 %[c13], a[20:23], a[60:63], %[c13]
buffer_load_dwordx4 v32, s[40:43], s46 offen sc1 lds
v_mfma_f32_16x16x32_bf16 %[c14], a[24:27], a[60:63], %[c14]
ds_read_b128 a[40:43], v40 offset:4096
v_mfma_f32_16x16x32_bf16 %[c15], a[28:31], a[60:63], %[c15]
buffer_load_dwordx4 v32, s[40:43], s46 offen offset:1024 sc1 lds
s_waitcnt lgkmcnt(1)
v_mfma_f32_16x16x32_bf16 %[c0], a[0:3], a[32:35], %[c0]
v_mfma_f32_16x16x32_bf16 %[c1], a[4:7], a[32:35], %[c1]
s_add_u32 s46, s46, %[wadv]
v_mfma_f32_16x16x32_bf16 %[c2], a[8:11], a[32:35], %[c2]
ds_read_b128 a[44:47], v40 offset:6144
v_mfma_f32_16x16x32_bf16 %[c3], a[12:15], a[32:35], %[c3]
s_add_u32 s45, s45, %[xadv]
v_mfma_f32_16x16x32_bf16 %[c4], a[0:3], a[36:39], %[c4]
ds_read_b64_tr_b16 a[16:17], v49 offset:8192
ds_read_b64_tr_b16 a[18:19], v51 offset:8192
v_mfma_f32_16x16x32_bf16 %[c5], a[4:7], a[36:39], %[c5]
ds_read_b64_tr_b16 a[20:21], v50 offset:8192
ds_read_b64_tr_b16 a[22:23], v52 offset:8192
v_mfma_f32_16x16x32_bf16 %[c6], a[8:11], a[36:39], %[c6]
s_waitcnt vmcnt(16)
s_barrier
ds_read_b128 a[48:51], v43
v_mfma_f32_16x16x32_bf16 %[c7], a[12:15], a[36:39], %[c7]
s_add_u32 m0, s50, 32768
s_waitcnt lgkmcnt(5)
v_mfma_f32_16x16x32_bf16 %[c8], a[0:3], a[40:43], %[c8]
ds_read_b64_tr_b16 a[24:25], v49 offset:8704
ds_read_b64_tr_b16 a[26:27], v51 offset:8704
v_mfma_f32_16x16x32_bf16 %[c9], a[4:7], a[40:43], %[c9]
buffer_load_dwordx4 v33, s[36:39], s45 offen lds
v_mfma_f32_16x16x32_bf16 %[c10], a[8:11], a[40:43], %[c10]
ds_read_b128 a[52:55], v43 offset:2048
v_mfma_f32_16x16x32_bf16 %[c11], a[12:15], a[40:43], %[c11]
ds_read_b64_tr_b16 a[28:29], v50 offset:8704
ds_read_b64_tr_b16 a[30:31], v52 offset:8704
buffer_load_dwordx4 v34, s[36:39], s45 offen offset:1024 lds
v_mfma_f32_16x16x32_bf16 %[c12], a[0:3], a[44:47], %[c12]
v_mfma_f32_16x16x32_bf16 %[c13], a[4:7], a[44:47], %[c13]
buffer_load_dwordx4 v35, s[36:39], s45 offen offset:2048 lds
v_mfma_f32_16x16x32_bf16 %[c14], a[8:11], a[44:47], %[c14]
ds_read_b128 a[56:59], v43 offset:4096
v_mfma_f32_16x16x32_bf16 %[c15], a[12:15], a[44:47], %[c15]
buffer_load_dwordx4 v36, s[36:39], s45 offen offset:3072 lds
s_waitcnt lgkmcnt(1)
v_mfma_f32_16x16x32_bf16 %[c0], a[16:19], a[48:51], %[c0]
v_mfma_f32_16x16x32_bf16 %[c1], a[20:23], a[48:51], %[c1]
v_mfma_f32_16x16x32_bf16 %[c2], a[24:27], a[48:51], %[c2]
s_add_u32 m0, s44, 49152
ds_read_b128 a[60:63], v43 offset:6144
v_mfma_f32_16x16x32_bf16 %[c3], a[28:31], a[48:51], %[c3]
v_mfma_f32_16x16x32_bf16 %[c4], a[16:19], a[52:55], %[c4]
ds_read_b64_tr_b16 a[0:1], v49 offset:32768
ds_read_b64_tr_b16 a[2:3], v51 offset:32768
buffer_load_dwordx4 v32, s[40:43], s46 offen sc1 lds
v_mfma_f32_16x16x32_bf16 %[c5], a[20:23], a[52:55], %[c5]
ds_read_b64_tr_b16 a[4:5], v50 offset:32768
ds_read_b64_tr_b16 a[6:7], v52 offset:32768
v_mfma_f32_16x16x32_bf16 %[c6], a[24:27], a[52:55], %[c6]
ds_read_b128 a[32:35], v40 offset:32768
buffer_load_dwordx4 v32, s[40:43], s46 offen offset:1024 sc1 lds
v_mfma_f32_16x16x32_bf16 %[c7], a[28:31], a[52:55], %[c7]
s_waitcnt lgkmcnt(5)
v_mfma_f32_16x16x32_bf16 %[c8], a[16:19], a[56:59], %[c8]
s_add_u32 s46, s46, %[wadv]
ds_read_b64_tr_b16 a[8:9], v49 offset:33280
ds_read_b64_tr_b16 a[10:11], v51 offset:33280
v_mfma_f32_16x16x32_bf16 %[c9], a[20:23], a[56:59], %[c9]
v_mfma_f32_16x16x32_bf16 %[c10], a[24:27], a[56:59], %[c10]
s_add_u32 m0, s44, 57344
ds_read_b128 a[36:39], v40 offset:34816
v_mfma_f32_16x16x32_bf16 %[c11], a[28:31], a[56:59], %[c11]
ds_read_b64_tr_b16 a[12:13], v50 offset:33280
ds_read_b64_tr_b16 a[14:15], v52 offset:33280
v_mfma_f32_16x16x32_bf16 %[c12], a[16:19], a[60:63], %[c12]
v_mfma_f32_16x16x32_bf16 %[c13], a[20:23], a[60:63], %[c13]
buffer_load_dwordx4 v32, s[40:43], s46 offen sc1 lds
v_mfma_f32_16x16x32_bf16 %[c14], a[24:27], a[60:63], %[c14]
ds_read_b128 a[40:43], v40 offset:36864
v_mfma_f32_16x16x32_bf16 %[c15], a[28:31], a[60:63], %[c15]
buffer_load_dwordx4 v32, s[40:43], s46 offen offset:1024 sc1 lds
s_waitcnt lgkmcnt(1)
v_mfma_f32_16x16x32_bf16 %[c0], a[0:3], a[32:35], %[c0]
v_mfma_f32_16x16x32_bf16 %[c1], a[4:7], a[32:35], %[c1]
s_add_u32 s46, s46, %[wadv]
v_mfma_f32_16x16x32_bf16 %[c2], a[8:11], a[32:35], %[c2]
ds_read_b128 a[44:47], v40 offset:38912
v_mfma_f32_16x16x32_bf16 %[c3], a[12:15], a[32:35], %[c3]
s_add_u32 s45, s45, %[xadv]
v_mfma_f32_16x16x32_bf16 %[c4], a[0:3], a[36:39], %[c4]
ds_read_b64_tr_b16 a[16:17], v49 offset:40960
ds_read_b64_tr_b16 a[18:19], v51 offset:40960
v_mfma_f32_16x16x32_bf16 %[c5], a[4:7], a[36:39], %[c5]
ds_read_b64_tr_b16 a[20:21], v50 offset:40960
ds_read_b64_tr_b16 a[22:23], v52 offset:40960
v_mfma_f32_16x16x32_bf16 %[c6], a[8:11], a[36:39], %[c6]
s_waitcnt vmcnt(16)
s_barrier
ds_read_b128 a[48:51], v43 offset:32768
v_mfma_f32_16x16x32_bf16 %[c7], a[12:15], a[36:39], %[c7]
s_add_u32 m0, s50, 65536
s_waitcnt lgkmcnt(5)
v_mfma_f32_16x16x32_bf16 %[c8], a[0:3], a[40:43], %[c8]
ds_read_b64_tr_b16 a[24:25], v49 offset:41472
ds_read_b64_tr_b16 a[26:27], v51 offset:41472
v_mfma_f32_16x16x32_bf16 %[c9], a[4:7], a[40:43], %[c9]
buffer_load_dwordx4 v33, s[36:39], s45 offen lds
v_mfma_f32_16x16x32_bf16 %[c10], a[8:11], a[40:43], %[c10]
ds_read_b128 a[52:55], v43 offset:34816
v_mfma_f32_16x16x32_bf16 %[c11], a[12:15], a[40:43], %[c11]
ds_read_b64_tr_b16 a[28:29], v50 offset:41472
ds_read_b64_tr_b16 a[30:31], v52 offset:41472
buffer_load_dwordx4 v34, s[36:39], s45 offen offset:1024 lds
v_mfma_f32_16x16x32_bf16 %[c12], a[0:3], a[44:47], %[c12]
v_mfma_f32_16x16x32_bf16 %[c13], a[4:7], a[44:47], %[c13]
buffer_load_dwordx4 v35, s[36:39], s45 offen offset:2048 lds
v_mfma_f32_16x16x32_bf16 %[c14], a[8:11], a[44:47], %[c14]
ds_read_b128 a[56:59], v43 offset:36864
v_mfma_f32_16x16x32_bf16 %[c15], a[12:15], a[44:47], %[c15]
buffer_load_dwordx4 v36, s[36:39], s45 offen offset:3072 lds
s_waitcnt lgkmcnt(1)
v_mfma_f32_16x16x32_bf16 %[c0], a[16:19], a[48:51], %[c0]
v_mfma_f32_16x16x32_bf16 %[c1], a[20:23], a[48:51], %[c1]
v_mfma_f32_16x16x32_bf16 %[c2], a[24:27], a[48:51], %[c2]
s_add_u32 m0, s44, 81920
ds_read_b128 a[60:63], v43 offset:38912
v_mfma_f32_16x16x32_bf16 %[c3], a[28:31], a[48:51], %[c3]
v_mfma_f32_16x16x32_bf16 %[c4], a[16:19], a[52:55], %[c4]
ds_read_b64_tr_b16 a[0:1], v53
ds_read_b64_tr_b16 a[2:3], v55
buffer_load_dwordx4 v32, s[40:43], s46 offen sc1 lds
v_mfma_f32_16x16x32_bf16 %[c5], a[20:23], a[52:55], %[c5]
ds_read_b64_tr_b16 a[4:5], v54
ds_read_b64_tr_b16 a[6:7], v56
v_mfma_f32_16x16x32_bf16 %[c6], a[24:27], a[52:55], %[c6]
ds_read_b128 a[32:35], v41
buffer_load_dwordx4 v32, s[40:43], s46 offen offset:1024 sc1 lds
v_mfma_f32_16x16x32_bf16 %[c7], a[28:31], a[52:55], %[c7]
s_waitcnt lgkmcnt(5)
v_mfma_f32_16x16x32_bf16 %[c8], a[16:19], a[56:59], %[c8]
s_add_u32 s46, s46, %[wadv]
ds_read_b64_tr_b16 a[8:9], v53 offset:512
ds_read_b64_tr_b16 a[10:11], v55 offset:512
v_mfma_f32_16x16x32_bf16 %[c9], a[20:23], a[56:59], %[c9]
v_mfma_f32_16x16x32_bf16 %[c10], a[24:27], a[56:59], %[c10]
s_add_u32 m0, s44, 90112
ds_read_b128 a[36:39], v41 offset:2048
v_mfma_f32_16x16x32_bf16 %[c11], a[28:31], a[56:59], %[c11]
ds_read_b64_tr_b16 a[12:13], v54 offset:512
ds_read_b64_tr_b16 a[14:15], v56 offset:512
v_mfma_f32_16x16x32_bf16 %[c12], a[16:19], a[60:63], %[c12]
v_mfma_f32_16x16x32_bf16 %[c13], a[20:23], a[60:63], %[c13]
buffer_load_dwordx4 v32, s[40:43], s46 offen sc1 lds
v_mfma_f32_16x16x32_bf16 %[c14], a[24:27], a[60:63], %[c14]
ds_read_b128 a[40:43], v41 offset:4096
v_mfma_f32_16x16x32_bf16 %[c15], a[28:31], a[60:63], %[c15]
buffer_load_dwordx4 v32, s[40:43], s46 offen offset:1024 sc1 lds
s_waitcnt lgkmcnt(1)
v_mfma_f32_16x16x32_bf16 %[c0], a[0:3], a[32:35], %[c0]
v_mfma_f32_16x16x32_bf16 %[c1], a[4:7], a[32:35], %[c1]
s_add_u32 s46, s46, %[wadv]
v_mfma_f32_16x16x32_bf16 %[c2], a[8:11], a[32:35], %[c2]
ds_read_b128 a[44:47], v41 offset:6144
v_mfma_f32_16x16x32_bf16 %[c3], a[12:15], a[32:35], %[c3]
s_add_u32 s45, s45, %[xadv]
v_mfma_f32_16x16x32_bf16 %[c4], a[0:3], a[36:39], %[c4]
ds_read_b64_tr_b16 a[16:17], v53 offset:8192
ds_read_b64_tr_b16 a[18:19], v55 offset:8192
v_mfma_f32_16x16x32_bf16 %[c5], a[4:7], a[36:39], %[c5]
ds_read_b64_tr_b16 a[20:21], v54 offset:8192
ds_read_b64_tr_b16 a[22:23], v56 offset:8192
v_mfma_f32_16x16x32_bf16 %[c6], a[8:11], a[36:39], %[c6]
s_waitcnt vmcnt(16)
s_barrier
ds_read_b128 a[48:51], v44
v_mfma_f32_16x16x32_bf16 %[c7], a[12:15], a[36:39], %[c7]
s_add_u32 m0, s50, 98304
s_waitcnt lgkmcnt(5)
v_mfma_f32_16x16x32_bf16 %[c8], a[0:3], a[40:43], %[c8]
ds_read_b64_tr_b16 a[24:25], v53 offset:8704
ds_read_b64_tr_b16 a[26:27], v55 offset:8704
v_mfma_f32_16x16x32_bf16 %[c9], a[4:7], a[40:43], %[c9]
buffer_load_dwordx4 v33, s[36:39], s45 offen lds
v_mfma_f32_16x16x32_bf16 %[c10], a[8:11], a[40:43], %[c10]
ds_read_b128 a[52:55], v44 offset:2048
v_mfma_f32_16x16x32_bf16 %[c11], a[12:15], a[40:43], %[c11]
ds_read_b64_tr_b16 a[28:29], v54 offset:8704
ds_read_b64_tr_b16 a[30:31], v56 offset:8704
buffer_load_dwordx4 v34, s[36:39], s45 offen offset:1024 lds
v_mfma_f32_16x16x32_bf16 %[c12], a[0:3], a[44:47], %[c12]
v_mfma_f32_16x16x32_bf16 %[c13], a[4:7], a[44:47], %[c13]
buffer_load_dwordx4 v35, s[36:39], s45 offen offset:2048 lds
v_mfma_f32_16x16x32_bf16 %[c14], a[8:11], a[44:47], %[c14]
ds_read_b128 a[56:59], v44 offset:4096
v_mfma_f32_16x16x32_bf16 %[c15], a[12:15], a[44:47], %[c15]
buffer_load_dwordx4 v36, s[36:39], s45 offen offset:3072 lds
s_waitcnt lgkmcnt(1)
v_mfma_f32_16x16x32_bf16 %[c0], a[16:19], a[48:51], %[c0]
v_mfma_f32_16x16x32_bf16 %[c1], a[20:23], a[48:51], %[c1]
v_mfma_f32_16x16x32_bf16 %[c2], a[24:27], a[48:51], %[c2]
s_add_u32 m0, s44, 114688
ds_read_b128 a[60:63], v44 offset:6144
v_mfma_f32_16x16x32_bf16 %[c3], a[28:31], a[48:51], %[c3]
v_mfma_f32_16x16x32_bf16 %[c4], a[16:19], a[52:55], %[c4]
ds_read_b64_tr_b16 a[0:1], v45
ds_read_b64_tr_b16 a[2:3], v47
buffer_load_dwordx4 v32, s[40:43], s46 offen sc1 lds
v_mfma_f32_16x16x32_bf16 %[c5], a[20:23], a[52:55], %[c5]
ds_read_b64_tr_b16 a[4:5], v46
ds_read_b64_tr_b16 a[6:7], v48
v_mfma_f32_16x16x32_bf16 %[c6], a[24:27], a[52:55], %[c6]
ds_read_b128 a[32:35], v39
buffer_load_dwordx4 v32, s[40:43], s46 offen offset:1024 sc1 lds
v_mfma_f32_16x16x32_bf16 %[c7], a[28:31], a[52:55], %[c7]
s_waitcnt lgkmcnt(5)
v_mfma_f32_16x16x32_bf16 %[c8], a[16:19], a[56:59], %[c8]
s_add_u32 s46, s46, %[wadv]
ds_read_b64_tr_b16 a[8:9], v45 offset:512
ds_read_b64_tr_b16 a[10:11], v47 offset:512
v_mfma_f32_16x16x32_bf16 %[c9], a[20:23], a[56:59], %[c9]
v_mfma_f32_16x16x32_bf16 %[c10], a[24:27], a[56:59], %[c10]
s_add_u32 m0, s44, 122880
ds_read_b128 a[36:39], v39 offset:2048
v_mfma_f32_16x16x32_bf16 %[c11], a[28:31], a[56:59], %[c11]
ds_read_b64_tr_b16 a[12:13], v46 offset:512
ds_read_b64_tr_b16 a[14:15], v48 offset:512
v_mfma_f32_16x16x32_bf16 %[c12], a[16:19], a[60:63], %[c12]
v_mfma_f32_16x16x32_bf16 %[c13], a[20:23], a[60:63], %[c13]
)ASM"
R"ASM(buffer_load_dwordx4 v32, s[40:43], s46 offen sc1 lds
v_mfma_f32_16x16x32_bf16 %[c14], a[24:27], a[60:63], %[c14]
ds_read_b128 a[40:43], v39 offset:4096
v_mfma_f32_16x16x32_bf16 %[c15], a[28:31], a[60:63], %[c15]
buffer_load_dwordx4 v32, s[40:43], s46 offen offset:1024 sc1 lds
s_waitcnt lgkmcnt(1)
v_mfma_f32_16x16x32_bf16 %[c0], a[0:3], a[32:35], %[c0]
v_mfma_f32_16x16x32_bf16 %[c1], a[4:7], a[32:35], %[c1]
s_add_u32 s46, s46, %[wadv]
v_mfma_f32_16x16x32_bf16 %[c2], a[8:11], a[32:35], %[c2]
ds_read_b128 a[44:47], v39 offset:6144
v_mfma_f32_16x16x32_bf16 %[c3], a[12:15], a[32:35], %[c3]
s_add_u32 s45, s45, %[xadv]
v_mfma_f32_16x16x32_bf16 %[c4], a[0:3], a[36:39], %[c4]
ds_read_b64_tr_b16 a[16:17], v45 offset:8192
ds_read_b64_tr_b16 a[18:19], v47 offset:8192
v_mfma_f32_16x16x32_bf16 %[c5], a[4:7], a[36:39], %[c5]
ds_read_b64_tr_b16 a[20:21], v46 offset:8192
ds_read_b64_tr_b16 a[22:23], v48 offset:8192
v_mfma_f32_16x16x32_bf16 %[c6], a[8:11], a[36:39], %[c6]
s_waitcnt vmcnt(16)
s_barrier
ds_read_b128 a[48:51], v42
v_mfma_f32_16x16x32_bf16 %[c7], a[12:15], a[36:39], %[c7]
s_add_u32 m0, s50, 131072
s_waitcnt lgkmcnt(5)
v_mfma_f32_16x16x32_bf16 %[c8], a[0:3], a[40:43], %[c8]
ds_read_b64_tr_b16 a[24:25], v45 offset:8704
ds_read_b64_tr_b16 a[26:27], v47 offset:8704
v_mfma_f32_16x16x32_bf16 %[c9], a[4:7], a[40:43], %[c9]
buffer_load_dwordx4 v33, s[36:39], s45 offen lds
v_mfma_f32_16x16x32_bf16 %[c10], a[8:11], a[40:43], %[c10]
ds_read_b128 a[52:55], v42 offset:2048
v_mfma_f32_16x16x32_bf16 %[c11], a[12:15], a[40:43], %[c11]
ds_read_b64_tr_b16 a[28:29], v46 offset:8704
ds_read_b64_tr_b16 a[30:31], v48 offset:8704
buffer_load_dwordx4 v34, s[36:39], s45 offen offset:1024 lds
v_mfma_f32_16x16x32_bf16 %[c12], a[0:3], a[44:47], %[c12]
v_mfma_f32_16x16x32_bf16 %[c13], a[4:7], a[44:47], %[c13]
buffer_load_dwordx4 v35, s[36:39], s45 offen offset:2048 lds
v_mfma_f32_16x16x32_bf16 %[c14], a[8:11], a[44:47], %[c14]
ds_read_b128 a[56:59], v42 offset:4096
v_mfma_f32_16x16x32_bf16 %[c15], a[12:15], a[44:47], %[c15]
buffer_load_dwordx4 v36, s[36:39], s45 offen offset:3072 lds
s_waitcnt lgkmcnt(1)
v_mfma_f32_16x16x32_bf16 %[c0], a[16:19], a[48:51], %[c0]
v_mfma_f32_16x16x32_bf16 %[c1], a[20:23], a[48:51], %[c1]
v_mfma_f32_16x16x32_bf16 %[c2], a[24:27], a[48:51], %[c2]
s_add_u32 m0, s44, 147456
ds_read_b128 a[60:63], v42 offset:6144
v_mfma_f32_16x16x32_bf16 %[c3], a[28:31], a[48:51], %[c3]
v_mfma_f32_16x16x32_bf16 %[c4], a[16:19], a[52:55], %[c4]
ds_read_b64_tr_b16 a[0:1], v45 offset:32768
ds_read_b64_tr_b16 a[2:3], v47 offset:32768
buffer_load_dwordx4 v32, s[40:43], s46 offen sc1 lds
v_mfma_f32_16x16x32_bf16 %[c5], a[20:23], a[52:55], %[c5]
ds_read_b64_tr_b16 a[4:5], v46 offset:32768
ds_read_b64_tr_b16 a[6:7], v48 offset:32768
v_mfma_f32_16x16x32_bf16 %[c6], a[24:27], a[52:55], %[c6]
ds_read_b128 a[32:35], v39 offset:32768
buffer_load_dwordx4 v32, s[40:43], s46 offen offset:1024 sc1 lds
v_mfma_f32_16x16x32_bf16 %[c7], a[28:31], a[52:55], %[c7]
s_waitcnt lgkmcnt(5)
v_mfma_f32_16x16x32_bf16 %[c8], a[16:19], a[56:59], %[c8]
s_add_u32 s46, s46, %[wadv]
ds_read_b64_tr_b16 a[8:9], v45 offset:33280
ds_read_b64_tr_b16 a[10:11], v47 offset:33280
v_mfma_f32_16x16x32_bf16 %[c9], a[20:23], a[56:59], %[c9]
v_mfma_f32_16x16x32_bf16 %[c10], a[24:27], a[56:59], %[c10]
s_add_u32 m0, s44, 155648
ds_read_b128 a[36:39], v39 offset:34816
v_mfma_f32_16x16x32_bf16 %[c11], a[28:31], a[56:59], %[c11]
ds_read_b64_tr_b16 a[12:13], v46 offset:33280
ds_read_b64_tr_b16 a[14:15], v48 offset:33280
v_mfma_f32_16x16x32_bf16 %[c12], a[16:19], a[60:63], %[c12]
v_mfma_f32_16x16x32_bf16 %[c13], a[20:23], a[60:63], %[c13]
buffer_load_dwordx4 v32, s[40:43], s46 offen sc1 lds
v_mfma_f32_16x16x32_bf16 %[c14], a[24:27], a[60:63], %[c14]
ds_read_b128 a[40:43], v39 offset:36864
v_mfma_f32_16x16x32_bf16 %[c15], a[28:31], a[60:63], %[c15]
buffer_load_dwordx4 v32, s[40:43], s46 offen offset:1024 sc1 lds
s_waitcnt lgkmcnt(1)
v_mfma_f32_16x16x32_bf16 %[c0], a[0:3], a[32:35], %[c0]
v_mfma_f32_16x16x32_bf16 %[c1], a[4:7], a[32:35], %[c1]
s_add_u32 s46, s46, %[wadv]
v_mfma_f32_16x16x32_bf16 %[c2], a[8:11], a[32:35], %[c2]
ds_read_b128 a[44:47], v39 offset:38912
v_mfma_f32_16x16x32_bf16 %[c3], a[12:15], a[32:35], %[c3]
s_add_u32 s45, s45, %[xadv]
v_mfma_f32_16x16x32_bf16 %[c4], a[0:3], a[36:39], %[c4]
ds_read_b64_tr_b16 a[16:17], v45 offset:40960
ds_read_b64_tr_b16 a[18:19], v47 offset:40960
v_mfma_f32_16x16x32_bf16 %[c5], a[4:7], a[36:39], %[c5]
ds_read_b64_tr_b16 a[20:21], v46 offset:40960
ds_read_b64_tr_b16 a[22:23], v48 offset:40960
v_mfma_f32_16x16x32_bf16 %[c6], a[8:11], a[36:39], %[c6]
s_waitcnt vmcnt(16)
s_barrier
ds_read_b128 a[48:51], v42 offset:32768
v_mfma_f32_16x16x32_bf16 %[c7], a[12:15], a[36:39], %[c7]
s_add_u32 m0, s50, 0
s_waitcnt lgkmcnt(5)
v_mfma_f32_16x16x32_bf16 %[c8], a[0:3], a[40:43], %[c8]
ds_read_b64_tr_b16 a[24:25], v45 offset:41472
ds_read_b64_tr_b16 a[26:27], v47 offset:41472
v_mfma_f32_16x16x32_bf16 %[c9], a[4:7], a[40:43], %[c9]
buffer_load_dwordx4 v33, s[36:39], s45 offen lds
v_mfma_f32_16x16x32_bf16 %[c10], a[8:11], a[40:43], %[c10]
ds_read_b128 a[52:55], v42 offset:34816
v_mfma_f32_16x16x32_bf16 %[c11], a[12:15], a[40:43], %[c11]
ds_read_b64_tr_b16 a[28:29], v46 offset:41472
ds_read_b64_tr_b16 a[30:31], v48 offset:41472
buffer_load_dwordx4 v34, s[36:39], s45 offen offset:1024 lds
v_mfma_f32_16x16x32_bf16 %[c12], a[0:3], a[44:47], %[c12]
v_mfma_f32_16x16x32_bf16 %[c13], a[4:7], a[44:47], %[c13]
buffer_load_dwordx4 v35, s[36:39], s45 offen offset:2048 lds
v_mfma_f32_16x16x32_bf16 %[c14], a[8:11], a[44:47], %[c14]
ds_read_b128 a[56:59], v42 offset:36864
v_mfma_f32_16x16x32_bf16 %[c15], a[12:15], a[44:47], %[c15]
buffer_load_dwordx4 v36, s[36:39], s45 offen offset:3072 lds
s_waitcnt lgkmcnt(1)
v_mfma_f32_16x16x32_bf16 %[c0], a[16:19], a[48:51], %[c0]
v_mfma_f32_16x16x32_bf16 %[c1], a[20:23], a[48:51], %[c1]
v_mfma_f32_16x16x32_bf16 %[c2], a[24:27], a[48:51], %[c2]
s_add_u32 m0, s44, 16384
ds_read_b128 a[60:63], v42 offset:38912
v_mfma_f32_16x16x32_bf16 %[c3], a[28:31], a[48:51], %[c3]
v_mfma_f32_16x16x32_bf16 %[c4], a[16:19], a[52:55], %[c4]
ds_read_b64_tr_b16 a[0:1], v49
ds_read_b64_tr_b16 a[2:3], v51
buffer_load_dwordx4 v32, s[40:43], s46 offen sc1 lds
v_mfma_f32_16x16x32_bf16 %[c5], a[20:23], a[52:55], %[c5]
ds_read_b64_tr_b16 a[4:5], v50
ds_read_b64_tr_b16 a[6:7], v52
v_mfma_f32_16x16x32_bf16 %[c6], a[24:27], a[52:55], %[c6]
ds_read_b128 a[32:35], v40
buffer_load_dwordx4 v32, s[40:43], s46 offen offset:1024 sc1 lds
v_mfma_f32_16x16x32_bf16 %[c7], a[28:31], a[52:55], %[c7]
s_waitcnt lgkmcnt(5)
v_mfma_f32_16x16x32_bf16 %[c8], a[16:19], a[56:59], %[c8]
s_add_u32 s46, s46, %[wadv]
ds_read_b64_tr_b16 a[8:9], v49 offset:512
ds_read_b64_tr_b16 a[10:11], v51 offset:512
v_mfma_f32_16x16x32_bf16 %[c9], a[20:23], a[56:59], %[c9]
v_mfma_f32_16x16x32_bf16 %[c10], a[24:27], a[56:59], %[c10]
s_add_u32 m0, s44, 24576
ds_read_b128 a[36:39], v40 offset:2048
v_mfma_f32_16x16x32_bf16 %[c11], a[28:31], a[56:59], %[c11]
ds_read_b64_tr_b16 a[12:13], v50 offset:512
ds_read_b64_tr_b16 a[14:15], v52 offset:512
v_mfma_f32_16x16x32_bf16 %[c12], a[16:19], a[60:63], %[c12]
v_mfma_f32_16x16x32_bf16 %[c13], a[20:23], a[60:63], %[c13]
buffer_load_dwordx4 v32, s[40:43], s46 offen sc1 lds
v_mfma_f32_16x16x32_bf16 %[c14], a[24:27], a[60:63], %[c14]
ds_read_b128 a[40:43], v40 offset:4096
v_mfma_f32_16x16x32_bf16 %[c15], a[28:31], a[60:63], %[c15]
buffer_load_dwordx4 v32, s[40:43], s46 offen offset:1024 sc1 lds
s_waitcnt lgkmcnt(1)
v_mfma_f32_16x16x32_bf16 %[c0], a[0:3], a[32:35], %[c0]
v_mfma_f32_16x16x32_bf16 %[c1], a[4:7], a[32:35], %[c1]
s_add_u32 s46, s46, %[wadv]
v_mfma_f32_16x16x32_bf16 %[c2], a[8:11], a[32:35], %[c2]
ds_read_b128 a[44:47], v40 offset:6144
v_mfma_f32_16x16x32_bf16 %[c3], a[12:15], a[32:35], %[c3]
s_add_u32 s45, s45, %[xadv]
v_mfma_f32_16x16x32_bf16 %[c4], a[0:3], a[36:39], %[c4]
ds_read_b64_tr_b16 a[16:17], v49 offset:8192
ds_read_b64_tr_b16 a[18:19], v51 offset:8192
v_mfma_f32_16x16x32_bf16 %[c5], a[4:7], a[36:39], %[c5]
ds_read_b64_tr_b16 a[20:21], v50 offset:8192
ds_read_b64_tr_b16 a[22:23], v52 offset:8192
v_mfma_f32_16x16x32_bf16 %[c6], a[8:11], a[36:39], %[c6]
s_waitcnt vmcnt(16)
s_barrier
ds_read_b128 a[48:51], v43
v_mfma_f32_16x16x32_bf16 %[c7], a[12:15], a[36:39], %[c7]
s_add_u32 m0, s50, 32768
s_waitcnt lgkmcnt(5)
v_mfma_f32_16x16x32_bf16 %[c8], a[0:3], a[40:43], %[c8]
ds_read_b64_tr_b16 a[24:25], v49 offset:8704
ds_read_b64_tr_b16 a[26:27], v51 offset:8704
v_mfma_f32_16x16x32_bf16 %[c9], a[4:7], a[40:43], %[c9]
buffer_load_dwordx4 v33, s[36:39], s45 offen lds
v_mfma_f32_16x16x32_bf16 %[c10], a[8:11], a[40:43], %[c10]
ds_read_b128 a[52:55], v43 offset:2048
v_mfma_f32_16x16x32_bf16 %[c11], a[12:15], a[40:43], %[c11]
ds_read_b64_tr_b16 a[28:29], v50 offset:8704
ds_read_b64_tr_b16 a[30:31], v52 offset:8704
buffer_load_dwordx4 v34, s[36:39], s45 offen offset:1024 lds
v_mfma_f32_16x16x32_bf16 %[c12], a[0:3], a[44:47], %[c12]
v_mfma_f32_16x16x32_bf16 %[c13], a[4:7], a[44:47], %[c13]
buffer_load_dwordx4 v35, s[36:39], s45 offen offset:2048 lds
v_mfma_f32_16x16x32_bf16 %[c14], a[8:11], a[44:47], %[c14]
ds_read_b128 a[56:59], v43 offset:4096
v_mfma_f32_16x16x32_bf16 %[c15], a[12:15], a[44:47], %[c15]
buffer_load_dwordx4 v36, s[36:39], s45 offen offset:3072 lds
s_waitcnt lgkmcnt(1)
v_mfma_f32_16x16x32_bf16 %[c0], a[16:19], a[48:51], %[c0]
v_mfma_f32_16x16x32_bf16 %[c1], a[20:23], a[48:51], %[c1]
v_mfma_f32_16x16x32_bf16 %[c2], a[24:27], a[48:51], %[c2]
s_add_u32 m0, s44, 49152
ds_read_b128 a[60:63], v43 offset:6144
v_mfma_f32_16x16x32_bf16 %[c3], a[28:31], a[48:51], %[c3]
v_mfma_f32_16x16x32_bf16 %[c4], a[16:19], a[52:55], %[c4]
ds_read_b64_tr_b16 a[0:1], v49 offset:32768
ds_read_b64_tr_b16 a[2:3], v51 offset:32768
buffer_load_dwordx4 v32, s[40:43], s46 offen sc1 lds
v_mfma_f32_16x16x32_bf16 %[c5], a[20:23], a[52:55], %[c5]
ds_read_b64_tr_b16 a[4:5], v50 offset:32768
ds_read_b64_tr_b16 a[6:7], v52 offset:32768
v_mfma_f32_16x16x32_bf16 %[c6], a[24:27], a[52:55], %[c6]
ds_read_b128 a[32:35], v40 offset:32768
buffer_load_dwordx4 v32, s[40:43], s46 offen offset:1024 sc1 lds
v_mfma_f32_16x16x32_bf16 %[c7], a[28:31], a[52:55], %[c7]
s_waitcnt lgkmcnt(5)
v_mfma_f32_16x16x32_bf16 %[c8], a[16:19], a[56:59], %[c8]
s_add_u32 s46, s46, %[wadv]
ds_read_b64_tr_b16 a[8:9], v49 offset:33280
ds_read_b64_tr_b16 a[10:11], v51 offset:33280
v_mfma_f32_16x16x32_bf16 %[c9], a[20:23], a[56:59], %[c9]
v_mfma_f32_16x16x32_bf16 %[c10], a[24:27], a[56:59], %[c10]
s_add_u32 m0, s44, 57344
ds_read_b128 a[36:39], v40 offset:34816
v_mfma_f32_16x16x32_bf16 %[c11], a[28:31], a[56:59], %[c11]
ds_read_b64_tr_b16 a[12:13], v50 offset:33280
ds_read_b64_tr_b16 a[14:15], v52 offset:33280
v_mfma_f32_16x16x32_bf16 %[c12], a[16:19], a[60:63], %[c12]
v_mfma_f32_16x16x32_bf16 %[c13], a[20:23], a[60:63], %[c13]
buffer_load_dwordx4 v32, s[40:43], s46 offen sc1 lds
v_mfma_f32_16x16x32_bf16 %[c14], a[24:27], a[60:63], %[c14]
ds_read_b128 a[40:43], v40 offset:36864
v_mfma_f32_16x16x32_bf16 %[c15], a[28:31], a[60:63], %[c15]
buffer_load_dwordx4 v32, s[40:43], s46 offen offset:1024 sc1 lds
s_waitcnt lgkmcnt(1)
v_mfma_f32_16x16x32_bf16 %[c0], a[0:3], a[32:35], %[c0]
v_mfma_f32_16x16x32_bf16 %[c1], a[4:7], a[32:35], %[c1]
s_add_u32 s46, s46, %[wadv]
v_mfma_f32_16x16x32_bf16 %[c2], a[8:11], a[32:35], %[c2]
ds_read_b128 a[44:47], v40 offset:38912
v_mfma_f32_16x16x32_bf16 %[c3], a[12:15], a[32:35], %[c3]
s_add_u32 s45, s45, %[xadv]
v_mfma_f32_16x16x32_bf16 %[c4], a[0:3], a[36:39], %[c4]
ds_read_b64_tr_b16 a[16:17], v49 offset:40960
ds_read_b64_tr_b16 a[18:19], v51 offset:40960
v_mfma_f32_16x16x32_bf16 %[c5], a[4:7], a[36:39], %[c5]
ds_read_b64_tr_b16 a[20:21], v50 offset:40960
ds_read_b64_tr_b16 a[22:23], v52 offset:40960
v_mfma_f32_16x16x32_bf16 %[c6], a[8:11], a[36:39], %[c6]
s_waitcnt vmcnt(16)
s_barrier
ds_read_b128 a[48:51], v43 offset:32768
v_mfma_f32_16x16x32_bf16 %[c7], a[12:15], a[36:39], %[c7]
s_waitcnt lgkmcnt(5)
v_mfma_f32_16x16x32_bf16 %[c8], a[0:3], a[40:43], %[c8]
ds_read_b64_tr_b16 a[24:25], v49 offset:41472
)ASM"
R"ASM(ds_read_b64_tr_b16 a[26:27], v51 offset:41472
v_mfma_f32_16x16x32_bf16 %[c9], a[4:7], a[40:43], %[c9]
v_mfma_f32_16x16x32_bf16 %[c10], a[8:11], a[40:43], %[c10]
ds_read_b128 a[52:55], v43 offset:34816
v_mfma_f32_16x16x32_bf16 %[c11], a[12:15], a[40:43], %[c11]
ds_read_b64_tr_b16 a[28:29], v50 offset:41472
ds_read_b64_tr_b16 a[30:31], v52 offset:41472
v_mfma_f32_16x16x32_bf16 %[c12], a[0:3], a[44:47], %[c12]
v_mfma_f32_16x16x32_bf16 %[c13], a[4:7], a[44:47], %[c13]
v_mfma_f32_16x16x32_bf16 %[c14], a[8:11], a[44:47], %[c14]
ds_read_b128 a[56:59], v43 offset:36864
v_mfma_f32_16x16x32_bf16 %[c15], a[12:15], a[44:47], %[c15]
s_waitcnt lgkmcnt(1)
v_mfma_f32_16x16x32_bf16 %[c0], a[16:19], a[48:51], %[c0]
v_mfma_f32_16x16x32_bf16 %[c1], a[20:23], a[48:51], %[c1]
v_mfma_f32_16x16x32_bf16 %[c2], a[24:27], a[48:51], %[c2]
ds_read_b128 a[60:63], v43 offset:38912
v_mfma_f32_16x16x32_bf16 %[c3], a[28:31], a[48:51], %[c3]
v_mfma_f32_16x16x32_bf16 %[c4], a[16:19], a[52:55], %[c4]
ds_read_b64_tr_b16 a[0:1], v53
ds_read_b64_tr_b16 a[2:3], v55
v_mfma_f32_16x16x32_bf16 %[c5], a[20:23], a[52:55], %[c5]
ds_read_b64_tr_b16 a[4:5], v54
ds_read_b64_tr_b16 a[6:7], v56
v_mfma_f32_16x16x32_bf16 %[c6], a[24:27], a[52:55], %[c6]
ds_read_b128 a[32:35], v41
v_mfma_f32_16x16x32_bf16 %[c7], a[28:31], a[52:55], %[c7]
s_waitcnt lgkmcnt(5)
v_mfma_f32_16x16x32_bf16 %[c8], a[16:19], a[56:59], %[c8]
ds_read_b64_tr_b16 a[8:9], v53 offset:512
ds_read_b64_tr_b16 a[10:11], v55 offset:512
v_mfma_f32_16x16x32_bf16 %[c9], a[20:23], a[56:59], %[c9]
v_mfma_f32_16x16x32_bf16 %[c10], a[24:27], a[56:59], %[c10]
ds_read_b128 a[36:39], v41 offset:2048
v_mfma_f32_16x16x32_bf16 %[c11], a[28:31], a[56:59], %[c11]
ds_read_b64_tr_b16 a[12:13], v54 offset:512
ds_read_b64_tr_b16 a[14:15], v56 offset:512
v_mfma_f32_16x16x32_bf16 %[c12], a[16:19], a[60:63], %[c12]
v_mfma_f32_16x16x32_bf16 %[c13], a[20:23], a[60:63], %[c13]
v_mfma_f32_16x16x32_bf16 %[c14], a[24:27], a[60:63], %[c14]
ds_read_b128 a[40:43], v41 offset:4096
v_mfma_f32_16x16x32_bf16 %[c15], a[28:31], a[60:63], %[c15]
s_waitcnt lgkmcnt(1)
v_mfma_f32_16x16x32_bf16 %[c0], a[0:3], a[32:35], %[c0]
v_mfma_f32_16x16x32_bf16 %[c1], a[4:7], a[32:35], %[c1]
v_mfma_f32_16x16x32_bf16 %[c2], a[8:11], a[32:35], %[c2]
ds_read_b128 a[44:47], v41 offset:6144
v_mfma_f32_16x16x32_bf16 %[c3], a[12:15], a[32:35], %[c3]
v_mfma_f32_16x16x32_bf16 %[c4], a[0:3], a[36:39], %[c4]
ds_read_b64_tr_b16 a[16:17], v53 offset:8192
ds_read_b64_tr_b16 a[18:19], v55 offset:8192
v_mfma_f32_16x16x32_bf16 %[c5], a[4:7], a[36:39], %[c5]
ds_read_b64_tr_b16 a[20:21], v54 offset:8192
ds_read_b64_tr_b16 a[22:23], v56 offset:8192
v_mfma_f32_16x16x32_bf16 %[c6], a[8:11], a[36:39], %[c6]
s_waitcnt vmcnt(8)
s_barrier
ds_read_b128 a[48:51], v44
v_mfma_f32_16x16x32_bf16 %[c7], a[12:15], a[36:39], %[c7]
s_waitcnt lgkmcnt(5)
v_mfma_f32_16x16x32_bf16 %[c8], a[0:3], a[40:43], %[c8]
ds_read_b64_tr_b16 a[24:25], v53 offset:8704
ds_read_b64_tr_b16 a[26:27], v55 offset:8704
v_mfma_f32_16x16x32_bf16 %[c9], a[4:7], a[40:43], %[c9]
v_mfma_f32_16x16x32_bf16 %[c10], a[8:11], a[40:43], %[c10]
ds_read_b128 a[52:55], v44 offset:2048
v_mfma_f32_16x16x32_bf16 %[c11], a[12:15], a[40:43], %[c11]
ds_read_b64_tr_b16 a[28:29], v54 offset:8704
ds_read_b64_tr_b16 a[30:31], v56 offset:8704
v_mfma_f32_16x16x32_bf16 %[c12], a[0:3], a[44:47], %[c12]
v_mfma_f32_16x16x32_bf16 %[c13], a[4:7], a[44:47], %[c13]
v_mfma_f32_16x16x32_bf16 %[c14], a[8:11], a[44:47], %[c14]
ds_read_b128 a[56:59], v44 offset:4096
v_mfma_f32_16x16x32_bf16 %[c15], a[12:15], a[44:47], %[c15]
s_waitcnt lgkmcnt(1)
v_mfma_f32_16x16x32_bf16 %[c0], a[16:19], a[48:51], %[c0]
v_mfma_f32_16x16x32_bf16 %[c1], a[20:23], a[48:51], %[c1]
v_mfma_f32_16x16x32_bf16 %[c2], a[24:27], a[48:51], %[c2]
ds_read_b128 a[60:63], v44 offset:6144
v_mfma_f32_16x16x32_bf16 %[c3], a[28:31], a[48:51], %[c3]
v_mfma_f32_16x16x32_bf16 %[c4], a[16:19], a[52:55], %[c4]
ds_read_b64_tr_b16 a[0:1], v45
ds_read_b64_tr_b16 a[2:3], v47
v_mfma_f32_16x16x32_bf16 %[c5], a[20:23], a[52:55], %[c5]
ds_read_b64_tr_b16 a[4:5], v46
ds_read_b64_tr_b16 a[6:7], v48
v_mfma_f32_16x16x32_bf16 %[c6], a[24:27], a[52:55], %[c6]
ds_read_b128 a[32:35], v39
v_mfma_f32_16x16x32_bf16 %[c7], a[28:31], a[52:55], %[c7]
s_waitcnt lgkmcnt(5)
v_mfma_f32_16x16x32_bf16 %[c8], a[16:19], a[56:59], %[c8]
ds_read_b64_tr_b16 a[8:9], v45 offset:512
ds_read_b64_tr_b16 a[10:11], v47 offset:512
v_mfma_f32_16x16x32_bf16 %[c9], a[20:23], a[56:59], %[c9]
v_mfma_f32_16x16x32_bf16 %[c10], a[24:27], a[56:59], %[c10]
ds_read_b128 a[36:39], v39 offset:2048
v_mfma_f32_16x16x32_bf16 %[c11], a[28:31], a[56:59], %[c11]
ds_read_b64_tr_b16 a[12:13], v46 offset:512
ds_read_b64_tr_b16 a[14:15], v48 offset:512
v_mfma_f32_16x16x32_bf16 %[c12], a[16:19], a[60:63], %[c12]
v_mfma_f32_16x16x32_bf16 %[c13], a[20:23], a[60:63], %[c13]
v_mfma_f32_16x16x32_bf16 %[c14], a[24:27], a[60:63], %[c14]
ds_read_b128 a[40:43], v39 offset:4096
v_mfma_f32_16x16x32_bf16 %[c15], a[28:31], a[60:63], %[c15]
s_waitcnt lgkmcnt(1)
v_mfma_f32_16x16x32_bf16 %[c0], a[0:3], a[32:35], %[c0]
v_mfma_f32_16x16x32_bf16 %[c1], a[4:7], a[32:35], %[c1]
v_mfma_f32_16x16x32_bf16 %[c2], a[8:11], a[32:35], %[c2]
ds_read_b128 a[44:47], v39 offset:6144
v_mfma_f32_16x16x32_bf16 %[c3], a[12:15], a[32:35], %[c3]
v_mfma_f32_16x16x32_bf16 %[c4], a[0:3], a[36:39], %[c4]
ds_read_b64_tr_b16 a[16:17], v45 offset:8192
ds_read_b64_tr_b16 a[18:19], v47 offset:8192
v_mfma_f32_16x16x32_bf16 %[c5], a[4:7], a[36:39], %[c5]
ds_read_b64_tr_b16 a[20:21], v46 offset:8192
ds_read_b64_tr_b16 a[22:23], v48 offset:8192
v_mfma_f32_16x16x32_bf16 %[c6], a[8:11], a[36:39], %[c6]
s_waitcnt vmcnt(0)
s_barrier
ds_read_b128 a[48:51], v42
v_mfma_f32_16x16x32_bf16 %[c7], a[12:15], a[36:39], %[c7]
s_waitcnt lgkmcnt(5)
v_mfma_f32_16x16x32_bf16 %[c8], a[0:3], a[40:43], %[c8]
ds_read_b64_tr_b16 a[24:25], v45 offset:8704
ds_read_b64_tr_b16 a[26:27], v47 offset:8704
v_mfma_f32_16x16x32_bf16 %[c9], a[4:7], a[40:43], %[c9]
v_mfma_f32_16x16x32_bf16 %[c10], a[8:11], a[40:43], %[c10]
ds_read_b128 a[52:55], v42 offset:2048
v_mfma_f32_16x16x32_bf16 %[c11], a[12:15], a[40:43], %[c11]
ds_read_b64_tr_b16 a[28:29], v46 offset:8704
ds_read_b64_tr_b16 a[30:31], v48 offset:8704
v_mfma_f32_16x16x32_bf16 %[c12], a[0:3], a[44:47], %[c12]
v_mfma_f32_16x16x32_bf16 %[c13], a[4:7], a[44:47], %[c13]
v_mfma_f32_16x16x32_bf16 %[c14], a[8:11], a[44:47], %[c14]
ds_read_b128 a[56:59], v42 offset:4096
v_mfma_f32_16x16x32_bf16 %[c15], a[12:15], a[44:47], %[c15]
s_waitcnt lgkmcnt(1)
v_mfma_f32_16x16x32_bf16 %[c0], a[16:19], a[48:51], %[c0]
v_mfma_f32_16x16x32_bf16 %[c1], a[20:23], a[48:51], %[c1]
v_mfma_f32_16x16x32_bf16 %[c2], a[24:27], a[48:51], %[c2]
ds_read_b128 a[60:63], v42 offset:6144
v_mfma_f32_16x16x32_bf16 %[c3], a[28:31], a[48:51], %[c3]
v_mfma_f32_16x16x32_bf16 %[c4], a[16:19], a[52:55], %[c4]
ds_read_b64_tr_b16 a[0:1], v45 offset:32768
ds_read_b64_tr_b16 a[2:3], v47 offset:32768
v_mfma_f32_16x16x32_bf16 %[c5], a[20:23], a[52:55], %[c5]
ds_read_b64_tr_b16 a[4:5], v46 offset:32768
ds_read_b64_tr_b16 a[6:7], v48 offset:32768
v_mfma_f32_16x16x32_bf16 %[c6], a[24:27], a[52:55], %[c6]
ds_read_b128 a[32:35], v39 offset:32768
v_mfma_f32_16x16x32_bf16 %[c7], a[28:31], a[52:55], %[c7]
s_waitcnt lgkmcnt(5)
v_mfma_f32_16x16x32_bf16 %[c8], a[16:19], a[56:59], %[c8]
ds_read_b64_tr_b16 a[8:9], v45 offset:33280
ds_read_b64_tr_b16 a[10:11], v47 offset:33280
v_mfma_f32_16x16x32_bf16 %[c9], a[20:23], a[56:59], %[c9]
v_mfma_f32_16x16x32_bf16 %[c10], a[24:27], a[56:59], %[c10]
ds_read_b128 a[36:39], v39 offset:34816
v_mfma_f32_16x16x32_bf16 %[c11], a[28:31], a[56:59], %[c11]
ds_read_b64_tr_b16 a[12:13], v46 offset:33280
ds_read_b64_tr_b16 a[14:15], v48 offset:33280
v_mfma_f32_16x16x32_bf16 %[c12], a[16:19], a[60:63], %[c12]
v_mfma_f32_16x16x32_bf16 %[c13], a[20:23], a[60:63], %[c13]
v_mfma_f32_16x16x32_bf16 %[c14], a[24:27], a[60:63], %[c14]
ds_read_b128 a[40:43], v39 offset:36864
v_mfma_f32_16x16x32_bf16 %[c15], a[28:31], a[60:63], %[c15]
s_waitcnt lgkmcnt(1)
v_mfma_f32_16x16x32_bf16 %[c0], a[0:3], a[32:35], %[c0]
v_mfma_f32_16x16x32_bf16 %[c1], a[4:7], a[32:35], %[c1]
v_mfma_f32_16x16x32_bf16 %[c2], a[8:11], a[32:35], %[c2]
ds_read_b128 a[44:47], v39 offset:38912
v_mfma_f32_16x16x32_bf16 %[c3], a[12:15], a[32:35], %[c3]
v_mfma_f32_16x16x32_bf16 %[c4], a[0:3], a[36:39], %[c4]
ds_read_b64_tr_b16 a[16:17], v45 offset:40960
ds_read_b64_tr_b16 a[18:19], v47 offset:40960
v_mfma_f32_16x16x32_bf16 %[c5], a[4:7], a[36:39], %[c5]
ds_read_b64_tr_b16 a[20:21], v46 offset:40960
ds_read_b64_tr_b16 a[22:23], v48 offset:40960
v_mfma_f32_16x16x32_bf16 %[c6], a[8:11], a[36:39], %[c6]
ds_read_b128 a[48:51], v42 offset:32768
v_mfma_f32_16x16x32_bf16 %[c7], a[12:15], a[36:39], %[c7]
s_waitcnt lgkmcnt(5)
v_mfma_f32_16x16x32_bf16 %[c8], a[0:3], a[40:43], %[c8]
ds_read_b64_tr_b16 a[24:25], v45 offset:41472
ds_read_b64_tr_b16 a[26:27], v47 offset:41472
v_mfma_f32_16x16x32_bf16 %[c9], a[4:7], a[40:43], %[c9]
v_mfma_f32_16x16x32_bf16 %[c10], a[8:11], a[40:43], %[c10]
ds_read_b128 a[52:55], v42 offset:34816
v_mfma_f32_16x16x32_bf16 %[c11], a[12:15], a[40:43], %[c11]
ds_read_b64_tr_b16 a[28:29], v46 offset:41472
ds_read_b64_tr_b16 a[30:31], v48 offset:41472
v_mfma_f32_16x16x32_bf16 %[c12], a[0:3], a[44:47], %[c12]
v_mfma_f32_16x16x32_bf16 %[c13], a[4:7], a[44:47], %[c13]
v_mfma_f32_16x16x32_bf16 %[c14], a[8:11], a[44:47], %[c14]
ds_read_b128 a[56:59], v42 offset:36864
v_mfma_f32_16x16x32_bf16 %[c15], a[12:15], a[44:47], %[c15]
s_waitcnt lgkmcnt(1)
v_mfma_f32_16x16x32_bf16 %[c0], a[16:19], a[48:51], %[c0]
v_mfma_f32_16x16x32_bf16 %[c1], a[20:23], a[48:51], %[c1]
v_mfma_f32_16x16x32_bf16 %[c2], a[24:27], a[48:51], %[c2]
ds_read_b128 a[60:63], v42 offset:38912
v_mfma_f32_16x16x32_bf16 %[c3], a[28:31], a[48:51], %[c3]
v_mfma_f32_16x16x32_bf16 %[c4], a[16:19], a[52:55], %[c4]
v_mfma_f32_16x16x32_bf16 %[c5], a[20:23], a[52:55], %[c5]
v_mfma_f32_16x16x32_bf16 %[c6], a[24:27], a[52:55], %[c6]
v_mfma_f32_16x16x32_bf16 %[c7], a[28:31], a[52:55], %[c7]
s_waitcnt lgkmcnt(0)
v_mfma_f32_16x16x32_bf16 %[c8], a[16:19], a[56:59], %[c8]
v_mfma_f32_16x16x32_bf16 %[c9], a[20:23], a[56:59], %[c9]
v_mfma_f32_16x16x32_bf16 %[c10], a[24:27], a[56:59], %[c10]
v_mfma_f32_16x16x32_bf16 %[c11], a[28:31], a[56:59], %[c11]
v_mfma_f32_16x16x32_bf16 %[c12], a[16:19], a[60:63], %[c12]
v_mfma_f32_16x16x32_bf16 %[c13], a[20:23], a[60:63], %[c13]
v_mfma_f32_16x16x32_bf16 %[c14], a[24:27], a[60:63], %[c14]
v_mfma_f32_16x16x32_bf16 %[c15], a[28:31], a[60:63], %[c15]
s_nop 7
)ASM"
                : ACC_OUT
                : [x0] "s"(LO32(wsp)), [x1] "s"(HI16(wsp)), [xn] "i"(S * S * 2), [w0] "s"(LO32(wb)), [w1] "s"(HI16(wb)), [wn] "i"(64 * 7 * 8192), [w] "s"(w), [lds] "s"((unsigned)(size_t)lds),
                  [xo] "s"((m0 + 32 * w) * S * 2), [wo] "s"(cts * 8192 + 2048 * w), [xadv] "i"(128), [wadv] "i"(7 * 8192), [xs] "i"(S * 2), [xp1] "i"(8 * S * 2 - 1024), [xp2] "i"(16 * S * 2 - 2048),
                  [tag0] "s"((unsigned)tag), [tag1] "s"((unsigned)(tag >> 32)), [flags] "s"(ws + OFF_CTL), [fdef] "s"(8 * (F_CTX + bl * 32 + h)), [fdef2] "s"(8 * (F_CTX + bs * 32 + h)),
                  [dbg] "s"(ws + OFF_DBG + 393216 + (bid * 4 + w) * 64)
                :
"v32", "v33", "v34", "v35", "v36", "v37", "v38", "v39", "v40", "v41", "v42", "v43", "v44", "v45", "v46", "v47", "v48", "v49", "v50", "v51", "v52", "v53", "v54", "v55", "v56", "v57", "v58", "v59", "v60", "v61", "v62", "v63", "v64", "v65", "v66", "v67", "v68", "v69", "v70", "v71", "a0", "a1", "a2", "a3", "a4", "a5", "a6", "a7", "a8", "a9", "a10", "a11", "a12", "a13", "a14", "a15", "a16", "a17", "a18", "a19", "a20", "a21", "a22", "a23", "a24", "a25", "a26", "a27", "a28", "a29", "a30", "a31", "a32", "a33", "a34", "a35", "a36", "a37", "a38", "a39", "a40", "a41", "a42", "a43", "a44", "a45", "a46", "a47", "a48", "a49", "a50", "a51", "a52", "a53", "a54", "a55", "a56", "a57", "a58", "a59", "a60", "a61", "a62", "a63", "a64", "a65", "a66", "a67", "a68", "a69", "a70", "a71", "a72", "a73", "a74", "a75", "a76", "a77", "a78", "a79", "a80", "a81", "a82", "a83", "a84", "a85", "a86", "a87", "a88", "a89", "a90", "a91", "a92", "a93", "a94", "a95", "s36", "s37", "s38", "s39", "s40", "s41", "s42", "s43", "s44", "s45", "s46", "s47", "s48", "s49", "s50", "s51", "s52", "s53", "s54", "s55", "s56", "s57", "s58", "s59", "s60", "s61", "s62", "s63", "s64", "s65", "s66", "s67", "s68", "s69", "s70", "s71", "s72", "s73", "vcc", "scc", "memory"
            );
            const int r16 = lane & 15, fq = lane >> 4, wr = w >> 1, wc = w & 1;
#pragma unroll
            for (int m = 0; m < 4; ++m) {
                const int row = m0 + 64 * wr + 16 * m + r16;
                const float b = bflo(ap->bsp[row]);
#pragma unroll
                for (int G = 0; G < 2; ++G) {
                    const int fg = 4 * cts + 2 * wc + G - 1;
                    const u32x4 z = __builtin_bit_cast(u32x4, __builtin_amdgcn_raw_buffer_load_b128(wsr, fg < 0 ? 0xfffffff0u : (unsigned)OFF_Z1 + (row * FP + fg * 32 + 8 * fq) * 2, 0, 16));
                    const f32x4 p = c[4 * m + 2 * G], d = c[4 * m + 2 * G + 1];
                    const u32x4 v = {cvtpk(bflo(z[0]) * (p[0] + b), bfhi(z[0]) * (p[1] + b)), cvtpk(bflo(z[1]) * (p[2] + b), bfhi(z[1]) * (p[3] + b)),
                                     cvtpk(bflo(z[2]) * (d[0] + b), bfhi(z[2]) * (d[1] + b)), cvtpk(bflo(z[3]) * (d[2] + b), bfhi(z[3]) * (d[3] + b))};
                    st16_wt(wsr, (unsigned)OFF_CU + ((128 + (fg < 0 ? 27 : fg)) * S + row) * 64 + 16 * (fq ^ swz4(row)), fg < 0 ? (u32x4){0, 0, 0, 0} : v);
                }
            }
            if (idx >= 30) PUBLISH(F_U + rbs * 7 + cts); else SYNC();
        }
    }
    {
        PHASE_VARS();
        if (idx < 30) {
            const int L = xcd * 30 + idx, sp = L / 80, tile = L - sp * 80, frb = tile / 5, fct = tile - frb * 5;
            const int h0 = 13 * sp, nh = sp == 2 ? 6 : 13;
            CST(8);
            const unsigned pc = 8u * (lane < nh ? F_CTX + frb * 32 + h0 + lane : F_KV + bid), pu = 8u * ((sp == 2 && lane < 7) ? F_U + frb * 7 + lane : F_KV + bid);
            const unsigned fd1 = 8u * (srole ? F_U + rbs * 7 + cts : F_CTX + bl * 32 + h), fd2 = 8u * (srole ? F_KV + bid : F_CTX + bs * 32 + h);
            const int m0 = frb * 128, n0 = fct * 128;
            const char* xb = ws + OFF_CU; const char* wb = (const char*)ap->wo - 896; const char* w2 = (const char*)ap->wout - 896;
            f32x4 c[16];
            asm volatile(
R"ASM(s_mov_b32 s36, %[x0]
s_mov_b32 s37, %[x1]
s_mov_b32 s38, %[xn]
s_mov_b32 s39, 0x20000
s_mov_b32 s40, %[w0]
s_mov_b32 s41, %[w1]
s_mov_b32 s42, %[wn]
s_mov_b32 s43, 0x20000
s_mov_b32 s52, %[w20]
s_mov_b32 s53, %[w21]
s_mov_b32 s54, %[w2n]
s_mov_b32 s55, %[wo2]
s_mov_b32 s49, %[k0]
s_mov_b32 s47, %[w]
s_mov_b32 s70, %[tag0]
s_mov_b32 s71, %[tag1]
s_lshl_b32 s48, s47, 11
s_add_u32 s44, %[lds], s48
s_mov_b32 s45, %[xo]
s_mov_b32 s46, %[wo]
v_mbcnt_lo_u32_b32 v57, -1, 0
v_mbcnt_hi_u32_b32 v57, -1, v57
v_lshlrev_b32_e32 v32, 4, v57
v_lshrrev_b32_e32 v62, 4, v57
v_and_b32_e32 v62, 1, v62
s_lshl_b32 s48, s47, 1
v_add_u32_e32 v62, s48, v62
v_and_b32_e32 v62, 3, v62
v_and_b32_e32 v63, 3, v57
v_xor_b32_e32 v62, v62, v63
v_lshrrev_b32_e32 v63, 5, v57
v_lshl_add_u32 v62, v63, 2, v62
v_lshlrev_b32_e32 v62, 4, v62
v_lshrrev_b32_e32 v63, 2, v57
v_and_b32_e32 v63, 7, v63
v_lshrrev_b32_e32 v64, 2, v63
v_and_b32_e32 v63, 3, v63
v_lshl_add_u32 v63, v64, 4, v63
v_mul_u32_u24_e32 v63, %[ws], v63
v_add_u32_e32 v38, v62, v63
v_add_u32_e32 v37, 896, v38
s_cmp_eq_u32 s49, 128
s_cselect_b32 s40, s52, s40
s_cselect_b32 s41, s53, s41
s_cselect_b32 s42, s54, s42
s_cselect_b32 s46, s55, s46
s_add_u32 s49, s49, 1
s_add_u32 m0, s44, 8192
s_nop 0
buffer_load_dwordx4 v37, s[40:43], s46 offen lds
buffer_load_dwordx4 v38, s[40:43], s46 offen offset:1024 lds
s_add_u32 s46, s46, %[wadv]
s_cmp_eq_u32 s49, 128
s_cselect_b32 s40, s52, s40
s_cselect_b32 s41, s53, s41
s_cselect_b32 s42, s54, s42
s_cselect_b32 s46, s55, s46
s_add_u32 s49, s49, 1
s_add_u32 m0, s44, 24576
s_nop 0
buffer_load_dwordx4 v37, s[40:43], s46 offen lds
buffer_load_dwordx4 v38, s[40:43], s46 offen offset:1024 lds
s_add_u32 s46, s46, %[wadv]
s_cmp_eq_u32 s49, 128
s_cselect_b32 s40, s52, s40
s_cselect_b32 s41, s53, s41
s_cselect_b32 s42, s54, s42
s_cselect_b32 s46, s55, s46
s_add_u32 s49, s49, 1
s_add_u32 m0, s44, 40960
s_nop 0
buffer_load_dwordx4 v37, s[40:43], s46 offen lds
buffer_load_dwordx4 v38, s[40:43], s46 offen offset:1024 lds
s_add_u32 s46, s46, %[wadv]
s_cmp_eq_u32 s49, 128
s_cselect_b32 s40, s52, s40
s_cselect_b32 s41, s53, s41
s_cselect_b32 s42, s54, s42
s_cselect_b32 s46, s55, s46
s_add_u32 s49, s49, 1
s_add_u32 m0, s44, 57344
s_nop 0
buffer_load_dwordx4 v37, s[40:43], s46 offen lds
buffer_load_dwordx4 v38, s[40:43], s46 offen offset:1024 lds
s_add_u32 s46, s46, %[wadv]
v_and_b32_e32 v58, 15, v57
v_lshrrev_b32_e32 v59, 4, v57
v_lshrrev_b32_e32 v62, 2, v58
v_sub_u32_e32 v62, 4, v62
v_and_b32_e32 v62, 3, v62
v_xor_b32_e32 v62, v62, v59
v_lshlrev_b32_e32 v62, 4, v62
v_lshl_add_u32 v62, v58, 6, v62
s_lshr_b32 s48, s47, 1
s_lshl_b32 s48, s48, 12
s_add_u32 s48, s48, %[lds]
v_add_u32_e32 v62, s48, v62
v_add_u32_e32 v39, 0, v62
v_add_u32_e32 v40, 65536, v62
v_add_u32_e32 v41, 131072, v62
v_lshrrev_b32_e32 v60, 2, v58
v_and_b32_e32 v61, 3, v57
s_and_b32 s48, s47, 1
s_lshl_b32 s48, s48, 10
s_add_u32 s48, s48, %[lds]
s_add_u32 s48, s48, 8192
v_lshlrev_b32_e32 v62, 1, v59
v_add_u32_e32 v62, 0, v62
v_and_b32_e32 v62, 3, v62
v_lshrrev_b32_e32 v63, 1, v61
v_or_b32_e32 v63, 0, v63
v_xor_b32_e32 v62, v62, v63
v_lshlrev_b32_e32 v62, 4, v62
v_and_b32_e32 v63, 1, v61
v_lshl_add_u32 v62, v63, 3, v62
v_lshl_add_u32 v62, v59, 11, v62
v_add_u32_e32 v63, 0, v60
v_lshl_add_u32 v62, v63, 6, v62
v_add_u32_e32 v62, s48, v62
v_add_u32_e32 v45, 0, v62
v_add_u32_e32 v49, 65536, v62
v_add_u32_e32 v53, 131072, v62
v_lshlrev_b32_e32 v62, 1, v59
v_add_u32_e32 v62, 0, v62
v_and_b32_e32 v62, 3, v62
v_lshrrev_b32_e32 v63, 1, v61
v_or_b32_e32 v63, 2, v63
v_xor_b32_e32 v62, v62, v63
v_lshlrev_b32_e32 v62, 4, v62
v_and_b32_e32 v63, 1, v61
v_lshl_add_u32 v62, v63, 3, v62
v_lshl_add_u32 v62, v59, 11, v62
v_add_u32_e32 v63, 0, v60
v_lshl_add_u32 v62, v63, 6, v62
v_add_u32_e32 v62, s48, v62
v_add_u32_e32 v46, 0, v62
v_add_u32_e32 v50, 65536, v62
v_add_u32_e32 v54, 131072, v62
v_lshlrev_b32_e32 v62, 1, v59
v_add_u32_e32 v62, 1, v62
v_and_b32_e32 v62, 3, v62
v_lshrrev_b32_e32 v63, 1, v61
v_or_b32_e32 v63, 0, v63
v_xor_b32_e32 v62, v62, v63
v_lshlrev_b32_e32 v62, 4, v62
v_and_b32_e32 v63, 1, v61
v_lshl_add_u32 v62, v63, 3, v62
v_lshl_add_u32 v62, v59, 11, v62
v_add_u32_e32 v63, 4, v60
v_lshl_add_u32 v62, v63, 6, v62
v_add_u32_e32 v62, s48, v62
v_add_u32_e32 v47, 0, v62
v_add_u32_e32 v51, 65536, v62
v_add_u32_e32 v55, 131072, v62
v_lshlrev_b32_e32 v62, 1, v59
v_add_u32_e32 v62, 1, v62
v_and_b32_e32 v62, 3, v62
v_lshrrev_b32_e32 v63, 1, v61
v_or_b32_e32 v63, 2, v63
v_xor_b32_e32 v62, v62, v63
v_lshlrev_b32_e32 v62, 4, v62
v_and_b32_e32 v63, 1, v61
v_lshl_add_u32 v62, v63, 3, v62
v_lshl_add_u32 v62, v59, 11, v62
v_add_u32_e32 v63, 4, v60
v_lshl_add_u32 v62, v63, 6, v62
v_add_u32_e32 v62, s48, v62
v_add_u32_e32 v48, 0, v62
v_add_u32_e32 v52, 65536, v62
v_add_u32_e32 v56, 131072, v62
s_waitcnt vmcnt(4)
s_barrier
s_cmp_lg_u32 s47, 0
s_cbranch_scc1 L_fs4_%=
v_mov_b32 v60, s70
v_mov_b32 v61, s71
s_mov_b64 exec, 1
v_mov_b32 v62, %[fd1]
global_store_dwordx2 v62, v[60:61], %[flags] sc1
v_mov_b32 v63, %[fd2]
global_store_dwordx2 v63, v[60:61], %[flags] sc1
v_mov_b32 v64, %[fd3]
global_store_dwordx2 v64, v[60:61], %[flags] sc1
s_mov_b64 exec, -1
L_fs4_%=:
global_load_dwordx2 v[66:67], %[pc], %[flags] sc1
s_waitcnt vmcnt(0)
v_cmp_ne_u64_e32 vcc, s[70:71], v[66:67]
s_nop 4
s_cmp_lg_u64 vcc, 0
s_cbranch_scc0 L_pok5_%=
s_mov_b32 s72, 0x10000
L_plp5_%=:
s_sleep 2
global_load_dwordx2 v[66:67], %[pc], %[flags] sc1
s_waitcnt vmcnt(0)
v_cmp_ne_u64_e32 vcc, s[70:71], v[66:67]
s_nop 4
s_cmp_lg_u64 vcc, 0
s_cbranch_scc0 L_pok5_%=
s_sub_u32 s72, s72, 1
s_cmp_lg_u32 s72, 0
s_cbranch_scc1 L_plp5_%=
L_pok5_%=:
s_add_u32 m0, s44, 0
s_nop 0
buffer_load_dwordx4 v32, s[36:39], s45 offen sc1 lds
buffer_load_dwordx4 v32, s[36:39], s45 offen offset:1024 sc1 lds
s_add_u32 s45, s45, %[xadv]
s_add_u32 m0, s44, 16384
s_nop 0
buffer_load_dwordx4 v32, s[36:39], s45 offen sc1 lds
buffer_load_dwordx4 v32, s[36:39], s45 offen offset:1024 sc1 lds
s_add_u32 s45, s45, %[xadv]
s_add_u32 m0, s44, 32768
s_nop 0
buffer_load_dwordx4 v32, s[36:39], s45 offen sc1 lds
buffer_load_dwordx4 v32, s[36:39], s45 offen offset:1024 sc1 lds
s_add_u32 s45, s45, %[xadv]
s_add_u32 m0, s44, 49152
s_nop 0
buffer_load_dwordx4 v32, s[36:39], s45 offen sc1 lds
buffer_load_dwordx4 v32, s[36:39], s45 offen offset:1024 sc1 lds
s_add_u32 s45, s45, %[xadv]
s_waitcnt vmcnt(4)
s_barrier
ds_read_b64_tr_b16 a[0:1], v45
ds_read_b64_tr_b16 a[2:3], v47
ds_read_b64_tr_b16 a[4:5], v46
ds_read_b64_tr_b16 a[6:7], v48
ds_read_b128 a[32:35], v39
ds_read_b64_tr_b16 a[8:9], v45 offset:512
ds_read_b64_tr_b16 a[10:11], v47 offset:512
ds_read_b128 a[36:39], v39 offset:1024
ds_read_b64_tr_b16 a[12:13], v46 offset:512
ds_read_b64_tr_b16 a[14:15], v48 offset:512
ds_read_b128 a[40:43], v39 offset:2048
s_waitcnt lgkmcnt(1)
v_mfma_f32_16x16x32_bf16 %[c0], a[0:3], a[32:35], 0
v_mfma_f32_16x16x32_bf16 %[c1], a[4:7], a[32:35], 0
v_mfma_f32_16x16x32_bf16 %[c2], a[8:11], a[32:35], 0
ds_read_b128 a[44:47], v39 offset:3072
v_mfma_f32_16x16x32_bf16 %[c3], a[12:15], a[32:35], 0
v_mfma_f32_16x16x32_bf16 %[c4], a[0:3], a[36:39], 0
ds_read_b64_tr_b16 a[16:17], v45 offset:16384
ds_read_b64_tr_b16 a[18:19], v47 offset:16384
v_mfma_f32_16x16x32_bf16 %[c5], a[4:7], a[36:39], 0
ds_read_b64_tr_b16 a[20:21], v46 offset:16384
ds_read_b64_tr_b16 a[22:23], v48 offset:16384
v_mfma_f32_16x16x32_bf16 %[c6], a[8:11], a[36:39], 0
s_waitcnt vmcnt(0)
s_barrier
ds_read_b128 a[48:51], v39 offset:16384
v_mfma_f32_16x16x32_bf16 %[c7], a[12:15], a[36:39], 0
s_cmp_eq_u32 s49, 128
s_cselect_b32 s40, s52, s40
s_waitcnt lgkmcnt(5)
v_mfma_f32_16x16x32_bf16 %[c8], a[0:3], a[40:43], 0
s_cselect_b32 s41, s53, s41
s_cselect_b32 s42, s54, s42
ds_read_b64_tr_b16 a[24:25], v45 offset:16896
ds_read_b64_tr_b16 a[26:27], v47 offset:16896
v_mfma_f32_16x16x32_bf16 %[c9], a[4:7], a[40:43], 0
s_cselect_b32 s46, s55, s46
s_add_u32 s49, s49, 1
v_mfma_f32_16x16x32_bf16 %[c10], a[8:11], a[40:43], 0
s_add_u32 m0, s44, 65536
s_nop 0
buffer_load_dwordx4 v32, s[36:39], s45 offen sc1 lds
ds_read_b128 a[52:55], v39 offset:17408
v_mfma_f32_16x16x32_bf16 %[c11], a[12:15], a[40:43], 0
buffer_load_dwordx4 v32, s[36:39], s45 offen offset:1024 sc1 lds
s_add_u32 m0, s44, 73728
ds_read_b64_tr_b16 a[28:29], v46 offset:16896
ds_read_b64_tr_b16 a[30:31], v48 offset:16896
v_mfma_f32_16x16x32_bf16 %[c12], a[0:3], a[44:47], 0
buffer_load_dwordx4 v37, s[40:43], s46 offen lds
v_mfma_f32_16x16x32_bf16 %[c13], a[4:7], a[44:47], 0
buffer_load_dwordx4 v38, s[40:43], s46 offen offset:1024 lds
s_add_u32 s45, s45, %[xadv]
v_mfma_f32_16x16x32_bf16 %[c14], a[8:11], a[44:47], 0
s_add_u32 s46, s46, %[wadv]
s_cmp_eq_u32 s49, 128
ds_read_b128 a[56:59], v39 offset:18432
v_mfma_f32_16x16x32_bf16 %[c15], a[12:15], a[44:47], 0
s_cselect_b32 s40, s52, s40
s_cselect_b32 s41, s53, s41
s_waitcnt lgkmcnt(1)
v_mfma_f32_16x16x32_bf16 %[c0], a[16:19], a[48:51], %[c0]
s_cselect_b32 s42, s54, s42
s_cselect_b32 s46, s55, s46
v_mfma_f32_16x16x32_bf16 %[c1], a[20:23], a[48:51], %[c1]
s_add_u32 s49, s49, 1
v_mfma_f32_16x16x32_bf16 %[c2], a[24:27], a[48:51], %[c2]
s_add_u32 m0, s44, 81920
s_nop 0
buffer_load_dwordx4 v32, s[36:39], s45 offen sc1 lds
ds_read_b128 a[60:63], v39 offset:19456
v_mfma_f32_16x16x32_bf16 %[c3], a[28:31], a[48:51], %[c3]
buffer_load_dwordx4 v32, s[36:39], s45 offen offset:1024 sc1 lds
s_add_u32 m0, s44, 90112
v_mfma_f32_16x16x32_bf16 %[c4], a[16:19], a[52:55], %[c4]
buffer_load_dwordx4 v37, s[40:43], s46 offen lds
buffer_load_dwordx4 v38, s[40:43], s46 offen offset:1024 lds
ds_read_b64_tr_b16 a[0:1], v45 offset:32768
ds_read_b64_tr_b16 a[2:3], v47 offset:32768
v_mfma_f32_16x16x32_bf16 %[c5], a[20:23], a[52:55], %[c5]
s_add_u32 s45, s45, %[xadv]
s_add_u32 s46, s46, %[wadv]
ds_read_b64_tr_b16 a[4:5], v46 offset:32768
ds_read_b64_tr_b16 a[6:7], v48 offset:32768
v_mfma_f32_16x16x32_bf16 %[c6], a[24:27], a[52:55], %[c6]
s_cmp_eq_u32 s49, 128
ds_read_b128 a[32:35], v39 offset:32768
v_mfma_f32_16x16x32_bf16 %[c7], a[28:31], a[52:55], %[c7]
s_cselect_b32 s40, s52, s40
s_cselect_b32 s41, s53, s41
s_waitcnt lgkmcnt(5)
v_mfma_f32_16x16x32_bf16 %[c8], a[16:19], a[56:59], %[c8]
s_cselect_b32 s42, s54, s42
s_cselect_b32 s46, s55, s46
ds_read_b64_tr_b16 a[8:9], v45 offset:33280
ds_read_b64_tr_b16 a[10:11], v47 offset:33280
v_mfma_f32_16x16x32_bf16 %[c9], a[20:23], a[56:59], %[c9]
s_add_u32 s49, s49, 1
s_add_u32 m0, s44, 98304
v_mfma_f32_16x16x32_bf16 %[c10], a[24:27], a[56:59], %[c10]
buffer_load_dwordx4 v32, s[36:39], s45 offen sc1 lds
buffer_load_dwordx4 v32, s[36:39], s45 offen offset:1024 sc1 lds
ds_read_b128 a[36:39], v39 offset:33792
v_mfma_f32_16x16x32_bf16 %[c11], a[28:31], a[56:59], %[c11]
s_add_u32 m0, s44, 106496
ds_read_b64_tr_b16 a[12:13], v46 offset:33280
ds_read_b64_tr_b16 a[14:15], v48 offset:33280
v_mfma_f32_16x16x32_bf16 %[c12], a[16:19], a[60:63], %[c12]
buffer_load_dwordx4 v37, s[40:43], s46 offen lds
buffer_load_dwordx4 v38, s[40:43], s46 offen offset:1024 lds
v_mfma_f32_16x16x32_bf16 %[c13], a[20:23], a[60:63], %[c13]
s_add_u32 s45, s45, %[xadv]
s_add_u32 s46, s46, %[wadv]
v_mfma_f32_16x16x32_bf16 %[c14], a[24:27], a[60:63], %[c14]
s_cmp_eq_u32 s49, 128
s_cselect_b32 s40, s52, s40
ds_read_b128 a[40:43], v39 offset:34816
v_mfma_f32_16x16x32_bf16 %[c15], a[28:31], a[60:63], %[c15]
s_cselect_b32 s41, s53, s41
s_cselect_b32 s42, s54, s42
s_waitcnt lgkmcnt(1)
v_mfma_f32_16x16x32_bf16 %[c0], a[0:3], a[32:35], %[c0]
s_cselect_b32 s46, s55, s46
v_mfma_f32_16x16x32_bf16 %[c1], a[4:7], a[32:35], %[c1]
s_add_u32 s49, s49, 1
s_add_u32 m0, s44, 114688
v_mfma_f32_16x16x32_bf16 %[c2], a[8:11], a[32:35], %[c2]
buffer_load_dwordx4 v32, s[36:39], s45 offen sc1 lds
buffer_load_dwordx4 v32, s[36:39], s45 offen offset:1024 sc1 lds
ds_read_b128 a[44:47], v39 offset:35840
v_mfma_f32_16x16x32_bf16 %[c3], a[12:15], a[32:35], %[c3]
s_add_u32 m0, s44, 122880
s_nop 0
buffer_load_dwordx4 v37, s[40:43], s46 offen lds
v_mfma_f32_16x16x32_bf16 %[c4], a[0:3], a[36:39], %[c4]
buffer_load_dwordx4 v38, s[40:43], s46 offen offset:1024 lds
s_add_u32 s45, s45, %[xadv]
ds_read_b64_tr_b16 a[16:17], v45 offset:49152
)ASM"
R"ASM(ds_read_b64_tr_b16 a[18:19], v47 offset:49152
v_mfma_f32_16x16x32_bf16 %[c5], a[4:7], a[36:39], %[c5]
s_add_u32 s46, s46, %[wadv]
ds_read_b64_tr_b16 a[20:21], v46 offset:49152
ds_read_b64_tr_b16 a[22:23], v48 offset:49152
v_mfma_f32_16x16x32_bf16 %[c6], a[8:11], a[36:39], %[c6]
s_waitcnt vmcnt(8)
s_barrier
ds_read_b128 a[48:51], v39 offset:49152
v_mfma_f32_16x16x32_bf16 %[c7], a[12:15], a[36:39], %[c7]
s_cmp_eq_u32 s49, 128
s_cselect_b32 s40, s52, s40
s_waitcnt lgkmcnt(5)
v_mfma_f32_16x16x32_bf16 %[c8], a[0:3], a[40:43], %[c8]
s_cselect_b32 s41, s53, s41
s_cselect_b32 s42, s54, s42
ds_read_b64_tr_b16 a[24:25], v45 offset:49664
ds_read_b64_tr_b16 a[26:27], v47 offset:49664
v_mfma_f32_16x16x32_bf16 %[c9], a[4:7], a[40:43], %[c9]
s_cselect_b32 s46, s55, s46
s_add_u32 s49, s49, 1
v_mfma_f32_16x16x32_bf16 %[c10], a[8:11], a[40:43], %[c10]
s_add_u32 m0, s44, 131072
s_nop 0
buffer_load_dwordx4 v32, s[36:39], s45 offen sc1 lds
ds_read_b128 a[52:55], v39 offset:50176
v_mfma_f32_16x16x32_bf16 %[c11], a[12:15], a[40:43], %[c11]
buffer_load_dwordx4 v32, s[36:39], s45 offen offset:1024 sc1 lds
s_add_u32 m0, s44, 139264
ds_read_b64_tr_b16 a[28:29], v46 offset:49664
ds_read_b64_tr_b16 a[30:31], v48 offset:49664
v_mfma_f32_16x16x32_bf16 %[c12], a[0:3], a[44:47], %[c12]
buffer_load_dwordx4 v37, s[40:43], s46 offen lds
v_mfma_f32_16x16x32_bf16 %[c13], a[4:7], a[44:47], %[c13]
buffer_load_dwordx4 v38, s[40:43], s46 offen offset:1024 lds
s_add_u32 s45, s45, %[xadv]
v_mfma_f32_16x16x32_bf16 %[c14], a[8:11], a[44:47], %[c14]
s_add_u32 s46, s46, %[wadv]
s_cmp_eq_u32 s49, 128
ds_read_b128 a[56:59], v39 offset:51200
v_mfma_f32_16x16x32_bf16 %[c15], a[12:15], a[44:47], %[c15]
s_cselect_b32 s40, s52, s40
s_cselect_b32 s41, s53, s41
s_waitcnt lgkmcnt(1)
v_mfma_f32_16x16x32_bf16 %[c0], a[16:19], a[48:51], %[c0]
s_cselect_b32 s42, s54, s42
s_cselect_b32 s46, s55, s46
v_mfma_f32_16x16x32_bf16 %[c1], a[20:23], a[48:51], %[c1]
s_add_u32 s49, s49, 1
v_mfma_f32_16x16x32_bf16 %[c2], a[24:27], a[48:51], %[c2]
s_add_u32 m0, s44, 147456
s_nop 0
buffer_load_dwordx4 v32, s[36:39], s45 offen sc1 lds
ds_read_b128 a[60:63], v39 offset:52224
v_mfma_f32_16x16x32_bf16 %[c3], a[28:31], a[48:51], %[c3]
buffer_load_dwordx4 v32, s[36:39], s45 offen offset:1024 sc1 lds
s_add_u32 m0, s44, 155648
v_mfma_f32_16x16x32_bf16 %[c4], a[16:19], a[52:55], %[c4]
buffer_load_dwordx4 v37, s[40:43], s46 offen lds
buffer_load_dwordx4 v38, s[40:43], s46 offen offset:1024 lds
ds_read_b64_tr_b16 a[0:1], v49
ds_read_b64_tr_b16 a[2:3], v51
v_mfma_f32_16x16x32_bf16 %[c5], a[20:23], a[52:55], %[c5]
s_add_u32 s45, s45, %[xadv]
s_add_u32 s46, s46, %[wadv]
ds_read_b64_tr_b16 a[4:5], v50
ds_read_b64_tr_b16 a[6:7], v52
v_mfma_f32_16x16x32_bf16 %[c6], a[24:27], a[52:55], %[c6]
s_cmp_eq_u32 s49, 128
ds_read_b128 a[32:35], v40
v_mfma_f32_16x16x32_bf16 %[c7], a[28:31], a[52:55], %[c7]
s_cselect_b32 s40, s52, s40
s_cselect_b32 s41, s53, s41
s_waitcnt lgkmcnt(5)
v_mfma_f32_16x16x32_bf16 %[c8], a[16:19], a[56:59], %[c8]
s_cselect_b32 s42, s54, s42
s_cselect_b32 s46, s55, s46
ds_read_b64_tr_b16 a[8:9], v49 offset:512
ds_read_b64_tr_b16 a[10:11], v51 offset:512
v_mfma_f32_16x16x32_bf16 %[c9], a[20:23], a[56:59], %[c9]
s_add_u32 s49, s49, 1
s_add_u32 m0, s44, 0
v_mfma_f32_16x16x32_bf16 %[c10], a[24:27], a[56:59], %[c10]
buffer_load_dwordx4 v32, s[36:39], s45 offen sc1 lds
buffer_load_dwordx4 v32, s[36:39], s45 offen offset:1024 sc1 lds
ds_read_b128 a[36:39], v40 offset:1024
v_mfma_f32_16x16x32_bf16 %[c11], a[28:31], a[56:59], %[c11]
s_add_u32 m0, s44, 8192
ds_read_b64_tr_b16 a[12:13], v50 offset:512
ds_read_b64_tr_b16 a[14:15], v52 offset:512
v_mfma_f32_16x16x32_bf16 %[c12], a[16:19], a[60:63], %[c12]
buffer_load_dwordx4 v37, s[40:43], s46 offen lds
buffer_load_dwordx4 v38, s[40:43], s46 offen offset:1024 lds
v_mfma_f32_16x16x32_bf16 %[c13], a[20:23], a[60:63], %[c13]
s_add_u32 s45, s45, %[xadv]
s_add_u32 s46, s46, %[wadv]
v_mfma_f32_16x16x32_bf16 %[c14], a[24:27], a[60:63], %[c14]
s_cmp_eq_u32 s49, 128
s_cselect_b32 s40, s52, s40
ds_read_b128 a[40:43], v40 offset:2048
v_mfma_f32_16x16x32_bf16 %[c15], a[28:31], a[60:63], %[c15]
s_cselect_b32 s41, s53, s41
s_cselect_b32 s42, s54, s42
s_waitcnt lgkmcnt(1)
v_mfma_f32_16x16x32_bf16 %[c0], a[0:3], a[32:35], %[c0]
s_cselect_b32 s46, s55, s46
v_mfma_f32_16x16x32_bf16 %[c1], a[4:7], a[32:35], %[c1]
s_add_u32 s49, s49, 1
s_add_u32 m0, s44, 16384
v_mfma_f32_16x16x32_bf16 %[c2], a[8:11], a[32:35], %[c2]
buffer_load_dwordx4 v32, s[36:39], s45 offen sc1 lds
buffer_load_dwordx4 v32, s[36:39], s45 offen offset:1024 sc1 lds
ds_read_b128 a[44:47], v40 offset:3072
v_mfma_f32_16x16x32_bf16 %[c3], a[12:15], a[32:35], %[c3]
s_add_u32 m0, s44, 24576
s_nop 0
buffer_load_dwordx4 v37, s[40:43], s46 offen lds
v_mfma_f32_16x16x32_bf16 %[c4], a[0:3], a[36:39], %[c4]
buffer_load_dwordx4 v38, s[40:43], s46 offen offset:1024 lds
s_add_u32 s45, s45, %[xadv]
ds_read_b64_tr_b16 a[16:17], v49 offset:16384
ds_read_b64_tr_b16 a[18:19], v51 offset:16384
v_mfma_f32_16x16x32_bf16 %[c5], a[4:7], a[36:39], %[c5]
s_add_u32 s46, s46, %[wadv]
ds_read_b64_tr_b16 a[20:21], v50 offset:16384
ds_read_b64_tr_b16 a[22:23], v52 offset:16384
v_mfma_f32_16x16x32_bf16 %[c6], a[8:11], a[36:39], %[c6]
s_waitcnt vmcnt(16)
s_barrier
ds_read_b128 a[48:51], v40 offset:16384
v_mfma_f32_16x16x32_bf16 %[c7], a[12:15], a[36:39], %[c7]
s_cmp_eq_u32 s49, 128
s_waitcnt lgkmcnt(5)
v_mfma_f32_16x16x32_bf16 %[c8], a[0:3], a[40:43], %[c8]
s_cselect_b32 s40, s52, s40
ds_read_b64_tr_b16 a[24:25], v49 offset:16896
ds_read_b64_tr_b16 a[26:27], v51 offset:16896
v_mfma_f32_16x16x32_bf16 %[c9], a[4:7], a[40:43], %[c9]
s_cselect_b32 s41, s53, s41
v_mfma_f32_16x16x32_bf16 %[c10], a[8:11], a[40:43], %[c10]
s_cselect_b32 s42, s54, s42
ds_read_b128 a[52:55], v40 offset:17408
v_mfma_f32_16x16x32_bf16 %[c11], a[12:15], a[40:43], %[c11]
s_cselect_b32 s46, s55, s46
ds_read_b64_tr_b16 a[28:29], v50 offset:16896
ds_read_b64_tr_b16 a[30:31], v52 offset:16896
v_mfma_f32_16x16x32_bf16 %[c12], a[0:3], a[44:47], %[c12]
s_add_u32 s49, s49, 1
v_mfma_f32_16x16x32_bf16 %[c13], a[4:7], a[44:47], %[c13]
s_add_u32 m0, s44, 32768
v_mfma_f32_16x16x32_bf16 %[c14], a[8:11], a[44:47], %[c14]
ds_read_b128 a[56:59], v40 offset:18432
buffer_load_dwordx4 v32, s[36:39], s45 offen sc1 lds
v_mfma_f32_16x16x32_bf16 %[c15], a[12:15], a[44:47], %[c15]
buffer_load_dwordx4 v32, s[36:39], s45 offen offset:1024 sc1 lds
s_waitcnt lgkmcnt(1)
v_mfma_f32_16x16x32_bf16 %[c0], a[16:19], a[48:51], %[c0]
s_add_u32 m0, s44, 40960
v_mfma_f32_16x16x32_bf16 %[c1], a[20:23], a[48:51], %[c1]
v_mfma_f32_16x16x32_bf16 %[c2], a[24:27], a[48:51], %[c2]
ds_read_b128 a[60:63], v40 offset:19456
buffer_load_dwordx4 v37, s[40:43], s46 offen lds
v_mfma_f32_16x16x32_bf16 %[c3], a[28:31], a[48:51], %[c3]
buffer_load_dwordx4 v38, s[40:43], s46 offen offset:1024 lds
v_mfma_f32_16x16x32_bf16 %[c4], a[16:19], a[52:55], %[c4]
s_add_u32 s45, s45, %[xadv]
ds_read_b64_tr_b16 a[0:1], v49 offset:32768
ds_read_b64_tr_b16 a[2:3], v51 offset:32768
v_mfma_f32_16x16x32_bf16 %[c5], a[20:23], a[52:55], %[c5]
s_add_u32 s46, s46, %[wadv]
ds_read_b64_tr_b16 a[4:5], v50 offset:32768
ds_read_b64_tr_b16 a[6:7], v52 offset:32768
v_mfma_f32_16x16x32_bf16 %[c6], a[24:27], a[52:55], %[c6]
s_cmp_eq_u32 s49, 128
ds_read_b128 a[32:35], v40 offset:32768
v_mfma_f32_16x16x32_bf16 %[c7], a[28:31], a[52:55], %[c7]
s_cselect_b32 s40, s52, s40
s_waitcnt lgkmcnt(5)
v_mfma_f32_16x16x32_bf16 %[c8], a[16:19], a[56:59], %[c8]
s_cselect_b32 s41, s53, s41
ds_read_b64_tr_b16 a[8:9], v49 offset:33280
ds_read_b64_tr_b16 a[10:11], v51 offset:33280
v_mfma_f32_16x16x32_bf16 %[c9], a[20:23], a[56:59], %[c9]
s_cselect_b32 s42, s54, s42
v_mfma_f32_16x16x32_bf16 %[c10], a[24:27], a[56:59], %[c10]
s_cselect_b32 s46, s55, s46
ds_read_b128 a[36:39], v40 offset:33792
v_mfma_f32_16x16x32_bf16 %[c11], a[28:31], a[56:59], %[c11]
ds_read_b64_tr_b16 a[12:13], v50 offset:33280
ds_read_b64_tr_b16 a[14:15], v52 offset:33280
v_mfma_f32_16x16x32_bf16 %[c12], a[16:19], a[60:63], %[c12]
s_add_u32 s49, s49, 1
v_mfma_f32_16x16x32_bf16 %[c13], a[20:23], a[60:63], %[c13]
s_add_u32 m0, s44, 49152
v_mfma_f32_16x16x32_bf16 %[c14], a[24:27], a[60:63], %[c14]
ds_read_b128 a[40:43], v40 offset:34816
buffer_load_dwordx4 v32, s[36:39], s45 offen sc1 lds
v_mfma_f32_16x16x32_bf16 %[c15], a[28:31], a[60:63], %[c15]
buffer_load_dwordx4 v32, s[36:39], s45 offen offset:1024 sc1 lds
s_waitcnt lgkmcnt(1)
v_mfma_f32_16x16x32_bf16 %[c0], a[0:3], a[32:35], %[c0]
s_add_u32 m0, s44, 57344
v_mfma_f32_16x16x32_bf16 %[c1], a[4:7], a[32:35], %[c1]
buffer_load_dwordx4 v37, s[40:43], s46 offen lds
v_mfma_f32_16x16x32_bf16 %[c2], a[8:11], a[32:35], %[c2]
ds_read_b128 a[44:47], v40 offset:35840
buffer_load_dwordx4 v38, s[40:43], s46 offen offset:1024 lds
v_mfma_f32_16x16x32_bf16 %[c3], a[12:15], a[32:35], %[c3]
s_add_u32 s45, s45, %[xadv]
v_mfma_f32_16x16x32_bf16 %[c4], a[0:3], a[36:39], %[c4]
s_add_u32 s46, s46, %[wadv]
ds_read_b64_tr_b16 a[16:17], v49 offset:49152
ds_read_b64_tr_b16 a[18:19], v51 offset:49152
v_mfma_f32_16x16x32_bf16 %[c5], a[4:7], a[36:39], %[c5]
ds_read_b64_tr_b16 a[20:21], v50 offset:49152
ds_read_b64_tr_b16 a[22:23], v52 offset:49152
v_mfma_f32_16x16x32_bf16 %[c6], a[8:11], a[36:39], %[c6]
s_waitcnt vmcnt(16)
s_barrier
ds_read_b128 a[48:51], v40 offset:49152
v_mfma_f32_16x16x32_bf16 %[c7], a[12:15], a[36:39], %[c7]
s_cmp_eq_u32 s49, 128
s_waitcnt lgkmcnt(5)
v_mfma_f32_16x16x32_bf16 %[c8], a[0:3], a[40:43], %[c8]
s_cselect_b32 s40, s52, s40
ds_read_b64_tr_b16 a[24:25], v49 offset:49664
ds_read_b64_tr_b16 a[26:27], v51 offset:49664
v_mfma_f32_16x16x32_bf16 %[c9], a[4:7], a[40:43], %[c9]
s_cselect_b32 s41, s53, s41
v_mfma_f32_16x16x32_bf16 %[c10], a[8:11], a[40:43], %[c10]
s_cselect_b32 s42, s54, s42
ds_read_b128 a[52:55], v40 offset:50176
v_mfma_f32_16x16x32_bf16 %[c11], a[12:15], a[40:43], %[c11]
s_cselect_b32 s46, s55, s46
ds_read_b64_tr_b16 a[28:29], v50 offset:49664
ds_read_b64_tr_b16 a[30:31], v52 offset:49664
v_mfma_f32_16x16x32_bf16 %[c12], a[0:3], a[44:47], %[c12]
s_add_u32 s49, s49, 1
v_mfma_f32_16x16x32_bf16 %[c13], a[4:7], a[44:47], %[c13]
s_add_u32 m0, s44, 65536
v_mfma_f32_16x16x32_bf16 %[c14], a[8:11], a[44:47], %[c14]
ds_read_b128 a[56:59], v40 offset:51200
buffer_load_dwordx4 v32, s[36:39], s45 offen sc1 lds
v_mfma_f32_16x16x32_bf16 %[c15], a[12:15], a[44:47], %[c15]
buffer_load_dwordx4 v32, s[36:39], s45 offen offset:1024 sc1 lds
s_waitcnt lgkmcnt(1)
v_mfma_f32_16x16x32_bf16 %[c0], a[16:19], a[48:51], %[c0]
s_add_u32 m0, s44, 73728
v_mfma_f32_16x16x32_bf16 %[c1], a[20:23], a[48:51], %[c1]
v_mfma_f32_16x16x32_bf16 %[c2], a[24:27], a[48:51], %[c2]
ds_read_b128 a[60:63], v40 offset:52224
buffer_load_dwordx4 v37, s[40:43], s46 offen lds
v_mfma_f32_16x16x32_bf16 %[c3], a[28:31], a[48:51], %[c3]
buffer_load_dwordx4 v38, s[40:43], s46 offen offset:1024 lds
v_mfma_f32_16x16x32_bf16 %[c4], a[16:19], a[52:55], %[c4]
s_add_u32 s45, s45, %[xadv]
ds_read_b64_tr_b16 a[0:1], v53
ds_read_b64_tr_b16 a[2:3], v55
v_mfma_f32_16x16x32_bf16 %[c5], a[20:23], a[52:55], %[c5]
s_add_u32 s46, s46, %[wadv]
ds_read_b64_tr_b16 a[4:5], v54
ds_read_b64_tr_b16 a[6:7], v56
v_mfma_f32_16x16x32_bf16 %[c6], a[24:27], a[52:55], %[c6]
s_cmp_eq_u32 s49, 128
ds_read_b128 a[32:35], v41
v_mfma_f32_16x16x32_bf16 %[c7], a[28:31], a[52:55], %[c7]
s_cselect_b32 s40, s52, s40
s_waitcnt lgkmcnt(5)
v_mfma_f32_16x16x32_bf16 %[c8], a[16:19], a[56:59], %[c8]
s_cselect_b32 s41, s53, s41
ds_read_b64_tr_b16 a[8:9], v53 offset:512
ds_read_b64_tr_b16 a[10:11], v55 offset:512
v_mfma_f32_16x16x32_bf16 %[c9], a[20:23], a[56:59], %[c9]
s_cselect_b32 s42, s54, s42
v_mfma_f32_16x16x32_bf16 %[c10], a[24:27], a[56:59], %[c10]
s_cselect_b32 s46, s55, s46
ds_read_b128 a[36:39], v41 offset:1024
v_mfma_f32_16x16x32_bf16 %[c11], a[28:31], a[56:59], %[c11]
ds_read_b64_tr_b16 a[12:13], v54 offset:512
ds_read_b64_tr_b16 a[14:15], v56 offset:512
v_mfma_f32_16x16x32_bf16 %[c12], a[16:19], a[60:63], %[c12]
s_add_u32 s49, s49, 1
v_mfma_f32_16x16x32_bf16 %[c13], a[20:23], a[60:63], %[c13]
s_add_u32 m0, s44, 81920
)ASM"
R"ASM(v_mfma_f32_16x16x32_bf16 %[c14], a[24:27], a[60:63], %[c14]
ds_read_b128 a[40:43], v41 offset:2048
buffer_load_dwordx4 v32, s[36:39], s45 offen sc1 lds
v_mfma_f32_16x16x32_bf16 %[c15], a[28:31], a[60:63], %[c15]
buffer_load_dwordx4 v32, s[36:39], s45 offen offset:1024 sc1 lds
s_waitcnt lgkmcnt(1)
v_mfma_f32_16x16x32_bf16 %[c0], a[0:3], a[32:35], %[c0]
s_add_u32 m0, s44, 90112
v_mfma_f32_16x16x32_bf16 %[c1], a[4:7], a[32:35], %[c1]
buffer_load_dwordx4 v37, s[40:43], s46 offen lds
v_mfma_f32_16x16x32_bf16 %[c2], a[8:11], a[32:35], %[c2]
ds_read_b128 a[44:47], v41 offset:3072
buffer_load_dwordx4 v38, s[40:43], s46 offen offset:1024 lds
v_mfma_f32_16x16x32_bf16 %[c3], a[12:15], a[32:35], %[c3]
s_add_u32 s45, s45, %[xadv]
v_mfma_f32_16x16x32_bf16 %[c4], a[0:3], a[36:39], %[c4]
s_add_u32 s46, s46, %[wadv]
ds_read_b64_tr_b16 a[16:17], v53 offset:16384
ds_read_b64_tr_b16 a[18:19], v55 offset:16384
v_mfma_f32_16x16x32_bf16 %[c5], a[4:7], a[36:39], %[c5]
ds_read_b64_tr_b16 a[20:21], v54 offset:16384
ds_read_b64_tr_b16 a[22:23], v56 offset:16384
v_mfma_f32_16x16x32_bf16 %[c6], a[8:11], a[36:39], %[c6]
s_waitcnt vmcnt(16)
s_barrier
ds_read_b128 a[48:51], v41 offset:16384
v_mfma_f32_16x16x32_bf16 %[c7], a[12:15], a[36:39], %[c7]
s_cmp_eq_u32 s49, 128
s_waitcnt lgkmcnt(5)
v_mfma_f32_16x16x32_bf16 %[c8], a[0:3], a[40:43], %[c8]
s_cselect_b32 s40, s52, s40
ds_read_b64_tr_b16 a[24:25], v53 offset:16896
ds_read_b64_tr_b16 a[26:27], v55 offset:16896
v_mfma_f32_16x16x32_bf16 %[c9], a[4:7], a[40:43], %[c9]
s_cselect_b32 s41, s53, s41
v_mfma_f32_16x16x32_bf16 %[c10], a[8:11], a[40:43], %[c10]
s_cselect_b32 s42, s54, s42
ds_read_b128 a[52:55], v41 offset:17408
v_mfma_f32_16x16x32_bf16 %[c11], a[12:15], a[40:43], %[c11]
s_cselect_b32 s46, s55, s46
ds_read_b64_tr_b16 a[28:29], v54 offset:16896
ds_read_b64_tr_b16 a[30:31], v56 offset:16896
v_mfma_f32_16x16x32_bf16 %[c12], a[0:3], a[44:47], %[c12]
s_add_u32 s49, s49, 1
v_mfma_f32_16x16x32_bf16 %[c13], a[4:7], a[44:47], %[c13]
s_add_u32 m0, s44, 98304
v_mfma_f32_16x16x32_bf16 %[c14], a[8:11], a[44:47], %[c14]
ds_read_b128 a[56:59], v41 offset:18432
buffer_load_dwordx4 v32, s[36:39], s45 offen sc1 lds
v_mfma_f32_16x16x32_bf16 %[c15], a[12:15], a[44:47], %[c15]
buffer_load_dwordx4 v32, s[36:39], s45 offen offset:1024 sc1 lds
s_waitcnt lgkmcnt(1)
v_mfma_f32_16x16x32_bf16 %[c0], a[16:19], a[48:51], %[c0]
s_add_u32 m0, s44, 106496
v_mfma_f32_16x16x32_bf16 %[c1], a[20:23], a[48:51], %[c1]
v_mfma_f32_16x16x32_bf16 %[c2], a[24:27], a[48:51], %[c2]
ds_read_b128 a[60:63], v41 offset:19456
buffer_load_dwordx4 v37, s[40:43], s46 offen lds
v_mfma_f32_16x16x32_bf16 %[c3], a[28:31], a[48:51], %[c3]
buffer_load_dwordx4 v38, s[40:43], s46 offen offset:1024 lds
v_mfma_f32_16x16x32_bf16 %[c4], a[16:19], a[52:55], %[c4]
s_add_u32 s45, s45, %[xadv]
ds_read_b64_tr_b16 a[0:1], v45
ds_read_b64_tr_b16 a[2:3], v47
v_mfma_f32_16x16x32_bf16 %[c5], a[20:23], a[52:55], %[c5]
s_add_u32 s46, s46, %[wadv]
ds_read_b64_tr_b16 a[4:5], v46
ds_read_b64_tr_b16 a[6:7], v48
v_mfma_f32_16x16x32_bf16 %[c6], a[24:27], a[52:55], %[c6]
s_cmp_eq_u32 s49, 128
ds_read_b128 a[32:35], v39
v_mfma_f32_16x16x32_bf16 %[c7], a[28:31], a[52:55], %[c7]
s_cselect_b32 s40, s52, s40
s_waitcnt lgkmcnt(5)
v_mfma_f32_16x16x32_bf16 %[c8], a[16:19], a[56:59], %[c8]
s_cselect_b32 s41, s53, s41
ds_read_b64_tr_b16 a[8:9], v45 offset:512
ds_read_b64_tr_b16 a[10:11], v47 offset:512
v_mfma_f32_16x16x32_bf16 %[c9], a[20:23], a[56:59], %[c9]
s_cselect_b32 s42, s54, s42
v_mfma_f32_16x16x32_bf16 %[c10], a[24:27], a[56:59], %[c10]
s_cselect_b32 s46, s55, s46
ds_read_b128 a[36:39], v39 offset:1024
v_mfma_f32_16x16x32_bf16 %[c11], a[28:31], a[56:59], %[c11]
ds_read_b64_tr_b16 a[12:13], v46 offset:512
ds_read_b64_tr_b16 a[14:15], v48 offset:512
v_mfma_f32_16x16x32_bf16 %[c12], a[16:19], a[60:63], %[c12]
s_add_u32 s49, s49, 1
v_mfma_f32_16x16x32_bf16 %[c13], a[20:23], a[60:63], %[c13]
s_add_u32 m0, s44, 114688
v_mfma_f32_16x16x32_bf16 %[c14], a[24:27], a[60:63], %[c14]
ds_read_b128 a[40:43], v39 offset:2048
buffer_load_dwordx4 v32, s[36:39], s45 offen sc1 lds
v_mfma_f32_16x16x32_bf16 %[c15], a[28:31], a[60:63], %[c15]
buffer_load_dwordx4 v32, s[36:39], s45 offen offset:1024 sc1 lds
s_waitcnt lgkmcnt(1)
v_mfma_f32_16x16x32_bf16 %[c0], a[0:3], a[32:35], %[c0]
s_add_u32 m0, s44, 122880
v_mfma_f32_16x16x32_bf16 %[c1], a[4:7], a[32:35], %[c1]
buffer_load_dwordx4 v37, s[40:43], s46 offen lds
v_mfma_f32_16x16x32_bf16 %[c2], a[8:11], a[32:35], %[c2]
ds_read_b128 a[44:47], v39 offset:3072
buffer_load_dwordx4 v38, s[40:43], s46 offen offset:1024 lds
v_mfma_f32_16x16x32_bf16 %[c3], a[12:15], a[32:35], %[c3]
s_add_u32 s45, s45, %[xadv]
v_mfma_f32_16x16x32_bf16 %[c4], a[0:3], a[36:39], %[c4]
s_add_u32 s46, s46, %[wadv]
ds_read_b64_tr_b16 a[16:17], v45 offset:16384
ds_read_b64_tr_b16 a[18:19], v47 offset:16384
v_mfma_f32_16x16x32_bf16 %[c5], a[4:7], a[36:39], %[c5]
ds_read_b64_tr_b16 a[20:21], v46 offset:16384
ds_read_b64_tr_b16 a[22:23], v48 offset:16384
v_mfma_f32_16x16x32_bf16 %[c6], a[8:11], a[36:39], %[c6]
s_waitcnt vmcnt(16)
s_barrier
global_load_dwordx2 v[68:69], %[pu], %[flags] sc1
ds_read_b128 a[48:51], v39 offset:16384
v_mfma_f32_16x16x32_bf16 %[c7], a[12:15], a[36:39], %[c7]
s_cmp_eq_u32 s49, 128
s_waitcnt lgkmcnt(5)
v_mfma_f32_16x16x32_bf16 %[c8], a[0:3], a[40:43], %[c8]
s_cselect_b32 s40, s52, s40
ds_read_b64_tr_b16 a[24:25], v45 offset:16896
ds_read_b64_tr_b16 a[26:27], v47 offset:16896
v_mfma_f32_16x16x32_bf16 %[c9], a[4:7], a[40:43], %[c9]
s_cselect_b32 s41, s53, s41
v_mfma_f32_16x16x32_bf16 %[c10], a[8:11], a[40:43], %[c10]
s_cselect_b32 s42, s54, s42
ds_read_b128 a[52:55], v39 offset:17408
v_mfma_f32_16x16x32_bf16 %[c11], a[12:15], a[40:43], %[c11]
s_cselect_b32 s46, s55, s46
ds_read_b64_tr_b16 a[28:29], v46 offset:16896
ds_read_b64_tr_b16 a[30:31], v48 offset:16896
v_mfma_f32_16x16x32_bf16 %[c12], a[0:3], a[44:47], %[c12]
s_add_u32 s49, s49, 1
v_mfma_f32_16x16x32_bf16 %[c13], a[4:7], a[44:47], %[c13]
s_add_u32 m0, s44, 131072
v_mfma_f32_16x16x32_bf16 %[c14], a[8:11], a[44:47], %[c14]
ds_read_b128 a[56:59], v39 offset:18432
buffer_load_dwordx4 v32, s[36:39], s45 offen sc1 lds
v_mfma_f32_16x16x32_bf16 %[c15], a[12:15], a[44:47], %[c15]
buffer_load_dwordx4 v32, s[36:39], s45 offen offset:1024 sc1 lds
s_waitcnt lgkmcnt(1)
v_mfma_f32_16x16x32_bf16 %[c0], a[16:19], a[48:51], %[c0]
s_add_u32 m0, s44, 139264
v_mfma_f32_16x16x32_bf16 %[c1], a[20:23], a[48:51], %[c1]
v_mfma_f32_16x16x32_bf16 %[c2], a[24:27], a[48:51], %[c2]
ds_read_b128 a[60:63], v39 offset:19456
buffer_load_dwordx4 v37, s[40:43], s46 offen lds
v_mfma_f32_16x16x32_bf16 %[c3], a[28:31], a[48:51], %[c3]
buffer_load_dwordx4 v38, s[40:43], s46 offen offset:1024 lds
v_mfma_f32_16x16x32_bf16 %[c4], a[16:19], a[52:55], %[c4]
s_add_u32 s45, s45, %[xadv]
ds_read_b64_tr_b16 a[0:1], v45 offset:32768
ds_read_b64_tr_b16 a[2:3], v47 offset:32768
v_mfma_f32_16x16x32_bf16 %[c5], a[20:23], a[52:55], %[c5]
s_add_u32 s46, s46, %[wadv]
ds_read_b64_tr_b16 a[4:5], v46 offset:32768
ds_read_b64_tr_b16 a[6:7], v48 offset:32768
v_mfma_f32_16x16x32_bf16 %[c6], a[24:27], a[52:55], %[c6]
s_cmp_eq_u32 s49, 128
ds_read_b128 a[32:35], v39 offset:32768
v_mfma_f32_16x16x32_bf16 %[c7], a[28:31], a[52:55], %[c7]
s_cselect_b32 s40, s52, s40
s_waitcnt lgkmcnt(5)
v_mfma_f32_16x16x32_bf16 %[c8], a[16:19], a[56:59], %[c8]
s_cselect_b32 s41, s53, s41
ds_read_b64_tr_b16 a[8:9], v45 offset:33280
ds_read_b64_tr_b16 a[10:11], v47 offset:33280
v_mfma_f32_16x16x32_bf16 %[c9], a[20:23], a[56:59], %[c9]
s_cselect_b32 s42, s54, s42
v_mfma_f32_16x16x32_bf16 %[c10], a[24:27], a[56:59], %[c10]
s_cselect_b32 s46, s55, s46
ds_read_b128 a[36:39], v39 offset:33792
v_mfma_f32_16x16x32_bf16 %[c11], a[28:31], a[56:59], %[c11]
ds_read_b64_tr_b16 a[12:13], v46 offset:33280
ds_read_b64_tr_b16 a[14:15], v48 offset:33280
v_mfma_f32_16x16x32_bf16 %[c12], a[16:19], a[60:63], %[c12]
s_add_u32 s49, s49, 1
v_mfma_f32_16x16x32_bf16 %[c13], a[20:23], a[60:63], %[c13]
s_add_u32 m0, s44, 147456
v_mfma_f32_16x16x32_bf16 %[c14], a[24:27], a[60:63], %[c14]
ds_read_b128 a[40:43], v39 offset:34816
buffer_load_dwordx4 v32, s[36:39], s45 offen sc1 lds
v_mfma_f32_16x16x32_bf16 %[c15], a[28:31], a[60:63], %[c15]
buffer_load_dwordx4 v32, s[36:39], s45 offen offset:1024 sc1 lds
s_waitcnt lgkmcnt(1)
v_mfma_f32_16x16x32_bf16 %[c0], a[0:3], a[32:35], %[c0]
s_add_u32 m0, s44, 155648
v_mfma_f32_16x16x32_bf16 %[c1], a[4:7], a[32:35], %[c1]
buffer_load_dwordx4 v37, s[40:43], s46 offen lds
v_mfma_f32_16x16x32_bf16 %[c2], a[8:11], a[32:35], %[c2]
ds_read_b128 a[44:47], v39 offset:35840
buffer_load_dwordx4 v38, s[40:43], s46 offen offset:1024 lds
v_mfma_f32_16x16x32_bf16 %[c3], a[12:15], a[32:35], %[c3]
s_add_u32 s45, s45, %[xadv]
v_mfma_f32_16x16x32_bf16 %[c4], a[0:3], a[36:39], %[c4]
s_add_u32 s46, s46, %[wadv]
ds_read_b64_tr_b16 a[16:17], v45 offset:49152
ds_read_b64_tr_b16 a[18:19], v47 offset:49152
v_mfma_f32_16x16x32_bf16 %[c5], a[4:7], a[36:39], %[c5]
ds_read_b64_tr_b16 a[20:21], v46 offset:49152
ds_read_b64_tr_b16 a[22:23], v48 offset:49152
v_mfma_f32_16x16x32_bf16 %[c6], a[8:11], a[36:39], %[c6]
s_waitcnt vmcnt(17)
s_barrier
ds_read_b128 a[48:51], v39 offset:49152
v_mfma_f32_16x16x32_bf16 %[c7], a[12:15], a[36:39], %[c7]
s_cmp_eq_u32 s49, 128
s_waitcnt lgkmcnt(5)
v_mfma_f32_16x16x32_bf16 %[c8], a[0:3], a[40:43], %[c8]
s_cselect_b32 s40, s52, s40
ds_read_b64_tr_b16 a[24:25], v45 offset:49664
ds_read_b64_tr_b16 a[26:27], v47 offset:49664
v_mfma_f32_16x16x32_bf16 %[c9], a[4:7], a[40:43], %[c9]
s_cselect_b32 s41, s53, s41
v_mfma_f32_16x16x32_bf16 %[c10], a[8:11], a[40:43], %[c10]
s_cselect_b32 s42, s54, s42
ds_read_b128 a[52:55], v39 offset:50176
v_mfma_f32_16x16x32_bf16 %[c11], a[12:15], a[40:43], %[c11]
s_cselect_b32 s46, s55, s46
ds_read_b64_tr_b16 a[28:29], v46 offset:49664
ds_read_b64_tr_b16 a[30:31], v48 offset:49664
v_mfma_f32_16x16x32_bf16 %[c12], a[0:3], a[44:47], %[c12]
s_add_u32 s49, s49, 1
v_mfma_f32_16x16x32_bf16 %[c13], a[4:7], a[44:47], %[c13]
s_add_u32 m0, s44, 0
v_mfma_f32_16x16x32_bf16 %[c14], a[8:11], a[44:47], %[c14]
ds_read_b128 a[56:59], v39 offset:51200
buffer_load_dwordx4 v32, s[36:39], s45 offen sc1 lds
v_mfma_f32_16x16x32_bf16 %[c15], a[12:15], a[44:47], %[c15]
buffer_load_dwordx4 v32, s[36:39], s45 offen offset:1024 sc1 lds
s_waitcnt lgkmcnt(1)
v_mfma_f32_16x16x32_bf16 %[c0], a[16:19], a[48:51], %[c0]
s_add_u32 m0, s44, 8192
v_mfma_f32_16x16x32_bf16 %[c1], a[20:23], a[48:51], %[c1]
v_mfma_f32_16x16x32_bf16 %[c2], a[24:27], a[48:51], %[c2]
ds_read_b128 a[60:63], v39 offset:52224
buffer_load_dwordx4 v37, s[40:43], s46 offen lds
v_mfma_f32_16x16x32_bf16 %[c3], a[28:31], a[48:51], %[c3]
buffer_load_dwordx4 v38, s[40:43], s46 offen offset:1024 lds
v_mfma_f32_16x16x32_bf16 %[c4], a[16:19], a[52:55], %[c4]
s_add_u32 s45, s45, %[xadv]
ds_read_b64_tr_b16 a[0:1], v49
ds_read_b64_tr_b16 a[2:3], v51
v_mfma_f32_16x16x32_bf16 %[c5], a[20:23], a[52:55], %[c5]
s_add_u32 s46, s46, %[wadv]
ds_read_b64_tr_b16 a[4:5], v50
ds_read_b64_tr_b16 a[6:7], v52
v_mfma_f32_16x16x32_bf16 %[c6], a[24:27], a[52:55], %[c6]
s_cmp_eq_u32 s49, 128
ds_read_b128 a[32:35], v40
v_mfma_f32_16x16x32_bf16 %[c7], a[28:31], a[52:55], %[c7]
s_cselect_b32 s40, s52, s40
s_waitcnt lgkmcnt(5)
v_mfma_f32_16x16x32_bf16 %[c8], a[16:19], a[56:59], %[c8]
s_cselect_b32 s41, s53, s41
ds_read_b64_tr_b16 a[8:9], v49 offset:512
ds_read_b64_tr_b16 a[10:11], v51 offset:512
v_mfma_f32_16x16x32_bf16 %[c9], a[20:23], a[56:59], %[c9]
s_cselect_b32 s42, s54, s42
v_mfma_f32_16x16x32_bf16 %[c10], a[24:27], a[56:59], %[c10]
s_cselect_b32 s46, s55, s46
ds_read_b128 a[36:39], v40 offset:1024
v_mfma_f32_16x16x32_bf16 %[c11], a[28:31], a[56:59], %[c11]
ds_read_b64_tr_b16 a[12:13], v50 offset:512
ds_read_b64_tr_b16 a[14:15], v52 offset:512
v_mfma_f32_16x16x32_bf16 %[c12], a[16:19], a[60:63], %[c12]
s_add_u32 s49, s49, 1
v_mfma_f32_16x16x32_bf16 %[c13], a[20:23], a[60:63], %[c13]
s_add_u32 m0, s44, 16384
v_mfma_f32_16x16x32_bf16 %[c14], a[24:27], a[60:63], %[c14]
ds_read_b128 a[40:43], v40 offset:2048
buffer_load_dwordx4 v32, s[36:39], s45 offen sc1 lds
)ASM"
R"ASM(v_mfma_f32_16x16x32_bf16 %[c15], a[28:31], a[60:63], %[c15]
buffer_load_dwordx4 v32, s[36:39], s45 offen offset:1024 sc1 lds
s_waitcnt lgkmcnt(1)
v_mfma_f32_16x16x32_bf16 %[c0], a[0:3], a[32:35], %[c0]
s_add_u32 m0, s44, 24576
v_mfma_f32_16x16x32_bf16 %[c1], a[4:7], a[32:35], %[c1]
buffer_load_dwordx4 v37, s[40:43], s46 offen lds
v_mfma_f32_16x16x32_bf16 %[c2], a[8:11], a[32:35], %[c2]
ds_read_b128 a[44:47], v40 offset:3072
buffer_load_dwordx4 v38, s[40:43], s46 offen offset:1024 lds
v_mfma_f32_16x16x32_bf16 %[c3], a[12:15], a[32:35], %[c3]
s_add_u32 s45, s45, %[xadv]
v_mfma_f32_16x16x32_bf16 %[c4], a[0:3], a[36:39], %[c4]
s_add_u32 s46, s46, %[wadv]
ds_read_b64_tr_b16 a[16:17], v49 offset:16384
ds_read_b64_tr_b16 a[18:19], v51 offset:16384
v_mfma_f32_16x16x32_bf16 %[c5], a[4:7], a[36:39], %[c5]
ds_read_b64_tr_b16 a[20:21], v50 offset:16384
ds_read_b64_tr_b16 a[22:23], v52 offset:16384
v_mfma_f32_16x16x32_bf16 %[c6], a[8:11], a[36:39], %[c6]
s_waitcnt vmcnt(17)
s_barrier
ds_read_b128 a[48:51], v40 offset:16384
v_mfma_f32_16x16x32_bf16 %[c7], a[12:15], a[36:39], %[c7]
s_cmp_eq_u32 s49, 128
s_waitcnt lgkmcnt(5)
v_mfma_f32_16x16x32_bf16 %[c8], a[0:3], a[40:43], %[c8]
s_cselect_b32 s40, s52, s40
ds_read_b64_tr_b16 a[24:25], v49 offset:16896
ds_read_b64_tr_b16 a[26:27], v51 offset:16896
v_mfma_f32_16x16x32_bf16 %[c9], a[4:7], a[40:43], %[c9]
s_cselect_b32 s41, s53, s41
v_mfma_f32_16x16x32_bf16 %[c10], a[8:11], a[40:43], %[c10]
s_cselect_b32 s42, s54, s42
ds_read_b128 a[52:55], v40 offset:17408
v_mfma_f32_16x16x32_bf16 %[c11], a[12:15], a[40:43], %[c11]
s_cselect_b32 s46, s55, s46
ds_read_b64_tr_b16 a[28:29], v50 offset:16896
ds_read_b64_tr_b16 a[30:31], v52 offset:16896
v_mfma_f32_16x16x32_bf16 %[c12], a[0:3], a[44:47], %[c12]
s_add_u32 s49, s49, 1
v_mfma_f32_16x16x32_bf16 %[c13], a[4:7], a[44:47], %[c13]
s_add_u32 m0, s44, 32768
v_mfma_f32_16x16x32_bf16 %[c14], a[8:11], a[44:47], %[c14]
ds_read_b128 a[56:59], v40 offset:18432
buffer_load_dwordx4 v32, s[36:39], s45 offen sc1 lds
v_mfma_f32_16x16x32_bf16 %[c15], a[12:15], a[44:47], %[c15]
buffer_load_dwordx4 v32, s[36:39], s45 offen offset:1024 sc1 lds
s_waitcnt lgkmcnt(1)
v_mfma_f32_16x16x32_bf16 %[c0], a[16:19], a[48:51], %[c0]
s_add_u32 m0, s44, 40960
v_mfma_f32_16x16x32_bf16 %[c1], a[20:23], a[48:51], %[c1]
v_mfma_f32_16x16x32_bf16 %[c2], a[24:27], a[48:51], %[c2]
ds_read_b128 a[60:63], v40 offset:19456
buffer_load_dwordx4 v37, s[40:43], s46 offen lds
v_mfma_f32_16x16x32_bf16 %[c3], a[28:31], a[48:51], %[c3]
buffer_load_dwordx4 v38, s[40:43], s46 offen offset:1024 lds
v_mfma_f32_16x16x32_bf16 %[c4], a[16:19], a[52:55], %[c4]
s_add_u32 s45, s45, %[xadv]
ds_read_b64_tr_b16 a[0:1], v49 offset:32768
ds_read_b64_tr_b16 a[2:3], v51 offset:32768
v_mfma_f32_16x16x32_bf16 %[c5], a[20:23], a[52:55], %[c5]
s_add_u32 s46, s46, %[wadv]
ds_read_b64_tr_b16 a[4:5], v50 offset:32768
ds_read_b64_tr_b16 a[6:7], v52 offset:32768
v_mfma_f32_16x16x32_bf16 %[c6], a[24:27], a[52:55], %[c6]
s_cmp_eq_u32 s49, 128
ds_read_b128 a[32:35], v40 offset:32768
v_mfma_f32_16x16x32_bf16 %[c7], a[28:31], a[52:55], %[c7]
s_cselect_b32 s40, s52, s40
s_waitcnt lgkmcnt(5)
v_mfma_f32_16x16x32_bf16 %[c8], a[16:19], a[56:59], %[c8]
s_cselect_b32 s41, s53, s41
ds_read_b64_tr_b16 a[8:9], v49 offset:33280
ds_read_b64_tr_b16 a[10:11], v51 offset:33280
v_mfma_f32_16x16x32_bf16 %[c9], a[20:23], a[56:59], %[c9]
s_cselect_b32 s42, s54, s42
v_mfma_f32_16x16x32_bf16 %[c10], a[24:27], a[56:59], %[c10]
s_cselect_b32 s46, s55, s46
ds_read_b128 a[36:39], v40 offset:33792
v_mfma_f32_16x16x32_bf16 %[c11], a[28:31], a[56:59], %[c11]
ds_read_b64_tr_b16 a[12:13], v50 offset:33280
ds_read_b64_tr_b16 a[14:15], v52 offset:33280
v_mfma_f32_16x16x32_bf16 %[c12], a[16:19], a[60:63], %[c12]
s_add_u32 s49, s49, 1
v_mfma_f32_16x16x32_bf16 %[c13], a[20:23], a[60:63], %[c13]
s_add_u32 m0, s44, 49152
v_mfma_f32_16x16x32_bf16 %[c14], a[24:27], a[60:63], %[c14]
ds_read_b128 a[40:43], v40 offset:34816
buffer_load_dwordx4 v32, s[36:39], s45 offen sc1 lds
v_mfma_f32_16x16x32_bf16 %[c15], a[28:31], a[60:63], %[c15]
buffer_load_dwordx4 v32, s[36:39], s45 offen offset:1024 sc1 lds
s_waitcnt lgkmcnt(1)
v_mfma_f32_16x16x32_bf16 %[c0], a[0:3], a[32:35], %[c0]
s_add_u32 m0, s44, 57344
v_mfma_f32_16x16x32_bf16 %[c1], a[4:7], a[32:35], %[c1]
buffer_load_dwordx4 v37, s[40:43], s46 offen lds
v_mfma_f32_16x16x32_bf16 %[c2], a[8:11], a[32:35], %[c2]
ds_read_b128 a[44:47], v40 offset:35840
buffer_load_dwordx4 v38, s[40:43], s46 offen offset:1024 lds
v_mfma_f32_16x16x32_bf16 %[c3], a[12:15], a[32:35], %[c3]
s_add_u32 s45, s45, %[xadv]
v_mfma_f32_16x16x32_bf16 %[c4], a[0:3], a[36:39], %[c4]
s_add_u32 s46, s46, %[wadv]
ds_read_b64_tr_b16 a[16:17], v49 offset:49152
ds_read_b64_tr_b16 a[18:19], v51 offset:49152
v_mfma_f32_16x16x32_bf16 %[c5], a[4:7], a[36:39], %[c5]
ds_read_b64_tr_b16 a[20:21], v50 offset:49152
ds_read_b64_tr_b16 a[22:23], v52 offset:49152
v_mfma_f32_16x16x32_bf16 %[c6], a[8:11], a[36:39], %[c6]
s_waitcnt vmcnt(16)
s_barrier
v_cmp_ne_u64_e32 vcc, s[70:71], v[68:69]
s_nop 4
s_cmp_lg_u64 vcc, 0
s_cbranch_scc0 L_pok3_%=
s_mov_b32 s72, 0x10000
L_plp3_%=:
s_sleep 2
global_load_dwordx2 v[68:69], %[pu], %[flags] sc1
s_waitcnt vmcnt(0)
v_cmp_ne_u64_e32 vcc, s[70:71], v[68:69]
s_nop 4
s_cmp_lg_u64 vcc, 0
s_cbranch_scc0 L_pok3_%=
s_sub_u32 s72, s72, 1
s_cmp_lg_u32 s72, 0
s_cbranch_scc1 L_plp3_%=
L_pok3_%=:
ds_read_b128 a[48:51], v40 offset:49152
v_mfma_f32_16x16x32_bf16 %[c7], a[12:15], a[36:39], %[c7]
s_cmp_eq_u32 s49, 128
s_waitcnt lgkmcnt(5)
v_mfma_f32_16x16x32_bf16 %[c8], a[0:3], a[40:43], %[c8]
s_cselect_b32 s40, s52, s40
ds_read_b64_tr_b16 a[24:25], v49 offset:49664
ds_read_b64_tr_b16 a[26:27], v51 offset:49664
v_mfma_f32_16x16x32_bf16 %[c9], a[4:7], a[40:43], %[c9]
s_cselect_b32 s41, s53, s41
v_mfma_f32_16x16x32_bf16 %[c10], a[8:11], a[40:43], %[c10]
s_cselect_b32 s42, s54, s42
ds_read_b128 a[52:55], v40 offset:50176
v_mfma_f32_16x16x32_bf16 %[c11], a[12:15], a[40:43], %[c11]
s_cselect_b32 s46, s55, s46
ds_read_b64_tr_b16 a[28:29], v50 offset:49664
ds_read_b64_tr_b16 a[30:31], v52 offset:49664
v_mfma_f32_16x16x32_bf16 %[c12], a[0:3], a[44:47], %[c12]
s_add_u32 s49, s49, 1
v_mfma_f32_16x16x32_bf16 %[c13], a[4:7], a[44:47], %[c13]
s_add_u32 m0, s44, 65536
v_mfma_f32_16x16x32_bf16 %[c14], a[8:11], a[44:47], %[c14]
ds_read_b128 a[56:59], v40 offset:51200
buffer_load_dwordx4 v32, s[36:39], s45 offen sc1 lds
v_mfma_f32_16x16x32_bf16 %[c15], a[12:15], a[44:47], %[c15]
buffer_load_dwordx4 v32, s[36:39], s45 offen offset:1024 sc1 lds
s_waitcnt lgkmcnt(1)
v_mfma_f32_16x16x32_bf16 %[c0], a[16:19], a[48:51], %[c0]
s_add_u32 m0, s44, 73728
v_mfma_f32_16x16x32_bf16 %[c1], a[20:23], a[48:51], %[c1]
v_mfma_f32_16x16x32_bf16 %[c2], a[24:27], a[48:51], %[c2]
ds_read_b128 a[60:63], v40 offset:52224
buffer_load_dwordx4 v37, s[40:43], s46 offen lds
v_mfma_f32_16x16x32_bf16 %[c3], a[28:31], a[48:51], %[c3]
buffer_load_dwordx4 v38, s[40:43], s46 offen offset:1024 lds
v_mfma_f32_16x16x32_bf16 %[c4], a[16:19], a[52:55], %[c4]
s_add_u32 s45, s45, %[xadv]
ds_read_b64_tr_b16 a[0:1], v53
ds_read_b64_tr_b16 a[2:3], v55
v_mfma_f32_16x16x32_bf16 %[c5], a[20:23], a[52:55], %[c5]
s_add_u32 s46, s46, %[wadv]
ds_read_b64_tr_b16 a[4:5], v54
ds_read_b64_tr_b16 a[6:7], v56
v_mfma_f32_16x16x32_bf16 %[c6], a[24:27], a[52:55], %[c6]
s_cmp_eq_u32 s49, 128
ds_read_b128 a[32:35], v41
v_mfma_f32_16x16x32_bf16 %[c7], a[28:31], a[52:55], %[c7]
s_cselect_b32 s40, s52, s40
s_waitcnt lgkmcnt(5)
v_mfma_f32_16x16x32_bf16 %[c8], a[16:19], a[56:59], %[c8]
s_cselect_b32 s41, s53, s41
ds_read_b64_tr_b16 a[8:9], v53 offset:512
ds_read_b64_tr_b16 a[10:11], v55 offset:512
v_mfma_f32_16x16x32_bf16 %[c9], a[20:23], a[56:59], %[c9]
s_cselect_b32 s42, s54, s42
v_mfma_f32_16x16x32_bf16 %[c10], a[24:27], a[56:59], %[c10]
s_cselect_b32 s46, s55, s46
ds_read_b128 a[36:39], v41 offset:1024
v_mfma_f32_16x16x32_bf16 %[c11], a[28:31], a[56:59], %[c11]
ds_read_b64_tr_b16 a[12:13], v54 offset:512
ds_read_b64_tr_b16 a[14:15], v56 offset:512
v_mfma_f32_16x16x32_bf16 %[c12], a[16:19], a[60:63], %[c12]
s_add_u32 s49, s49, 1
v_mfma_f32_16x16x32_bf16 %[c13], a[20:23], a[60:63], %[c13]
s_add_u32 m0, s44, 81920
v_mfma_f32_16x16x32_bf16 %[c14], a[24:27], a[60:63], %[c14]
ds_read_b128 a[40:43], v41 offset:2048
buffer_load_dwordx4 v32, s[36:39], s45 offen sc1 lds
v_mfma_f32_16x16x32_bf16 %[c15], a[28:31], a[60:63], %[c15]
buffer_load_dwordx4 v32, s[36:39], s45 offen offset:1024 sc1 lds
s_waitcnt lgkmcnt(1)
v_mfma_f32_16x16x32_bf16 %[c0], a[0:3], a[32:35], %[c0]
s_add_u32 m0, s44, 90112
v_mfma_f32_16x16x32_bf16 %[c1], a[4:7], a[32:35], %[c1]
buffer_load_dwordx4 v37, s[40:43], s46 offen lds
v_mfma_f32_16x16x32_bf16 %[c2], a[8:11], a[32:35], %[c2]
ds_read_b128 a[44:47], v41 offset:3072
buffer_load_dwordx4 v38, s[40:43], s46 offen offset:1024 lds
v_mfma_f32_16x16x32_bf16 %[c3], a[12:15], a[32:35], %[c3]
s_add_u32 s45, s45, %[xadv]
v_mfma_f32_16x16x32_bf16 %[c4], a[0:3], a[36:39], %[c4]
s_add_u32 s46, s46, %[wadv]
ds_read_b64_tr_b16 a[16:17], v53 offset:16384
ds_read_b64_tr_b16 a[18:19], v55 offset:16384
v_mfma_f32_16x16x32_bf16 %[c5], a[4:7], a[36:39], %[c5]
ds_read_b64_tr_b16 a[20:21], v54 offset:16384
ds_read_b64_tr_b16 a[22:23], v56 offset:16384
v_mfma_f32_16x16x32_bf16 %[c6], a[8:11], a[36:39], %[c6]
s_waitcnt vmcnt(16)
s_barrier
ds_read_b128 a[48:51], v41 offset:16384
v_mfma_f32_16x16x32_bf16 %[c7], a[12:15], a[36:39], %[c7]
s_cmp_eq_u32 s49, 128
s_waitcnt lgkmcnt(5)
v_mfma_f32_16x16x32_bf16 %[c8], a[0:3], a[40:43], %[c8]
s_cselect_b32 s40, s52, s40
ds_read_b64_tr_b16 a[24:25], v53 offset:16896
ds_read_b64_tr_b16 a[26:27], v55 offset:16896
v_mfma_f32_16x16x32_bf16 %[c9], a[4:7], a[40:43], %[c9]
s_cselect_b32 s41, s53, s41
v_mfma_f32_16x16x32_bf16 %[c10], a[8:11], a[40:43], %[c10]
s_cselect_b32 s42, s54, s42
ds_read_b128 a[52:55], v41 offset:17408
v_mfma_f32_16x16x32_bf16 %[c11], a[12:15], a[40:43], %[c11]
s_cselect_b32 s46, s55, s46
ds_read_b64_tr_b16 a[28:29], v54 offset:16896
ds_read_b64_tr_b16 a[30:31], v56 offset:16896
v_mfma_f32_16x16x32_bf16 %[c12], a[0:3], a[44:47], %[c12]
s_add_u32 s49, s49, 1
v_mfma_f32_16x16x32_bf16 %[c13], a[4:7], a[44:47], %[c13]
s_add_u32 m0, s44, 98304
v_mfma_f32_16x16x32_bf16 %[c14], a[8:11], a[44:47], %[c14]
ds_read_b128 a[56:59], v41 offset:18432
buffer_load_dwordx4 v32, s[36:39], s45 offen sc1 lds
v_mfma_f32_16x16x32_bf16 %[c15], a[12:15], a[44:47], %[c15]
buffer_load_dwordx4 v32, s[36:39], s45 offen offset:1024 sc1 lds
s_waitcnt lgkmcnt(1)
v_mfma_f32_16x16x32_bf16 %[c0], a[16:19], a[48:51], %[c0]
s_add_u32 m0, s44, 106496
v_mfma_f32_16x16x32_bf16 %[c1], a[20:23], a[48:51], %[c1]
v_mfma_f32_16x16x32_bf16 %[c2], a[24:27], a[48:51], %[c2]
ds_read_b128 a[60:63], v41 offset:19456
buffer_load_dwordx4 v37, s[40:43], s46 offen lds
v_mfma_f32_16x16x32_bf16 %[c3], a[28:31], a[48:51], %[c3]
buffer_load_dwordx4 v38, s[40:43], s46 offen offset:1024 lds
v_mfma_f32_16x16x32_bf16 %[c4], a[16:19], a[52:55], %[c4]
s_add_u32 s45, s45, %[xadv]
ds_read_b64_tr_b16 a[0:1], v45
ds_read_b64_tr_b16 a[2:3], v47
v_mfma_f32_16x16x32_bf16 %[c5], a[20:23], a[52:55], %[c5]
s_add_u32 s46, s46, %[wadv]
ds_read_b64_tr_b16 a[4:5], v46
ds_read_b64_tr_b16 a[6:7], v48
v_mfma_f32_16x16x32_bf16 %[c6], a[24:27], a[52:55], %[c6]
s_cmp_eq_u32 s49, 128
ds_read_b128 a[32:35], v39
v_mfma_f32_16x16x32_bf16 %[c7], a[28:31], a[52:55], %[c7]
s_cselect_b32 s40, s52, s40
s_waitcnt lgkmcnt(5)
v_mfma_f32_16x16x32_bf16 %[c8], a[16:19], a[56:59], %[c8]
s_cselect_b32 s41, s53, s41
ds_read_b64_tr_b16 a[8:9], v45 offset:512
ds_read_b64_tr_b16 a[10:11], v47 offset:512
v_mfma_f32_16x16x32_bf16 %[c9], a[20:23], a[56:59], %[c9]
s_cselect_b32 s42, s54, s42
v_mfma_f32_16x16x32_bf16 %[c10], a[24:27], a[56:59], %[c10]
s_cselect_b32 s46, s55, s46
ds_read_b128 a[36:39], v39 offset:1024
v_mfma_f32_16x16x32_bf16 %[c11], a[28:31], a[56:59], %[c11]
ds_read_b64_tr_b16 a[12:13], v46 offset:512
ds_read_b64_tr_b16 a[14:15], v48 offset:512
v_mfma_f32_16x16x32_bf16 %[c12], a[16:19], a[60:63], %[c12]
s_add_u32 s49, s49, 1
v_mfma_f32_16x16x32_bf16 %[c13], a[20:23], a[60:63], %[c13]
)ASM"
R"ASM(s_add_u32 m0, s44, 114688
v_mfma_f32_16x16x32_bf16 %[c14], a[24:27], a[60:63], %[c14]
ds_read_b128 a[40:43], v39 offset:2048
buffer_load_dwordx4 v32, s[36:39], s45 offen sc1 lds
v_mfma_f32_16x16x32_bf16 %[c15], a[28:31], a[60:63], %[c15]
buffer_load_dwordx4 v32, s[36:39], s45 offen offset:1024 sc1 lds
s_waitcnt lgkmcnt(1)
v_mfma_f32_16x16x32_bf16 %[c0], a[0:3], a[32:35], %[c0]
s_add_u32 m0, s44, 122880
v_mfma_f32_16x16x32_bf16 %[c1], a[4:7], a[32:35], %[c1]
buffer_load_dwordx4 v37, s[40:43], s46 offen lds
v_mfma_f32_16x16x32_bf16 %[c2], a[8:11], a[32:35], %[c2]
ds_read_b128 a[44:47], v39 offset:3072
buffer_load_dwordx4 v38, s[40:43], s46 offen offset:1024 lds
v_mfma_f32_16x16x32_bf16 %[c3], a[12:15], a[32:35], %[c3]
s_add_u32 s45, s45, %[xadv]
v_mfma_f32_16x16x32_bf16 %[c4], a[0:3], a[36:39], %[c4]
s_add_u32 s46, s46, %[wadv]
ds_read_b64_tr_b16 a[16:17], v45 offset:16384
ds_read_b64_tr_b16 a[18:19], v47 offset:16384
v_mfma_f32_16x16x32_bf16 %[c5], a[4:7], a[36:39], %[c5]
ds_read_b64_tr_b16 a[20:21], v46 offset:16384
ds_read_b64_tr_b16 a[22:23], v48 offset:16384
v_mfma_f32_16x16x32_bf16 %[c6], a[8:11], a[36:39], %[c6]
s_waitcnt vmcnt(16)
s_barrier
ds_read_b128 a[48:51], v39 offset:16384
v_mfma_f32_16x16x32_bf16 %[c7], a[12:15], a[36:39], %[c7]
s_cmp_eq_u32 s49, 128
s_waitcnt lgkmcnt(5)
v_mfma_f32_16x16x32_bf16 %[c8], a[0:3], a[40:43], %[c8]
s_cselect_b32 s40, s52, s40
ds_read_b64_tr_b16 a[24:25], v45 offset:16896
ds_read_b64_tr_b16 a[26:27], v47 offset:16896
v_mfma_f32_16x16x32_bf16 %[c9], a[4:7], a[40:43], %[c9]
s_cselect_b32 s41, s53, s41
v_mfma_f32_16x16x32_bf16 %[c10], a[8:11], a[40:43], %[c10]
s_cselect_b32 s42, s54, s42
ds_read_b128 a[52:55], v39 offset:17408
v_mfma_f32_16x16x32_bf16 %[c11], a[12:15], a[40:43], %[c11]
s_cselect_b32 s46, s55, s46
ds_read_b64_tr_b16 a[28:29], v46 offset:16896
ds_read_b64_tr_b16 a[30:31], v48 offset:16896
v_mfma_f32_16x16x32_bf16 %[c12], a[0:3], a[44:47], %[c12]
s_add_u32 s49, s49, 1
v_mfma_f32_16x16x32_bf16 %[c13], a[4:7], a[44:47], %[c13]
s_add_u32 m0, s44, 131072
v_mfma_f32_16x16x32_bf16 %[c14], a[8:11], a[44:47], %[c14]
ds_read_b128 a[56:59], v39 offset:18432
buffer_load_dwordx4 v32, s[36:39], s45 offen sc1 lds
v_mfma_f32_16x16x32_bf16 %[c15], a[12:15], a[44:47], %[c15]
buffer_load_dwordx4 v32, s[36:39], s45 offen offset:1024 sc1 lds
s_waitcnt lgkmcnt(1)
v_mfma_f32_16x16x32_bf16 %[c0], a[16:19], a[48:51], %[c0]
s_add_u32 m0, s44, 139264
v_mfma_f32_16x16x32_bf16 %[c1], a[20:23], a[48:51], %[c1]
v_mfma_f32_16x16x32_bf16 %[c2], a[24:27], a[48:51], %[c2]
ds_read_b128 a[60:63], v39 offset:19456
buffer_load_dwordx4 v37, s[40:43], s46 offen lds
v_mfma_f32_16x16x32_bf16 %[c3], a[28:31], a[48:51], %[c3]
buffer_load_dwordx4 v38, s[40:43], s46 offen offset:1024 lds
v_mfma_f32_16x16x32_bf16 %[c4], a[16:19], a[52:55], %[c4]
s_add_u32 s45, s45, %[xadv]
ds_read_b64_tr_b16 a[0:1], v45 offset:32768
ds_read_b64_tr_b16 a[2:3], v47 offset:32768
v_mfma_f32_16x16x32_bf16 %[c5], a[20:23], a[52:55], %[c5]
s_add_u32 s46, s46, %[wadv]
ds_read_b64_tr_b16 a[4:5], v46 offset:32768
ds_read_b64_tr_b16 a[6:7], v48 offset:32768
v_mfma_f32_16x16x32_bf16 %[c6], a[24:27], a[52:55], %[c6]
s_cmp_eq_u32 s49, 128
ds_read_b128 a[32:35], v39 offset:32768
v_mfma_f32_16x16x32_bf16 %[c7], a[28:31], a[52:55], %[c7]
s_cselect_b32 s40, s52, s40
s_waitcnt lgkmcnt(5)
v_mfma_f32_16x16x32_bf16 %[c8], a[16:19], a[56:59], %[c8]
s_cselect_b32 s41, s53, s41
ds_read_b64_tr_b16 a[8:9], v45 offset:33280
ds_read_b64_tr_b16 a[10:11], v47 offset:33280
v_mfma_f32_16x16x32_bf16 %[c9], a[20:23], a[56:59], %[c9]
s_cselect_b32 s42, s54, s42
v_mfma_f32_16x16x32_bf16 %[c10], a[24:27], a[56:59], %[c10]
s_cselect_b32 s46, s55, s46
ds_read_b128 a[36:39], v39 offset:33792
v_mfma_f32_16x16x32_bf16 %[c11], a[28:31], a[56:59], %[c11]
ds_read_b64_tr_b16 a[12:13], v46 offset:33280
ds_read_b64_tr_b16 a[14:15], v48 offset:33280
v_mfma_f32_16x16x32_bf16 %[c12], a[16:19], a[60:63], %[c12]
s_add_u32 s49, s49, 1
v_mfma_f32_16x16x32_bf16 %[c13], a[20:23], a[60:63], %[c13]
s_add_u32 m0, s44, 147456
v_mfma_f32_16x16x32_bf16 %[c14], a[24:27], a[60:63], %[c14]
ds_read_b128 a[40:43], v39 offset:34816
buffer_load_dwordx4 v32, s[36:39], s45 offen sc1 lds
v_mfma_f32_16x16x32_bf16 %[c15], a[28:31], a[60:63], %[c15]
buffer_load_dwordx4 v32, s[36:39], s45 offen offset:1024 sc1 lds
s_waitcnt lgkmcnt(1)
v_mfma_f32_16x16x32_bf16 %[c0], a[0:3], a[32:35], %[c0]
s_add_u32 m0, s44, 155648
v_mfma_f32_16x16x32_bf16 %[c1], a[4:7], a[32:35], %[c1]
buffer_load_dwordx4 v37, s[40:43], s46 offen lds
v_mfma_f32_16x16x32_bf16 %[c2], a[8:11], a[32:35], %[c2]
ds_read_b128 a[44:47], v39 offset:35840
buffer_load_dwordx4 v38, s[40:43], s46 offen offset:1024 lds
v_mfma_f32_16x16x32_bf16 %[c3], a[12:15], a[32:35], %[c3]
s_add_u32 s45, s45, %[xadv]
v_mfma_f32_16x16x32_bf16 %[c4], a[0:3], a[36:39], %[c4]
s_add_u32 s46, s46, %[wadv]
ds_read_b64_tr_b16 a[16:17], v45 offset:49152
ds_read_b64_tr_b16 a[18:19], v47 offset:49152
v_mfma_f32_16x16x32_bf16 %[c5], a[4:7], a[36:39], %[c5]
ds_read_b64_tr_b16 a[20:21], v46 offset:49152
ds_read_b64_tr_b16 a[22:23], v48 offset:49152
v_mfma_f32_16x16x32_bf16 %[c6], a[8:11], a[36:39], %[c6]
s_waitcnt vmcnt(16)
s_barrier
ds_read_b128 a[48:51], v39 offset:49152
v_mfma_f32_16x16x32_bf16 %[c7], a[12:15], a[36:39], %[c7]
s_cmp_eq_u32 s49, 128
s_waitcnt lgkmcnt(5)
v_mfma_f32_16x16x32_bf16 %[c8], a[0:3], a[40:43], %[c8]
s_cselect_b32 s40, s52, s40
ds_read_b64_tr_b16 a[24:25], v45 offset:49664
ds_read_b64_tr_b16 a[26:27], v47 offset:49664
v_mfma_f32_16x16x32_bf16 %[c9], a[4:7], a[40:43], %[c9]
s_cselect_b32 s41, s53, s41
v_mfma_f32_16x16x32_bf16 %[c10], a[8:11], a[40:43], %[c10]
s_cselect_b32 s42, s54, s42
ds_read_b128 a[52:55], v39 offset:50176
v_mfma_f32_16x16x32_bf16 %[c11], a[12:15], a[40:43], %[c11]
s_cselect_b32 s46, s55, s46
ds_read_b64_tr_b16 a[28:29], v46 offset:49664
ds_read_b64_tr_b16 a[30:31], v48 offset:49664
v_mfma_f32_16x16x32_bf16 %[c12], a[0:3], a[44:47], %[c12]
s_add_u32 s49, s49, 1
v_mfma_f32_16x16x32_bf16 %[c13], a[4:7], a[44:47], %[c13]
s_add_u32 m0, s44, 0
v_mfma_f32_16x16x32_bf16 %[c14], a[8:11], a[44:47], %[c14]
ds_read_b128 a[56:59], v39 offset:51200
buffer_load_dwordx4 v32, s[36:39], s45 offen sc1 lds
v_mfma_f32_16x16x32_bf16 %[c15], a[12:15], a[44:47], %[c15]
buffer_load_dwordx4 v32, s[36:39], s45 offen offset:1024 sc1 lds
s_waitcnt lgkmcnt(1)
v_mfma_f32_16x16x32_bf16 %[c0], a[16:19], a[48:51], %[c0]
s_add_u32 m0, s44, 8192
v_mfma_f32_16x16x32_bf16 %[c1], a[20:23], a[48:51], %[c1]
v_mfma_f32_16x16x32_bf16 %[c2], a[24:27], a[48:51], %[c2]
ds_read_b128 a[60:63], v39 offset:52224
buffer_load_dwordx4 v37, s[40:43], s46 offen lds
v_mfma_f32_16x16x32_bf16 %[c3], a[28:31], a[48:51], %[c3]
buffer_load_dwordx4 v38, s[40:43], s46 offen offset:1024 lds
v_mfma_f32_16x16x32_bf16 %[c4], a[16:19], a[52:55], %[c4]
s_add_u32 s45, s45, %[xadv]
ds_read_b64_tr_b16 a[0:1], v49
ds_read_b64_tr_b16 a[2:3], v51
v_mfma_f32_16x16x32_bf16 %[c5], a[20:23], a[52:55], %[c5]
s_add_u32 s46, s46, %[wadv]
ds_read_b64_tr_b16 a[4:5], v50
ds_read_b64_tr_b16 a[6:7], v52
v_mfma_f32_16x16x32_bf16 %[c6], a[24:27], a[52:55], %[c6]
s_cmp_eq_u32 s49, 128
ds_read_b128 a[32:35], v40
v_mfma_f32_16x16x32_bf16 %[c7], a[28:31], a[52:55], %[c7]
s_cselect_b32 s40, s52, s40
s_waitcnt lgkmcnt(5)
v_mfma_f32_16x16x32_bf16 %[c8], a[16:19], a[56:59], %[c8]
s_cselect_b32 s41, s53, s41
ds_read_b64_tr_b16 a[8:9], v49 offset:512
ds_read_b64_tr_b16 a[10:11], v51 offset:512
v_mfma_f32_16x16x32_bf16 %[c9], a[20:23], a[56:59], %[c9]
s_cselect_b32 s42, s54, s42
v_mfma_f32_16x16x32_bf16 %[c10], a[24:27], a[56:59], %[c10]
s_cselect_b32 s46, s55, s46
ds_read_b128 a[36:39], v40 offset:1024
v_mfma_f32_16x16x32_bf16 %[c11], a[28:31], a[56:59], %[c11]
ds_read_b64_tr_b16 a[12:13], v50 offset:512
ds_read_b64_tr_b16 a[14:15], v52 offset:512
v_mfma_f32_16x16x32_bf16 %[c12], a[16:19], a[60:63], %[c12]
s_add_u32 s49, s49, 1
v_mfma_f32_16x16x32_bf16 %[c13], a[20:23], a[60:63], %[c13]
s_add_u32 m0, s44, 16384
v_mfma_f32_16x16x32_bf16 %[c14], a[24:27], a[60:63], %[c14]
ds_read_b128 a[40:43], v40 offset:2048
buffer_load_dwordx4 v32, s[36:39], s45 offen sc1 lds
v_mfma_f32_16x16x32_bf16 %[c15], a[28:31], a[60:63], %[c15]
buffer_load_dwordx4 v32, s[36:39], s45 offen offset:1024 sc1 lds
s_waitcnt lgkmcnt(1)
v_mfma_f32_16x16x32_bf16 %[c0], a[0:3], a[32:35], %[c0]
s_add_u32 m0, s44, 24576
v_mfma_f32_16x16x32_bf16 %[c1], a[4:7], a[32:35], %[c1]
buffer_load_dwordx4 v37, s[40:43], s46 offen lds
v_mfma_f32_16x16x32_bf16 %[c2], a[8:11], a[32:35], %[c2]
ds_read_b128 a[44:47], v40 offset:3072
buffer_load_dwordx4 v38, s[40:43], s46 offen offset:1024 lds
v_mfma_f32_16x16x32_bf16 %[c3], a[12:15], a[32:35], %[c3]
s_add_u32 s45, s45, %[xadv]
v_mfma_f32_16x16x32_bf16 %[c4], a[0:3], a[36:39], %[c4]
s_add_u32 s46, s46, %[wadv]
ds_read_b64_tr_b16 a[16:17], v49 offset:16384
ds_read_b64_tr_b16 a[18:19], v51 offset:16384
v_mfma_f32_16x16x32_bf16 %[c5], a[4:7], a[36:39], %[c5]
ds_read_b64_tr_b16 a[20:21], v50 offset:16384
ds_read_b64_tr_b16 a[22:23], v52 offset:16384
v_mfma_f32_16x16x32_bf16 %[c6], a[8:11], a[36:39], %[c6]
s_waitcnt vmcnt(16)
s_barrier
ds_read_b128 a[48:51], v40 offset:16384
v_mfma_f32_16x16x32_bf16 %[c7], a[12:15], a[36:39], %[c7]
s_cmp_eq_u32 s49, 128
s_waitcnt lgkmcnt(5)
v_mfma_f32_16x16x32_bf16 %[c8], a[0:3], a[40:43], %[c8]
s_cselect_b32 s40, s52, s40
ds_read_b64_tr_b16 a[24:25], v49 offset:16896
ds_read_b64_tr_b16 a[26:27], v51 offset:16896
v_mfma_f32_16x16x32_bf16 %[c9], a[4:7], a[40:43], %[c9]
s_cselect_b32 s41, s53, s41
v_mfma_f32_16x16x32_bf16 %[c10], a[8:11], a[40:43], %[c10]
s_cselect_b32 s42, s54, s42
ds_read_b128 a[52:55], v40 offset:17408
v_mfma_f32_16x16x32_bf16 %[c11], a[12:15], a[40:43], %[c11]
s_cselect_b32 s46, s55, s46
ds_read_b64_tr_b16 a[28:29], v50 offset:16896
ds_read_b64_tr_b16 a[30:31], v52 offset:16896
v_mfma_f32_16x16x32_bf16 %[c12], a[0:3], a[44:47], %[c12]
s_add_u32 s49, s49, 1
v_mfma_f32_16x16x32_bf16 %[c13], a[4:7], a[44:47], %[c13]
s_add_u32 m0, s44, 32768
v_mfma_f32_16x16x32_bf16 %[c14], a[8:11], a[44:47], %[c14]
ds_read_b128 a[56:59], v40 offset:18432
buffer_load_dwordx4 v32, s[36:39], s45 offen sc1 lds
v_mfma_f32_16x16x32_bf16 %[c15], a[12:15], a[44:47], %[c15]
buffer_load_dwordx4 v32, s[36:39], s45 offen offset:1024 sc1 lds
s_waitcnt lgkmcnt(1)
v_mfma_f32_16x16x32_bf16 %[c0], a[16:19], a[48:51], %[c0]
s_add_u32 m0, s44, 40960
v_mfma_f32_16x16x32_bf16 %[c1], a[20:23], a[48:51], %[c1]
v_mfma_f32_16x16x32_bf16 %[c2], a[24:27], a[48:51], %[c2]
ds_read_b128 a[60:63], v40 offset:19456
buffer_load_dwordx4 v37, s[40:43], s46 offen lds
v_mfma_f32_16x16x32_bf16 %[c3], a[28:31], a[48:51], %[c3]
buffer_load_dwordx4 v38, s[40:43], s46 offen offset:1024 lds
v_mfma_f32_16x16x32_bf16 %[c4], a[16:19], a[52:55], %[c4]
s_add_u32 s45, s45, %[xadv]
ds_read_b64_tr_b16 a[0:1], v49 offset:32768
ds_read_b64_tr_b16 a[2:3], v51 offset:32768
v_mfma_f32_16x16x32_bf16 %[c5], a[20:23], a[52:55], %[c5]
s_add_u32 s46, s46, %[wadv]
ds_read_b64_tr_b16 a[4:5], v50 offset:32768
ds_read_b64_tr_b16 a[6:7], v52 offset:32768
v_mfma_f32_16x16x32_bf16 %[c6], a[24:27], a[52:55], %[c6]
s_cmp_eq_u32 s49, 128
ds_read_b128 a[32:35], v40 offset:32768
v_mfma_f32_16x16x32_bf16 %[c7], a[28:31], a[52:55], %[c7]
s_cselect_b32 s40, s52, s40
s_waitcnt lgkmcnt(5)
v_mfma_f32_16x16x32_bf16 %[c8], a[16:19], a[56:59], %[c8]
s_cselect_b32 s41, s53, s41
ds_read_b64_tr_b16 a[8:9], v49 offset:33280
ds_read_b64_tr_b16 a[10:11], v51 offset:33280
v_mfma_f32_16x16x32_bf16 %[c9], a[20:23], a[56:59], %[c9]
s_cselect_b32 s42, s54, s42
v_mfma_f32_16x16x32_bf16 %[c10], a[24:27], a[56:59], %[c10]
s_cselect_b32 s46, s55, s46
ds_read_b128 a[36:39], v40 offset:33792
v_mfma_f32_16x16x32_bf16 %[c11], a[28:31], a[56:59], %[c11]
ds_read_b64_tr_b16 a[12:13], v50 offset:33280
ds_read_b64_tr_b16 a[14:15], v52 offset:33280
v_mfma_f32_16x16x32_bf16 %[c12], a[16:19], a[60:63], %[c12]
s_add_u32 s49, s49, 1
v_mfma_f32_16x16x32_bf16 %[c13], a[20:23], a[60:63], %[c13]
s_add_u32 m0, s44, 49152
v_mfma_f32_16x16x32_bf16 %[c14], a[24:27], a[60:63], %[c14]
)ASM"
R"ASM(ds_read_b128 a[40:43], v40 offset:34816
buffer_load_dwordx4 v32, s[36:39], s45 offen sc1 lds
v_mfma_f32_16x16x32_bf16 %[c15], a[28:31], a[60:63], %[c15]
buffer_load_dwordx4 v32, s[36:39], s45 offen offset:1024 sc1 lds
s_waitcnt lgkmcnt(1)
v_mfma_f32_16x16x32_bf16 %[c0], a[0:3], a[32:35], %[c0]
s_add_u32 m0, s44, 57344
v_mfma_f32_16x16x32_bf16 %[c1], a[4:7], a[32:35], %[c1]
buffer_load_dwordx4 v37, s[40:43], s46 offen lds
v_mfma_f32_16x16x32_bf16 %[c2], a[8:11], a[32:35], %[c2]
ds_read_b128 a[44:47], v40 offset:35840
buffer_load_dwordx4 v38, s[40:43], s46 offen offset:1024 lds
v_mfma_f32_16x16x32_bf16 %[c3], a[12:15], a[32:35], %[c3]
s_add_u32 s45, s45, %[xadv]
v_mfma_f32_16x16x32_bf16 %[c4], a[0:3], a[36:39], %[c4]
s_add_u32 s46, s46, %[wadv]
ds_read_b64_tr_b16 a[16:17], v49 offset:49152
ds_read_b64_tr_b16 a[18:19], v51 offset:49152
v_mfma_f32_16x16x32_bf16 %[c5], a[4:7], a[36:39], %[c5]
ds_read_b64_tr_b16 a[20:21], v50 offset:49152
ds_read_b64_tr_b16 a[22:23], v52 offset:49152
v_mfma_f32_16x16x32_bf16 %[c6], a[8:11], a[36:39], %[c6]
s_waitcnt vmcnt(16)
s_barrier
ds_read_b128 a[48:51], v40 offset:49152
v_mfma_f32_16x16x32_bf16 %[c7], a[12:15], a[36:39], %[c7]
s_cmp_eq_u32 s49, 128
s_waitcnt lgkmcnt(5)
v_mfma_f32_16x16x32_bf16 %[c8], a[0:3], a[40:43], %[c8]
s_cselect_b32 s40, s52, s40
ds_read_b64_tr_b16 a[24:25], v49 offset:49664
ds_read_b64_tr_b16 a[26:27], v51 offset:49664
v_mfma_f32_16x16x32_bf16 %[c9], a[4:7], a[40:43], %[c9]
s_cselect_b32 s41, s53, s41
v_mfma_f32_16x16x32_bf16 %[c10], a[8:11], a[40:43], %[c10]
s_cselect_b32 s42, s54, s42
ds_read_b128 a[52:55], v40 offset:50176
v_mfma_f32_16x16x32_bf16 %[c11], a[12:15], a[40:43], %[c11]
s_cselect_b32 s46, s55, s46
ds_read_b64_tr_b16 a[28:29], v50 offset:49664
ds_read_b64_tr_b16 a[30:31], v52 offset:49664
v_mfma_f32_16x16x32_bf16 %[c12], a[0:3], a[44:47], %[c12]
s_add_u32 s49, s49, 1
v_mfma_f32_16x16x32_bf16 %[c13], a[4:7], a[44:47], %[c13]
s_add_u32 m0, s44, 65536
v_mfma_f32_16x16x32_bf16 %[c14], a[8:11], a[44:47], %[c14]
ds_read_b128 a[56:59], v40 offset:51200
buffer_load_dwordx4 v32, s[36:39], s45 offen sc1 lds
v_mfma_f32_16x16x32_bf16 %[c15], a[12:15], a[44:47], %[c15]
buffer_load_dwordx4 v32, s[36:39], s45 offen offset:1024 sc1 lds
s_waitcnt lgkmcnt(1)
v_mfma_f32_16x16x32_bf16 %[c0], a[16:19], a[48:51], %[c0]
s_add_u32 m0, s44, 73728
v_mfma_f32_16x16x32_bf16 %[c1], a[20:23], a[48:51], %[c1]
v_mfma_f32_16x16x32_bf16 %[c2], a[24:27], a[48:51], %[c2]
ds_read_b128 a[60:63], v40 offset:52224
buffer_load_dwordx4 v37, s[40:43], s46 offen lds
v_mfma_f32_16x16x32_bf16 %[c3], a[28:31], a[48:51], %[c3]
buffer_load_dwordx4 v38, s[40:43], s46 offen offset:1024 lds
v_mfma_f32_16x16x32_bf16 %[c4], a[16:19], a[52:55], %[c4]
s_add_u32 s45, s45, %[xadv]
ds_read_b64_tr_b16 a[0:1], v53
ds_read_b64_tr_b16 a[2:3], v55
v_mfma_f32_16x16x32_bf16 %[c5], a[20:23], a[52:55], %[c5]
s_add_u32 s46, s46, %[wadv]
ds_read_b64_tr_b16 a[4:5], v54
ds_read_b64_tr_b16 a[6:7], v56
v_mfma_f32_16x16x32_bf16 %[c6], a[24:27], a[52:55], %[c6]
s_cmp_eq_u32 s49, 128
ds_read_b128 a[32:35], v41
v_mfma_f32_16x16x32_bf16 %[c7], a[28:31], a[52:55], %[c7]
s_cselect_b32 s40, s52, s40
s_waitcnt lgkmcnt(5)
v_mfma_f32_16x16x32_bf16 %[c8], a[16:19], a[56:59], %[c8]
s_cselect_b32 s41, s53, s41
ds_read_b64_tr_b16 a[8:9], v53 offset:512
ds_read_b64_tr_b16 a[10:11], v55 offset:512
v_mfma_f32_16x16x32_bf16 %[c9], a[20:23], a[56:59], %[c9]
s_cselect_b32 s42, s54, s42
v_mfma_f32_16x16x32_bf16 %[c10], a[24:27], a[56:59], %[c10]
s_cselect_b32 s46, s55, s46
ds_read_b128 a[36:39], v41 offset:1024
v_mfma_f32_16x16x32_bf16 %[c11], a[28:31], a[56:59], %[c11]
ds_read_b64_tr_b16 a[12:13], v54 offset:512
ds_read_b64_tr_b16 a[14:15], v56 offset:512
v_mfma_f32_16x16x32_bf16 %[c12], a[16:19], a[60:63], %[c12]
s_add_u32 s49, s49, 1
v_mfma_f32_16x16x32_bf16 %[c13], a[20:23], a[60:63], %[c13]
s_add_u32 m0, s44, 81920
v_mfma_f32_16x16x32_bf16 %[c14], a[24:27], a[60:63], %[c14]
ds_read_b128 a[40:43], v41 offset:2048
buffer_load_dwordx4 v32, s[36:39], s45 offen sc1 lds
v_mfma_f32_16x16x32_bf16 %[c15], a[28:31], a[60:63], %[c15]
buffer_load_dwordx4 v32, s[36:39], s45 offen offset:1024 sc1 lds
s_waitcnt lgkmcnt(1)
v_mfma_f32_16x16x32_bf16 %[c0], a[0:3], a[32:35], %[c0]
s_add_u32 m0, s44, 90112
v_mfma_f32_16x16x32_bf16 %[c1], a[4:7], a[32:35], %[c1]
buffer_load_dwordx4 v37, s[40:43], s46 offen lds
v_mfma_f32_16x16x32_bf16 %[c2], a[8:11], a[32:35], %[c2]
ds_read_b128 a[44:47], v41 offset:3072
buffer_load_dwordx4 v38, s[40:43], s46 offen offset:1024 lds
v_mfma_f32_16x16x32_bf16 %[c3], a[12:15], a[32:35], %[c3]
s_add_u32 s45, s45, %[xadv]
v_mfma_f32_16x16x32_bf16 %[c4], a[0:3], a[36:39], %[c4]
s_add_u32 s46, s46, %[wadv]
ds_read_b64_tr_b16 a[16:17], v53 offset:16384
ds_read_b64_tr_b16 a[18:19], v55 offset:16384
v_mfma_f32_16x16x32_bf16 %[c5], a[4:7], a[36:39], %[c5]
ds_read_b64_tr_b16 a[20:21], v54 offset:16384
ds_read_b64_tr_b16 a[22:23], v56 offset:16384
v_mfma_f32_16x16x32_bf16 %[c6], a[8:11], a[36:39], %[c6]
s_waitcnt vmcnt(16)
s_barrier
ds_read_b128 a[48:51], v41 offset:16384
v_mfma_f32_16x16x32_bf16 %[c7], a[12:15], a[36:39], %[c7]
s_cmp_eq_u32 s49, 128
s_waitcnt lgkmcnt(5)
v_mfma_f32_16x16x32_bf16 %[c8], a[0:3], a[40:43], %[c8]
s_cselect_b32 s40, s52, s40
ds_read_b64_tr_b16 a[24:25], v53 offset:16896
ds_read_b64_tr_b16 a[26:27], v55 offset:16896
v_mfma_f32_16x16x32_bf16 %[c9], a[4:7], a[40:43], %[c9]
s_cselect_b32 s41, s53, s41
v_mfma_f32_16x16x32_bf16 %[c10], a[8:11], a[40:43], %[c10]
s_cselect_b32 s42, s54, s42
ds_read_b128 a[52:55], v41 offset:17408
v_mfma_f32_16x16x32_bf16 %[c11], a[12:15], a[40:43], %[c11]
s_cselect_b32 s46, s55, s46
ds_read_b64_tr_b16 a[28:29], v54 offset:16896
ds_read_b64_tr_b16 a[30:31], v56 offset:16896
v_mfma_f32_16x16x32_bf16 %[c12], a[0:3], a[44:47], %[c12]
s_add_u32 s49, s49, 1
v_mfma_f32_16x16x32_bf16 %[c13], a[4:7], a[44:47], %[c13]
s_add_u32 m0, s44, 98304
v_mfma_f32_16x16x32_bf16 %[c14], a[8:11], a[44:47], %[c14]
ds_read_b128 a[56:59], v41 offset:18432
buffer_load_dwordx4 v32, s[36:39], s45 offen sc1 lds
v_mfma_f32_16x16x32_bf16 %[c15], a[12:15], a[44:47], %[c15]
buffer_load_dwordx4 v32, s[36:39], s45 offen offset:1024 sc1 lds
s_waitcnt lgkmcnt(1)
v_mfma_f32_16x16x32_bf16 %[c0], a[16:19], a[48:51], %[c0]
s_add_u32 m0, s44, 106496
v_mfma_f32_16x16x32_bf16 %[c1], a[20:23], a[48:51], %[c1]
v_mfma_f32_16x16x32_bf16 %[c2], a[24:27], a[48:51], %[c2]
ds_read_b128 a[60:63], v41 offset:19456
buffer_load_dwordx4 v37, s[40:43], s46 offen lds
v_mfma_f32_16x16x32_bf16 %[c3], a[28:31], a[48:51], %[c3]
buffer_load_dwordx4 v38, s[40:43], s46 offen offset:1024 lds
v_mfma_f32_16x16x32_bf16 %[c4], a[16:19], a[52:55], %[c4]
s_add_u32 s45, s45, %[xadv]
ds_read_b64_tr_b16 a[0:1], v45
ds_read_b64_tr_b16 a[2:3], v47
v_mfma_f32_16x16x32_bf16 %[c5], a[20:23], a[52:55], %[c5]
s_add_u32 s46, s46, %[wadv]
ds_read_b64_tr_b16 a[4:5], v46
ds_read_b64_tr_b16 a[6:7], v48
v_mfma_f32_16x16x32_bf16 %[c6], a[24:27], a[52:55], %[c6]
s_cmp_eq_u32 s49, 128
ds_read_b128 a[32:35], v39
v_mfma_f32_16x16x32_bf16 %[c7], a[28:31], a[52:55], %[c7]
s_cselect_b32 s40, s52, s40
s_waitcnt lgkmcnt(5)
v_mfma_f32_16x16x32_bf16 %[c8], a[16:19], a[56:59], %[c8]
s_cselect_b32 s41, s53, s41
ds_read_b64_tr_b16 a[8:9], v45 offset:512
ds_read_b64_tr_b16 a[10:11], v47 offset:512
v_mfma_f32_16x16x32_bf16 %[c9], a[20:23], a[56:59], %[c9]
s_cselect_b32 s42, s54, s42
v_mfma_f32_16x16x32_bf16 %[c10], a[24:27], a[56:59], %[c10]
s_cselect_b32 s46, s55, s46
ds_read_b128 a[36:39], v39 offset:1024
v_mfma_f32_16x16x32_bf16 %[c11], a[28:31], a[56:59], %[c11]
ds_read_b64_tr_b16 a[12:13], v46 offset:512
ds_read_b64_tr_b16 a[14:15], v48 offset:512
v_mfma_f32_16x16x32_bf16 %[c12], a[16:19], a[60:63], %[c12]
s_add_u32 s49, s49, 1
v_mfma_f32_16x16x32_bf16 %[c13], a[20:23], a[60:63], %[c13]
s_add_u32 m0, s44, 114688
v_mfma_f32_16x16x32_bf16 %[c14], a[24:27], a[60:63], %[c14]
ds_read_b128 a[40:43], v39 offset:2048
buffer_load_dwordx4 v32, s[36:39], s45 offen sc1 lds
v_mfma_f32_16x16x32_bf16 %[c15], a[28:31], a[60:63], %[c15]
buffer_load_dwordx4 v32, s[36:39], s45 offen offset:1024 sc1 lds
s_waitcnt lgkmcnt(1)
v_mfma_f32_16x16x32_bf16 %[c0], a[0:3], a[32:35], %[c0]
s_add_u32 m0, s44, 122880
v_mfma_f32_16x16x32_bf16 %[c1], a[4:7], a[32:35], %[c1]
buffer_load_dwordx4 v37, s[40:43], s46 offen lds
v_mfma_f32_16x16x32_bf16 %[c2], a[8:11], a[32:35], %[c2]
ds_read_b128 a[44:47], v39 offset:3072
buffer_load_dwordx4 v38, s[40:43], s46 offen offset:1024 lds
v_mfma_f32_16x16x32_bf16 %[c3], a[12:15], a[32:35], %[c3]
s_add_u32 s45, s45, %[xadv]
v_mfma_f32_16x16x32_bf16 %[c4], a[0:3], a[36:39], %[c4]
s_add_u32 s46, s46, %[wadv]
ds_read_b64_tr_b16 a[16:17], v45 offset:16384
ds_read_b64_tr_b16 a[18:19], v47 offset:16384
v_mfma_f32_16x16x32_bf16 %[c5], a[4:7], a[36:39], %[c5]
ds_read_b64_tr_b16 a[20:21], v46 offset:16384
ds_read_b64_tr_b16 a[22:23], v48 offset:16384
v_mfma_f32_16x16x32_bf16 %[c6], a[8:11], a[36:39], %[c6]
s_waitcnt vmcnt(16)
s_barrier
ds_read_b128 a[48:51], v39 offset:16384
v_mfma_f32_16x16x32_bf16 %[c7], a[12:15], a[36:39], %[c7]
s_cmp_eq_u32 s49, 128
s_waitcnt lgkmcnt(5)
v_mfma_f32_16x16x32_bf16 %[c8], a[0:3], a[40:43], %[c8]
s_cselect_b32 s40, s52, s40
ds_read_b64_tr_b16 a[24:25], v45 offset:16896
ds_read_b64_tr_b16 a[26:27], v47 offset:16896
v_mfma_f32_16x16x32_bf16 %[c9], a[4:7], a[40:43], %[c9]
s_cselect_b32 s41, s53, s41
v_mfma_f32_16x16x32_bf16 %[c10], a[8:11], a[40:43], %[c10]
s_cselect_b32 s42, s54, s42
ds_read_b128 a[52:55], v39 offset:17408
v_mfma_f32_16x16x32_bf16 %[c11], a[12:15], a[40:43], %[c11]
s_cselect_b32 s46, s55, s46
ds_read_b64_tr_b16 a[28:29], v46 offset:16896
ds_read_b64_tr_b16 a[30:31], v48 offset:16896
v_mfma_f32_16x16x32_bf16 %[c12], a[0:3], a[44:47], %[c12]
s_add_u32 s49, s49, 1
v_mfma_f32_16x16x32_bf16 %[c13], a[4:7], a[44:47], %[c13]
s_add_u32 m0, s44, 131072
v_mfma_f32_16x16x32_bf16 %[c14], a[8:11], a[44:47], %[c14]
ds_read_b128 a[56:59], v39 offset:18432
buffer_load_dwordx4 v32, s[36:39], s45 offen sc1 lds
v_mfma_f32_16x16x32_bf16 %[c15], a[12:15], a[44:47], %[c15]
buffer_load_dwordx4 v32, s[36:39], s45 offen offset:1024 sc1 lds
s_waitcnt lgkmcnt(1)
v_mfma_f32_16x16x32_bf16 %[c0], a[16:19], a[48:51], %[c0]
s_add_u32 m0, s44, 139264
v_mfma_f32_16x16x32_bf16 %[c1], a[20:23], a[48:51], %[c1]
v_mfma_f32_16x16x32_bf16 %[c2], a[24:27], a[48:51], %[c2]
ds_read_b128 a[60:63], v39 offset:19456
buffer_load_dwordx4 v37, s[40:43], s46 offen lds
v_mfma_f32_16x16x32_bf16 %[c3], a[28:31], a[48:51], %[c3]
buffer_load_dwordx4 v38, s[40:43], s46 offen offset:1024 lds
v_mfma_f32_16x16x32_bf16 %[c4], a[16:19], a[52:55], %[c4]
s_add_u32 s45, s45, %[xadv]
ds_read_b64_tr_b16 a[0:1], v45 offset:32768
ds_read_b64_tr_b16 a[2:3], v47 offset:32768
v_mfma_f32_16x16x32_bf16 %[c5], a[20:23], a[52:55], %[c5]
s_add_u32 s46, s46, %[wadv]
ds_read_b64_tr_b16 a[4:5], v46 offset:32768
ds_read_b64_tr_b16 a[6:7], v48 offset:32768
v_mfma_f32_16x16x32_bf16 %[c6], a[24:27], a[52:55], %[c6]
s_cmp_eq_u32 s49, 128
ds_read_b128 a[32:35], v39 offset:32768
v_mfma_f32_16x16x32_bf16 %[c7], a[28:31], a[52:55], %[c7]
s_cselect_b32 s40, s52, s40
s_waitcnt lgkmcnt(5)
v_mfma_f32_16x16x32_bf16 %[c8], a[16:19], a[56:59], %[c8]
s_cselect_b32 s41, s53, s41
ds_read_b64_tr_b16 a[8:9], v45 offset:33280
ds_read_b64_tr_b16 a[10:11], v47 offset:33280
v_mfma_f32_16x16x32_bf16 %[c9], a[20:23], a[56:59], %[c9]
s_cselect_b32 s42, s54, s42
v_mfma_f32_16x16x32_bf16 %[c10], a[24:27], a[56:59], %[c10]
s_cselect_b32 s46, s55, s46
ds_read_b128 a[36:39], v39 offset:33792
v_mfma_f32_16x16x32_bf16 %[c11], a[28:31], a[56:59], %[c11]
ds_read_b64_tr_b16 a[12:13], v46 offset:33280
ds_read_b64_tr_b16 a[14:15], v48 offset:33280
v_mfma_f32_16x16x32_bf16 %[c12], a[16:19], a[60:63], %[c12]
s_add_u32 s49, s49, 1
v_mfma_f32_16x16x32_bf16 %[c13], a[20:23], a[60:63], %[c13]
s_add_u32 m0, s44, 147456
v_mfma_f32_16x16x32_bf16 %[c14], a[24:27], a[60:63], %[c14]
ds_read_b128 a[40:43], v39 offset:34816
buffer_load_dwordx4 v32, s[36:39], s45 offen sc1 lds
v_mfma_f32_16x16x32_bf16 %[c15], a[28:31], a[60:63], %[c15]
)ASM"
R"ASM(buffer_load_dwordx4 v32, s[36:39], s45 offen offset:1024 sc1 lds
s_waitcnt lgkmcnt(1)
v_mfma_f32_16x16x32_bf16 %[c0], a[0:3], a[32:35], %[c0]
s_add_u32 m0, s44, 155648
v_mfma_f32_16x16x32_bf16 %[c1], a[4:7], a[32:35], %[c1]
buffer_load_dwordx4 v37, s[40:43], s46 offen lds
v_mfma_f32_16x16x32_bf16 %[c2], a[8:11], a[32:35], %[c2]
ds_read_b128 a[44:47], v39 offset:35840
buffer_load_dwordx4 v38, s[40:43], s46 offen offset:1024 lds
v_mfma_f32_16x16x32_bf16 %[c3], a[12:15], a[32:35], %[c3]
s_add_u32 s45, s45, %[xadv]
v_mfma_f32_16x16x32_bf16 %[c4], a[0:3], a[36:39], %[c4]
s_add_u32 s46, s46, %[wadv]
ds_read_b64_tr_b16 a[16:17], v45 offset:49152
ds_read_b64_tr_b16 a[18:19], v47 offset:49152
v_mfma_f32_16x16x32_bf16 %[c5], a[4:7], a[36:39], %[c5]
ds_read_b64_tr_b16 a[20:21], v46 offset:49152
ds_read_b64_tr_b16 a[22:23], v48 offset:49152
v_mfma_f32_16x16x32_bf16 %[c6], a[8:11], a[36:39], %[c6]
s_waitcnt vmcnt(16)
s_barrier
ds_read_b128 a[48:51], v39 offset:49152
v_mfma_f32_16x16x32_bf16 %[c7], a[12:15], a[36:39], %[c7]
s_cmp_eq_u32 s49, 128
s_waitcnt lgkmcnt(5)
v_mfma_f32_16x16x32_bf16 %[c8], a[0:3], a[40:43], %[c8]
s_cselect_b32 s40, s52, s40
ds_read_b64_tr_b16 a[24:25], v45 offset:49664
ds_read_b64_tr_b16 a[26:27], v47 offset:49664
v_mfma_f32_16x16x32_bf16 %[c9], a[4:7], a[40:43], %[c9]
s_cselect_b32 s41, s53, s41
v_mfma_f32_16x16x32_bf16 %[c10], a[8:11], a[40:43], %[c10]
s_cselect_b32 s42, s54, s42
ds_read_b128 a[52:55], v39 offset:50176
v_mfma_f32_16x16x32_bf16 %[c11], a[12:15], a[40:43], %[c11]
s_cselect_b32 s46, s55, s46
ds_read_b64_tr_b16 a[28:29], v46 offset:49664
ds_read_b64_tr_b16 a[30:31], v48 offset:49664
v_mfma_f32_16x16x32_bf16 %[c12], a[0:3], a[44:47], %[c12]
s_add_u32 s49, s49, 1
v_mfma_f32_16x16x32_bf16 %[c13], a[4:7], a[44:47], %[c13]
s_add_u32 m0, s44, 0
v_mfma_f32_16x16x32_bf16 %[c14], a[8:11], a[44:47], %[c14]
ds_read_b128 a[56:59], v39 offset:51200
buffer_load_dwordx4 v32, s[36:39], s45 offen sc1 lds
v_mfma_f32_16x16x32_bf16 %[c15], a[12:15], a[44:47], %[c15]
buffer_load_dwordx4 v32, s[36:39], s45 offen offset:1024 sc1 lds
s_waitcnt lgkmcnt(1)
v_mfma_f32_16x16x32_bf16 %[c0], a[16:19], a[48:51], %[c0]
s_add_u32 m0, s44, 8192
v_mfma_f32_16x16x32_bf16 %[c1], a[20:23], a[48:51], %[c1]
v_mfma_f32_16x16x32_bf16 %[c2], a[24:27], a[48:51], %[c2]
ds_read_b128 a[60:63], v39 offset:52224
buffer_load_dwordx4 v37, s[40:43], s46 offen lds
v_mfma_f32_16x16x32_bf16 %[c3], a[28:31], a[48:51], %[c3]
buffer_load_dwordx4 v38, s[40:43], s46 offen offset:1024 lds
v_mfma_f32_16x16x32_bf16 %[c4], a[16:19], a[52:55], %[c4]
s_add_u32 s45, s45, %[xadv]
ds_read_b64_tr_b16 a[0:1], v49
ds_read_b64_tr_b16 a[2:3], v51
v_mfma_f32_16x16x32_bf16 %[c5], a[20:23], a[52:55], %[c5]
s_add_u32 s46, s46, %[wadv]
ds_read_b64_tr_b16 a[4:5], v50
ds_read_b64_tr_b16 a[6:7], v52
v_mfma_f32_16x16x32_bf16 %[c6], a[24:27], a[52:55], %[c6]
s_cmp_eq_u32 s49, 128
ds_read_b128 a[32:35], v40
v_mfma_f32_16x16x32_bf16 %[c7], a[28:31], a[52:55], %[c7]
s_cselect_b32 s40, s52, s40
s_waitcnt lgkmcnt(5)
v_mfma_f32_16x16x32_bf16 %[c8], a[16:19], a[56:59], %[c8]
s_cselect_b32 s41, s53, s41
ds_read_b64_tr_b16 a[8:9], v49 offset:512
ds_read_b64_tr_b16 a[10:11], v51 offset:512
v_mfma_f32_16x16x32_bf16 %[c9], a[20:23], a[56:59], %[c9]
s_cselect_b32 s42, s54, s42
v_mfma_f32_16x16x32_bf16 %[c10], a[24:27], a[56:59], %[c10]
s_cselect_b32 s46, s55, s46
ds_read_b128 a[36:39], v40 offset:1024
v_mfma_f32_16x16x32_bf16 %[c11], a[28:31], a[56:59], %[c11]
ds_read_b64_tr_b16 a[12:13], v50 offset:512
ds_read_b64_tr_b16 a[14:15], v52 offset:512
v_mfma_f32_16x16x32_bf16 %[c12], a[16:19], a[60:63], %[c12]
s_add_u32 s49, s49, 1
v_mfma_f32_16x16x32_bf16 %[c13], a[20:23], a[60:63], %[c13]
s_add_u32 m0, s44, 16384
v_mfma_f32_16x16x32_bf16 %[c14], a[24:27], a[60:63], %[c14]
ds_read_b128 a[40:43], v40 offset:2048
buffer_load_dwordx4 v32, s[36:39], s45 offen sc1 lds
v_mfma_f32_16x16x32_bf16 %[c15], a[28:31], a[60:63], %[c15]
buffer_load_dwordx4 v32, s[36:39], s45 offen offset:1024 sc1 lds
s_waitcnt lgkmcnt(1)
v_mfma_f32_16x16x32_bf16 %[c0], a[0:3], a[32:35], %[c0]
s_add_u32 m0, s44, 24576
v_mfma_f32_16x16x32_bf16 %[c1], a[4:7], a[32:35], %[c1]
buffer_load_dwordx4 v37, s[40:43], s46 offen lds
v_mfma_f32_16x16x32_bf16 %[c2], a[8:11], a[32:35], %[c2]
ds_read_b128 a[44:47], v40 offset:3072
buffer_load_dwordx4 v38, s[40:43], s46 offen offset:1024 lds
v_mfma_f32_16x16x32_bf16 %[c3], a[12:15], a[32:35], %[c3]
s_add_u32 s45, s45, %[xadv]
v_mfma_f32_16x16x32_bf16 %[c4], a[0:3], a[36:39], %[c4]
s_add_u32 s46, s46, %[wadv]
ds_read_b64_tr_b16 a[16:17], v49 offset:16384
ds_read_b64_tr_b16 a[18:19], v51 offset:16384
v_mfma_f32_16x16x32_bf16 %[c5], a[4:7], a[36:39], %[c5]
ds_read_b64_tr_b16 a[20:21], v50 offset:16384
ds_read_b64_tr_b16 a[22:23], v52 offset:16384
v_mfma_f32_16x16x32_bf16 %[c6], a[8:11], a[36:39], %[c6]
s_waitcnt vmcnt(16)
s_barrier
ds_read_b128 a[48:51], v40 offset:16384
v_mfma_f32_16x16x32_bf16 %[c7], a[12:15], a[36:39], %[c7]
s_cmp_eq_u32 s49, 128
s_waitcnt lgkmcnt(5)
v_mfma_f32_16x16x32_bf16 %[c8], a[0:3], a[40:43], %[c8]
s_cselect_b32 s40, s52, s40
ds_read_b64_tr_b16 a[24:25], v49 offset:16896
ds_read_b64_tr_b16 a[26:27], v51 offset:16896
v_mfma_f32_16x16x32_bf16 %[c9], a[4:7], a[40:43], %[c9]
s_cselect_b32 s41, s53, s41
v_mfma_f32_16x16x32_bf16 %[c10], a[8:11], a[40:43], %[c10]
s_cselect_b32 s42, s54, s42
ds_read_b128 a[52:55], v40 offset:17408
v_mfma_f32_16x16x32_bf16 %[c11], a[12:15], a[40:43], %[c11]
s_cselect_b32 s46, s55, s46
ds_read_b64_tr_b16 a[28:29], v50 offset:16896
ds_read_b64_tr_b16 a[30:31], v52 offset:16896
v_mfma_f32_16x16x32_bf16 %[c12], a[0:3], a[44:47], %[c12]
s_add_u32 s49, s49, 1
v_mfma_f32_16x16x32_bf16 %[c13], a[4:7], a[44:47], %[c13]
s_add_u32 m0, s44, 32768
v_mfma_f32_16x16x32_bf16 %[c14], a[8:11], a[44:47], %[c14]
ds_read_b128 a[56:59], v40 offset:18432
buffer_load_dwordx4 v32, s[36:39], s45 offen sc1 lds
v_mfma_f32_16x16x32_bf16 %[c15], a[12:15], a[44:47], %[c15]
buffer_load_dwordx4 v32, s[36:39], s45 offen offset:1024 sc1 lds
s_waitcnt lgkmcnt(1)
v_mfma_f32_16x16x32_bf16 %[c0], a[16:19], a[48:51], %[c0]
s_add_u32 m0, s44, 40960
v_mfma_f32_16x16x32_bf16 %[c1], a[20:23], a[48:51], %[c1]
v_mfma_f32_16x16x32_bf16 %[c2], a[24:27], a[48:51], %[c2]
ds_read_b128 a[60:63], v40 offset:19456
buffer_load_dwordx4 v37, s[40:43], s46 offen lds
v_mfma_f32_16x16x32_bf16 %[c3], a[28:31], a[48:51], %[c3]
buffer_load_dwordx4 v38, s[40:43], s46 offen offset:1024 lds
v_mfma_f32_16x16x32_bf16 %[c4], a[16:19], a[52:55], %[c4]
s_add_u32 s45, s45, %[xadv]
ds_read_b64_tr_b16 a[0:1], v49 offset:32768
ds_read_b64_tr_b16 a[2:3], v51 offset:32768
v_mfma_f32_16x16x32_bf16 %[c5], a[20:23], a[52:55], %[c5]
s_add_u32 s46, s46, %[wadv]
ds_read_b64_tr_b16 a[4:5], v50 offset:32768
ds_read_b64_tr_b16 a[6:7], v52 offset:32768
v_mfma_f32_16x16x32_bf16 %[c6], a[24:27], a[52:55], %[c6]
s_cmp_eq_u32 s49, 128
ds_read_b128 a[32:35], v40 offset:32768
v_mfma_f32_16x16x32_bf16 %[c7], a[28:31], a[52:55], %[c7]
s_cselect_b32 s40, s52, s40
s_waitcnt lgkmcnt(5)
v_mfma_f32_16x16x32_bf16 %[c8], a[16:19], a[56:59], %[c8]
s_cselect_b32 s41, s53, s41
ds_read_b64_tr_b16 a[8:9], v49 offset:33280
ds_read_b64_tr_b16 a[10:11], v51 offset:33280
v_mfma_f32_16x16x32_bf16 %[c9], a[20:23], a[56:59], %[c9]
s_cselect_b32 s42, s54, s42
v_mfma_f32_16x16x32_bf16 %[c10], a[24:27], a[56:59], %[c10]
s_cselect_b32 s46, s55, s46
ds_read_b128 a[36:39], v40 offset:33792
v_mfma_f32_16x16x32_bf16 %[c11], a[28:31], a[56:59], %[c11]
ds_read_b64_tr_b16 a[12:13], v50 offset:33280
ds_read_b64_tr_b16 a[14:15], v52 offset:33280
v_mfma_f32_16x16x32_bf16 %[c12], a[16:19], a[60:63], %[c12]
s_add_u32 s49, s49, 1
v_mfma_f32_16x16x32_bf16 %[c13], a[20:23], a[60:63], %[c13]
s_add_u32 m0, s44, 49152
v_mfma_f32_16x16x32_bf16 %[c14], a[24:27], a[60:63], %[c14]
ds_read_b128 a[40:43], v40 offset:34816
buffer_load_dwordx4 v32, s[36:39], s45 offen sc1 lds
v_mfma_f32_16x16x32_bf16 %[c15], a[28:31], a[60:63], %[c15]
buffer_load_dwordx4 v32, s[36:39], s45 offen offset:1024 sc1 lds
s_waitcnt lgkmcnt(1)
v_mfma_f32_16x16x32_bf16 %[c0], a[0:3], a[32:35], %[c0]
s_add_u32 m0, s44, 57344
v_mfma_f32_16x16x32_bf16 %[c1], a[4:7], a[32:35], %[c1]
buffer_load_dwordx4 v37, s[40:43], s46 offen lds
v_mfma_f32_16x16x32_bf16 %[c2], a[8:11], a[32:35], %[c2]
ds_read_b128 a[44:47], v40 offset:35840
buffer_load_dwordx4 v38, s[40:43], s46 offen offset:1024 lds
v_mfma_f32_16x16x32_bf16 %[c3], a[12:15], a[32:35], %[c3]
s_add_u32 s45, s45, %[xadv]
v_mfma_f32_16x16x32_bf16 %[c4], a[0:3], a[36:39], %[c4]
s_add_u32 s46, s46, %[wadv]
ds_read_b64_tr_b16 a[16:17], v49 offset:49152
ds_read_b64_tr_b16 a[18:19], v51 offset:49152
v_mfma_f32_16x16x32_bf16 %[c5], a[4:7], a[36:39], %[c5]
ds_read_b64_tr_b16 a[20:21], v50 offset:49152
ds_read_b64_tr_b16 a[22:23], v52 offset:49152
v_mfma_f32_16x16x32_bf16 %[c6], a[8:11], a[36:39], %[c6]
s_waitcnt vmcnt(16)
s_barrier
ds_read_b128 a[48:51], v40 offset:49152
v_mfma_f32_16x16x32_bf16 %[c7], a[12:15], a[36:39], %[c7]
s_cmp_eq_u32 s49, 128
s_waitcnt lgkmcnt(5)
v_mfma_f32_16x16x32_bf16 %[c8], a[0:3], a[40:43], %[c8]
s_cselect_b32 s40, s52, s40
ds_read_b64_tr_b16 a[24:25], v49 offset:49664
ds_read_b64_tr_b16 a[26:27], v51 offset:49664
v_mfma_f32_16x16x32_bf16 %[c9], a[4:7], a[40:43], %[c9]
s_cselect_b32 s41, s53, s41
v_mfma_f32_16x16x32_bf16 %[c10], a[8:11], a[40:43], %[c10]
s_cselect_b32 s42, s54, s42
ds_read_b128 a[52:55], v40 offset:50176
v_mfma_f32_16x16x32_bf16 %[c11], a[12:15], a[40:43], %[c11]
s_cselect_b32 s46, s55, s46
ds_read_b64_tr_b16 a[28:29], v50 offset:49664
ds_read_b64_tr_b16 a[30:31], v52 offset:49664
v_mfma_f32_16x16x32_bf16 %[c12], a[0:3], a[44:47], %[c12]
s_add_u32 s49, s49, 1
v_mfma_f32_16x16x32_bf16 %[c13], a[4:7], a[44:47], %[c13]
s_add_u32 m0, s44, 65536
v_mfma_f32_16x16x32_bf16 %[c14], a[8:11], a[44:47], %[c14]
ds_read_b128 a[56:59], v40 offset:51200
buffer_load_dwordx4 v32, s[36:39], s45 offen sc1 lds
v_mfma_f32_16x16x32_bf16 %[c15], a[12:15], a[44:47], %[c15]
buffer_load_dwordx4 v32, s[36:39], s45 offen offset:1024 sc1 lds
s_waitcnt lgkmcnt(1)
v_mfma_f32_16x16x32_bf16 %[c0], a[16:19], a[48:51], %[c0]
s_add_u32 m0, s44, 73728
v_mfma_f32_16x16x32_bf16 %[c1], a[20:23], a[48:51], %[c1]
v_mfma_f32_16x16x32_bf16 %[c2], a[24:27], a[48:51], %[c2]
ds_read_b128 a[60:63], v40 offset:52224
buffer_load_dwordx4 v37, s[40:43], s46 offen lds
v_mfma_f32_16x16x32_bf16 %[c3], a[28:31], a[48:51], %[c3]
buffer_load_dwordx4 v38, s[40:43], s46 offen offset:1024 lds
v_mfma_f32_16x16x32_bf16 %[c4], a[16:19], a[52:55], %[c4]
s_add_u32 s45, s45, %[xadv]
ds_read_b64_tr_b16 a[0:1], v53
ds_read_b64_tr_b16 a[2:3], v55
v_mfma_f32_16x16x32_bf16 %[c5], a[20:23], a[52:55], %[c5]
s_add_u32 s46, s46, %[wadv]
ds_read_b64_tr_b16 a[4:5], v54
ds_read_b64_tr_b16 a[6:7], v56
v_mfma_f32_16x16x32_bf16 %[c6], a[24:27], a[52:55], %[c6]
s_cmp_eq_u32 s49, 128
ds_read_b128 a[32:35], v41
v_mfma_f32_16x16x32_bf16 %[c7], a[28:31], a[52:55], %[c7]
s_cselect_b32 s40, s52, s40
s_waitcnt lgkmcnt(5)
v_mfma_f32_16x16x32_bf16 %[c8], a[16:19], a[56:59], %[c8]
s_cselect_b32 s41, s53, s41
ds_read_b64_tr_b16 a[8:9], v53 offset:512
ds_read_b64_tr_b16 a[10:11], v55 offset:512
v_mfma_f32_16x16x32_bf16 %[c9], a[20:23], a[56:59], %[c9]
s_cselect_b32 s42, s54, s42
v_mfma_f32_16x16x32_bf16 %[c10], a[24:27], a[56:59], %[c10]
s_cselect_b32 s46, s55, s46
ds_read_b128 a[36:39], v41 offset:1024
v_mfma_f32_16x16x32_bf16 %[c11], a[28:31], a[56:59], %[c11]
ds_read_b64_tr_b16 a[12:13], v54 offset:512
ds_read_b64_tr_b16 a[14:15], v56 offset:512
v_mfma_f32_16x16x32_bf16 %[c12], a[16:19], a[60:63], %[c12]
s_add_u32 s49, s49, 1
v_mfma_f32_16x16x32_bf16 %[c13], a[20:23], a[60:63], %[c13]
s_add_u32 m0, s44, 81920
v_mfma_f32_16x16x32_bf16 %[c14], a[24:27], a[60:63], %[c14]
ds_read_b128 a[40:43], v41 offset:2048
buffer_load_dwordx4 v32, s[36:39], s45 offen sc1 lds
v_mfma_f32_16x16x32_bf16 %[c15], a[28:31], a[60:63], %[c15]
buffer_load_dwordx4 v32, s[36:39], s45 offen offset:1024 sc1 lds
s_waitcnt lgkmcnt(1)
v_mfma_f32_16x16x32_bf16 %[c0], a[0:3], a[32:35], %[c0]
s_add_u32 m0, s44, 90112
)ASM"
R"ASM(v_mfma_f32_16x16x32_bf16 %[c1], a[4:7], a[32:35], %[c1]
buffer_load_dwordx4 v37, s[40:43], s46 offen lds
v_mfma_f32_16x16x32_bf16 %[c2], a[8:11], a[32:35], %[c2]
ds_read_b128 a[44:47], v41 offset:3072
buffer_load_dwordx4 v38, s[40:43], s46 offen offset:1024 lds
v_mfma_f32_16x16x32_bf16 %[c3], a[12:15], a[32:35], %[c3]
s_add_u32 s45, s45, %[xadv]
v_mfma_f32_16x16x32_bf16 %[c4], a[0:3], a[36:39], %[c4]
s_add_u32 s46, s46, %[wadv]
ds_read_b64_tr_b16 a[16:17], v53 offset:16384
ds_read_b64_tr_b16 a[18:19], v55 offset:16384
v_mfma_f32_16x16x32_bf16 %[c5], a[4:7], a[36:39], %[c5]
ds_read_b64_tr_b16 a[20:21], v54 offset:16384
ds_read_b64_tr_b16 a[22:23], v56 offset:16384
v_mfma_f32_16x16x32_bf16 %[c6], a[8:11], a[36:39], %[c6]
s_waitcnt vmcnt(16)
s_barrier
ds_read_b128 a[48:51], v41 offset:16384
v_mfma_f32_16x16x32_bf16 %[c7], a[12:15], a[36:39], %[c7]
s_cmp_eq_u32 s49, 128
s_waitcnt lgkmcnt(5)
v_mfma_f32_16x16x32_bf16 %[c8], a[0:3], a[40:43], %[c8]
s_cselect_b32 s40, s52, s40
ds_read_b64_tr_b16 a[24:25], v53 offset:16896
ds_read_b64_tr_b16 a[26:27], v55 offset:16896
v_mfma_f32_16x16x32_bf16 %[c9], a[4:7], a[40:43], %[c9]
s_cselect_b32 s41, s53, s41
v_mfma_f32_16x16x32_bf16 %[c10], a[8:11], a[40:43], %[c10]
s_cselect_b32 s42, s54, s42
ds_read_b128 a[52:55], v41 offset:17408
v_mfma_f32_16x16x32_bf16 %[c11], a[12:15], a[40:43], %[c11]
s_cselect_b32 s46, s55, s46
ds_read_b64_tr_b16 a[28:29], v54 offset:16896
ds_read_b64_tr_b16 a[30:31], v56 offset:16896
v_mfma_f32_16x16x32_bf16 %[c12], a[0:3], a[44:47], %[c12]
s_add_u32 s49, s49, 1
v_mfma_f32_16x16x32_bf16 %[c13], a[4:7], a[44:47], %[c13]
s_add_u32 m0, s44, 98304
v_mfma_f32_16x16x32_bf16 %[c14], a[8:11], a[44:47], %[c14]
ds_read_b128 a[56:59], v41 offset:18432
buffer_load_dwordx4 v32, s[36:39], s45 offen sc1 lds
v_mfma_f32_16x16x32_bf16 %[c15], a[12:15], a[44:47], %[c15]
buffer_load_dwordx4 v32, s[36:39], s45 offen offset:1024 sc1 lds
s_waitcnt lgkmcnt(1)
v_mfma_f32_16x16x32_bf16 %[c0], a[16:19], a[48:51], %[c0]
s_add_u32 m0, s44, 106496
v_mfma_f32_16x16x32_bf16 %[c1], a[20:23], a[48:51], %[c1]
v_mfma_f32_16x16x32_bf16 %[c2], a[24:27], a[48:51], %[c2]
ds_read_b128 a[60:63], v41 offset:19456
buffer_load_dwordx4 v37, s[40:43], s46 offen lds
v_mfma_f32_16x16x32_bf16 %[c3], a[28:31], a[48:51], %[c3]
buffer_load_dwordx4 v38, s[40:43], s46 offen offset:1024 lds
v_mfma_f32_16x16x32_bf16 %[c4], a[16:19], a[52:55], %[c4]
s_add_u32 s45, s45, %[xadv]
ds_read_b64_tr_b16 a[0:1], v45
ds_read_b64_tr_b16 a[2:3], v47
v_mfma_f32_16x16x32_bf16 %[c5], a[20:23], a[52:55], %[c5]
s_add_u32 s46, s46, %[wadv]
ds_read_b64_tr_b16 a[4:5], v46
ds_read_b64_tr_b16 a[6:7], v48
v_mfma_f32_16x16x32_bf16 %[c6], a[24:27], a[52:55], %[c6]
s_cmp_eq_u32 s49, 128
ds_read_b128 a[32:35], v39
v_mfma_f32_16x16x32_bf16 %[c7], a[28:31], a[52:55], %[c7]
s_cselect_b32 s40, s52, s40
s_waitcnt lgkmcnt(5)
v_mfma_f32_16x16x32_bf16 %[c8], a[16:19], a[56:59], %[c8]
s_cselect_b32 s41, s53, s41
ds_read_b64_tr_b16 a[8:9], v45 offset:512
ds_read_b64_tr_b16 a[10:11], v47 offset:512
v_mfma_f32_16x16x32_bf16 %[c9], a[20:23], a[56:59], %[c9]
s_cselect_b32 s42, s54, s42
v_mfma_f32_16x16x32_bf16 %[c10], a[24:27], a[56:59], %[c10]
s_cselect_b32 s46, s55, s46
ds_read_b128 a[36:39], v39 offset:1024
v_mfma_f32_16x16x32_bf16 %[c11], a[28:31], a[56:59], %[c11]
ds_read_b64_tr_b16 a[12:13], v46 offset:512
ds_read_b64_tr_b16 a[14:15], v48 offset:512
v_mfma_f32_16x16x32_bf16 %[c12], a[16:19], a[60:63], %[c12]
s_add_u32 s49, s49, 1
v_mfma_f32_16x16x32_bf16 %[c13], a[20:23], a[60:63], %[c13]
s_add_u32 m0, s44, 114688
v_mfma_f32_16x16x32_bf16 %[c14], a[24:27], a[60:63], %[c14]
ds_read_b128 a[40:43], v39 offset:2048
buffer_load_dwordx4 v32, s[36:39], s45 offen sc1 lds
v_mfma_f32_16x16x32_bf16 %[c15], a[28:31], a[60:63], %[c15]
buffer_load_dwordx4 v32, s[36:39], s45 offen offset:1024 sc1 lds
s_waitcnt lgkmcnt(1)
v_mfma_f32_16x16x32_bf16 %[c0], a[0:3], a[32:35], %[c0]
s_add_u32 m0, s44, 122880
v_mfma_f32_16x16x32_bf16 %[c1], a[4:7], a[32:35], %[c1]
buffer_load_dwordx4 v37, s[40:43], s46 offen lds
v_mfma_f32_16x16x32_bf16 %[c2], a[8:11], a[32:35], %[c2]
ds_read_b128 a[44:47], v39 offset:3072
buffer_load_dwordx4 v38, s[40:43], s46 offen offset:1024 lds
v_mfma_f32_16x16x32_bf16 %[c3], a[12:15], a[32:35], %[c3]
s_add_u32 s45, s45, %[xadv]
v_mfma_f32_16x16x32_bf16 %[c4], a[0:3], a[36:39], %[c4]
s_add_u32 s46, s46, %[wadv]
ds_read_b64_tr_b16 a[16:17], v45 offset:16384
ds_read_b64_tr_b16 a[18:19], v47 offset:16384
v_mfma_f32_16x16x32_bf16 %[c5], a[4:7], a[36:39], %[c5]
ds_read_b64_tr_b16 a[20:21], v46 offset:16384
ds_read_b64_tr_b16 a[22:23], v48 offset:16384
v_mfma_f32_16x16x32_bf16 %[c6], a[8:11], a[36:39], %[c6]
s_waitcnt vmcnt(16)
s_barrier
ds_read_b128 a[48:51], v39 offset:16384
v_mfma_f32_16x16x32_bf16 %[c7], a[12:15], a[36:39], %[c7]
s_cmp_eq_u32 s49, 128
s_waitcnt lgkmcnt(5)
v_mfma_f32_16x16x32_bf16 %[c8], a[0:3], a[40:43], %[c8]
s_cselect_b32 s40, s52, s40
ds_read_b64_tr_b16 a[24:25], v45 offset:16896
ds_read_b64_tr_b16 a[26:27], v47 offset:16896
v_mfma_f32_16x16x32_bf16 %[c9], a[4:7], a[40:43], %[c9]
s_cselect_b32 s41, s53, s41
v_mfma_f32_16x16x32_bf16 %[c10], a[8:11], a[40:43], %[c10]
s_cselect_b32 s42, s54, s42
ds_read_b128 a[52:55], v39 offset:17408
v_mfma_f32_16x16x32_bf16 %[c11], a[12:15], a[40:43], %[c11]
s_cselect_b32 s46, s55, s46
ds_read_b64_tr_b16 a[28:29], v46 offset:16896
ds_read_b64_tr_b16 a[30:31], v48 offset:16896
v_mfma_f32_16x16x32_bf16 %[c12], a[0:3], a[44:47], %[c12]
s_add_u32 s49, s49, 1
v_mfma_f32_16x16x32_bf16 %[c13], a[4:7], a[44:47], %[c13]
s_add_u32 m0, s44, 131072
v_mfma_f32_16x16x32_bf16 %[c14], a[8:11], a[44:47], %[c14]
ds_read_b128 a[56:59], v39 offset:18432
buffer_load_dwordx4 v32, s[36:39], s45 offen sc1 lds
v_mfma_f32_16x16x32_bf16 %[c15], a[12:15], a[44:47], %[c15]
buffer_load_dwordx4 v32, s[36:39], s45 offen offset:1024 sc1 lds
s_waitcnt lgkmcnt(1)
v_mfma_f32_16x16x32_bf16 %[c0], a[16:19], a[48:51], %[c0]
s_add_u32 m0, s44, 139264
v_mfma_f32_16x16x32_bf16 %[c1], a[20:23], a[48:51], %[c1]
v_mfma_f32_16x16x32_bf16 %[c2], a[24:27], a[48:51], %[c2]
ds_read_b128 a[60:63], v39 offset:19456
buffer_load_dwordx4 v37, s[40:43], s46 offen lds
v_mfma_f32_16x16x32_bf16 %[c3], a[28:31], a[48:51], %[c3]
buffer_load_dwordx4 v38, s[40:43], s46 offen offset:1024 lds
v_mfma_f32_16x16x32_bf16 %[c4], a[16:19], a[52:55], %[c4]
s_add_u32 s45, s45, %[xadv]
ds_read_b64_tr_b16 a[0:1], v45 offset:32768
ds_read_b64_tr_b16 a[2:3], v47 offset:32768
v_mfma_f32_16x16x32_bf16 %[c5], a[20:23], a[52:55], %[c5]
s_add_u32 s46, s46, %[wadv]
ds_read_b64_tr_b16 a[4:5], v46 offset:32768
ds_read_b64_tr_b16 a[6:7], v48 offset:32768
v_mfma_f32_16x16x32_bf16 %[c6], a[24:27], a[52:55], %[c6]
s_cmp_eq_u32 s49, 128
ds_read_b128 a[32:35], v39 offset:32768
v_mfma_f32_16x16x32_bf16 %[c7], a[28:31], a[52:55], %[c7]
s_cselect_b32 s40, s52, s40
s_waitcnt lgkmcnt(5)
v_mfma_f32_16x16x32_bf16 %[c8], a[16:19], a[56:59], %[c8]
s_cselect_b32 s41, s53, s41
ds_read_b64_tr_b16 a[8:9], v45 offset:33280
ds_read_b64_tr_b16 a[10:11], v47 offset:33280
v_mfma_f32_16x16x32_bf16 %[c9], a[20:23], a[56:59], %[c9]
s_cselect_b32 s42, s54, s42
v_mfma_f32_16x16x32_bf16 %[c10], a[24:27], a[56:59], %[c10]
s_cselect_b32 s46, s55, s46
ds_read_b128 a[36:39], v39 offset:33792
v_mfma_f32_16x16x32_bf16 %[c11], a[28:31], a[56:59], %[c11]
ds_read_b64_tr_b16 a[12:13], v46 offset:33280
ds_read_b64_tr_b16 a[14:15], v48 offset:33280
v_mfma_f32_16x16x32_bf16 %[c12], a[16:19], a[60:63], %[c12]
s_add_u32 s49, s49, 1
v_mfma_f32_16x16x32_bf16 %[c13], a[20:23], a[60:63], %[c13]
s_add_u32 m0, s44, 147456
v_mfma_f32_16x16x32_bf16 %[c14], a[24:27], a[60:63], %[c14]
ds_read_b128 a[40:43], v39 offset:34816
buffer_load_dwordx4 v32, s[36:39], s45 offen sc1 lds
v_mfma_f32_16x16x32_bf16 %[c15], a[28:31], a[60:63], %[c15]
buffer_load_dwordx4 v32, s[36:39], s45 offen offset:1024 sc1 lds
s_waitcnt lgkmcnt(1)
v_mfma_f32_16x16x32_bf16 %[c0], a[0:3], a[32:35], %[c0]
s_add_u32 m0, s44, 155648
v_mfma_f32_16x16x32_bf16 %[c1], a[4:7], a[32:35], %[c1]
buffer_load_dwordx4 v37, s[40:43], s46 offen lds
v_mfma_f32_16x16x32_bf16 %[c2], a[8:11], a[32:35], %[c2]
ds_read_b128 a[44:47], v39 offset:35840
buffer_load_dwordx4 v38, s[40:43], s46 offen offset:1024 lds
v_mfma_f32_16x16x32_bf16 %[c3], a[12:15], a[32:35], %[c3]
s_add_u32 s45, s45, %[xadv]
v_mfma_f32_16x16x32_bf16 %[c4], a[0:3], a[36:39], %[c4]
s_add_u32 s46, s46, %[wadv]
ds_read_b64_tr_b16 a[16:17], v45 offset:49152
ds_read_b64_tr_b16 a[18:19], v47 offset:49152
v_mfma_f32_16x16x32_bf16 %[c5], a[4:7], a[36:39], %[c5]
ds_read_b64_tr_b16 a[20:21], v46 offset:49152
ds_read_b64_tr_b16 a[22:23], v48 offset:49152
v_mfma_f32_16x16x32_bf16 %[c6], a[8:11], a[36:39], %[c6]
s_waitcnt vmcnt(16)
s_barrier
ds_read_b128 a[48:51], v39 offset:49152
v_mfma_f32_16x16x32_bf16 %[c7], a[12:15], a[36:39], %[c7]
s_cmp_eq_u32 s49, 128
s_waitcnt lgkmcnt(5)
v_mfma_f32_16x16x32_bf16 %[c8], a[0:3], a[40:43], %[c8]
s_cselect_b32 s40, s52, s40
ds_read_b64_tr_b16 a[24:25], v45 offset:49664
ds_read_b64_tr_b16 a[26:27], v47 offset:49664
v_mfma_f32_16x16x32_bf16 %[c9], a[4:7], a[40:43], %[c9]
s_cselect_b32 s41, s53, s41
v_mfma_f32_16x16x32_bf16 %[c10], a[8:11], a[40:43], %[c10]
s_cselect_b32 s42, s54, s42
ds_read_b128 a[52:55], v39 offset:50176
v_mfma_f32_16x16x32_bf16 %[c11], a[12:15], a[40:43], %[c11]
s_cselect_b32 s46, s55, s46
ds_read_b64_tr_b16 a[28:29], v46 offset:49664
ds_read_b64_tr_b16 a[30:31], v48 offset:49664
v_mfma_f32_16x16x32_bf16 %[c12], a[0:3], a[44:47], %[c12]
s_add_u32 s49, s49, 1
v_mfma_f32_16x16x32_bf16 %[c13], a[4:7], a[44:47], %[c13]
s_add_u32 m0, s44, 0
v_mfma_f32_16x16x32_bf16 %[c14], a[8:11], a[44:47], %[c14]
ds_read_b128 a[56:59], v39 offset:51200
buffer_load_dwordx4 v32, s[36:39], s45 offen sc1 lds
v_mfma_f32_16x16x32_bf16 %[c15], a[12:15], a[44:47], %[c15]
buffer_load_dwordx4 v32, s[36:39], s45 offen offset:1024 sc1 lds
s_waitcnt lgkmcnt(1)
v_mfma_f32_16x16x32_bf16 %[c0], a[16:19], a[48:51], %[c0]
s_add_u32 m0, s44, 8192
v_mfma_f32_16x16x32_bf16 %[c1], a[20:23], a[48:51], %[c1]
v_mfma_f32_16x16x32_bf16 %[c2], a[24:27], a[48:51], %[c2]
ds_read_b128 a[60:63], v39 offset:52224
buffer_load_dwordx4 v37, s[40:43], s46 offen lds
v_mfma_f32_16x16x32_bf16 %[c3], a[28:31], a[48:51], %[c3]
buffer_load_dwordx4 v38, s[40:43], s46 offen offset:1024 lds
v_mfma_f32_16x16x32_bf16 %[c4], a[16:19], a[52:55], %[c4]
s_add_u32 s45, s45, %[xadv]
ds_read_b64_tr_b16 a[0:1], v49
ds_read_b64_tr_b16 a[2:3], v51
v_mfma_f32_16x16x32_bf16 %[c5], a[20:23], a[52:55], %[c5]
s_add_u32 s46, s46, %[wadv]
ds_read_b64_tr_b16 a[4:5], v50
ds_read_b64_tr_b16 a[6:7], v52
v_mfma_f32_16x16x32_bf16 %[c6], a[24:27], a[52:55], %[c6]
s_cmp_eq_u32 s49, 128
ds_read_b128 a[32:35], v40
v_mfma_f32_16x16x32_bf16 %[c7], a[28:31], a[52:55], %[c7]
s_cselect_b32 s40, s52, s40
s_waitcnt lgkmcnt(5)
v_mfma_f32_16x16x32_bf16 %[c8], a[16:19], a[56:59], %[c8]
s_cselect_b32 s41, s53, s41
ds_read_b64_tr_b16 a[8:9], v49 offset:512
ds_read_b64_tr_b16 a[10:11], v51 offset:512
v_mfma_f32_16x16x32_bf16 %[c9], a[20:23], a[56:59], %[c9]
s_cselect_b32 s42, s54, s42
v_mfma_f32_16x16x32_bf16 %[c10], a[24:27], a[56:59], %[c10]
s_cselect_b32 s46, s55, s46
ds_read_b128 a[36:39], v40 offset:1024
v_mfma_f32_16x16x32_bf16 %[c11], a[28:31], a[56:59], %[c11]
ds_read_b64_tr_b16 a[12:13], v50 offset:512
ds_read_b64_tr_b16 a[14:15], v52 offset:512
v_mfma_f32_16x16x32_bf16 %[c12], a[16:19], a[60:63], %[c12]
s_add_u32 s49, s49, 1
v_mfma_f32_16x16x32_bf16 %[c13], a[20:23], a[60:63], %[c13]
s_add_u32 m0, s44, 16384
v_mfma_f32_16x16x32_bf16 %[c14], a[24:27], a[60:63], %[c14]
ds_read_b128 a[40:43], v40 offset:2048
buffer_load_dwordx4 v32, s[36:39], s45 offen sc1 lds
v_mfma_f32_16x16x32_bf16 %[c15], a[28:31], a[60:63], %[c15]
buffer_load_dwordx4 v32, s[36:39], s45 offen offset:1024 sc1 lds
s_waitcnt lgkmcnt(1)
v_mfma_f32_16x16x32_bf16 %[c0], a[0:3], a[32:35], %[c0]
s_add_u32 m0, s44, 24576
v_mfma_f32_16x16x32_bf16 %[c1], a[4:7], a[32:35], %[c1]
buffer_load_dwordx4 v37, s[40:43], s46 offen lds
v_mfma_f32_16x16x32_bf16 %[c2], a[8:11], a[32:35], %[c2]
)ASM"
R"ASM(ds_read_b128 a[44:47], v40 offset:3072
buffer_load_dwordx4 v38, s[40:43], s46 offen offset:1024 lds
v_mfma_f32_16x16x32_bf16 %[c3], a[12:15], a[32:35], %[c3]
s_add_u32 s45, s45, %[xadv]
v_mfma_f32_16x16x32_bf16 %[c4], a[0:3], a[36:39], %[c4]
s_add_u32 s46, s46, %[wadv]
ds_read_b64_tr_b16 a[16:17], v49 offset:16384
ds_read_b64_tr_b16 a[18:19], v51 offset:16384
v_mfma_f32_16x16x32_bf16 %[c5], a[4:7], a[36:39], %[c5]
ds_read_b64_tr_b16 a[20:21], v50 offset:16384
ds_read_b64_tr_b16 a[22:23], v52 offset:16384
v_mfma_f32_16x16x32_bf16 %[c6], a[8:11], a[36:39], %[c6]
s_waitcnt vmcnt(16)
s_barrier
ds_read_b128 a[48:51], v40 offset:16384
v_mfma_f32_16x16x32_bf16 %[c7], a[12:15], a[36:39], %[c7]
s_waitcnt lgkmcnt(5)
v_mfma_f32_16x16x32_bf16 %[c8], a[0:3], a[40:43], %[c8]
ds_read_b64_tr_b16 a[24:25], v49 offset:16896
ds_read_b64_tr_b16 a[26:27], v51 offset:16896
v_mfma_f32_16x16x32_bf16 %[c9], a[4:7], a[40:43], %[c9]
v_mfma_f32_16x16x32_bf16 %[c10], a[8:11], a[40:43], %[c10]
ds_read_b128 a[52:55], v40 offset:17408
v_mfma_f32_16x16x32_bf16 %[c11], a[12:15], a[40:43], %[c11]
ds_read_b64_tr_b16 a[28:29], v50 offset:16896
ds_read_b64_tr_b16 a[30:31], v52 offset:16896
v_mfma_f32_16x16x32_bf16 %[c12], a[0:3], a[44:47], %[c12]
v_mfma_f32_16x16x32_bf16 %[c13], a[4:7], a[44:47], %[c13]
v_mfma_f32_16x16x32_bf16 %[c14], a[8:11], a[44:47], %[c14]
ds_read_b128 a[56:59], v40 offset:18432
v_mfma_f32_16x16x32_bf16 %[c15], a[12:15], a[44:47], %[c15]
s_waitcnt lgkmcnt(1)
v_mfma_f32_16x16x32_bf16 %[c0], a[16:19], a[48:51], %[c0]
v_mfma_f32_16x16x32_bf16 %[c1], a[20:23], a[48:51], %[c1]
v_mfma_f32_16x16x32_bf16 %[c2], a[24:27], a[48:51], %[c2]
ds_read_b128 a[60:63], v40 offset:19456
v_mfma_f32_16x16x32_bf16 %[c3], a[28:31], a[48:51], %[c3]
v_mfma_f32_16x16x32_bf16 %[c4], a[16:19], a[52:55], %[c4]
ds_read_b64_tr_b16 a[0:1], v49 offset:32768
ds_read_b64_tr_b16 a[2:3], v51 offset:32768
v_mfma_f32_16x16x32_bf16 %[c5], a[20:23], a[52:55], %[c5]
ds_read_b64_tr_b16 a[4:5], v50 offset:32768
ds_read_b64_tr_b16 a[6:7], v52 offset:32768
v_mfma_f32_16x16x32_bf16 %[c6], a[24:27], a[52:55], %[c6]
ds_read_b128 a[32:35], v40 offset:32768
v_mfma_f32_16x16x32_bf16 %[c7], a[28:31], a[52:55], %[c7]
s_waitcnt lgkmcnt(5)
v_mfma_f32_16x16x32_bf16 %[c8], a[16:19], a[56:59], %[c8]
ds_read_b64_tr_b16 a[8:9], v49 offset:33280
ds_read_b64_tr_b16 a[10:11], v51 offset:33280
v_mfma_f32_16x16x32_bf16 %[c9], a[20:23], a[56:59], %[c9]
v_mfma_f32_16x16x32_bf16 %[c10], a[24:27], a[56:59], %[c10]
ds_read_b128 a[36:39], v40 offset:33792
v_mfma_f32_16x16x32_bf16 %[c11], a[28:31], a[56:59], %[c11]
ds_read_b64_tr_b16 a[12:13], v50 offset:33280
ds_read_b64_tr_b16 a[14:15], v52 offset:33280
v_mfma_f32_16x16x32_bf16 %[c12], a[16:19], a[60:63], %[c12]
v_mfma_f32_16x16x32_bf16 %[c13], a[20:23], a[60:63], %[c13]
v_mfma_f32_16x16x32_bf16 %[c14], a[24:27], a[60:63], %[c14]
ds_read_b128 a[40:43], v40 offset:34816
v_mfma_f32_16x16x32_bf16 %[c15], a[28:31], a[60:63], %[c15]
s_waitcnt lgkmcnt(1)
v_mfma_f32_16x16x32_bf16 %[c0], a[0:3], a[32:35], %[c0]
v_mfma_f32_16x16x32_bf16 %[c1], a[4:7], a[32:35], %[c1]
v_mfma_f32_16x16x32_bf16 %[c2], a[8:11], a[32:35], %[c2]
ds_read_b128 a[44:47], v40 offset:35840
v_mfma_f32_16x16x32_bf16 %[c3], a[12:15], a[32:35], %[c3]
v_mfma_f32_16x16x32_bf16 %[c4], a[0:3], a[36:39], %[c4]
ds_read_b64_tr_b16 a[16:17], v49 offset:49152
ds_read_b64_tr_b16 a[18:19], v51 offset:49152
v_mfma_f32_16x16x32_bf16 %[c5], a[4:7], a[36:39], %[c5]
ds_read_b64_tr_b16 a[20:21], v50 offset:49152
ds_read_b64_tr_b16 a[22:23], v52 offset:49152
v_mfma_f32_16x16x32_bf16 %[c6], a[8:11], a[36:39], %[c6]
s_waitcnt vmcnt(8)
s_barrier
ds_read_b128 a[48:51], v40 offset:49152
v_mfma_f32_16x16x32_bf16 %[c7], a[12:15], a[36:39], %[c7]
s_waitcnt lgkmcnt(5)
v_mfma_f32_16x16x32_bf16 %[c8], a[0:3], a[40:43], %[c8]
ds_read_b64_tr_b16 a[24:25], v49 offset:49664
ds_read_b64_tr_b16 a[26:27], v51 offset:49664
v_mfma_f32_16x16x32_bf16 %[c9], a[4:7], a[40:43], %[c9]
v_mfma_f32_16x16x32_bf16 %[c10], a[8:11], a[40:43], %[c10]
ds_read_b128 a[52:55], v40 offset:50176
v_mfma_f32_16x16x32_bf16 %[c11], a[12:15], a[40:43], %[c11]
ds_read_b64_tr_b16 a[28:29], v50 offset:49664
ds_read_b64_tr_b16 a[30:31], v52 offset:49664
v_mfma_f32_16x16x32_bf16 %[c12], a[0:3], a[44:47], %[c12]
v_mfma_f32_16x16x32_bf16 %[c13], a[4:7], a[44:47], %[c13]
v_mfma_f32_16x16x32_bf16 %[c14], a[8:11], a[44:47], %[c14]
ds_read_b128 a[56:59], v40 offset:51200
v_mfma_f32_16x16x32_bf16 %[c15], a[12:15], a[44:47], %[c15]
s_waitcnt lgkmcnt(1)
v_mfma_f32_16x16x32_bf16 %[c0], a[16:19], a[48:51], %[c0]
v_mfma_f32_16x16x32_bf16 %[c1], a[20:23], a[48:51], %[c1]
v_mfma_f32_16x16x32_bf16 %[c2], a[24:27], a[48:51], %[c2]
ds_read_b128 a[60:63], v40 offset:52224
v_mfma_f32_16x16x32_bf16 %[c3], a[28:31], a[48:51], %[c3]
v_mfma_f32_16x16x32_bf16 %[c4], a[16:19], a[52:55], %[c4]
ds_read_b64_tr_b16 a[0:1], v53
ds_read_b64_tr_b16 a[2:3], v55
v_mfma_f32_16x16x32_bf16 %[c5], a[20:23], a[52:55], %[c5]
ds_read_b64_tr_b16 a[4:5], v54
ds_read_b64_tr_b16 a[6:7], v56
v_mfma_f32_16x16x32_bf16 %[c6], a[24:27], a[52:55], %[c6]
ds_read_b128 a[32:35], v41
v_mfma_f32_16x16x32_bf16 %[c7], a[28:31], a[52:55], %[c7]
s_waitcnt lgkmcnt(5)
v_mfma_f32_16x16x32_bf16 %[c8], a[16:19], a[56:59], %[c8]
ds_read_b64_tr_b16 a[8:9], v53 offset:512
ds_read_b64_tr_b16 a[10:11], v55 offset:512
v_mfma_f32_16x16x32_bf16 %[c9], a[20:23], a[56:59], %[c9]
v_mfma_f32_16x16x32_bf16 %[c10], a[24:27], a[56:59], %[c10]
ds_read_b128 a[36:39], v41 offset:1024
v_mfma_f32_16x16x32_bf16 %[c11], a[28:31], a[56:59], %[c11]
ds_read_b64_tr_b16 a[12:13], v54 offset:512
ds_read_b64_tr_b16 a[14:15], v56 offset:512
v_mfma_f32_16x16x32_bf16 %[c12], a[16:19], a[60:63], %[c12]
v_mfma_f32_16x16x32_bf16 %[c13], a[20:23], a[60:63], %[c13]
v_mfma_f32_16x16x32_bf16 %[c14], a[24:27], a[60:63], %[c14]
ds_read_b128 a[40:43], v41 offset:2048
v_mfma_f32_16x16x32_bf16 %[c15], a[28:31], a[60:63], %[c15]
s_waitcnt lgkmcnt(1)
v_mfma_f32_16x16x32_bf16 %[c0], a[0:3], a[32:35], %[c0]
v_mfma_f32_16x16x32_bf16 %[c1], a[4:7], a[32:35], %[c1]
v_mfma_f32_16x16x32_bf16 %[c2], a[8:11], a[32:35], %[c2]
ds_read_b128 a[44:47], v41 offset:3072
v_mfma_f32_16x16x32_bf16 %[c3], a[12:15], a[32:35], %[c3]
v_mfma_f32_16x16x32_bf16 %[c4], a[0:3], a[36:39], %[c4]
ds_read_b64_tr_b16 a[16:17], v53 offset:16384
ds_read_b64_tr_b16 a[18:19], v55 offset:16384
v_mfma_f32_16x16x32_bf16 %[c5], a[4:7], a[36:39], %[c5]
ds_read_b64_tr_b16 a[20:21], v54 offset:16384
ds_read_b64_tr_b16 a[22:23], v56 offset:16384
v_mfma_f32_16x16x32_bf16 %[c6], a[8:11], a[36:39], %[c6]
s_waitcnt vmcnt(0)
s_barrier
ds_read_b128 a[48:51], v41 offset:16384
v_mfma_f32_16x16x32_bf16 %[c7], a[12:15], a[36:39], %[c7]
s_waitcnt lgkmcnt(5)
v_mfma_f32_16x16x32_bf16 %[c8], a[0:3], a[40:43], %[c8]
ds_read_b64_tr_b16 a[24:25], v53 offset:16896
ds_read_b64_tr_b16 a[26:27], v55 offset:16896
v_mfma_f32_16x16x32_bf16 %[c9], a[4:7], a[40:43], %[c9]
v_mfma_f32_16x16x32_bf16 %[c10], a[8:11], a[40:43], %[c10]
ds_read_b128 a[52:55], v41 offset:17408
v_mfma_f32_16x16x32_bf16 %[c11], a[12:15], a[40:43], %[c11]
ds_read_b64_tr_b16 a[28:29], v54 offset:16896
ds_read_b64_tr_b16 a[30:31], v56 offset:16896
v_mfma_f32_16x16x32_bf16 %[c12], a[0:3], a[44:47], %[c12]
v_mfma_f32_16x16x32_bf16 %[c13], a[4:7], a[44:47], %[c13]
v_mfma_f32_16x16x32_bf16 %[c14], a[8:11], a[44:47], %[c14]
ds_read_b128 a[56:59], v41 offset:18432
v_mfma_f32_16x16x32_bf16 %[c15], a[12:15], a[44:47], %[c15]
s_waitcnt lgkmcnt(1)
v_mfma_f32_16x16x32_bf16 %[c0], a[16:19], a[48:51], %[c0]
v_mfma_f32_16x16x32_bf16 %[c1], a[20:23], a[48:51], %[c1]
v_mfma_f32_16x16x32_bf16 %[c2], a[24:27], a[48:51], %[c2]
ds_read_b128 a[60:63], v41 offset:19456
v_mfma_f32_16x16x32_bf16 %[c3], a[28:31], a[48:51], %[c3]
v_mfma_f32_16x16x32_bf16 %[c4], a[16:19], a[52:55], %[c4]
ds_read_b64_tr_b16 a[0:1], v45
ds_read_b64_tr_b16 a[2:3], v47
v_mfma_f32_16x16x32_bf16 %[c5], a[20:23], a[52:55], %[c5]
ds_read_b64_tr_b16 a[4:5], v46
ds_read_b64_tr_b16 a[6:7], v48
v_mfma_f32_16x16x32_bf16 %[c6], a[24:27], a[52:55], %[c6]
ds_read_b128 a[32:35], v39
v_mfma_f32_16x16x32_bf16 %[c7], a[28:31], a[52:55], %[c7]
s_waitcnt lgkmcnt(5)
v_mfma_f32_16x16x32_bf16 %[c8], a[16:19], a[56:59], %[c8]
ds_read_b64_tr_b16 a[8:9], v45 offset:512
ds_read_b64_tr_b16 a[10:11], v47 offset:512
v_mfma_f32_16x16x32_bf16 %[c9], a[20:23], a[56:59], %[c9]
v_mfma_f32_16x16x32_bf16 %[c10], a[24:27], a[56:59], %[c10]
ds_read_b128 a[36:39], v39 offset:1024
v_mfma_f32_16x16x32_bf16 %[c11], a[28:31], a[56:59], %[c11]
ds_read_b64_tr_b16 a[12:13], v46 offset:512
ds_read_b64_tr_b16 a[14:15], v48 offset:512
v_mfma_f32_16x16x32_bf16 %[c12], a[16:19], a[60:63], %[c12]
v_mfma_f32_16x16x32_bf16 %[c13], a[20:23], a[60:63], %[c13]
v_mfma_f32_16x16x32_bf16 %[c14], a[24:27], a[60:63], %[c14]
ds_read_b128 a[40:43], v39 offset:2048
v_mfma_f32_16x16x32_bf16 %[c15], a[28:31], a[60:63], %[c15]
s_waitcnt lgkmcnt(1)
v_mfma_f32_16x16x32_bf16 %[c0], a[0:3], a[32:35], %[c0]
v_mfma_f32_16x16x32_bf16 %[c1], a[4:7], a[32:35], %[c1]
v_mfma_f32_16x16x32_bf16 %[c2], a[8:11], a[32:35], %[c2]
ds_read_b128 a[44:47], v39 offset:3072
v_mfma_f32_16x16x32_bf16 %[c3], a[12:15], a[32:35], %[c3]
v_mfma_f32_16x16x32_bf16 %[c4], a[0:3], a[36:39], %[c4]
ds_read_b64_tr_b16 a[16:17], v45 offset:16384
ds_read_b64_tr_b16 a[18:19], v47 offset:16384
v_mfma_f32_16x16x32_bf16 %[c5], a[4:7], a[36:39], %[c5]
ds_read_b64_tr_b16 a[20:21], v46 offset:16384
ds_read_b64_tr_b16 a[22:23], v48 offset:16384
v_mfma_f32_16x16x32_bf16 %[c6], a[8:11], a[36:39], %[c6]
ds_read_b128 a[48:51], v39 offset:16384
v_mfma_f32_16x16x32_bf16 %[c7], a[12:15], a[36:39], %[c7]
s_waitcnt lgkmcnt(5)
v_mfma_f32_16x16x32_bf16 %[c8], a[0:3], a[40:43], %[c8]
ds_read_b64_tr_b16 a[24:25], v45 offset:16896
ds_read_b64_tr_b16 a[26:27], v47 offset:16896
v_mfma_f32_16x16x32_bf16 %[c9], a[4:7], a[40:43], %[c9]
v_mfma_f32_16x16x32_bf16 %[c10], a[8:11], a[40:43], %[c10]
ds_read_b128 a[52:55], v39 offset:17408
v_mfma_f32_16x16x32_bf16 %[c11], a[12:15], a[40:43], %[c11]
ds_read_b64_tr_b16 a[28:29], v46 offset:16896
ds_read_b64_tr_b16 a[30:31], v48 offset:16896
v_mfma_f32_16x16x32_bf16 %[c12], a[0:3], a[44:47], %[c12]
v_mfma_f32_16x16x32_bf16 %[c13], a[4:7], a[44:47], %[c13]
v_mfma_f32_16x16x32_bf16 %[c14], a[8:11], a[44:47], %[c14]
ds_read_b128 a[56:59], v39 offset:18432
v_mfma_f32_16x16x32_bf16 %[c15], a[12:15], a[44:47], %[c15]
s_waitcnt lgkmcnt(1)
v_mfma_f32_16x16x32_bf16 %[c0], a[16:19], a[48:51], %[c0]
v_mfma_f32_16x16x32_bf16 %[c1], a[20:23], a[48:51], %[c1]
v_mfma_f32_16x16x32_bf16 %[c2], a[24:27], a[48:51], %[c2]
ds_read_b128 a[60:63], v39 offset:19456
v_mfma_f32_16x16x32_bf16 %[c3], a[28:31], a[48:51], %[c3]
v_mfma_f32_16x16x32_bf16 %[c4], a[16:19], a[52:55], %[c4]
v_mfma_f32_16x16x32_bf16 %[c5], a[20:23], a[52:55], %[c5]
v_mfma_f32_16x16x32_bf16 %[c6], a[24:27], a[52:55], %[c6]
v_mfma_f32_16x16x32_bf16 %[c7], a[28:31], a[52:55], %[c7]
s_waitcnt lgkmcnt(0)
v_mfma_f32_16x16x32_bf16 %[c8], a[16:19], a[56:59], %[c8]
v_mfma_f32_16x16x32_bf16 %[c9], a[20:23], a[56:59], %[c9]
v_mfma_f32_16x16x32_bf16 %[c10], a[24:27], a[56:59], %[c10]
v_mfma_f32_16x16x32_bf16 %[c11], a[28:31], a[56:59], %[c11]
v_mfma_f32_16x16x32_bf16 %[c12], a[16:19], a[60:63], %[c12]
v_mfma_f32_16x16x32_bf16 %[c13], a[20:23], a[60:63], %[c13]
v_mfma_f32_16x16x32_bf16 %[c14], a[24:27], a[60:63], %[c14]
v_mfma_f32_16x16x32_bf16 %[c15], a[28:31], a[60:63], %[c15]
s_nop 7
)ASM"
                : ACC_OUT
                : [x0] "s"(LO32(xb)), [x1] "s"(HI16(xb)), [xn] "i"(156 * S * 64), [w0] "s"(LO32(wb)), [w1] "s"(HI16(wb)), [wn] "i"(HD * M * 2 + 896), [w] "s"(w), [lds] "s"((unsigned)(size_t)lds),
                  [xo] "s"((sp * 52 * S + m0 + 32 * w) * 64), [wo] "s"(sp * 52 * 32 * M * 2 + n0 * 2 + 4 * w * M * 2), [xadv] "i"(S * 64), [wadv] "i"(32 * M * 2), [ws] "i"(M * 2),
                  [w20] "s"(LO32(w2)), [w21] "s"(HI16(w2)), [w2n] "i"(FH * M * 2 + 896), [wo2] "s"(n0 * 2 + 4 * w * M * 2), [k0] "s"(sp * 52),
                  [tag0] "s"((unsigned)tag), [tag1] "s"((unsigned)(tag >> 32)), [flags] "s"(ws + OFF_CTL), [fd1] "s"(fd1), [fd2] "s"(fd2), [fd3] "s"(8u * (F_KV + bid)), [pc] "v"(pc), [pu] "v"(pu), [dbg] "s"(ws + OFF_DBG + 524288 + (bid * 4 + w) * 64)
                :
"v32", "v33", "v34", "v35", "v36", "v37", "v38", "v39", "v40", "v41", "v42", "v43", "v44", "v45", "v46", "v47", "v48", "v49", "v50", "v51", "v52", "v53", "v54", "v55", "v56", "v57", "v58", "v59", "v60", "v61", "v62", "v63", "v64", "v65", "v66", "v67", "v68", "v69", "v70", "v71", "a0", "a1", "a2", "a3", "a4", "a5", "a6", "a7", "a8", "a9", "a10", "a11", "a12", "a13", "a14", "a15", "a16", "a17", "a18", "a19", "a20", "a21", "a22", "a23", "a24", "a25", "a26", "a27", "a28", "a29", "a30", "a31", "a32", "a33", "a34", "a35", "a36", "a37", "a38", "a39", "a40", "a41", "a42", "a43", "a44", "a45", "a46", "a47", "a48", "a49", "a50", "a51", "a52", "a53", "a54", "a55", "a56", "a57", "a58", "a59", "a60", "a61", "a62", "a63", "a64", "a65", "a66", "a67", "a68", "a69", "a70", "a71", "a72", "a73", "a74", "a75", "a76", "a77", "a78", "a79", "a80", "a81", "a82", "a83", "a84", "a85", "a86", "a87", "a88", "a89", "a90", "a91", "a92", "a93", "a94", "a95", "s36", "s37", "s38", "s39", "s40", "s41", "s42", "s43", "s44", "s45", "s46", "s47", "s48", "s49", "s50", "s51", "s52", "s53", "s54", "s55", "s56", "s57", "s58", "s59", "s60", "s61", "s62", "s63", "s64", "s65", "s66", "s67", "s68", "s69", "s70", "s71", "s72", "s73", "vcc", "scc", "memory"
            );
            const int r16 = lane & 15, fq = lane >> 4, wr = w >> 1, wc = w & 1;
            CST(10);
            const unsigned pbase = (unsigned)OFF_PART + tile * (3 * 8 * 2 * 4096) + tid * 16, tg = (unsigned)tag;
#pragma unroll
            for (int u = 0; u < 8; ++u) if (u % 3 != sp) {
#pragma unroll
                for (int k = 0; k < 2; ++k) { const f32x4 v = c[2 * u + k];
                    st16_wt(wsr, pbase + ((sp * 8 + u) * 2 + k) * 4096, (u32x4){cvtpk(v[0], v[1]), tg, cvtpk(v[2], v[3]), tg}); }
            }
            CST(11);
            u32x4 g[3][2][2], xv[3];
#pragma unroll
            for (int i = 0; i < 3; ++i) { const int u = sp + 3 * i, uu = u < 8 ? u : sp;
                xv[i] = *(const u32x4*)(ap->x + (size_t)(m0 + 64 * wr + 16 * (uu >> 1) + r16) * M + n0 + 64 * wc + 32 * (uu & 1) + 16 * (fq & 1) + 8 * (fq >> 1)); }
            for (unsigned spins = 0; spins < (1u << 16); ++spins) {
                bool ok = true;
#pragma unroll
                for (int i = 0; i < 3; ++i) { const int u = sp + 3 * i, uu = u < 8 ? u : sp;
#pragma unroll
                    for (int o = 0; o < 2; ++o)
#pragma unroll
                        for (int k = 0; k < 2; ++k) g[i][o][k] = __builtin_bit_cast(u32x4, __builtin_amdgcn_raw_buffer_load_b128(wsr, pbase + ((((sp + 1 + o) % 3) * 8 + uu) * 2 + k) * 4096, 0, 16)); }
#pragma unroll
                for (int i = 0; i < 3; ++i)
#pragma unroll
                    for (int o = 0; o < 2; ++o)
#pragma unroll
                        for (int k = 0; k < 2; ++k) ok = ok && g[i][o][k][1] == tg && g[i][o][k][3] == tg;
                if (__all(ok)) break;
                __builtin_amdgcn_s_sleep(4);
            }
#pragma unroll
            for (int i = 0; i < 3; ++i) if (sp + 3 * i < 8) {
                const int u = sp + 3 * i;
                f32x4 p = sp == 0 ? c[2 * (3 * i)] : sp == 1 ? c[2 * (1 + 3 * i < 8 ? 1 + 3 * i : 0)] : c[2 * (2 + 3 * i < 8 ? 2 + 3 * i : 0)];
                f32x4 b = sp == 0 ? c[2 * (3 * i) + 1] : sp == 1 ? c[2 * (1 + 3 * i < 8 ? 1 + 3 * i : 0) + 1] : c[2 * (2 + 3 * i < 8 ? 2 + 3 * i : 0) + 1];
                const int row = m0 + 64 * wr + 16 * (u >> 1) + r16, col = n0 + 64 * wc + 32 * (u & 1) + 16 * (fq & 1) + 8 * (fq >> 1);
#pragma unroll
                for (int o = 0; o < 2; ++o) {
                    p += (f32x4){bflo(g[i][o][0][0]), bfhi(g[i][o][0][0]), bflo(g[i][o][0][2]), bfhi(g[i][o][0][2])};
                    b += (f32x4){bflo(g[i][o][1][0]), bfhi(g[i][o][1][0]), bflo(g[i][o][1][2]), bfhi(g[i][o][1][2])};
                }
#pragma unroll
                for (int r = 0; r < 4; ++r) { auto t = __builtin_amdgcn_permlane16_swap(__float_as_uint(p[r]), __float_as_uint(b[r]), false, false); p[r] = __uint_as_float(t[0]); b[r] = __uint_as_float(t[1]); }
                *(u32x4*)(ap->out + (size_t)row * M + col) = (u32x4){cvtpk(p[0] + bflo(xv[i][0]), p[1] + bfhi(xv[i][0])), cvtpk(p[2] + bflo(xv[i][1]), p[3] + bfhi(xv[i][1])),
                                                                  cvtpk(b[0] + bflo(xv[i][2]), b[1] + bfhi(xv[i][2])), cvtpk(b[2] + bflo(xv[i][3]), b[3] + bfhi(xv[i][3]))};
            }
            CST(12);
        }
    }
    { PHASE_VARS(); CST(13);
    }
    {
        PHASE_VARS();
        if (bid == 255 && w == 0) {
            for (int k = 0; k < 4; ++k) await_wave(ws, F_KV + 64 * k + lane, tag);
            if (lane == 0) __hip_atomic_store(flagp(ws, F_EPOCH), tag, __ATOMIC_RELAXED, __HIP_MEMORY_SCOPE_AGENT);
        }
    }
    asm volatile(".Lmega_code_end:");
}

extern "C" long long ws_offset(const char* n) {
    if (!strcmp(n, "dbg")) return OFF_DBG; if (!strcmp(n, "kvi")) return OFF_KV; if (!strcmp(n, "xni")) return OFF_XNI; if (!strcmp(n, "cui")) return OFF_CU;
    if (!strcmp(n, "z1s")) return OFF_Z1; if (!strcmp(n, "z2i")) return OFF_Z2I; return -1; }

extern "C" void kernel_launch(void* const* d_in, const int* in_sizes, int n_in, void* d_out, int out_size, void* d_ws, size_t ws_size, hipStream_t stream) {
    if (ws_size < WS_USED) return;
    Args a;
    a.x = (const u16*)d_in[0]; a.ckv = (const u16*)d_in[1]; a.wq = (const u16*)d_in[2]; a.wuk = (const u16*)d_in[3]; a.wuv = (const u16*)d_in[4]; a.wo = (const u16*)d_in[5];
    a.win = (const u16*)d_in[7]; a.wsp = (const u16*)d_in[8]; a.bsp = (const u16*)d_in[9]; a.wout = (const u16*)d_in[10]; a.ws = (char*)d_ws; a.out = (u16*)d_out;
    (void)hipFuncSetAttribute((const void*)k_mega, hipFuncAttributeMaxDynamicSharedMemorySize, 163840);
    k_mega<<<256, 256, 163840, stream>>>(a);
}
```
